```python
import torch

def _softmax(x, axis=-1):
    return torch.softmax(x, dim=axis)

def _gelu(x):
    return torch.nn.functional.gelu(x, approximate='tanh')

def _silu(x):
    return torch.nn.functional.silu(x)

def _sigmoid(x):
    return torch.sigmoid(x)

def _layernorm(x, axis=-1, eps=1e-05):
    m = x.mean(dim=axis, keepdim=True)
    v = x.var(dim=axis, keepdim=True, unbiased=False)
    return (x - m) / torch.sqrt(v + eps)

def _scatter_add(tgt, idx, src, axis=0):
    return tgt.clone().index_add_(axis, idx, src)

def _segment_sum(src, seg, n):
    out = torch.zeros((n,) + src.shape[1:], dtype=src.dtype)
    return out.index_add_(0, seg, src)

def _pad(x, pw):
    flat = tuple((v for pair in reversed(pw) for v in pair))
    return torch.nn.functional.pad(x, flat)
kernel_name = 'adaLN_cond__sliding_block_cached__mixer_gmlp__prompt'

def setup_inputs():

    _w = lambda a: (lambda t: t.to(torch.bfloat16) if t.is_floating_point() else t)(torch.as_tensor(a))

    def _A_setup():
        B, Sq, D = (1, 2048, 448)
        Hq, Hkv, Sk, Dh = (16, 8, 2048, 256)
        assert Hq % Hkv == 0, (Hq, Hkv)
        return {'x': _w((lambda *s: torch.randn(*s, dtype=torch.bfloat16) * 0.1)(B, Sq, D)), 'k_cache': _w((lambda *s: torch.randn(*s, dtype=torch.bfloat16) * 0.8)(B, Hkv, Sk, Dh)), 'v_cache': _w((lambda *s: torch.randn(*s, dtype=torch.bfloat16) * 1.6)(B, Hkv, Sk, Dh)), 'wq': _w((lambda *s: torch.randn(*s, dtype=torch.bfloat16) * 0.8)(D, Hq * Dh) * D ** (-0.5)), 'wo': _w((lambda *s: torch.randn(*s, dtype=torch.bfloat16) * 1.6)(Hq * Dh, D) * (Hq * Dh) ** (-0.5))}
    _A_in = _A_setup()

    def _B_setup():
        B, S, D, F = (1, 2048, 448, 1792)
        Fh = F // 2
        return {'x': _w((lambda *s: torch.randn(*s, dtype=torch.bfloat16) * 0.1)(B, S, D)), 'w_in': _w((lambda *s: torch.randn(*s, dtype=torch.bfloat16) * 1.6)(D, 2 * Fh)), 'w_sp': _w(0.01 * (lambda *s: torch.randn(*s, dtype=torch.bfloat16) * 1.6)(S, S)), 'b_sp': _w(1.0 + 0.01 * (lambda *s: torch.randn(*s, dtype=torch.bfloat16) * 0.2)(S)), 'w_out': _w((lambda *s: torch.randn(*s, dtype=torch.bfloat16) * 0.2)(Fh, D))}
    _B_in = _B_setup()
    return {'A_x': _A_in['x'], 'A_k_cache': _A_in['k_cache'], 'A_v_cache': _A_in['v_cache'], 'A_wq': _A_in['wq'], 'A_wo': _A_in['wo'], 'B_x': _B_in['x'], 'B_w_in': _B_in['w_in'], 'B_w_sp': _B_in['w_sp'], 'B_b_sp': _B_in['b_sp'], 'B_w_out': _B_in['w_out'], 'c': _w((lambda *s: torch.randn(*s, dtype=torch.bfloat16) * 0.8)(1, 448) * 8.0), 'w_mod': _w((lambda *s: torch.randn(*s, dtype=torch.bfloat16) * 0.8)(448, 6 * 448) * 448 ** (-0.5) * 8.0)}

def reference(A_x, A_k_cache, A_v_cache, A_wq, A_wo, B_x, B_w_in, B_w_sp, B_b_sp, B_w_out, c, w_mod):

    def _A_ref(x, k_cache, v_cache, wq, wo):
        B, Sq, D = x.shape
        Hq, Dh = (16, 256)
        Hkv, Sk = (k_cache.shape[1], k_cache.shape[2])
        W = min(4096, max(256, Sk // 4))
        rep = Hq // Hkv
        q = (lambda x, *d: x.permute(*d))((x @ wq).reshape(B, Sq, Hq, Dh), 0, 2, 1, 3)
        k = (lambda x, n, axis: torch.repeat_interleave(x, n, dim=axis))(k_cache[:, :, Sk - W:, :], rep, axis=1)
        v = (lambda x, n, axis: torch.repeat_interleave(x, n, dim=axis))(v_cache[:, :, Sk - W:, :], rep, axis=1)
        scores = torch.einsum('bhqd,bhkd->bhqk', q, k) * Dh ** (-0.5)
        p = _softmax(scores, axis=-1)
        o = torch.einsum('bhqk,bhkd->bhqd', p, v)
        return (lambda x, *d: x.permute(*d))(o, 0, 2, 1, 3).reshape(B, Sq, Hq * Dh) @ wo

    def _B_ref(x, w_in, w_sp, b_sp, w_out):
        B, S, D = x.shape
        F = w_in.shape[-1]
        Fh = F // 2
        z = _gelu(torch.einsum('bsd,df->bsf', x, w_in))
        z1 = z[..., :Fh].clone()
        z2 = _layernorm(z[..., Fh:], axis=-1)
        g = torch.einsum('ts,bsf->btf', w_sp, z2) + b_sp[None, :, None]
        return torch.einsum('bsf,fd->bsd', z1 * g, w_out)
    x = A_x
    mod = (_silu(c) @ w_mod).reshape(c.shape[0], 6, 1, -1)
    sa, ca, ga, sb, cb, gb = (mod[:, 0].clone(), mod[:, 1].clone(), mod[:, 2].clone(), mod[:, 3].clone(), mod[:, 4].clone(), mod[:, 5].clone())
    h = _layernorm(x, axis=-1) * (1 + ca) + sa
    x = x + ga * _A_ref(h, A_k_cache, A_v_cache, A_wq, A_wo)
    h = _layernorm(x, axis=-1) * (1 + cb) + sb
    x = x + gb * _B_ref(h, B_w_in, B_w_sp, B_b_sp, B_w_out)
    return x
```

```cpp
#include <hip/hip_runtime.h>

__global__ __launch_bounds__(256, 1) void fused_layer(const void* x, const void* kc, const void* vc, const void* wq, const void* wo, const void* win, const void* wsp, const void* bsp, const void* wout, const void* c, const void* wmod, void* out, void* ws) {
  auto ka = __builtin_amdgcn_kernarg_segment_ptr();
  unsigned bid = blockIdx.x, tid = threadIdx.x;
  asm volatile(
    "s_load_dwordx16 s[64:79], s[0:1], 0x0\n"
    "s_load_dwordx8 s[80:87], s[0:1], 0x40\n"
    "s_load_dwordx2 s[88:89], s[0:1], 0x60\n"
    "v_and_b32 v1, 63, v0\n"
    "v_lshrrev_b32 v2, 6, v0\n"
    "s_nop 0\n"
    "v_readfirstlane_b32 s40, v2\n"
    "s_and_b32 s41, s2, 7\n"
    "s_lshr_b32 s42, s2, 3\n"
    "s_lshr_b32 s43, s40, 1\n"
    "s_and_b32 s44, s40, 1\n"
    "s_mov_b32 s7, 0xffff0000\n"
    "s_waitcnt lgkmcnt(0)\n"
    "s_mov_b32 s28, s88\n"
    "s_and_b32 s29, s89, 0xffff\n"
    "s_mov_b32 s30, 0xc000000\n"
    "s_mov_b32 s31, 0x20000\n"
    "s_mov_b32 s24, s64\n"
    "s_and_b32 s25, s65, 0xffff\n"
    "s_mov_b32 s26, 0x1c0000\n"
    "s_mov_b32 s27, 0x20000\n"
    "v_mov_b32 v2, 0\n"
    "buffer_load_dwordx2 v[4:5], v2, s[28:31], 0 offen sc1\n"
    "s_getpc_b64 s[32:33]\n"
    ".LCT_a:\n"
    "s_nop 0\n"
    "s_and_b32 s33, s33, 0xffff\n"
    "s_mov_b32 s34, .LCODE_END-.LCT_a\n"
    "s_mov_b32 s35, 0x20000\n"
    "s_lshl_b32 s58, s42, 2\n"
    "s_add_u32 s58, s58, s40\n"
    "s_lshl_b32 s58, s58, 10\n"
    "v_min_u32 v3, 7, v1\n"
    "v_lshlrev_b32 v3, 7, v3\n"
    "v_add_u32 v3, s58, v3\n"
    "buffer_load_dword a219, v3, s[32:35], 0 offen\n"
    "s_lshr_b32 s58, s42, 2\n"
    "s_lshl_b32 s58, s58, 5\n"
    "s_lshl_b32 s97, s41, 2\n"
    "s_add_u32 s97, s97, s58\n"
    "s_and_b32 s58, s42, 3\n"
    "s_add_u32 s97, s97, s58\n"
    "s_cmp_lt_u32 s97, 168\n"
    "s_cbranch_scc1 .LF_xa_1\n"
    "v_lshlrev_b32 v6, 4, v1\n"
    "s_lshl_b32 s58, s42, 6\n"
    "s_lshl_b32 s59, s40, 4\n"
    "s_add_u32 s58, s58, s59\n"
    "s_mul_i32 s58, s58, 0x380\n"
    "s_mul_i32 s59, s40, 0x3800\n"
    "s_add_u32 s59, s59, 0x18000\n"
    "s_mov_b32 exec_lo, 0xffffffff\n"
    "s_mov_b32 exec_hi, 0xffffff\n"
    "s_add_u32 m0, s59, 0\n"
    "v_xor_b32 v7, 0, v6\n"
    "buffer_load_dwordx4 v7, s[24:27], s58 offen lds\n"
    "buffer_load_dwordx4 v7, s[24:27], s58 offen offset:896 lds\n"
    "v_xor_b32 v7, 16, v6\n"
    "buffer_load_dwordx4 v7, s[24:27], s58 offen offset:1792 lds\n"
    "buffer_load_dwordx4 v7, s[24:27], s58 offen offset:2688 lds\n"
    "s_add_u32 s58, s58, 0xe00\n"
    "s_add_u32 m0, s59, 0xe00\n"
    "v_xor_b32 v7, 32, v6\n"
    "buffer_load_dwordx4 v7, s[24:27], s58 offen lds\n"
    "buffer_load_dwordx4 v7, s[24:27], s58 offen offset:896 lds\n"
    "v_xor_b32 v7, 48, v6\n"
    "buffer_load_dwordx4 v7, s[24:27], s58 offen offset:1792 lds\n"
    "buffer_load_dwordx4 v7, s[24:27], s58 offen offset:2688 lds\n"
    "s_add_u32 s58, s58, 0xe00\n"
    "s_add_u32 m0, s59, 0x1c00\n"
    "v_xor_b32 v7, 64, v6\n"
    "buffer_load_dwordx4 v7, s[24:27], s58 offen lds\n"
    "buffer_load_dwordx4 v7, s[24:27], s58 offen offset:896 lds\n"
    "v_xor_b32 v7, 0x50, v6\n"
    "buffer_load_dwordx4 v7, s[24:27], s58 offen offset:1792 lds\n"
    "buffer_load_dwordx4 v7, s[24:27], s58 offen offset:2688 lds\n"
    "s_add_u32 s58, s58, 0xe00\n"
    "s_add_u32 m0, s59, 0x2a00\n"
    "v_xor_b32 v7, 0x60, v6\n"
    "buffer_load_dwordx4 v7, s[24:27], s58 offen lds\n"
    "buffer_load_dwordx4 v7, s[24:27], s58 offen offset:896 lds\n"
    "v_xor_b32 v7, 0x70, v6\n"
    "buffer_load_dwordx4 v7, s[24:27], s58 offen offset:1792 lds\n"
    "buffer_load_dwordx4 v7, s[24:27], s58 offen offset:2688 lds\n"
    "s_add_u32 s58, s58, 0xe00\n"
    "s_mov_b64 exec, -1\n"
    ".LF_xa_1:\n"
    "s_nop 4\n"
    "s_cmp_ge_u32 s97, 168\n"
    "s_cbranch_scc1 .LF_nomod_3\n"
    "s_mov_b32 s32, s82\n"
    "s_and_b32 s33, s83, 0xffff\n"
    "s_mov_b32 s34, 0x380\n"
    "s_mov_b32 s35, 0x20000\n"
    "s_mov_b32 s36, s84\n"
    "s_and_b32 s37, s85, 0xffff\n"
    "s_mov_b32 s38, 0x24c000\n"
    "s_mov_b32 s39, 0x20000\n"
    "v_lshlrev_b32 v6, 4, v1\n"
    "buffer_load_dwordx4 v[12:15], v6, s[32:35], 0 offen\n"
    "v_lshrrev_b32 v7, 1, v1\n"
    "v_mul_u32_u24 v7, 0x1500, v7\n"
    "v_and_b32 v8, 1, v1\n"
    "v_lshl_add_u32 v7, v8, 4, v7\n"
    "s_mul_i32 s58, s40, 0xa8000\n"
    "s_lshl_b32 s59, s97, 5\n"
    "s_add_u32 s58, s58, s59\n"
    "v_add_u32 v7, s58, v7\n"
    "s_lshl_b32 s59, s40, 12\n"
    "s_add_u32 m0, s59, 0x14000\n"
    "s_mov_b32 s60, 0\n"
    "buffer_load_dwordx4 v7, s[36:39], s60 offen lds\n"
    "s_mov_b32 s60, 0x29c00\n"
    "buffer_load_dwordx4 v7, s[36:39], s60 offen offset:1024 lds\n"
    "s_mov_b32 s60, 0x53800\n"
    "buffer_load_dwordx4 v7, s[36:39], s60 offen offset:2048 lds\n"
    "s_mov_b32 s60, 0x7d400\n"
    "buffer_load_dwordx4 v7, s[36:39], s60 offen offset:3072 lds\n"
    "v_lshlrev_b32 v6, 4, v1\n"
    "s_lshl_b32 s58, s42, 6\n"
    "s_lshl_b32 s59, s40, 4\n"
    "s_add_u32 s58, s58, s59\n"
    "s_mul_i32 s58, s58, 0x380\n"
    "s_mul_i32 s59, s40, 0x3800\n"
    "s_add_u32 s59, s59, 0x18000\n"
    "s_mov_b32 exec_lo, 0xffffffff\n"
    "s_mov_b32 exec_hi, 0xffffff\n"
    "s_add_u32 m0, s59, 0\n"
    "v_xor_b32 v7, 0, v6\n"
    "buffer_load_dwordx4 v7, s[24:27], s58 offen lds\n"
    "buffer_load_dwordx4 v7, s[24:27], s58 offen offset:896 lds\n"
    "v_xor_b32 v7, 16, v6\n"
    "buffer_load_dwordx4 v7, s[24:27], s58 offen offset:1792 lds\n"
    "buffer_load_dwordx4 v7, s[24:27], s58 offen offset:2688 lds\n"
    "s_add_u32 s58, s58, 0xe00\n"
    "s_add_u32 m0, s59, 0xe00\n"
    "v_xor_b32 v7, 32, v6\n"
    "buffer_load_dwordx4 v7, s[24:27], s58 offen lds\n"
    "buffer_load_dwordx4 v7, s[24:27], s58 offen offset:896 lds\n"
    "v_xor_b32 v7, 48, v6\n"
    "buffer_load_dwordx4 v7, s[24:27], s58 offen offset:1792 lds\n"
    "buffer_load_dwordx4 v7, s[24:27], s58 offen offset:2688 lds\n"
    "s_add_u32 s58, s58, 0xe00\n"
    "s_add_u32 m0, s59, 0x1c00\n"
    "v_xor_b32 v7, 64, v6\n"
    "buffer_load_dwordx4 v7, s[24:27], s58 offen lds\n"
    "buffer_load_dwordx4 v7, s[24:27], s58 offen offset:896 lds\n"
    "v_xor_b32 v7, 0x50, v6\n"
    "buffer_load_dwordx4 v7, s[24:27], s58 offen offset:1792 lds\n"
    "buffer_load_dwordx4 v7, s[24:27], s58 offen offset:2688 lds\n"
    "s_add_u32 s58, s58, 0xe00\n"
    "s_add_u32 m0, s59, 0x2a00\n"
    "v_xor_b32 v7, 0x60, v6\n"
    "buffer_load_dwordx4 v7, s[24:27], s58 offen lds\n"
    "buffer_load_dwordx4 v7, s[24:27], s58 offen offset:896 lds\n"
    "v_xor_b32 v7, 0x70, v6\n"
    "buffer_load_dwordx4 v7, s[24:27], s58 offen offset:1792 lds\n"
    "buffer_load_dwordx4 v7, s[24:27], s58 offen offset:2688 lds\n"
    "s_add_u32 s58, s58, 0xe00\n"
    "s_mov_b64 exec, -1\n"
    "s_cmp_lg_u32 s40, 0\n"
    "s_cbranch_scc1 .LF_w0_4\n"
    "s_waitcnt vmcnt(20)\n"
    "v_lshlrev_b32 v16, 16, v12\n"
    "v_and_b32 v17, 0xffff0000, v12\n"
    "v_lshlrev_b32 v18, 16, v13\n"
    "v_and_b32 v19, 0xffff0000, v13\n"
    "v_lshlrev_b32 v20, 16, v14\n"
    "v_and_b32 v21, 0xffff0000, v14\n"
    "v_lshlrev_b32 v22, 16, v15\n"
    "v_and_b32 v23, 0xffff0000, v15\n"
    "v_mul_f32 v24, 0xbfb8aa3b, v16\n"
    "v_mul_f32 v25, 0xbfb8aa3b, v17\n"
    "v_mul_f32 v26, 0xbfb8aa3b, v18\n"
    "v_mul_f32 v27, 0xbfb8aa3b, v19\n"
    "v_exp_f32 v24, v24\n"
    "v_exp_f32 v25, v25\n"
    "v_exp_f32 v26, v26\n"
    "v_exp_f32 v27, v27\n"
    "v_add_f32 v24, 1.0, v24\n"
    "v_add_f32 v25, 1.0, v25\n"
    "v_add_f32 v26, 1.0, v26\n"
    "v_add_f32 v27, 1.0, v27\n"
    "v_rcp_f32 v24, v24\n"
    "v_rcp_f32 v25, v25\n"
    "v_rcp_f32 v26, v26\n"
    "v_rcp_f32 v27, v27\n"
    "v_mul_f32 v16, v16, v24\n"
    "v_mul_f32 v17, v17, v25\n"
    "v_mul_f32 v18, v18, v26\n"
    "v_mul_f32 v19, v19, v27\n"
    "v_mul_f32 v24, 0xbfb8aa3b, v20\n"
    "v_mul_f32 v25, 0xbfb8aa3b, v21\n"
    "v_mul_f32 v26, 0xbfb8aa3b, v22\n"
    "v_mul_f32 v27, 0xbfb8aa3b, v23\n"
    "v_exp_f32 v24, v24\n"
    "v_exp_f32 v25, v25\n"
    "v_exp_f32 v26, v26\n"
    "v_exp_f32 v27, v27\n"
    "v_add_f32 v24, 1.0, v24\n"
    "v_add_f32 v25, 1.0, v25\n"
    "v_add_f32 v26, 1.0, v26\n"
    "v_add_f32 v27, 1.0, v27\n"
    "v_rcp_f32 v24, v24\n"
    "v_rcp_f32 v25, v25\n"
    "v_rcp_f32 v26, v26\n"
    "v_rcp_f32 v27, v27\n"
    "v_mul_f32 v20, v20, v24\n"
    "v_mul_f32 v21, v21, v25\n"
    "v_mul_f32 v22, v22, v26\n"
    "v_mul_f32 v23, v23, v27\n"
    "v_cvt_pk_bf16_f32 v12, v16, v17\n"
    "v_cvt_pk_bf16_f32 v13, v18, v19\n"
    "v_cvt_pk_bf16_f32 v14, v20, v21\n"
    "v_cvt_pk_bf16_f32 v15, v22, v23\n"
    "v_add_u32 v6, 0x13c00, v6\n"
    "s_mov_b32 exec_lo, 0xffffffff\n"
    "s_mov_b32 exec_hi, 0xffffff\n"
    "ds_write_b128 v6, v[12:15]\n"
    "s_mov_b64 exec, -1\n"
    ".LF_w0_4:\n"
    "s_nop 4\n"
    "s_waitcnt vmcnt(16)\n"
    "s_barrier\n"
    "s_cmp_lg_u32 s40, 0\n"
    "s_cbranch_scc1 .LF_w0_5\n"
    "v_lshrrev_b32 v8, 4, v1\n"
    "v_bfe_u32 v6, v1, 2, 2\n"
    "v_lshl_add_u32 v6, v8, 3, v6\n"
    "v_lshlrev_b32 v6, 5, v6\n"
    "v_and_b32 v7, 3, v1\n"
    "v_lshl_add_u32 v6, v7, 3, v6\n"
    "v_add_u32 v6, 0x14000, v6\n"
    "v_lshlrev_b32 v7, 4, v8\n"
    "v_add_u32 v7, 0x13c00, v7\n"
    "ds_read_b64_tr_b16 v[32:33], v6\n"
    "ds_read_b64_tr_b16 v[34:35], v6 offset:128\n"
    "ds_read_b128 v[36:39], v7\n"
    "ds_read_b64_tr_b16 v[40:41], v6 offset:1024\n"
    "ds_read_b64_tr_b16 v[42:43], v6 offset:1152\n"
    "ds_read_b128 v[44:47], v7 offset:64\n"
    "ds_read_b64_tr_b16 v[48:49], v6 offset:2048\n"
    "ds_read_b64_tr_b16 v[50:51], v6 offset:2176\n"
    "ds_read_b128 v[52:55], v7 offset:128\n"
    "ds_read_b64_tr_b16 v[56:57], v6 offset:3072\n"
    "ds_read_b64_tr_b16 v[58:59], v6 offset:3200\n"
    "ds_read_b128 v[60:63], v7 offset:192\n"
    "s_waitcnt lgkmcnt(9)\n"
    "v_mfma_f32_16x16x32_bf16 v[12:15], v[32:35], v[36:39], 0\n"
    "ds_read_b64_tr_b16 v[64:65], v6 offset:4096\n"
    "ds_read_b64_tr_b16 v[66:67], v6 offset:4224\n"
    "ds_read_b128 v[68:71], v7 offset:256\n"
    "s_waitcnt lgkmcnt(9)\n"
    "v_mfma_f32_16x16x32_bf16 v[12:15], v[40:43], v[44:47], v[12:15]\n"
    "ds_read_b64_tr_b16 v[72:73], v6 offset:5120\n"
    "ds_read_b64_tr_b16 v[74:75], v6 offset:5248\n"
    "ds_read_b128 v[76:79], v7 offset:320\n"
    "s_waitcnt lgkmcnt(9)\n"
    "v_mfma_f32_16x16x32_bf16 v[12:15], v[48:51], v[52:55], v[12:15]\n"
    "ds_read_b64_tr_b16 v[80:81], v6 offset:6144\n"
    "ds_read_b64_tr_b16 v[82:83], v6 offset:6272\n"
    "ds_read_b128 v[84:87], v7 offset:384\n"
    "s_waitcnt lgkmcnt(9)\n"
    "v_mfma_f32_16x16x32_bf16 v[12:15], v[56:59], v[60:63], v[12:15]\n"
    "ds_read_b64_tr_b16 v[88:89], v6 offset:7168\n"
    "ds_read_b64_tr_b16 v[90:91], v6 offset:7296\n"
    "ds_read_b128 v[92:95], v7 offset:448\n"
    "s_waitcnt lgkmcnt(9)\n"
    "v_mfma_f32_16x16x32_bf16 v[12:15], v[64:67], v[68:71], v[12:15]\n"
    "ds_read_b64_tr_b16 v[96:97], v6 offset:8192\n"
    "ds_read_b64_tr_b16 v[98:99], v6 offset:8320\n"
    "ds_read_b128 v[100:103], v7 offset:512\n"
    "s_waitcnt lgkmcnt(9)\n"
    "v_mfma_f32_16x16x32_bf16 v[12:15], v[72:75], v[76:79], v[12:15]\n"
    "ds_read_b64_tr_b16 v[104:105], v6 offset:9216\n"
    "ds_read_b64_tr_b16 v[106:107], v6 offset:9344\n"
    "ds_read_b128 v[108:111], v7 offset:576\n"
    "s_waitcnt lgkmcnt(9)\n"
    "v_mfma_f32_16x16x32_bf16 v[12:15], v[80:83], v[84:87], v[12:15]\n"
    "ds_read_b64_tr_b16 v[112:113], v6 offset:10240\n"
    "ds_read_b64_tr_b16 v[114:115], v6 offset:10368\n"
    "ds_read_b128 v[116:119], v7 offset:640\n"
    "s_waitcnt lgkmcnt(9)\n"
    "v_mfma_f32_16x16x32_bf16 v[12:15], v[88:91], v[92:95], v[12:15]\n"
    "ds_read_b64_tr_b16 v[120:121], v6 offset:11264\n"
    "ds_read_b64_tr_b16 v[122:123], v6 offset:11392\n"
    "ds_read_b128 v[124:127], v7 offset:704\n"
    "s_waitcnt lgkmcnt(9)\n"
    "v_mfma_f32_16x16x32_bf16 v[12:15], v[96:99], v[100:103], v[12:15]\n"
    "ds_read_b64_tr_b16 v[128:129], v6 offset:12288\n"
    "ds_read_b64_tr_b16 v[130:131], v6 offset:12416\n"
    "ds_read_b128 v[132:135], v7 offset:768\n"
    "s_waitcnt lgkmcnt(9)\n"
    "v_mfma_f32_16x16x32_bf16 v[12:15], v[104:107], v[108:111], v[12:15]\n"
    "ds_read_b64_tr_b16 v[136:137], v6 offset:13312\n"
    "ds_read_b64_tr_b16 v[138:139], v6 offset:13440\n"
    "ds_read_b128 v[140:143], v7 offset:832\n"
    "s_waitcnt lgkmcnt(9)\n"
    "v_mfma_f32_16x16x32_bf16 v[12:15], v[112:115], v[116:119], v[12:15]\n"
    "s_waitcnt lgkmcnt(6)\n"
    "v_mfma_f32_16x16x32_bf16 v[12:15], v[120:123], v[124:127], v[12:15]\n"
    "s_waitcnt lgkmcnt(3)\n"
    "v_mfma_f32_16x16x32_bf16 v[12:15], v[128:131], v[132:135], v[12:15]\n"
    "s_waitcnt lgkmcnt(0)\n"
    "v_mfma_f32_16x16x32_bf16 v[12:15], v[136:139], v[140:143], v[12:15]\n"
    "s_mul_hi_u32 s58, s97, 0x924924a\n"
    "s_mul_i32 s59, s58, 28\n"
    "s_sub_u32 s59, s97, s59\n"
    "s_lshl_b32 s59, s59, 4\n"
    "s_nop 3\n"
    "v_cvt_pk_bf16_f32 v12, v12, v12\n"
    "v_cvt_pk_bf16_f32 v13, v13, v13\n"
    "v_cvt_pk_bf16_f32 v14, v14, v14\n"
    "v_cvt_pk_bf16_f32 v15, v15, v15\n"
    "v_and_b32 v12, 0xffff0000, v12\n"
    "v_and_b32 v13, 0xffff0000, v13\n"
    "v_and_b32 v14, 0xffff0000, v14\n"
    "v_and_b32 v15, 0xffff0000, v15\n"
    "s_mov_b32 s61, 0\n"
    "s_cmp_eq_u32 s58, 1\n"
    "s_cselect_b32 s61, 1.0, s61\n"
    "s_cmp_eq_u32 s58, 4\n"
    "s_cselect_b32 s61, 1.0, s61\n"
    "v_add_f32 v12, s61, v12\n"
    "v_add_f32 v13, s61, v13\n"
    "v_add_f32 v14, s61, v14\n"
    "v_add_f32 v15, s61, v15\n"
    "v_cvt_pk_bf16_f32 v12, v12, v12\n"
    "v_cvt_pk_bf16_f32 v13, v13, v13\n"
    "v_cvt_pk_bf16_f32 v14, v14, v14\n"
    "v_cvt_pk_bf16_f32 v15, v15, v15\n"
    "v_and_b32 v12, 0xffff0000, v12\n"
    "v_and_b32 v13, 0xffff0000, v13\n"
    "v_and_b32 v14, 0xffff0000, v14\n"
    "v_and_b32 v15, 0xffff0000, v15\n"
    "v_readfirstlane_b32 s90, v4\n"
    "v_readfirstlane_b32 s91, v5\n"
    "s_nop 1\n"
    "s_add_u32 s90, s90, 1\n"
    "s_add_u32 s91, s91, 0x9e3779b9\n"
    "v_mov_b32 v16, v12\n"
    "v_mov_b32 v17, v13\n"
    "v_mov_b32 v18, s90\n"
    "v_mov_b32 v19, s91\n"
    "v_mov_b32 v20, v14\n"
    "v_mov_b32 v21, v15\n"
    "v_mov_b32 v22, s90\n"
    "v_mov_b32 v23, s91\n"
    "s_lshl_b32 s61, s59, 3\n"
    "s_cmp_eq_u32 s58, 0\n"
    "s_cselect_b32 s62, 3584, 0\n"
    "s_add_u32 s61, s61, s62\n"
    "s_add_u32 s61, s61, 0x6000\n"
    "s_cmp_ge_u32 s58, 2\n"
    "s_cselect_b32 s61, 0x40000000, s61\n"
    "v_lshlrev_b32 v7, 5, v8\n"
    "s_mov_b32 exec_lo, 0x10001\n"
    "s_mov_b32 exec_hi, 0x10001\n"
    "buffer_store_dwordx4 v[16:19], v7, s[28:31], s61 offen sc1\n"
    "buffer_store_dwordx4 v[20:23], v7, s[28:31], s61 offen offset:16 sc1\n"
    "s_mov_b32 s60, s58\n"
    "s_cmp_eq_u32 s58, 0\n"
    "s_cselect_b32 s60, 1, s60\n"
    "s_cmp_eq_u32 s58, 1\n"
    "s_cselect_b32 s60, 0, s60\n"
    "s_cmp_eq_u32 s58, 3\n"
    "s_cselect_b32 s60, 4, s60\n"
    "s_cmp_eq_u32 s58, 4\n"
    "s_cselect_b32 s60, 3, s60\n"
    "s_mul_i32 s60, s60, 0x1c0\n"
    "s_add_u32 s60, s60, s59\n"
    "s_lshl_b32 s60, s60, 2\n"
    "s_add_u32 s60, s60, 0x3000\n"
    "v_lshlrev_b32 v6, 4, v8\n"
    "buffer_store_dwordx4 v[12:15], v6, s[28:31], s60 offen sc1\n"
    "s_mov_b64 exec, -1\n"
    ".LF_w0_5:\n"
    "s_nop 4\n"
    ".LF_nomod_3:\n"
    "s_nop 4\n"
    "s_mov_b32 s8, s70\n"
    "s_and_b32 s9, s71, 0xffff\n"
    "s_mov_b32 s10, 0x380000\n"
    "s_mov_b32 s11, 0x20000\n"
    "s_mov_b32 s12, s66\n"
    "s_and_b32 s13, s67, 0xffff\n"
    "s_mov_b32 s14, 0x800000\n"
    "s_mov_b32 s15, 0x20000\n"
    "s_mov_b32 s16, s68\n"
    "s_and_b32 s17, s69, 0xffff\n"
    "s_mov_b32 s18, 0x800000\n"
    "s_mov_b32 s19, 0x20000\n"
    "s_mov_b32 s20, s72\n"
    "s_and_b32 s21, s73, 0xffff\n"
    "s_mov_b32 s22, 0x380000\n"
    "s_mov_b32 s23, 0x20000\n"
    "s_lshl_b32 s45, s40, 12\n"
    "s_mov_b32 s46, 0x3c00\n"
    "s_mov_b32 s53, 0x1800\n"
    "s_mov_b32 s47, 0x7800\n"
    "s_mov_b32 s54, 0x3000\n"
    "s_mov_b32 s48, 0xb400\n"
    "s_mov_b32 s55, 0x4800\n"
    "s_mov_b32 s49, 0xa80\n"
    "s_mov_b32 s50, 0x700\n"
    "s_mov_b32 s51, 0x380\n"
    "s_mov_b32 s52, 0\n"
    "s_mov_b32 s56, 0x3db8aa3b\n"
    "s_mov_b32 s57, 0xbdb8aa3b\n"
    "v_lshrrev_b32 v25, 5, v1\n"
    "v_bfe_u32 v26, v1, 2, 3\n"
    "v_and_b32 v27, 3, v1\n"
    "v_lshlrev_b32 v27, 4, v27\n"
    "v_xor_b32 v28, v26, v25\n"
    "v_lshl_or_b32 v28, v28, 6, v27\n"
    "v_xor_b32 v29, 0x80, v28\n"
    "s_lshl_b32 s58, s44, 16\n"
    "s_lshl_b32 s59, s41, 1\n"
    "s_add_u32 s59, s59, s43\n"
    "s_lshl_b32 s59, s59, 9\n"
    "s_add_u32 s58, s58, s59\n"
    "v_lshl_add_u32 v12, v25, 13, v28\n"
    "v_add_u32 v12, s58, v12\n"
    "v_lshl_add_u32 v13, v25, 13, v29\n"
    "v_add_u32 v13, s58, v13\n"
    "s_lshl_b32 s58, s41, 20\n"
    "s_lshl_b32 s59, s40, 12\n"
    "s_add_u32 s58, s58, s59\n"
    "s_add_u32 s58, s58, 0xc0000\n"
    "v_lshl_add_u32 v15, v25, 9, v28\n"
    "v_add_u32 v15, s58, v15\n"
    "v_lshl_add_u32 v16, v25, 9, v29\n"
    "v_add_u32 v16, s58, v16\n"
    "v_lshrrev_b32 v26, 4, v1\n"
    "s_lshl_b32 s59, s40, 2\n"
    "v_add_u32 v26, s59, v26\n"
    "v_xor_b32 v26, v26, v1\n"
    "v_and_b32 v26, 7, v26\n"
    "v_lshrrev_b32 v27, 3, v1\n"
    "v_lshlrev_b32 v27, 9, v27\n"
    "v_lshl_add_u32 v14, v26, 4, v27\n"
    "s_sub_u32 s58, s58, 0xa80\n"
    "v_add_u32 v14, s58, v14\n"
    "v_bfe_u32 v26, v1, 4, 1\n"
    "v_lshlrev_b32 v26, 2, v26\n"
    "v_xor_b32 v26, v26, v1\n"
    "v_and_b32 v26, 7, v26\n"
    "v_lshlrev_b32 v26, 4, v26\n"
    "v_lshrrev_b32 v27, 3, v1\n"
    "v_mul_u32_u24 v27, 0x380, v27\n"
    "v_add_u32 v17, v26, v27\n"
    "s_lshl_b32 s58, s41, 9\n"
    "s_lshl_b32 s59, s43, 8\n"
    "s_add_u32 s58, s58, s59\n"
    "s_lshl_b32 s59, s44, 5\n"
    "s_add_u32 s58, s58, s59\n"
    "s_mul_i32 s58, s58, 0x380\n"
    "v_add_u32 v17, s58, v17\n"
    "s_add_u32 m0, s45, 0\n"
    "s_nop 0\n"
    "buffer_load_dwordx4 v12, s[8:11], 0 offen lds\n"
    "buffer_load_dwordx4 v13, s[8:11], s46 offen offset:1024 lds\n"
    "buffer_load_dwordx4 v12, s[8:11], s47 offen offset:2048 lds\n"
    "buffer_load_dwordx4 v13, s[8:11], s48 offen offset:3072 lds\n"
    "v_add_u32 v12, 0x20000, v12\n"
    "v_add_u32 v13, 0x20000, v13\n"
    "s_add_u32 m0, s45, 0x4000\n"
    "s_nop 0\n"
    "buffer_load_dwordx4 v12, s[8:11], 0 offen lds\n"
    "buffer_load_dwordx4 v13, s[8:11], s46 offen offset:1024 lds\n"
    "buffer_load_dwordx4 v12, s[8:11], s47 offen offset:2048 lds\n"
    "buffer_load_dwordx4 v13, s[8:11], s48 offen offset:3072 lds\n"
    "v_add_u32 v12, 0x20000, v12\n"
    "v_add_u32 v13, 0x20000, v13\n"
    "s_lshl_b32 s58, s42, 2\n"
    "s_add_u32 s58, s58, s40\n"
    "s_mul_i32 s59, s58, 28\n"
    "v_min_u32 v25, 27, v1\n"
    "v_add_u32 v25, s59, v25\n"
    "v_lshrrev_b32 v9, 3, v25\n"
    "v_lshlrev_b32 v9, 13, v9\n"
    "v_and_b32 v27, 7, v25\n"
    "v_lshl_add_u32 v9, v27, 7, v9\n"
    "s_lshl_b32 s60, s41, 10\n"
    "v_add_u32 v9, s60, v9\n"
    "s_mul_i32 s60, s41, 0x70000\n"
    "v_lshl_add_u32 v10, v25, 7, s60\n"
    "s_lshl_b32 s59, s58, 4\n"
    "v_min_u32 v25, 15, v1\n"
    "v_add_u32 v25, s59, v25\n"
    "s_lshl_b32 s60, s41, 20\n"
    "s_add_u32 s60, s60, 0xc0000\n"
    "v_lshl_add_u32 v11, v25, 7, s60\n"
    "buffer_load_dword a204, v9, s[8:11], 0 offen\n"
    "s_waitcnt vmcnt(11)\n"
    "v_readfirstlane_b32 s90, v4\n"
    "v_readfirstlane_b32 s91, v5\n"
    "s_nop 1\n"
    "s_add_u32 s90, s90, 1\n"
    "s_add_u32 s91, s91, 0x9e3779b9\n"
    "v_bfe_u32 v25, v1, 2, 2\n"
    "v_and_b32 v26, 3, v1\n"
    "v_bfe_u32 v27, v1, 4, 1\n"
    "v_lshrrev_b32 v28, 5, v1\n"
    "v_and_b32 v29, 1, v26\n"
    "v_lshlrev_b32 v29, 4, v29\n"
    "v_lshrrev_b32 v26, 1, v26\n"
    "v_lshl_or_b32 v29, v26, 3, v29\n"
    "v_lshl_or_b32 v29, v27, 5, v29\n"
    "v_lshl_add_u32 v30, v28, 3, v25\n"
    "s_lshl_b32 s58, s43, 13\n"
    "v_xor_b32 v2, 0, v25\n"
    "v_lshl_add_u32 v2, v2, 6, v29\n"
    "v_lshl_add_u32 v2, v30, 9, v2\n"
    "v_add_u32 v2, s58, v2\n"
    "v_xor_b32 v3, 1, v25\n"
    "v_lshl_add_u32 v3, v3, 6, v29\n"
    "v_lshl_add_u32 v3, v30, 9, v3\n"
    "v_add_u32 v3, s58, v3\n"
    "v_xor_b32 v4, 2, v25\n"
    "v_lshl_add_u32 v4, v4, 6, v29\n"
    "v_lshl_add_u32 v4, v30, 9, v4\n"
    "v_add_u32 v4, s58, v4\n"
    "v_xor_b32 v5, 3, v25\n"
    "v_lshl_add_u32 v5, v5, 6, v29\n"
    "v_lshl_add_u32 v5, v30, 9, v5\n"
    "v_add_u32 v5, s58, v5\n"
    "v_lshrrev_b32 v25, 1, v25\n"
    "v_xor_b32 v25, s43, v25\n"
    "v_lshl_add_u32 v29, v25, 6, v29\n"
    "v_lshl_add_u32 v29, v30, 7, v29\n"
    "v_add_u32 v10, s58, v29\n"
    "s_xor_b32 s58, s58, 0x2000\n"
    "v_add_u32 v11, s58, v29\n"
    "v_and_b32 v25, 31, v1\n"
    "v_and_b32 v26, 19, v25\n"
    "v_bfe_u32 v27, v25, 2, 1\n"
    "v_lshl_or_b32 v26, v27, 3, v26\n"
    "v_bfe_u32 v27, v25, 3, 1\n"
    "v_lshl_or_b32 v26, v27, 2, v26\n"
    "v_lshrrev_b32 v28, 5, v1\n"
    "v_bfe_u32 v27, v26, 1, 3\n"
    "v_xor_b32 v27, v27, v28\n"
    "v_lshlrev_b32 v27, 4, v27\n"
    "v_and_b32 v29, 7, v26\n"
    "v_lshl_or_b32 v27, v29, 7, v27\n"
    "v_lshrrev_b32 v29, 3, v26\n"
    "v_lshl_or_b32 v27, v29, 12, v27\n"
    "v_xor_b32 v6, 0, v27\n"
    "v_xor_b32 v7, 32, v27\n"
    "v_xor_b32 v8, 64, v27\n"
    "v_xor_b32 v9, 0x60, v27\n"
    "v_and_b32 v250, 15, v1\n"
    "v_bfe_u32 v251, v1, 1, 3\n"
    "v_lshrrev_b32 v248, 4, v1\n"
    "v_xor_b32 v248, v248, v251\n"
    "v_lshlrev_b32 v248, 4, v248\n"
    "v_mul_u32_u24 v250, 0x380, v250\n"
    "v_add_u32 v248, v248, v250\n"
    "s_mul_i32 s58, s40, 0x3800\n"
    "s_add_u32 s58, s58, 0x18000\n"
    "v_add_u32 v248, s58, v248\n"
    "v_xor_b32 v249, 64, v248\n"
    "s_waitcnt vmcnt(9)\n"
    "ds_read_b128 v[32:35], v248\n"
    "ds_read_b128 v[36:39], v249\n"
    "ds_read_b128 v[40:43], v248 offset:128\n"
    "ds_read_b128 v[44:47], v249 offset:128\n"
    "ds_read_b128 v[48:51], v248 offset:256\n"
    "ds_read_b128 v[52:55], v249 offset:256\n"
    "ds_read_b128 v[56:59], v248 offset:384\n"
    "ds_read_b128 v[60:63], v249 offset:384\n"
    "ds_read_b128 v[64:67], v248 offset:512\n"
    "ds_read_b128 v[68:71], v249 offset:512\n"
    "ds_read_b128 v[72:75], v248 offset:640\n"
    "ds_read_b128 v[76:79], v249 offset:640\n"
    "ds_read_b128 v[80:83], v248 offset:768\n"
    "ds_read_b128 v[84:87], v249 offset:768\n"
    "s_mov_b32 s98, 0x3f803f80\n"
    "v_mov_b32 v252, 0\n"
    "v_mov_b32 v253, 0\n"
    "v_mov_b32 v250, 0\n"
    "v_mov_b32 v251, 0\n"
    "s_waitcnt lgkmcnt(13)\n"
    "v_dot2_f32_bf16 v252, v32, s98, v252\n"
    "v_dot2_f32_bf16 v253, v32, v32, v253\n"
    "v_dot2_f32_bf16 v250, v33, s98, v250\n"
    "v_dot2_f32_bf16 v251, v33, v33, v251\n"
    "v_dot2_f32_bf16 v252, v34, s98, v252\n"
    "v_dot2_f32_bf16 v253, v34, v34, v253\n"
    "v_dot2_f32_bf16 v250, v35, s98, v250\n"
    "v_dot2_f32_bf16 v251, v35, v35, v251\n"
    "s_waitcnt lgkmcnt(12)\n"
    "v_dot2_f32_bf16 v252, v36, s98, v252\n"
    "v_dot2_f32_bf16 v253, v36, v36, v253\n"
    "v_dot2_f32_bf16 v250, v37, s98, v250\n"
    "v_dot2_f32_bf16 v251, v37, v37, v251\n"
    "v_dot2_f32_bf16 v252, v38, s98, v252\n"
    "v_dot2_f32_bf16 v253, v38, v38, v253\n"
    "v_dot2_f32_bf16 v250, v39, s98, v250\n"
    "v_dot2_f32_bf16 v251, v39, v39, v251\n"
    "s_waitcnt lgkmcnt(11)\n"
    "v_dot2_f32_bf16 v252, v40, s98, v252\n"
    "v_dot2_f32_bf16 v253, v40, v40, v253\n"
    "v_dot2_f32_bf16 v250, v41, s98, v250\n"
    "v_dot2_f32_bf16 v251, v41, v41, v251\n"
    "v_dot2_f32_bf16 v252, v42, s98, v252\n"
    "v_dot2_f32_bf16 v253, v42, v42, v253\n"
    "v_dot2_f32_bf16 v250, v43, s98, v250\n"
    "v_dot2_f32_bf16 v251, v43, v43, v251\n"
    "s_waitcnt lgkmcnt(10)\n"
    "v_dot2_f32_bf16 v252, v44, s98, v252\n"
    "v_dot2_f32_bf16 v253, v44, v44, v253\n"
    "v_dot2_f32_bf16 v250, v45, s98, v250\n"
    "v_dot2_f32_bf16 v251, v45, v45, v251\n"
    "v_dot2_f32_bf16 v252, v46, s98, v252\n"
    "v_dot2_f32_bf16 v253, v46, v46, v253\n"
    "v_dot2_f32_bf16 v250, v47, s98, v250\n"
    "v_dot2_f32_bf16 v251, v47, v47, v251\n"
    "s_waitcnt lgkmcnt(9)\n"
    "v_dot2_f32_bf16 v252, v48, s98, v252\n"
    "v_dot2_f32_bf16 v253, v48, v48, v253\n"
    "v_dot2_f32_bf16 v250, v49, s98, v250\n"
    "v_dot2_f32_bf16 v251, v49, v49, v251\n"
    "v_dot2_f32_bf16 v252, v50, s98, v252\n"
    "v_dot2_f32_bf16 v253, v50, v50, v253\n"
    "v_dot2_f32_bf16 v250, v51, s98, v250\n"
    "v_dot2_f32_bf16 v251, v51, v51, v251\n"
    "s_waitcnt lgkmcnt(8)\n"
    "v_dot2_f32_bf16 v252, v52, s98, v252\n"
    "v_dot2_f32_bf16 v253, v52, v52, v253\n"
    "v_dot2_f32_bf16 v250, v53, s98, v250\n"
    "v_dot2_f32_bf16 v251, v53, v53, v251\n"
    "v_dot2_f32_bf16 v252, v54, s98, v252\n"
    "v_dot2_f32_bf16 v253, v54, v54, v253\n"
    "v_dot2_f32_bf16 v250, v55, s98, v250\n"
    "v_dot2_f32_bf16 v251, v55, v55, v251\n"
    "s_waitcnt lgkmcnt(7)\n"
    "v_dot2_f32_bf16 v252, v56, s98, v252\n"
    "v_dot2_f32_bf16 v253, v56, v56, v253\n"
    "v_dot2_f32_bf16 v250, v57, s98, v250\n"
    "v_dot2_f32_bf16 v251, v57, v57, v251\n"
    "v_dot2_f32_bf16 v252, v58, s98, v252\n"
    "v_dot2_f32_bf16 v253, v58, v58, v253\n"
    "v_dot2_f32_bf16 v250, v59, s98, v250\n"
    "v_dot2_f32_bf16 v251, v59, v59, v251\n"
    "s_waitcnt lgkmcnt(6)\n"
    "v_dot2_f32_bf16 v252, v60, s98, v252\n"
    "v_dot2_f32_bf16 v253, v60, v60, v253\n"
    "v_dot2_f32_bf16 v250, v61, s98, v250\n"
    "v_dot2_f32_bf16 v251, v61, v61, v251\n"
    "v_dot2_f32_bf16 v252, v62, s98, v252\n"
    "v_dot2_f32_bf16 v253, v62, v62, v253\n"
    "v_dot2_f32_bf16 v250, v63, s98, v250\n"
    "v_dot2_f32_bf16 v251, v63, v63, v251\n"
    "s_waitcnt lgkmcnt(5)\n"
    "v_dot2_f32_bf16 v252, v64, s98, v252\n"
    "v_dot2_f32_bf16 v253, v64, v64, v253\n"
    "v_dot2_f32_bf16 v250, v65, s98, v250\n"
    "v_dot2_f32_bf16 v251, v65, v65, v251\n"
    "v_dot2_f32_bf16 v252, v66, s98, v252\n"
    "v_dot2_f32_bf16 v253, v66, v66, v253\n"
    "v_dot2_f32_bf16 v250, v67, s98, v250\n"
    "v_dot2_f32_bf16 v251, v67, v67, v251\n"
    "s_waitcnt lgkmcnt(4)\n"
    "v_dot2_f32_bf16 v252, v68, s98, v252\n"
    "v_dot2_f32_bf16 v253, v68, v68, v253\n"
    "v_dot2_f32_bf16 v250, v69, s98, v250\n"
    "v_dot2_f32_bf16 v251, v69, v69, v251\n"
    "v_dot2_f32_bf16 v252, v70, s98, v252\n"
    "v_dot2_f32_bf16 v253, v70, v70, v253\n"
    "v_dot2_f32_bf16 v250, v71, s98, v250\n"
    "v_dot2_f32_bf16 v251, v71, v71, v251\n"
    "s_waitcnt lgkmcnt(3)\n"
    "v_dot2_f32_bf16 v252, v72, s98, v252\n"
    "v_dot2_f32_bf16 v253, v72, v72, v253\n"
    "v_dot2_f32_bf16 v250, v73, s98, v250\n"
    "v_dot2_f32_bf16 v251, v73, v73, v251\n"
    "v_dot2_f32_bf16 v252, v74, s98, v252\n"
    "v_dot2_f32_bf16 v253, v74, v74, v253\n"
    "v_dot2_f32_bf16 v250, v75, s98, v250\n"
    "v_dot2_f32_bf16 v251, v75, v75, v251\n"
    "s_waitcnt lgkmcnt(2)\n"
    "v_dot2_f32_bf16 v252, v76, s98, v252\n"
    "v_dot2_f32_bf16 v253, v76, v76, v253\n"
    "v_dot2_f32_bf16 v250, v77, s98, v250\n"
    "v_dot2_f32_bf16 v251, v77, v77, v251\n"
    "v_dot2_f32_bf16 v252, v78, s98, v252\n"
    "v_dot2_f32_bf16 v253, v78, v78, v253\n"
    "v_dot2_f32_bf16 v250, v79, s98, v250\n"
    "v_dot2_f32_bf16 v251, v79, v79, v251\n"
    "s_waitcnt lgkmcnt(1)\n"
    "v_dot2_f32_bf16 v252, v80, s98, v252\n"
    "v_dot2_f32_bf16 v253, v80, v80, v253\n"
    "v_dot2_f32_bf16 v250, v81, s98, v250\n"
    "v_dot2_f32_bf16 v251, v81, v81, v251\n"
    "v_dot2_f32_bf16 v252, v82, s98, v252\n"
    "v_dot2_f32_bf16 v253, v82, v82, v253\n"
    "v_dot2_f32_bf16 v250, v83, s98, v250\n"
    "v_dot2_f32_bf16 v251, v83, v83, v251\n"
    "s_waitcnt lgkmcnt(0)\n"
    "v_dot2_f32_bf16 v252, v84, s98, v252\n"
    "v_dot2_f32_bf16 v253, v84, v84, v253\n"
    "v_dot2_f32_bf16 v250, v85, s98, v250\n"
    "v_dot2_f32_bf16 v251, v85, v85, v251\n"
    "v_dot2_f32_bf16 v252, v86, s98, v252\n"
    "v_dot2_f32_bf16 v253, v86, v86, v253\n"
    "v_dot2_f32_bf16 v250, v87, s98, v250\n"
    "v_dot2_f32_bf16 v251, v87, v87, v251\n"
    "s_nop 1\n"
    "v_add_f32 v252, v252, v250\n"
    "v_add_f32 v253, v253, v251\n"
    "v_mov_b32 v250, v252\n"
    "s_nop 1\n"
    "v_permlane16_swap_b32 v250, v252\n"
    "v_add_f32 v252, v252, v250\n"
    "v_mov_b32 v250, v252\n"
    "s_nop 1\n"
    "v_permlane32_swap_b32 v250, v252\n"
    "v_add_f32 v252, v252, v250\n"
    "v_mov_b32 v250, v253\n"
    "s_nop 1\n"
    "v_permlane16_swap_b32 v250, v253\n"
    "v_add_f32 v253, v253, v250\n"
    "v_mov_b32 v250, v253\n"
    "s_nop 1\n"
    "v_permlane32_swap_b32 v250, v253\n"
    "v_add_f32 v253, v253, v250\n"
    "v_mul_f32 v252, 0x3b124925, v252\n"
    "v_mul_f32 v253, 0x3b124925, v253\n"
    "v_fma_f32 v253, -v252, v252, v253\n"
    "v_max_f32 v253, 0, v253\n"
    "v_add_f32 v253, 0x3727c5ac, v253\n"
    "v_rsq_f32 v253, v253\n"
    "s_nop 0\n"
    "v_mul_f32 v252, -v252, v253\n"
    "v_lshlrev_b32 v104, 16, v32\n"
    "v_and_b32 v105, s7, v32\n"
    "v_lshlrev_b32 v106, 16, v33\n"
    "v_and_b32 v107, s7, v33\n"
    "v_lshlrev_b32 v108, 16, v34\n"
    "v_and_b32 v109, s7, v34\n"
    "v_lshlrev_b32 v110, 16, v35\n"
    "v_and_b32 v111, s7, v35\n"
    "v_pk_fma_f32 v[104:105], v[104:105], v[252:253], v[252:253] op_sel:[0,1,0] op_sel_hi:[1,1,0]\n"
    "v_pk_fma_f32 v[106:107], v[106:107], v[252:253], v[252:253] op_sel:[0,1,0] op_sel_hi:[1,1,0]\n"
    "v_pk_fma_f32 v[108:109], v[108:109], v[252:253], v[252:253] op_sel:[0,1,0] op_sel_hi:[1,1,0]\n"
    "v_pk_fma_f32 v[110:111], v[110:111], v[252:253], v[252:253] op_sel:[0,1,0] op_sel_hi:[1,1,0]\n"
    "v_lshlrev_b32 v112, 16, v36\n"
    "v_and_b32 v113, s7, v36\n"
    "v_lshlrev_b32 v114, 16, v37\n"
    "v_and_b32 v115, s7, v37\n"
    "v_lshlrev_b32 v116, 16, v38\n"
    "v_and_b32 v117, s7, v38\n"
    "v_lshlrev_b32 v118, 16, v39\n"
    "v_and_b32 v119, s7, v39\n"
    "v_pk_fma_f32 v[112:113], v[112:113], v[252:253], v[252:253] op_sel:[0,1,0] op_sel_hi:[1,1,0]\n"
    "v_pk_fma_f32 v[114:115], v[114:115], v[252:253], v[252:253] op_sel:[0,1,0] op_sel_hi:[1,1,0]\n"
    "v_pk_fma_f32 v[116:117], v[116:117], v[252:253], v[252:253] op_sel:[0,1,0] op_sel_hi:[1,1,0]\n"
    "v_pk_fma_f32 v[118:119], v[118:119], v[252:253], v[252:253] op_sel:[0,1,0] op_sel_hi:[1,1,0]\n"
    "v_lshlrev_b32 v120, 16, v40\n"
    "v_and_b32 v121, s7, v40\n"
    "v_lshlrev_b32 v122, 16, v41\n"
    "v_and_b32 v123, s7, v41\n"
    "v_lshlrev_b32 v124, 16, v42\n"
    "v_and_b32 v125, s7, v42\n"
    "v_lshlrev_b32 v126, 16, v43\n"
    "v_and_b32 v127, s7, v43\n"
    "v_pk_fma_f32 v[120:121], v[120:121], v[252:253], v[252:253] op_sel:[0,1,0] op_sel_hi:[1,1,0]\n"
    "v_pk_fma_f32 v[122:123], v[122:123], v[252:253], v[252:253] op_sel:[0,1,0] op_sel_hi:[1,1,0]\n"
    "v_pk_fma_f32 v[124:125], v[124:125], v[252:253], v[252:253] op_sel:[0,1,0] op_sel_hi:[1,1,0]\n"
    "v_pk_fma_f32 v[126:127], v[126:127], v[252:253], v[252:253] op_sel:[0,1,0] op_sel_hi:[1,1,0]\n"
    "v_lshlrev_b32 v128, 16, v44\n"
    "v_and_b32 v129, s7, v44\n"
    "v_lshlrev_b32 v130, 16, v45\n"
    "v_and_b32 v131, s7, v45\n"
    "v_lshlrev_b32 v132, 16, v46\n"
    "v_and_b32 v133, s7, v46\n"
    "v_lshlrev_b32 v134, 16, v47\n"
    "v_and_b32 v135, s7, v47\n"
    "v_pk_fma_f32 v[128:129], v[128:129], v[252:253], v[252:253] op_sel:[0,1,0] op_sel_hi:[1,1,0]\n"
    "v_pk_fma_f32 v[130:131], v[130:131], v[252:253], v[252:253] op_sel:[0,1,0] op_sel_hi:[1,1,0]\n"
    "v_pk_fma_f32 v[132:133], v[132:133], v[252:253], v[252:253] op_sel:[0,1,0] op_sel_hi:[1,1,0]\n"
    "v_pk_fma_f32 v[134:135], v[134:135], v[252:253], v[252:253] op_sel:[0,1,0] op_sel_hi:[1,1,0]\n"
    "v_lshlrev_b32 v136, 16, v48\n"
    "v_and_b32 v137, s7, v48\n"
    "v_lshlrev_b32 v138, 16, v49\n"
    "v_and_b32 v139, s7, v49\n"
    "v_lshlrev_b32 v140, 16, v50\n"
    "v_and_b32 v141, s7, v50\n"
    "v_lshlrev_b32 v142, 16, v51\n"
    "v_and_b32 v143, s7, v51\n"
    "v_pk_fma_f32 v[136:137], v[136:137], v[252:253], v[252:253] op_sel:[0,1,0] op_sel_hi:[1,1,0]\n"
    "v_pk_fma_f32 v[138:139], v[138:139], v[252:253], v[252:253] op_sel:[0,1,0] op_sel_hi:[1,1,0]\n"
    "v_pk_fma_f32 v[140:141], v[140:141], v[252:253], v[252:253] op_sel:[0,1,0] op_sel_hi:[1,1,0]\n"
    "v_pk_fma_f32 v[142:143], v[142:143], v[252:253], v[252:253] op_sel:[0,1,0] op_sel_hi:[1,1,0]\n"
    "v_lshlrev_b32 v144, 16, v52\n"
    "v_and_b32 v145, s7, v52\n"
    "v_lshlrev_b32 v146, 16, v53\n"
    "v_and_b32 v147, s7, v53\n"
    "v_lshlrev_b32 v148, 16, v54\n"
    "v_and_b32 v149, s7, v54\n"
    "v_lshlrev_b32 v150, 16, v55\n"
    "v_and_b32 v151, s7, v55\n"
    "v_pk_fma_f32 v[144:145], v[144:145], v[252:253], v[252:253] op_sel:[0,1,0] op_sel_hi:[1,1,0]\n"
    "v_pk_fma_f32 v[146:147], v[146:147], v[252:253], v[252:253] op_sel:[0,1,0] op_sel_hi:[1,1,0]\n"
    "v_pk_fma_f32 v[148:149], v[148:149], v[252:253], v[252:253] op_sel:[0,1,0] op_sel_hi:[1,1,0]\n"
    "v_pk_fma_f32 v[150:151], v[150:151], v[252:253], v[252:253] op_sel:[0,1,0] op_sel_hi:[1,1,0]\n"
    "v_lshlrev_b32 v152, 16, v56\n"
    "v_and_b32 v153, s7, v56\n"
    "v_lshlrev_b32 v154, 16, v57\n"
    "v_and_b32 v155, s7, v57\n"
    "v_lshlrev_b32 v156, 16, v58\n"
    "v_and_b32 v157, s7, v58\n"
    "v_lshlrev_b32 v158, 16, v59\n"
    "v_and_b32 v159, s7, v59\n"
    "v_pk_fma_f32 v[152:153], v[152:153], v[252:253], v[252:253] op_sel:[0,1,0] op_sel_hi:[1,1,0]\n"
    "v_pk_fma_f32 v[154:155], v[154:155], v[252:253], v[252:253] op_sel:[0,1,0] op_sel_hi:[1,1,0]\n"
    "v_pk_fma_f32 v[156:157], v[156:157], v[252:253], v[252:253] op_sel:[0,1,0] op_sel_hi:[1,1,0]\n"
    "v_pk_fma_f32 v[158:159], v[158:159], v[252:253], v[252:253] op_sel:[0,1,0] op_sel_hi:[1,1,0]\n"
    "v_lshlrev_b32 v160, 16, v60\n"
    "v_and_b32 v161, s7, v60\n"
    "v_lshlrev_b32 v162, 16, v61\n"
    "v_and_b32 v163, s7, v61\n"
    "v_lshlrev_b32 v164, 16, v62\n"
    "v_and_b32 v165, s7, v62\n"
    "v_lshlrev_b32 v166, 16, v63\n"
    "v_and_b32 v167, s7, v63\n"
    "v_pk_fma_f32 v[160:161], v[160:161], v[252:253], v[252:253] op_sel:[0,1,0] op_sel_hi:[1,1,0]\n"
    "v_pk_fma_f32 v[162:163], v[162:163], v[252:253], v[252:253] op_sel:[0,1,0] op_sel_hi:[1,1,0]\n"
    "v_pk_fma_f32 v[164:165], v[164:165], v[252:253], v[252:253] op_sel:[0,1,0] op_sel_hi:[1,1,0]\n"
    "v_pk_fma_f32 v[166:167], v[166:167], v[252:253], v[252:253] op_sel:[0,1,0] op_sel_hi:[1,1,0]\n"
    "v_lshlrev_b32 v168, 16, v64\n"
    "v_and_b32 v169, s7, v64\n"
    "v_lshlrev_b32 v170, 16, v65\n"
    "v_and_b32 v171, s7, v65\n"
    "v_lshlrev_b32 v172, 16, v66\n"
    "v_and_b32 v173, s7, v66\n"
    "v_lshlrev_b32 v174, 16, v67\n"
    "v_and_b32 v175, s7, v67\n"
    "v_pk_fma_f32 v[168:169], v[168:169], v[252:253], v[252:253] op_sel:[0,1,0] op_sel_hi:[1,1,0]\n"
    "v_pk_fma_f32 v[170:171], v[170:171], v[252:253], v[252:253] op_sel:[0,1,0] op_sel_hi:[1,1,0]\n"
    "v_pk_fma_f32 v[172:173], v[172:173], v[252:253], v[252:253] op_sel:[0,1,0] op_sel_hi:[1,1,0]\n"
    "v_pk_fma_f32 v[174:175], v[174:175], v[252:253], v[252:253] op_sel:[0,1,0] op_sel_hi:[1,1,0]\n"
    "v_lshlrev_b32 v176, 16, v68\n"
    "v_and_b32 v177, s7, v68\n"
    "v_lshlrev_b32 v178, 16, v69\n"
    "v_and_b32 v179, s7, v69\n"
    "v_lshlrev_b32 v180, 16, v70\n"
    "v_and_b32 v181, s7, v70\n"
    "v_lshlrev_b32 v182, 16, v71\n"
    "v_and_b32 v183, s7, v71\n"
    "v_pk_fma_f32 v[176:177], v[176:177], v[252:253], v[252:253] op_sel:[0,1,0] op_sel_hi:[1,1,0]\n"
    "v_pk_fma_f32 v[178:179], v[178:179], v[252:253], v[252:253] op_sel:[0,1,0] op_sel_hi:[1,1,0]\n"
    "v_pk_fma_f32 v[180:181], v[180:181], v[252:253], v[252:253] op_sel:[0,1,0] op_sel_hi:[1,1,0]\n"
    "v_pk_fma_f32 v[182:183], v[182:183], v[252:253], v[252:253] op_sel:[0,1,0] op_sel_hi:[1,1,0]\n"
    "v_lshlrev_b32 v184, 16, v72\n"
    "v_and_b32 v185, s7, v72\n"
    "v_lshlrev_b32 v186, 16, v73\n"
    "v_and_b32 v187, s7, v73\n"
    "v_lshlrev_b32 v188, 16, v74\n"
    "v_and_b32 v189, s7, v74\n"
    "v_lshlrev_b32 v190, 16, v75\n"
    "v_and_b32 v191, s7, v75\n"
    "v_pk_fma_f32 v[184:185], v[184:185], v[252:253], v[252:253] op_sel:[0,1,0] op_sel_hi:[1,1,0]\n"
    "v_pk_fma_f32 v[186:187], v[186:187], v[252:253], v[252:253] op_sel:[0,1,0] op_sel_hi:[1,1,0]\n"
    "v_pk_fma_f32 v[188:189], v[188:189], v[252:253], v[252:253] op_sel:[0,1,0] op_sel_hi:[1,1,0]\n"
    "v_pk_fma_f32 v[190:191], v[190:191], v[252:253], v[252:253] op_sel:[0,1,0] op_sel_hi:[1,1,0]\n"
    "v_lshlrev_b32 v192, 16, v76\n"
    "v_and_b32 v193, s7, v76\n"
    "v_lshlrev_b32 v194, 16, v77\n"
    "v_and_b32 v195, s7, v77\n"
    "v_lshlrev_b32 v196, 16, v78\n"
    "v_and_b32 v197, s7, v78\n"
    "v_lshlrev_b32 v198, 16, v79\n"
    "v_and_b32 v199, s7, v79\n"
    "v_pk_fma_f32 v[192:193], v[192:193], v[252:253], v[252:253] op_sel:[0,1,0] op_sel_hi:[1,1,0]\n"
    "v_pk_fma_f32 v[194:195], v[194:195], v[252:253], v[252:253] op_sel:[0,1,0] op_sel_hi:[1,1,0]\n"
    "v_pk_fma_f32 v[196:197], v[196:197], v[252:253], v[252:253] op_sel:[0,1,0] op_sel_hi:[1,1,0]\n"
    "v_pk_fma_f32 v[198:199], v[198:199], v[252:253], v[252:253] op_sel:[0,1,0] op_sel_hi:[1,1,0]\n"
    "v_lshlrev_b32 v200, 16, v80\n"
    "v_and_b32 v201, s7, v80\n"
    "v_lshlrev_b32 v202, 16, v81\n"
    "v_and_b32 v203, s7, v81\n"
    "v_lshlrev_b32 v204, 16, v82\n"
    "v_and_b32 v205, s7, v82\n"
    "v_lshlrev_b32 v206, 16, v83\n"
    "v_and_b32 v207, s7, v83\n"
    "v_pk_fma_f32 v[200:201], v[200:201], v[252:253], v[252:253] op_sel:[0,1,0] op_sel_hi:[1,1,0]\n"
    "v_pk_fma_f32 v[202:203], v[202:203], v[252:253], v[252:253] op_sel:[0,1,0] op_sel_hi:[1,1,0]\n"
    "v_pk_fma_f32 v[204:205], v[204:205], v[252:253], v[252:253] op_sel:[0,1,0] op_sel_hi:[1,1,0]\n"
    "v_pk_fma_f32 v[206:207], v[206:207], v[252:253], v[252:253] op_sel:[0,1,0] op_sel_hi:[1,1,0]\n"
    "v_lshlrev_b32 v208, 16, v84\n"
    "v_and_b32 v209, s7, v84\n"
    "v_lshlrev_b32 v210, 16, v85\n"
    "v_and_b32 v211, s7, v85\n"
    "v_lshlrev_b32 v212, 16, v86\n"
    "v_and_b32 v213, s7, v86\n"
    "v_lshlrev_b32 v214, 16, v87\n"
    "v_and_b32 v215, s7, v87\n"
    "v_pk_fma_f32 v[208:209], v[208:209], v[252:253], v[252:253] op_sel:[0,1,0] op_sel_hi:[1,1,0]\n"
    "v_pk_fma_f32 v[210:211], v[210:211], v[252:253], v[252:253] op_sel:[0,1,0] op_sel_hi:[1,1,0]\n"
    "v_pk_fma_f32 v[212:213], v[212:213], v[252:253], v[252:253] op_sel:[0,1,0] op_sel_hi:[1,1,0]\n"
    "v_pk_fma_f32 v[214:215], v[214:215], v[252:253], v[252:253] op_sel:[0,1,0] op_sel_hi:[1,1,0]\n"
    "s_add_u32 m0, s45, 0x8000\n"
    "s_nop 0\n"
    "buffer_load_dwordx4 v12, s[8:11], 0 offen lds\n"
    "buffer_load_dwordx4 v13, s[8:11], s46 offen offset:1024 lds\n"
    "buffer_load_dwordx4 v12, s[8:11], s47 offen offset:2048 lds\n"
    "buffer_load_dwordx4 v13, s[8:11], s48 offen offset:3072 lds\n"
    "v_add_u32 v12, 0x20000, v12\n"
    "v_add_u32 v13, 0x20000, v13\n"
    "s_add_u32 m0, s45, 0xc000\n"
    "s_nop 0\n"
    "buffer_load_dwordx4 v12, s[8:11], 0 offen lds\n"
    "buffer_load_dwordx4 v13, s[8:11], s46 offen offset:1024 lds\n"
    "buffer_load_dwordx4 v12, s[8:11], s47 offen offset:2048 lds\n"
    "buffer_load_dwordx4 v13, s[8:11], s48 offen offset:3072 lds\n"
    "v_add_u32 v12, 0x20000, v12\n"
    "v_add_u32 v13, 0x20000, v13\n"
    "s_cmp_ge_u32 s97, 168\n"
    "s_cbranch_scc1 .LF_nomf_6\n"
    "s_lshl_b32 s93, s97, 3\n"
    "s_add_u32 s93, s93, 0x100\n"
    "s_waitcnt vmcnt(0)\n"
    "s_barrier\n"
    "s_cmp_lg_u32 s40, 0\n"
    "s_cbranch_scc1 .LF_w0_7\n"
    "s_mov_b32 exec_lo, 0x1\n"
    "s_mov_b32 exec_hi, 0x0\n"
    "v_mov_b32 v88, s90\n"
    "v_mov_b32 v89, s91\n"
    "v_mov_b32 v90, 0\n"
    "buffer_store_dwordx2 v[88:89], v90, s[28:31], s93 offen sc1\n"
    "s_mov_b64 exec, -1\n"
    ".LF_w0_7:\n"
    "s_nop 4\n"
    ".LF_nomf_6:\n"
    "s_nop 4\n"
    "s_lshl_b32 s58, s40, 6\n"
    "v_add_u32 v94, s58, v1\n"
    "v_lshlrev_b32 v88, 4, v94\n"
    "v_add_u32 v89, 0x100, v94\n"
    "v_min_u32 v89, 0x1bf, v89\n"
    "v_cmp_le_u32 vcc, 224, v94\n"
    "s_nop 1\n"
    "v_cndmask_b32 v90, 0, 1, vcc\n"
    "v_mul_u32_u24 v91, 0xe0, v90\n"
    "v_sub_u32 v91, v94, v91\n"
    "v_lshrrev_b32 v92, 2, v91\n"
    "v_lshlrev_b32 v92, 6, v92\n"
    "v_and_b32 v91, 3, v91\n"
    "v_lshl_add_u32 v92, v91, 3, v92\n"
    "v_lshl_add_u32 v92, v90, 5, v92\n"
    "v_add_u32 v92, 0x26000, v92\n"
    "v_cmp_le_u32 vcc, 224, v89\n"
    "s_nop 1\n"
    "v_cndmask_b32 v90, 0, 1, vcc\n"
    "v_mul_u32_u24 v91, 0xe0, v90\n"
    "v_sub_u32 v91, v89, v91\n"
    "v_lshrrev_b32 v93, 2, v91\n"
    "v_lshlrev_b32 v93, 6, v93\n"
    "v_and_b32 v91, 3, v91\n"
    "v_lshl_add_u32 v93, v91, 3, v93\n"
    "v_lshl_add_u32 v93, v90, 5, v93\n"
    "v_add_u32 v93, 0x26000, v93\n"
    "v_lshlrev_b32 v89, 4, v89\n"
    "s_mov_b32 s58, 0x6000\n"
    "s_mov_b32 s92, 0\n"
    "v_and_b32 v90, 31, v1\n"
    "v_bfe_u32 v96, v1, 1, 3\n"
    "v_lshrrev_b32 v97, 5, v1\n"
    "v_xor_b32 v96, v96, v97\n"
    "v_lshlrev_b32 v96, 4, v96\n"
    "v_mul_u32_u24 v90, 0x380, v90\n"
    "v_add_u32 v96, v96, v90\n"
    "s_mul_i32 s59, s44, 0x7000\n"
    "s_add_u32 s59, s59, 0x18000\n"
    "v_add_u32 v96, s59, v96\n"
    "v_xor_b32 v97, 32, v96\n"
    "v_xor_b32 v98, 64, v96\n"
    "v_xor_b32 v99, 0x60, v96\n"
    "v_lshrrev_b32 v100, 5, v1\n"
    "v_lshlrev_b32 v100, 6, v100\n"
    "v_add_u32 v100, 0x26000, v100\n"
    "s_waitcnt vmcnt(0)\n"
    ".LF_tpoll_8:\n"
    "s_nop 0\n"
    "buffer_load_dwordx4 v[216:219], v88, s[28:31], s58 offen sc1\n"
    "buffer_load_dwordx4 v[220:223], v89, s[28:31], s58 offen sc1\n"
    "s_waitcnt vmcnt(0)\n"
    "v_xor_b32 v90, s90, v218\n"
    "v_xor_b32 v91, s91, v219\n"
    "v_or_b32 v90, v90, v91\n"
    "v_xor_b32 v91, s90, v222\n"
    "v_or_b32 v90, v90, v91\n"
    "v_xor_b32 v91, s91, v223\n"
    "v_or_b32 v90, v90, v91\n"
    "v_cmp_ne_u32 vcc, 0, v90\n"
    "s_add_u32 s92, s92, 1\n"
    "s_nop 3\n"
    "s_cbranch_vccz .LF_tgo_9\n"
    "s_cmp_gt_u32 s92, 0x20000\n"
    "s_cbranch_scc1 .LF_tgo_9\n"
    "s_sleep 2\n"
    "s_branch .LF_tpoll_8\n"
    ".LF_tgo_9:\n"
    "s_nop 1\n"
    "ds_write_b64 v92, v[216:217]\n"
    "ds_write_b64 v93, v[220:221]\n"
    "s_waitcnt lgkmcnt(0)\n"
    "s_barrier\n"
    "v_lshrrev_b32 v250, 4, v1\n"
    "v_lshlrev_b32 v250, 6, v250\n"
    "v_add_u32 v250, 0x26000, v250\n"
    "ds_read_b128 v[216:219], v250\n"
    "ds_read_b128 v[220:223], v250 offset:16\n"
    "ds_read_b128 v[224:227], v250 offset:32\n"
    "ds_read_b128 v[228:231], v250 offset:48\n"
    "ds_read_b128 v[232:235], v250 offset:256\n"
    "ds_read_b128 v[236:239], v250 offset:272\n"
    "ds_read_b128 v[240:243], v250 offset:288\n"
    "ds_read_b128 v[244:247], v250 offset:304\n"
    "s_waitcnt lgkmcnt(5)\n"
    "v_pk_fma_f32 v[104:105], v[104:105], v[216:217], v[224:225]\n"
    "v_pk_fma_f32 v[106:107], v[106:107], v[218:219], v[226:227]\n"
    "s_waitcnt lgkmcnt(4)\n"
    "v_pk_fma_f32 v[108:109], v[108:109], v[220:221], v[228:229]\n"
    "v_pk_fma_f32 v[110:111], v[110:111], v[222:223], v[230:231]\n"
    "ds_read_b128 v[216:219], v250 offset:512\n"
    "ds_read_b128 v[220:223], v250 offset:528\n"
    "ds_read_b128 v[224:227], v250 offset:544\n"
    "ds_read_b128 v[228:231], v250 offset:560\n"
    "v_cvt_pk_bf16_f32 v32, v104, v105\n"
    "v_cvt_pk_bf16_f32 v33, v106, v107\n"
    "v_cvt_pk_bf16_f32 v34, v108, v109\n"
    "v_cvt_pk_bf16_f32 v35, v110, v111\n"
    "ds_write_b128 v248, v[32:35]\n"
    "s_waitcnt lgkmcnt(6)\n"
    "v_pk_fma_f32 v[112:113], v[112:113], v[232:233], v[240:241]\n"
    "v_pk_fma_f32 v[114:115], v[114:115], v[234:235], v[242:243]\n"
    "s_waitcnt lgkmcnt(5)\n"
    "v_pk_fma_f32 v[116:117], v[116:117], v[236:237], v[244:245]\n"
    "v_pk_fma_f32 v[118:119], v[118:119], v[238:239], v[246:247]\n"
    "ds_read_b128 v[232:235], v250 offset:768\n"
    "ds_read_b128 v[236:239], v250 offset:784\n"
    "ds_read_b128 v[240:243], v250 offset:800\n"
    "ds_read_b128 v[244:247], v250 offset:816\n"
    "v_cvt_pk_bf16_f32 v36, v112, v113\n"
    "v_cvt_pk_bf16_f32 v37, v114, v115\n"
    "v_cvt_pk_bf16_f32 v38, v116, v117\n"
    "v_cvt_pk_bf16_f32 v39, v118, v119\n"
    "ds_write_b128 v249, v[36:39]\n"
    "s_waitcnt lgkmcnt(7)\n"
    "v_pk_fma_f32 v[120:121], v[120:121], v[216:217], v[224:225]\n"
    "v_pk_fma_f32 v[122:123], v[122:123], v[218:219], v[226:227]\n"
    "s_waitcnt lgkmcnt(6)\n"
    "v_pk_fma_f32 v[124:125], v[124:125], v[220:221], v[228:229]\n"
    "v_pk_fma_f32 v[126:127], v[126:127], v[222:223], v[230:231]\n"
    "ds_read_b128 v[216:219], v250 offset:1024\n"
    "ds_read_b128 v[220:223], v250 offset:1040\n"
    "ds_read_b128 v[224:227], v250 offset:1056\n"
    "ds_read_b128 v[228:231], v250 offset:1072\n"
    "v_cvt_pk_bf16_f32 v40, v120, v121\n"
    "v_cvt_pk_bf16_f32 v41, v122, v123\n"
    "v_cvt_pk_bf16_f32 v42, v124, v125\n"
    "v_cvt_pk_bf16_f32 v43, v126, v127\n"
    "ds_write_b128 v248, v[40:43] offset:128\n"
    "s_waitcnt lgkmcnt(7)\n"
    "v_pk_fma_f32 v[128:129], v[128:129], v[232:233], v[240:241]\n"
    "v_pk_fma_f32 v[130:131], v[130:131], v[234:235], v[242:243]\n"
    "s_waitcnt lgkmcnt(6)\n"
    "v_pk_fma_f32 v[132:133], v[132:133], v[236:237], v[244:245]\n"
    "v_pk_fma_f32 v[134:135], v[134:135], v[238:239], v[246:247]\n"
    "ds_read_b128 v[232:235], v250 offset:1280\n"
    "ds_read_b128 v[236:239], v250 offset:1296\n"
    "ds_read_b128 v[240:243], v250 offset:1312\n"
    "ds_read_b128 v[244:247], v250 offset:1328\n"
    "v_cvt_pk_bf16_f32 v44, v128, v129\n"
    "v_cvt_pk_bf16_f32 v45, v130, v131\n"
    "v_cvt_pk_bf16_f32 v46, v132, v133\n"
    "v_cvt_pk_bf16_f32 v47, v134, v135\n"
    "ds_write_b128 v249, v[44:47] offset:128\n"
    "s_waitcnt lgkmcnt(7)\n"
    "v_pk_fma_f32 v[136:137], v[136:137], v[216:217], v[224:225]\n"
    "v_pk_fma_f32 v[138:139], v[138:139], v[218:219], v[226:227]\n"
    "s_waitcnt lgkmcnt(6)\n"
    "v_pk_fma_f32 v[140:141], v[140:141], v[220:221], v[228:229]\n"
    "v_pk_fma_f32 v[142:143], v[142:143], v[222:223], v[230:231]\n"
    "ds_read_b128 v[216:219], v250 offset:1536\n"
    "ds_read_b128 v[220:223], v250 offset:1552\n"
    "ds_read_b128 v[224:227], v250 offset:1568\n"
    "ds_read_b128 v[228:231], v250 offset:1584\n"
    "v_cvt_pk_bf16_f32 v48, v136, v137\n"
    "v_cvt_pk_bf16_f32 v49, v138, v139\n"
    "v_cvt_pk_bf16_f32 v50, v140, v141\n"
    "v_cvt_pk_bf16_f32 v51, v142, v143\n"
    "ds_write_b128 v248, v[48:51] offset:256\n"
    "s_waitcnt lgkmcnt(7)\n"
    "v_pk_fma_f32 v[144:145], v[144:145], v[232:233], v[240:241]\n"
    "v_pk_fma_f32 v[146:147], v[146:147], v[234:235], v[242:243]\n"
    "s_waitcnt lgkmcnt(6)\n"
    "v_pk_fma_f32 v[148:149], v[148:149], v[236:237], v[244:245]\n"
    "v_pk_fma_f32 v[150:151], v[150:151], v[238:239], v[246:247]\n"
    "ds_read_b128 v[232:235], v250 offset:1792\n"
    "ds_read_b128 v[236:239], v250 offset:1808\n"
    "ds_read_b128 v[240:243], v250 offset:1824\n"
    "ds_read_b128 v[244:247], v250 offset:1840\n"
    "v_cvt_pk_bf16_f32 v52, v144, v145\n"
    "v_cvt_pk_bf16_f32 v53, v146, v147\n"
    "v_cvt_pk_bf16_f32 v54, v148, v149\n"
    "v_cvt_pk_bf16_f32 v55, v150, v151\n"
    "ds_write_b128 v249, v[52:55] offset:256\n"
    "s_waitcnt lgkmcnt(7)\n"
    "v_pk_fma_f32 v[152:153], v[152:153], v[216:217], v[224:225]\n"
    "v_pk_fma_f32 v[154:155], v[154:155], v[218:219], v[226:227]\n"
    "s_waitcnt lgkmcnt(6)\n"
    "v_pk_fma_f32 v[156:157], v[156:157], v[220:221], v[228:229]\n"
    "v_pk_fma_f32 v[158:159], v[158:159], v[222:223], v[230:231]\n"
    "ds_read_b128 v[216:219], v250 offset:2048\n"
    "ds_read_b128 v[220:223], v250 offset:2064\n"
    "ds_read_b128 v[224:227], v250 offset:2080\n"
    "ds_read_b128 v[228:231], v250 offset:2096\n"
    "v_cvt_pk_bf16_f32 v56, v152, v153\n"
    "v_cvt_pk_bf16_f32 v57, v154, v155\n"
    "v_cvt_pk_bf16_f32 v58, v156, v157\n"
    "v_cvt_pk_bf16_f32 v59, v158, v159\n"
    "ds_write_b128 v248, v[56:59] offset:384\n"
    "s_waitcnt lgkmcnt(7)\n"
    "v_pk_fma_f32 v[160:161], v[160:161], v[232:233], v[240:241]\n"
    "v_pk_fma_f32 v[162:163], v[162:163], v[234:235], v[242:243]\n"
    "s_waitcnt lgkmcnt(6)\n"
    "v_pk_fma_f32 v[164:165], v[164:165], v[236:237], v[244:245]\n"
    "v_pk_fma_f32 v[166:167], v[166:167], v[238:239], v[246:247]\n"
    "ds_read_b128 v[232:235], v250 offset:2304\n"
    "ds_read_b128 v[236:239], v250 offset:2320\n"
    "ds_read_b128 v[240:243], v250 offset:2336\n"
    "ds_read_b128 v[244:247], v250 offset:2352\n"
    "v_cvt_pk_bf16_f32 v60, v160, v161\n"
    "v_cvt_pk_bf16_f32 v61, v162, v163\n"
    "v_cvt_pk_bf16_f32 v62, v164, v165\n"
    "v_cvt_pk_bf16_f32 v63, v166, v167\n"
    "ds_write_b128 v249, v[60:63] offset:384\n"
    "s_waitcnt lgkmcnt(7)\n"
    "v_pk_fma_f32 v[168:169], v[168:169], v[216:217], v[224:225]\n"
    "v_pk_fma_f32 v[170:171], v[170:171], v[218:219], v[226:227]\n"
    "s_waitcnt lgkmcnt(6)\n"
    "v_pk_fma_f32 v[172:173], v[172:173], v[220:221], v[228:229]\n"
    "v_pk_fma_f32 v[174:175], v[174:175], v[222:223], v[230:231]\n"
    "ds_read_b128 v[216:219], v250 offset:2560\n"
    "ds_read_b128 v[220:223], v250 offset:2576\n"
    "ds_read_b128 v[224:227], v250 offset:2592\n"
    "ds_read_b128 v[228:231], v250 offset:2608\n"
    "v_cvt_pk_bf16_f32 v64, v168, v169\n"
    "v_cvt_pk_bf16_f32 v65, v170, v171\n"
    "v_cvt_pk_bf16_f32 v66, v172, v173\n"
    "v_cvt_pk_bf16_f32 v67, v174, v175\n"
    "ds_write_b128 v248, v[64:67] offset:512\n"
    "s_waitcnt lgkmcnt(7)\n"
    "v_pk_fma_f32 v[176:177], v[176:177], v[232:233], v[240:241]\n"
    "v_pk_fma_f32 v[178:179], v[178:179], v[234:235], v[242:243]\n"
    "s_waitcnt lgkmcnt(6)\n"
    "v_pk_fma_f32 v[180:181], v[180:181], v[236:237], v[244:245]\n"
    "v_pk_fma_f32 v[182:183], v[182:183], v[238:239], v[246:247]\n"
    "ds_read_b128 v[232:235], v250 offset:2816\n"
    "ds_read_b128 v[236:239], v250 offset:2832\n"
    "ds_read_b128 v[240:243], v250 offset:2848\n"
    "ds_read_b128 v[244:247], v250 offset:2864\n"
    "v_cvt_pk_bf16_f32 v68, v176, v177\n"
    "v_cvt_pk_bf16_f32 v69, v178, v179\n"
    "v_cvt_pk_bf16_f32 v70, v180, v181\n"
    "v_cvt_pk_bf16_f32 v71, v182, v183\n"
    "ds_write_b128 v249, v[68:71] offset:512\n"
    "s_waitcnt lgkmcnt(7)\n"
    "v_pk_fma_f32 v[184:185], v[184:185], v[216:217], v[224:225]\n"
    "v_pk_fma_f32 v[186:187], v[186:187], v[218:219], v[226:227]\n"
    "s_waitcnt lgkmcnt(6)\n"
    "v_pk_fma_f32 v[188:189], v[188:189], v[220:221], v[228:229]\n"
    "v_pk_fma_f32 v[190:191], v[190:191], v[222:223], v[230:231]\n"
    "ds_read_b128 v[216:219], v250 offset:3072\n"
    "ds_read_b128 v[220:223], v250 offset:3088\n"
    "ds_read_b128 v[224:227], v250 offset:3104\n"
    "ds_read_b128 v[228:231], v250 offset:3120\n"
    "v_cvt_pk_bf16_f32 v72, v184, v185\n"
    "v_cvt_pk_bf16_f32 v73, v186, v187\n"
    "v_cvt_pk_bf16_f32 v74, v188, v189\n"
    "v_cvt_pk_bf16_f32 v75, v190, v191\n"
    "ds_write_b128 v248, v[72:75] offset:640\n"
    "s_waitcnt lgkmcnt(7)\n"
    "v_pk_fma_f32 v[192:193], v[192:193], v[232:233], v[240:241]\n"
    "v_pk_fma_f32 v[194:195], v[194:195], v[234:235], v[242:243]\n"
    "s_waitcnt lgkmcnt(6)\n"
    "v_pk_fma_f32 v[196:197], v[196:197], v[236:237], v[244:245]\n"
    "v_pk_fma_f32 v[198:199], v[198:199], v[238:239], v[246:247]\n"
    "ds_read_b128 v[232:235], v250 offset:3328\n"
    "ds_read_b128 v[236:239], v250 offset:3344\n"
    "ds_read_b128 v[240:243], v250 offset:3360\n"
    "ds_read_b128 v[244:247], v250 offset:3376\n"
    "v_cvt_pk_bf16_f32 v76, v192, v193\n"
    "v_cvt_pk_bf16_f32 v77, v194, v195\n"
    "v_cvt_pk_bf16_f32 v78, v196, v197\n"
    "v_cvt_pk_bf16_f32 v79, v198, v199\n"
    "ds_write_b128 v249, v[76:79] offset:640\n"
    "s_waitcnt lgkmcnt(7)\n"
    "v_pk_fma_f32 v[200:201], v[200:201], v[216:217], v[224:225]\n"
    "v_pk_fma_f32 v[202:203], v[202:203], v[218:219], v[226:227]\n"
    "s_waitcnt lgkmcnt(6)\n"
    "v_pk_fma_f32 v[204:205], v[204:205], v[220:221], v[228:229]\n"
    "v_pk_fma_f32 v[206:207], v[206:207], v[222:223], v[230:231]\n"
    "v_cvt_pk_bf16_f32 v80, v200, v201\n"
    "v_cvt_pk_bf16_f32 v81, v202, v203\n"
    "v_cvt_pk_bf16_f32 v82, v204, v205\n"
    "v_cvt_pk_bf16_f32 v83, v206, v207\n"
    "ds_write_b128 v248, v[80:83] offset:768\n"
    "s_waitcnt lgkmcnt(3)\n"
    "v_pk_fma_f32 v[208:209], v[208:209], v[232:233], v[240:241]\n"
    "v_pk_fma_f32 v[210:211], v[210:211], v[234:235], v[242:243]\n"
    "s_waitcnt lgkmcnt(2)\n"
    "v_pk_fma_f32 v[212:213], v[212:213], v[236:237], v[244:245]\n"
    "v_pk_fma_f32 v[214:215], v[214:215], v[238:239], v[246:247]\n"
    "v_cvt_pk_bf16_f32 v84, v208, v209\n"
    "v_cvt_pk_bf16_f32 v85, v210, v211\n"
    "v_cvt_pk_bf16_f32 v86, v212, v213\n"
    "v_cvt_pk_bf16_f32 v87, v214, v215\n"
    "ds_write_b128 v249, v[84:87] offset:768\n"
    "s_waitcnt lgkmcnt(0)\n"
    "s_barrier\n"
    "ds_read_b128 v[32:35], v96\n"
    "ds_read_b128 v[36:39], v97\n"
    "s_barrier\n"
    "s_add_u32 m0, s45, 0x10000\n"
    "ds_read_b64_tr_b16 a[224:225], v2\n"
    "ds_read_b64_tr_b16 a[226:227], v2 offset:2048\n"
    "ds_read_b64_tr_b16 a[228:229], v3\n"
    "ds_read_b64_tr_b16 a[230:231], v3 offset:2048\n"
    "ds_read_b64_tr_b16 a[232:233], v4\n"
    "ds_read_b64_tr_b16 a[234:235], v4 offset:2048\n"
    "ds_read_b64_tr_b16 a[236:237], v5\n"
    "ds_read_b64_tr_b16 a[238:239], v5 offset:2048\n"
    "ds_read_b64_tr_b16 a[240:241], v2 offset:256\n"
    "ds_read_b64_tr_b16 a[242:243], v2 offset:2304\n"
    "buffer_load_dwordx4 v12, s[8:11], 0 offen lds\n"
    "s_waitcnt lgkmcnt(8)\n"
    "v_mfma_f32_32x32x16_bf16 v[128:143], a[224:227], v[32:35], 0\n"
    "s_waitcnt lgkmcnt(6)\n"
    "ds_read_b64_tr_b16 a[244:245], v3 offset:256\n"
    "ds_read_b64_tr_b16 a[246:247], v3 offset:2304\n"
    "ds_read_b128 v[40:43], v98\n"
    "buffer_load_dwordx4 v13, s[8:11], s46 offen offset:1024 lds\n"
    "v_mfma_f32_32x32x16_bf16 v[144:159], a[228:231], v[32:35], 0\n"
    "s_waitcnt lgkmcnt(7)\n"
    "ds_read_b64_tr_b16 a[248:249], v4 offset:256\n"
    "ds_read_b64_tr_b16 a[250:251], v4 offset:2304\n"
    "buffer_load_dwordx4 v12, s[8:11], s47 offen offset:2048 lds\n"
    "v_mfma_f32_32x32x16_bf16 v[160:175], a[232:235], v[32:35], 0\n"
    "s_waitcnt lgkmcnt(7)\n"
    "ds_read_b64_tr_b16 a[252:253], v5 offset:256\n"
    "ds_read_b64_tr_b16 a[254:255], v5 offset:2304\n"
    "buffer_load_dwordx4 v13, s[8:11], s48 offen offset:3072 lds\n"
    "v_add_u32 v12, 0x20000, v12\n"
    "v_add_u32 v13, 0x20000, v13\n"
    "v_mfma_f32_32x32x16_bf16 v[176:191], a[236:239], v[32:35], 0\n"
    "s_waitcnt lgkmcnt(7)\n"
    "s_barrier\n"
    "s_add_u32 m0, s45, 0x14000\n"
    "ds_read_b64_tr_b16 a[224:225], v2 offset:16384\n"
    "ds_read_b64_tr_b16 a[226:227], v2 offset:18432\n"
    "buffer_load_dwordx4 v12, s[8:11], 0 offen lds\n"
    "v_mfma_f32_32x32x16_bf16 v[192:207], a[240:243], v[32:35], 0\n"
    "s_waitcnt lgkmcnt(7)\n"
    "ds_read_b64_tr_b16 a[228:229], v3 offset:16384\n"
    "ds_read_b64_tr_b16 a[230:231], v3 offset:18432\n"
    "v_mfma_f32_32x32x16_bf16 v[208:223], a[244:247], v[32:35], 0\n"
    "s_waitcnt lgkmcnt(6)\n"
    "ds_read_b64_tr_b16 a[232:233], v4 offset:16384\n"
    "ds_read_b64_tr_b16 a[234:235], v4 offset:18432\n"
    "buffer_load_dwordx4 v13, s[8:11], s46 offen offset:1024 lds\n"
    "v_mfma_f32_32x32x16_bf16 v[224:239], a[248:251], v[32:35], 0\n"
    "s_waitcnt lgkmcnt(6)\n"
    "ds_read_b64_tr_b16 a[236:237], v5 offset:16384\n"
    "ds_read_b64_tr_b16 a[238:239], v5 offset:18432\n"
    "v_mfma_f32_32x32x16_bf16 v[240:255], a[252:255], v[32:35], 0\n"
    "s_waitcnt lgkmcnt(6)\n"
    "ds_read_b64_tr_b16 a[240:241], v2 offset:16640\n"
    "ds_read_b64_tr_b16 a[242:243], v2 offset:18688\n"
    "buffer_load_dwordx4 v12, s[8:11], s47 offen offset:2048 lds\n"
    "v_mfma_f32_32x32x16_bf16 v[128:143], a[224:227], v[36:39], v[128:143]\n"
    "s_waitcnt lgkmcnt(6)\n"
    "ds_read_b64_tr_b16 a[244:245], v3 offset:16640\n"
    "ds_read_b64_tr_b16 a[246:247], v3 offset:18688\n"
    "ds_read_b128 v[44:47], v99\n"
    "v_mfma_f32_32x32x16_bf16 v[144:159], a[228:231], v[36:39], v[144:159]\n"
    "s_waitcnt lgkmcnt(7)\n"
    "ds_read_b64_tr_b16 a[248:249], v4 offset:16640\n"
    "ds_read_b64_tr_b16 a[250:251], v4 offset:18688\n"
    "buffer_load_dwordx4 v13, s[8:11], s48 offen offset:3072 lds\n"
    "v_add_u32 v12, 0x20000, v12\n"
    "v_add_u32 v13, 0x20000, v13\n"
    "v_mfma_f32_32x32x16_bf16 v[160:175], a[232:235], v[36:39], v[160:175]\n"
    "s_waitcnt lgkmcnt(7)\n"
    "ds_read_b64_tr_b16 a[252:253], v5 offset:16640\n"
    "ds_read_b64_tr_b16 a[254:255], v5 offset:18688\n"
    "v_mfma_f32_32x32x16_bf16 v[176:191], a[236:239], v[36:39], v[176:191]\n"
    "s_waitcnt lgkmcnt(7)\n"
    "s_barrier\n"
    "s_add_u32 m0, s45, 0\n"
    "ds_read_b64_tr_b16 a[224:225], v2 offset:32768\n"
    "ds_read_b64_tr_b16 a[226:227], v2 offset:34816\n"
    "buffer_load_dwordx4 v12, s[8:11], 0 offen lds\n"
    "v_mfma_f32_32x32x16_bf16 v[192:207], a[240:243], v[36:39], v[192:207]\n"
    "s_waitcnt lgkmcnt(7)\n"
    "ds_read_b64_tr_b16 a[228:229], v3 offset:32768\n"
    "ds_read_b64_tr_b16 a[230:231], v3 offset:34816\n"
    "v_mfma_f32_32x32x16_bf16 v[208:223], a[244:247], v[36:39], v[208:223]\n"
    "s_waitcnt lgkmcnt(6)\n"
    "ds_read_b64_tr_b16 a[232:233], v4 offset:32768\n"
    "ds_read_b64_tr_b16 a[234:235], v4 offset:34816\n"
    "buffer_load_dwordx4 v13, s[8:11], s46 offen offset:1024 lds\n"
    "v_mfma_f32_32x32x16_bf16 v[224:239], a[248:251], v[36:39], v[224:239]\n"
    "s_waitcnt lgkmcnt(6)\n"
    "ds_read_b64_tr_b16 a[236:237], v5 offset:32768\n"
    "ds_read_b64_tr_b16 a[238:239], v5 offset:34816\n"
    "v_mfma_f32_32x32x16_bf16 v[240:255], a[252:255], v[36:39], v[240:255]\n"
    "s_waitcnt lgkmcnt(6)\n"
    "ds_read_b64_tr_b16 a[240:241], v2 offset:33024\n"
    "ds_read_b64_tr_b16 a[242:243], v2 offset:35072\n"
    "buffer_load_dwordx4 v12, s[8:11], s47 offen offset:2048 lds\n"
    "v_mfma_f32_32x32x16_bf16 v[128:143], a[224:227], v[40:43], v[128:143]\n"
    "s_waitcnt lgkmcnt(6)\n"
    "ds_read_b64_tr_b16 a[244:245], v3 offset:33024\n"
    "ds_read_b64_tr_b16 a[246:247], v3 offset:35072\n"
    "ds_read_b128 v[32:35], v96 offset:128\n"
    "v_mfma_f32_32x32x16_bf16 v[144:159], a[228:231], v[40:43], v[144:159]\n"
    "s_waitcnt lgkmcnt(7)\n"
    "ds_read_b64_tr_b16 a[248:249], v4 offset:33024\n"
    "ds_read_b64_tr_b16 a[250:251], v4 offset:35072\n"
    "buffer_load_dwordx4 v13, s[8:11], s48 offen offset:3072 lds\n"
    "v_add_u32 v12, 0x20000, v12\n"
    "v_add_u32 v13, 0x20000, v13\n"
    "v_mfma_f32_32x32x16_bf16 v[160:175], a[232:235], v[40:43], v[160:175]\n"
    "s_waitcnt lgkmcnt(7)\n"
    "ds_read_b64_tr_b16 a[252:253], v5 offset:33024\n"
    "ds_read_b64_tr_b16 a[254:255], v5 offset:35072\n"
    "v_mfma_f32_32x32x16_bf16 v[176:191], a[236:239], v[40:43], v[176:191]\n"
    "s_waitcnt lgkmcnt(7)\n"
    "s_barrier\n"
    "s_add_u32 m0, s45, 0x4000\n"
    "ds_read_b64_tr_b16 a[224:225], v2 offset:49152\n"
    "ds_read_b64_tr_b16 a[226:227], v2 offset:51200\n"
    "buffer_load_dwordx4 v12, s[8:11], 0 offen lds\n"
    "v_mfma_f32_32x32x16_bf16 v[192:207], a[240:243], v[40:43], v[192:207]\n"
    "s_waitcnt lgkmcnt(7)\n"
    "ds_read_b64_tr_b16 a[228:229], v3 offset:49152\n"
    "ds_read_b64_tr_b16 a[230:231], v3 offset:51200\n"
    "v_mfma_f32_32x32x16_bf16 v[208:223], a[244:247], v[40:43], v[208:223]\n"
    "s_waitcnt lgkmcnt(6)\n"
    "ds_read_b64_tr_b16 a[232:233], v4 offset:49152\n"
    "ds_read_b64_tr_b16 a[234:235], v4 offset:51200\n"
    "buffer_load_dwordx4 v13, s[8:11], s46 offen offset:1024 lds\n"
    "v_mfma_f32_32x32x16_bf16 v[224:239], a[248:251], v[40:43], v[224:239]\n"
    "s_waitcnt lgkmcnt(6)\n"
    "ds_read_b64_tr_b16 a[236:237], v5 offset:49152\n"
    "ds_read_b64_tr_b16 a[238:239], v5 offset:51200\n"
    "v_mfma_f32_32x32x16_bf16 v[240:255], a[252:255], v[40:43], v[240:255]\n"
    "s_waitcnt lgkmcnt(6)\n"
    "ds_read_b64_tr_b16 a[240:241], v2 offset:49408\n"
    "ds_read_b64_tr_b16 a[242:243], v2 offset:51456\n"
    "buffer_load_dwordx4 v12, s[8:11], s47 offen offset:2048 lds\n"
    "v_mfma_f32_32x32x16_bf16 v[128:143], a[224:227], v[44:47], v[128:143]\n"
    "s_waitcnt lgkmcnt(6)\n"
    "ds_read_b64_tr_b16 a[244:245], v3 offset:49408\n"
    "ds_read_b64_tr_b16 a[246:247], v3 offset:51456\n"
    "ds_read_b128 v[36:39], v97 offset:128\n"
    "v_mfma_f32_32x32x16_bf16 v[144:159], a[228:231], v[44:47], v[144:159]\n"
    "s_waitcnt lgkmcnt(7)\n"
    "ds_read_b64_tr_b16 a[248:249], v4 offset:49408\n"
    "ds_read_b64_tr_b16 a[250:251], v4 offset:51456\n"
    "buffer_load_dwordx4 v13, s[8:11], s48 offen offset:3072 lds\n"
    "v_add_u32 v12, 0x20000, v12\n"
    "v_add_u32 v13, 0x20000, v13\n"
    "v_mfma_f32_32x32x16_bf16 v[160:175], a[232:235], v[44:47], v[160:175]\n"
    "s_waitcnt lgkmcnt(7)\n"
    "ds_read_b64_tr_b16 a[252:253], v5 offset:49408\n"
    "ds_read_b64_tr_b16 a[254:255], v5 offset:51456\n"
    "v_mfma_f32_32x32x16_bf16 v[176:191], a[236:239], v[44:47], v[176:191]\n"
    "s_waitcnt vmcnt(12) lgkmcnt(7)\n"
    "s_barrier\n"
    "s_add_u32 m0, s45, 0x8000\n"
    "v_xor_b32 v2, 0x10000, v2\n"
    "ds_read_b64_tr_b16 a[224:225], v2\n"
    "ds_read_b64_tr_b16 a[226:227], v2 offset:2048\n"
    "buffer_load_dwordx4 v12, s[8:11], 0 offen lds\n"
    "s_lshl_b32 s58, s42, 2\n"
    "s_add_u32 s58, s58, s40\n"
    "s_mul_i32 s59, s58, 28\n"
    "v_min_u32 v25, 27, v1\n"
    "v_add_u32 v25, s59, v25\n"
    "v_lshrrev_b32 v48, 3, v25\n"
    "v_lshlrev_b32 v48, 13, v48\n"
    "v_mfma_f32_32x32x16_bf16 v[192:207], a[240:243], v[44:47], v[192:207]\n"
    "s_waitcnt lgkmcnt(7)\n"
    "v_xor_b32 v3, 0x10000, v3\n"
    "ds_read_b64_tr_b16 a[228:229], v3\n"
    "ds_read_b64_tr_b16 a[230:231], v3 offset:2048\n"
    "v_mfma_f32_32x32x16_bf16 v[208:223], a[244:247], v[44:47], v[208:223]\n"
    "s_waitcnt lgkmcnt(6)\n"
    "v_xor_b32 v4, 0x10000, v4\n"
    "ds_read_b64_tr_b16 a[232:233], v4\n"
    "ds_read_b64_tr_b16 a[234:235], v4 offset:2048\n"
    "buffer_load_dwordx4 v13, s[8:11], s46 offen offset:1024 lds\n"
    "v_mfma_f32_32x32x16_bf16 v[224:239], a[248:251], v[44:47], v[224:239]\n"
    "s_waitcnt lgkmcnt(6)\n"
    "v_xor_b32 v5, 0x10000, v5\n"
    "ds_read_b64_tr_b16 a[236:237], v5\n"
    "ds_read_b64_tr_b16 a[238:239], v5 offset:2048\n"
    "v_mfma_f32_32x32x16_bf16 v[240:255], a[252:255], v[44:47], v[240:255]\n"
    "s_waitcnt lgkmcnt(6)\n"
    "ds_read_b64_tr_b16 a[240:241], v2 offset:256\n"
    "ds_read_b64_tr_b16 a[242:243], v2 offset:2304\n"
    "buffer_load_dwordx4 v12, s[8:11], s47 offen offset:2048 lds\n"
    "v_and_b32 v27, 7, v25\n"
    "v_lshl_add_u32 v48, v27, 7, v48\n"
    "s_lshl_b32 s60, s41, 10\n"
    "v_add_u32 v48, s60, v48\n"
    "s_mul_i32 s60, s41, 0x70000\n"
    "v_lshl_add_u32 v49, v25, 7, s60\n"
    "s_lshl_b32 s59, s58, 4\n"
    "v_mfma_f32_32x32x16_bf16 v[128:143], a[224:227], v[32:35], v[128:143]\n"
    "s_waitcnt lgkmcnt(6)\n"
    "ds_read_b64_tr_b16 a[244:245], v3 offset:256\n"
    "ds_read_b64_tr_b16 a[246:247], v3 offset:2304\n"
    "ds_read_b128 v[40:43], v98 offset:128\n"
    "v_mfma_f32_32x32x16_bf16 v[144:159], a[228:231], v[32:35], v[144:159]\n"
    "s_waitcnt lgkmcnt(7)\n"
    "ds_read_b64_tr_b16 a[248:249], v4 offset:256\n"
    "ds_read_b64_tr_b16 a[250:251], v4 offset:2304\n"
    "buffer_load_dwordx4 v13, s[8:11], s48 offen offset:3072 lds\n"
    "v_add_u32 v12, 0x20000, v12\n"
    "v_add_u32 v13, 0x20000, v13\n"
    "v_mfma_f32_32x32x16_bf16 v[160:175], a[232:235], v[32:35], v[160:175]\n"
    "s_waitcnt lgkmcnt(7)\n"
    "ds_read_b64_tr_b16 a[252:253], v5 offset:256\n"
    "ds_read_b64_tr_b16 a[254:255], v5 offset:2304\n"
    "v_mfma_f32_32x32x16_bf16 v[176:191], a[236:239], v[32:35], v[176:191]\n"
    "s_waitcnt vmcnt(12) lgkmcnt(7)\n"
    "s_barrier\n"
    "s_add_u32 m0, s45, 0xc000\n"
    "ds_read_b64_tr_b16 a[224:225], v2 offset:16384\n"
    "ds_read_b64_tr_b16 a[226:227], v2 offset:18432\n"
    "buffer_load_dwordx4 v12, s[8:11], 0 offen lds\n"
    "v_min_u32 v25, 15, v1\n"
    "v_add_u32 v25, s59, v25\n"
    "s_lshl_b32 s60, s41, 20\n"
    "s_add_u32 s60, s60, 0xc0000\n"
    "v_lshl_add_u32 v50, v25, 7, s60\n"
    "v_mfma_f32_32x32x16_bf16 v[192:207], a[240:243], v[32:35], v[192:207]\n"
    "s_waitcnt lgkmcnt(7)\n"
    "ds_read_b64_tr_b16 a[228:229], v3 offset:16384\n"
    "ds_read_b64_tr_b16 a[230:231], v3 offset:18432\n"
    "v_mfma_f32_32x32x16_bf16 v[208:223], a[244:247], v[32:35], v[208:223]\n"
    "s_waitcnt lgkmcnt(6)\n"
    "ds_read_b64_tr_b16 a[232:233], v4 offset:16384\n"
    "ds_read_b64_tr_b16 a[234:235], v4 offset:18432\n"
    "buffer_load_dwordx4 v13, s[8:11], s46 offen offset:1024 lds\n"
    "v_mfma_f32_32x32x16_bf16 v[224:239], a[248:251], v[32:35], v[224:239]\n"
    "s_waitcnt lgkmcnt(6)\n"
    "ds_read_b64_tr_b16 a[236:237], v5 offset:16384\n"
    "ds_read_b64_tr_b16 a[238:239], v5 offset:18432\n"
    "v_mfma_f32_32x32x16_bf16 v[240:255], a[252:255], v[32:35], v[240:255]\n"
    "s_waitcnt lgkmcnt(6)\n"
    "ds_read_b64_tr_b16 a[240:241], v2 offset:16640\n"
    "ds_read_b64_tr_b16 a[242:243], v2 offset:18688\n"
    "buffer_load_dwordx4 v12, s[8:11], s47 offen offset:2048 lds\n"
    "buffer_load_dword a206, v50, s[12:15], 0 offen\n"
    "buffer_load_dword a207, v50, s[16:19], 0 offen\n"
    "v_mfma_f32_32x32x16_bf16 v[128:143], a[224:227], v[36:39], v[128:143]\n"
    "s_waitcnt lgkmcnt(6)\n"
    "ds_read_b64_tr_b16 a[244:245], v3 offset:16640\n"
    "ds_read_b64_tr_b16 a[246:247], v3 offset:18688\n"
    "ds_read_b128 v[44:47], v99 offset:128\n"
    "v_mfma_f32_32x32x16_bf16 v[144:159], a[228:231], v[36:39], v[144:159]\n"
    "s_waitcnt lgkmcnt(7)\n"
    "ds_read_b64_tr_b16 a[248:249], v4 offset:16640\n"
    "ds_read_b64_tr_b16 a[250:251], v4 offset:18688\n"
    "buffer_load_dwordx4 v13, s[8:11], s48 offen offset:3072 lds\n"
    "v_add_u32 v12, 0x20000, v12\n"
    "v_add_u32 v13, 0x20000, v13\n"
    "v_mfma_f32_32x32x16_bf16 v[160:175], a[232:235], v[36:39], v[160:175]\n"
    "s_waitcnt lgkmcnt(7)\n"
    "ds_read_b64_tr_b16 a[252:253], v5 offset:16640\n"
    "ds_read_b64_tr_b16 a[254:255], v5 offset:18688\n"
    "v_mfma_f32_32x32x16_bf16 v[176:191], a[236:239], v[36:39], v[176:191]\n"
    "s_waitcnt vmcnt(14) lgkmcnt(7)\n"
    "s_barrier\n"
    "s_add_u32 m0, s45, 0x10000\n"
    "v_xor_b32 v2, 0x10000, v2\n"
    "ds_read_b64_tr_b16 a[224:225], v2\n"
    "ds_read_b64_tr_b16 a[226:227], v2 offset:2048\n"
    "buffer_load_dwordx4 v12, s[8:11], 0 offen lds\n"
    "v_mfma_f32_32x32x16_bf16 v[192:207], a[240:243], v[36:39], v[192:207]\n"
    "s_waitcnt lgkmcnt(7)\n"
    "v_xor_b32 v3, 0x10000, v3\n"
    "ds_read_b64_tr_b16 a[228:229], v3\n"
    "ds_read_b64_tr_b16 a[230:231], v3 offset:2048\n"
    "v_mfma_f32_32x32x16_bf16 v[208:223], a[244:247], v[36:39], v[208:223]\n"
    "s_waitcnt lgkmcnt(6)\n"
    "v_xor_b32 v4, 0x10000, v4\n"
    "ds_read_b64_tr_b16 a[232:233], v4\n"
    "ds_read_b64_tr_b16 a[234:235], v4 offset:2048\n"
    "buffer_load_dwordx4 v13, s[8:11], s46 offen offset:1024 lds\n"
    "v_mfma_f32_32x32x16_bf16 v[224:239], a[248:251], v[36:39], v[224:239]\n"
    "s_waitcnt lgkmcnt(6)\n"
    "v_xor_b32 v5, 0x10000, v5\n"
    "ds_read_b64_tr_b16 a[236:237], v5\n"
    "ds_read_b64_tr_b16 a[238:239], v5 offset:2048\n"
    "v_mfma_f32_32x32x16_bf16 v[240:255], a[252:255], v[36:39], v[240:255]\n"
    "s_waitcnt lgkmcnt(6)\n"
    "ds_read_b64_tr_b16 a[240:241], v2 offset:256\n"
    "ds_read_b64_tr_b16 a[242:243], v2 offset:2304\n"
    "buffer_load_dwordx4 v12, s[8:11], s47 offen offset:2048 lds\n"
    "v_mfma_f32_32x32x16_bf16 v[128:143], a[224:227], v[40:43], v[128:143]\n"
    "s_waitcnt lgkmcnt(6)\n"
    "ds_read_b64_tr_b16 a[244:245], v3 offset:256\n"
    "ds_read_b64_tr_b16 a[246:247], v3 offset:2304\n"
    "ds_read_b128 v[32:35], v96 offset:256\n"
    "v_mfma_f32_32x32x16_bf16 v[144:159], a[228:231], v[40:43], v[144:159]\n"
    "s_waitcnt lgkmcnt(7)\n"
    "ds_read_b64_tr_b16 a[248:249], v4 offset:256\n"
    "ds_read_b64_tr_b16 a[250:251], v4 offset:2304\n"
    "buffer_load_dwordx4 v13, s[8:11], s48 offen offset:3072 lds\n"
    "v_add_u32 v12, 0x20000, v12\n"
    "v_add_u32 v13, 0x20000, v13\n"
    "v_mfma_f32_32x32x16_bf16 v[160:175], a[232:235], v[40:43], v[160:175]\n"
    "s_waitcnt lgkmcnt(7)\n"
    "ds_read_b64_tr_b16 a[252:253], v5 offset:256\n"
    "ds_read_b64_tr_b16 a[254:255], v5 offset:2304\n"
    "v_mfma_f32_32x32x16_bf16 v[176:191], a[236:239], v[40:43], v[176:191]\n"
    "s_waitcnt vmcnt(14) lgkmcnt(7)\n"
    "s_barrier\n"
    "s_add_u32 m0, s45, 0x14000\n"
    "ds_read_b64_tr_b16 a[224:225], v2 offset:16384\n"
    "ds_read_b64_tr_b16 a[226:227], v2 offset:18432\n"
    "buffer_load_dwordx4 v12, s[8:11], 0 offen lds\n"
    "v_mfma_f32_32x32x16_bf16 v[192:207], a[240:243], v[40:43], v[192:207]\n"
    "s_waitcnt lgkmcnt(7)\n"
    "ds_read_b64_tr_b16 a[228:229], v3 offset:16384\n"
    "ds_read_b64_tr_b16 a[230:231], v3 offset:18432\n"
    "v_mfma_f32_32x32x16_bf16 v[208:223], a[244:247], v[40:43], v[208:223]\n"
    "s_waitcnt lgkmcnt(6)\n"
    "ds_read_b64_tr_b16 a[232:233], v4 offset:16384\n"
    "ds_read_b64_tr_b16 a[234:235], v4 offset:18432\n"
    "buffer_load_dwordx4 v13, s[8:11], s46 offen offset:1024 lds\n"
    "v_mfma_f32_32x32x16_bf16 v[224:239], a[248:251], v[40:43], v[224:239]\n"
    "s_waitcnt lgkmcnt(6)\n"
    "ds_read_b64_tr_b16 a[236:237], v5 offset:16384\n"
    "ds_read_b64_tr_b16 a[238:239], v5 offset:18432\n"
    "v_mfma_f32_32x32x16_bf16 v[240:255], a[252:255], v[40:43], v[240:255]\n"
    "s_waitcnt lgkmcnt(6)\n"
    "ds_read_b64_tr_b16 a[240:241], v2 offset:16640\n"
    "ds_read_b64_tr_b16 a[242:243], v2 offset:18688\n"
    "buffer_load_dwordx4 v12, s[8:11], s47 offen offset:2048 lds\n"
    "v_mfma_f32_32x32x16_bf16 v[128:143], a[224:227], v[44:47], v[128:143]\n"
    "s_waitcnt lgkmcnt(6)\n"
    "ds_read_b64_tr_b16 a[244:245], v3 offset:16640\n"
    "ds_read_b64_tr_b16 a[246:247], v3 offset:18688\n"
    "ds_read_b128 v[36:39], v97 offset:256\n"
    "v_mfma_f32_32x32x16_bf16 v[144:159], a[228:231], v[44:47], v[144:159]\n"
    "s_waitcnt lgkmcnt(7)\n"
    "ds_read_b64_tr_b16 a[248:249], v4 offset:16640\n"
    "ds_read_b64_tr_b16 a[250:251], v4 offset:18688\n"
    "buffer_load_dwordx4 v13, s[8:11], s48 offen offset:3072 lds\n"
    "v_add_u32 v12, 0x20000, v12\n"
    "v_add_u32 v13, 0x20000, v13\n"
    "v_mfma_f32_32x32x16_bf16 v[160:175], a[232:235], v[44:47], v[160:175]\n"
    "s_waitcnt lgkmcnt(7)\n"
    "ds_read_b64_tr_b16 a[252:253], v5 offset:16640\n"
    "ds_read_b64_tr_b16 a[254:255], v5 offset:18688\n"
    "v_mfma_f32_32x32x16_bf16 v[176:191], a[236:239], v[44:47], v[176:191]\n"
    "s_waitcnt vmcnt(14) lgkmcnt(7)\n"
    "s_barrier\n"
    "s_add_u32 m0, s45, 0\n"
    "ds_read_b64_tr_b16 a[224:225], v2 offset:32768\n"
    "ds_read_b64_tr_b16 a[226:227], v2 offset:34816\n"
    "buffer_load_dwordx4 v12, s[8:11], 0 offen lds\n"
    "s_lshl_b32 s58, s2, 2\n"
    "s_add_u32 s58, s58, s40\n"
    "s_lshl_b32 s59, s58, 13\n"
    "v_lshlrev_b32 v101, 7, v1\n"
    "v_mfma_f32_32x32x16_bf16 v[192:207], a[240:243], v[44:47], v[192:207]\n"
    "s_waitcnt lgkmcnt(7)\n"
    "ds_read_b64_tr_b16 a[228:229], v3 offset:32768\n"
    "ds_read_b64_tr_b16 a[230:231], v3 offset:34816\n"
    "v_mfma_f32_32x32x16_bf16 v[208:223], a[244:247], v[44:47], v[208:223]\n"
    "s_waitcnt lgkmcnt(6)\n"
    "ds_read_b64_tr_b16 a[232:233], v4 offset:32768\n"
    "ds_read_b64_tr_b16 a[234:235], v4 offset:34816\n"
    "buffer_load_dwordx4 v13, s[8:11], s46 offen offset:1024 lds\n"
    "v_add_u32 v101, s59, v101\n"
    "s_mul_i32 s59, s58, 0x680\n"
    "v_min_u32 v102, 12, v1\n"
    "v_lshlrev_b32 v102, 7, v102\n"
    "v_mfma_f32_32x32x16_bf16 v[224:239], a[248:251], v[44:47], v[224:239]\n"
    "s_waitcnt lgkmcnt(6)\n"
    "ds_read_b64_tr_b16 a[236:237], v5 offset:32768\n"
    "ds_read_b64_tr_b16 a[238:239], v5 offset:34816\n"
    "v_mfma_f32_32x32x16_bf16 v[240:255], a[252:255], v[44:47], v[240:255]\n"
    "s_waitcnt lgkmcnt(6)\n"
    "ds_read_b64_tr_b16 a[240:241], v2 offset:33024\n"
    "ds_read_b64_tr_b16 a[242:243], v2 offset:35072\n"
    "buffer_load_dwordx4 v12, s[8:11], s47 offen offset:2048 lds\n"
    "v_add_u32 v102, s59, v102\n"
    "s_mul_i32 s59, s58, 0x380\n"
    "v_min_u32 v103, 6, v1\n"
    "v_lshlrev_b32 v103, 7, v103\n"
    "v_mfma_f32_32x32x16_bf16 v[128:143], a[224:227], v[32:35], v[128:143]\n"
    "s_waitcnt lgkmcnt(6)\n"
    "ds_read_b64_tr_b16 a[244:245], v3 offset:33024\n"
    "ds_read_b64_tr_b16 a[246:247], v3 offset:35072\n"
    "ds_read_b128 v[40:43], v98 offset:256\n"
    "v_mfma_f32_32x32x16_bf16 v[144:159], a[228:231], v[32:35], v[144:159]\n"
    "s_waitcnt lgkmcnt(7)\n"
    "ds_read_b64_tr_b16 a[248:249], v4 offset:33024\n"
    "ds_read_b64_tr_b16 a[250:251], v4 offset:35072\n"
    "buffer_load_dwordx4 v13, s[8:11], s48 offen offset:3072 lds\n"
    "v_add_u32 v12, 0x20000, v12\n"
    "v_add_u32 v13, 0x20000, v13\n"
    "v_add_u32 v103, s59, v103\n"
    "v_mfma_f32_32x32x16_bf16 v[160:175], a[232:235], v[32:35], v[160:175]\n"
    "s_waitcnt lgkmcnt(7)\n"
    "ds_read_b64_tr_b16 a[252:253], v5 offset:33024\n"
    "ds_read_b64_tr_b16 a[254:255], v5 offset:35072\n"
    "v_mfma_f32_32x32x16_bf16 v[176:191], a[236:239], v[32:35], v[176:191]\n"
    "s_waitcnt vmcnt(12) lgkmcnt(7)\n"
    "s_barrier\n"
    "s_add_u32 m0, s45, 0x4000\n"
    "ds_read_b64_tr_b16 a[224:225], v2 offset:49152\n"
    "ds_read_b64_tr_b16 a[226:227], v2 offset:51200\n"
    "buffer_load_dwordx4 v12, s[8:11], 0 offen lds\n"
    "v_mfma_f32_32x32x16_bf16 v[192:207], a[240:243], v[32:35], v[192:207]\n"
    "s_waitcnt lgkmcnt(7)\n"
    "ds_read_b64_tr_b16 a[228:229], v3 offset:49152\n"
    "ds_read_b64_tr_b16 a[230:231], v3 offset:51200\n"
    "v_mfma_f32_32x32x16_bf16 v[208:223], a[244:247], v[32:35], v[208:223]\n"
    "s_waitcnt lgkmcnt(6)\n"
    "ds_read_b64_tr_b16 a[232:233], v4 offset:49152\n"
    "ds_read_b64_tr_b16 a[234:235], v4 offset:51200\n"
    "buffer_load_dwordx4 v13, s[8:11], s46 offen offset:1024 lds\n"
    "v_mfma_f32_32x32x16_bf16 v[224:239], a[248:251], v[32:35], v[224:239]\n"
    "s_waitcnt lgkmcnt(6)\n"
    "ds_read_b64_tr_b16 a[236:237], v5 offset:49152\n"
    "ds_read_b64_tr_b16 a[238:239], v5 offset:51200\n"
    "v_mfma_f32_32x32x16_bf16 v[240:255], a[252:255], v[32:35], v[240:255]\n"
    "s_waitcnt lgkmcnt(6)\n"
    "ds_read_b64_tr_b16 a[240:241], v2 offset:49408\n"
    "ds_read_b64_tr_b16 a[242:243], v2 offset:51456\n"
    "buffer_load_dwordx4 v12, s[8:11], s47 offen offset:2048 lds\n"
    "v_mfma_f32_32x32x16_bf16 v[128:143], a[224:227], v[36:39], v[128:143]\n"
    "s_waitcnt lgkmcnt(6)\n"
    "ds_read_b64_tr_b16 a[244:245], v3 offset:49408\n"
    "ds_read_b64_tr_b16 a[246:247], v3 offset:51456\n"
    "ds_read_b128 v[44:47], v99 offset:256\n"
    "v_mfma_f32_32x32x16_bf16 v[144:159], a[228:231], v[36:39], v[144:159]\n"
    "s_waitcnt lgkmcnt(7)\n"
    "ds_read_b64_tr_b16 a[248:249], v4 offset:49408\n"
    "ds_read_b64_tr_b16 a[250:251], v4 offset:51456\n"
    "buffer_load_dwordx4 v13, s[8:11], s48 offen offset:3072 lds\n"
    "v_add_u32 v12, 0x20000, v12\n"
    "v_add_u32 v13, 0x20000, v13\n"
    "v_mfma_f32_32x32x16_bf16 v[160:175], a[232:235], v[36:39], v[160:175]\n"
    "s_waitcnt lgkmcnt(7)\n"
    "ds_read_b64_tr_b16 a[252:253], v5 offset:49408\n"
    "ds_read_b64_tr_b16 a[254:255], v5 offset:51456\n"
    "v_mfma_f32_32x32x16_bf16 v[176:191], a[236:239], v[36:39], v[176:191]\n"
    "s_waitcnt vmcnt(12) lgkmcnt(7)\n"
    "s_barrier\n"
    "s_add_u32 m0, s45, 0x8000\n"
    "v_xor_b32 v2, 0x10000, v2\n"
    "ds_read_b64_tr_b16 a[224:225], v2\n"
    "ds_read_b64_tr_b16 a[226:227], v2 offset:2048\n"
    "buffer_load_dwordx4 v12, s[8:11], 0 offen lds\n"
    "v_mfma_f32_32x32x16_bf16 v[192:207], a[240:243], v[36:39], v[192:207]\n"
    "s_waitcnt lgkmcnt(7)\n"
    "v_xor_b32 v3, 0x10000, v3\n"
    "ds_read_b64_tr_b16 a[228:229], v3\n"
    "ds_read_b64_tr_b16 a[230:231], v3 offset:2048\n"
    "v_mfma_f32_32x32x16_bf16 v[208:223], a[244:247], v[36:39], v[208:223]\n"
    "s_waitcnt lgkmcnt(6)\n"
    "v_xor_b32 v4, 0x10000, v4\n"
    "ds_read_b64_tr_b16 a[232:233], v4\n"
    "ds_read_b64_tr_b16 a[234:235], v4 offset:2048\n"
    "buffer_load_dwordx4 v13, s[8:11], s46 offen offset:1024 lds\n"
    "v_mfma_f32_32x32x16_bf16 v[224:239], a[248:251], v[36:39], v[224:239]\n"
    "s_waitcnt lgkmcnt(6)\n"
    "v_xor_b32 v5, 0x10000, v5\n"
    "ds_read_b64_tr_b16 a[236:237], v5\n"
    "ds_read_b64_tr_b16 a[238:239], v5 offset:2048\n"
    "v_mfma_f32_32x32x16_bf16 v[240:255], a[252:255], v[36:39], v[240:255]\n"
    "s_waitcnt lgkmcnt(6)\n"
    "ds_read_b64_tr_b16 a[240:241], v2 offset:256\n"
    "ds_read_b64_tr_b16 a[242:243], v2 offset:2304\n"
    "buffer_load_dwordx4 v12, s[8:11], s47 offen offset:2048 lds\n"
    "v_mfma_f32_32x32x16_bf16 v[128:143], a[224:227], v[40:43], v[128:143]\n"
    "s_waitcnt lgkmcnt(6)\n"
    "ds_read_b64_tr_b16 a[244:245], v3 offset:256\n"
    "ds_read_b64_tr_b16 a[246:247], v3 offset:2304\n"
    "ds_read_b128 v[32:35], v96 offset:384\n"
    "v_mfma_f32_32x32x16_bf16 v[144:159], a[228:231], v[40:43], v[144:159]\n"
    "s_waitcnt lgkmcnt(7)\n"
    "ds_read_b64_tr_b16 a[248:249], v4 offset:256\n"
    "ds_read_b64_tr_b16 a[250:251], v4 offset:2304\n"
    "buffer_load_dwordx4 v13, s[8:11], s48 offen offset:3072 lds\n"
    "v_add_u32 v12, 0x20000, v12\n"
    "v_add_u32 v13, 0x20000, v13\n"
    "v_mfma_f32_32x32x16_bf16 v[160:175], a[232:235], v[40:43], v[160:175]\n"
    "s_waitcnt lgkmcnt(7)\n"
    "ds_read_b64_tr_b16 a[252:253], v5 offset:256\n"
    "ds_read_b64_tr_b16 a[254:255], v5 offset:2304\n"
    "v_mfma_f32_32x32x16_bf16 v[176:191], a[236:239], v[40:43], v[176:191]\n"
    "s_waitcnt vmcnt(12) lgkmcnt(7)\n"
    "s_barrier\n"
    "s_add_u32 m0, s45, 0xc000\n"
    "ds_read_b64_tr_b16 a[224:225], v2 offset:16384\n"
    "ds_read_b64_tr_b16 a[226:227], v2 offset:18432\n"
    "buffer_load_dwordx4 v12, s[8:11], 0 offen lds\n"
    "v_mfma_f32_32x32x16_bf16 v[192:207], a[240:243], v[40:43], v[192:207]\n"
    "s_waitcnt lgkmcnt(7)\n"
    "ds_read_b64_tr_b16 a[228:229], v3 offset:16384\n"
    "ds_read_b64_tr_b16 a[230:231], v3 offset:18432\n"
    "v_mfma_f32_32x32x16_bf16 v[208:223], a[244:247], v[40:43], v[208:223]\n"
    "s_waitcnt lgkmcnt(6)\n"
    "ds_read_b64_tr_b16 a[232:233], v4 offset:16384\n"
    "ds_read_b64_tr_b16 a[234:235], v4 offset:18432\n"
    "buffer_load_dwordx4 v13, s[8:11], s46 offen offset:1024 lds\n"
    "v_mfma_f32_32x32x16_bf16 v[224:239], a[248:251], v[40:43], v[224:239]\n"
    "s_waitcnt lgkmcnt(6)\n"
    "ds_read_b64_tr_b16 a[236:237], v5 offset:16384\n"
    "ds_read_b64_tr_b16 a[238:239], v5 offset:18432\n"
    "v_mfma_f32_32x32x16_bf16 v[240:255], a[252:255], v[40:43], v[240:255]\n"
    "s_waitcnt lgkmcnt(6)\n"
    "ds_read_b64_tr_b16 a[240:241], v2 offset:16640\n"
    "ds_read_b64_tr_b16 a[242:243], v2 offset:18688\n"
    "buffer_load_dwordx4 v12, s[8:11], s47 offen offset:2048 lds\n"
    "v_mfma_f32_32x32x16_bf16 v[128:143], a[224:227], v[44:47], v[128:143]\n"
    "s_waitcnt lgkmcnt(6)\n"
    "ds_read_b64_tr_b16 a[244:245], v3 offset:16640\n"
    "ds_read_b64_tr_b16 a[246:247], v3 offset:18688\n"
    "ds_read_b128 v[36:39], v97 offset:384\n"
    "v_mfma_f32_32x32x16_bf16 v[144:159], a[228:231], v[44:47], v[144:159]\n"
    "s_waitcnt lgkmcnt(7)\n"
    "ds_read_b64_tr_b16 a[248:249], v4 offset:16640\n"
    "ds_read_b64_tr_b16 a[250:251], v4 offset:18688\n"
    "buffer_load_dwordx4 v13, s[8:11], s48 offen offset:3072 lds\n"
    "v_add_u32 v12, 0x20000, v12\n"
    "v_add_u32 v13, 0x20000, v13\n"
    "v_min_u32 v51, 15, v1\n"
    "v_lshlrev_b32 v51, 21, v51\n"
    "buffer_load_dword a208, v51, s[28:31], 0 offen\n"
    "s_mov_b32 s32, s86\n"
    "s_and_b32 s33, s87, 0xffff\n"
    "s_mov_b32 s34, 0x1c0000\n"
    "s_mov_b32 s35, 0x20000\n"
    "v_min_u32 v51, 1, v1\n"
    "v_lshlrev_b32 v51, 20, v51\n"
    "buffer_load_dword a209, v51, s[32:35], 0 offen\n"
    "v_mfma_f32_32x32x16_bf16 v[160:175], a[232:235], v[44:47], v[160:175]\n"
    "s_waitcnt lgkmcnt(7)\n"
    "ds_read_b64_tr_b16 a[252:253], v5 offset:16640\n"
    "ds_read_b64_tr_b16 a[254:255], v5 offset:18688\n"
    "v_mfma_f32_32x32x16_bf16 v[176:191], a[236:239], v[44:47], v[176:191]\n"
    "s_waitcnt vmcnt(14) lgkmcnt(7)\n"
    "s_barrier\n"
    "s_add_u32 m0, s45, 0x10000\n"
    "v_xor_b32 v2, 0x10000, v2\n"
    "ds_read_b64_tr_b16 a[224:225], v2\n"
    "ds_read_b64_tr_b16 a[226:227], v2 offset:2048\n"
    "buffer_load_dwordx4 v12, s[8:11], 0 offen lds\n"
    "v_mfma_f32_32x32x16_bf16 v[192:207], a[240:243], v[44:47], v[192:207]\n"
    "s_waitcnt lgkmcnt(7)\n"
    "v_xor_b32 v3, 0x10000, v3\n"
    "ds_read_b64_tr_b16 a[228:229], v3\n"
    "ds_read_b64_tr_b16 a[230:231], v3 offset:2048\n"
    "v_mfma_f32_32x32x16_bf16 v[208:223], a[244:247], v[44:47], v[208:223]\n"
    "s_waitcnt lgkmcnt(6)\n"
    "v_xor_b32 v4, 0x10000, v4\n"
    "ds_read_b64_tr_b16 a[232:233], v4\n"
    "ds_read_b64_tr_b16 a[234:235], v4 offset:2048\n"
    "buffer_load_dwordx4 v13, s[8:11], s46 offen offset:1024 lds\n"
    "v_mfma_f32_32x32x16_bf16 v[224:239], a[248:251], v[44:47], v[224:239]\n"
    "s_waitcnt lgkmcnt(6)\n"
    "v_xor_b32 v5, 0x10000, v5\n"
    "ds_read_b64_tr_b16 a[236:237], v5\n"
    "ds_read_b64_tr_b16 a[238:239], v5 offset:2048\n"
    "v_mfma_f32_32x32x16_bf16 v[240:255], a[252:255], v[44:47], v[240:255]\n"
    "s_waitcnt lgkmcnt(6)\n"
    "ds_read_b64_tr_b16 a[240:241], v2 offset:256\n"
    "ds_read_b64_tr_b16 a[242:243], v2 offset:2304\n"
    "buffer_load_dwordx4 v12, s[8:11], s47 offen offset:2048 lds\n"
    "v_mfma_f32_32x32x16_bf16 v[128:143], a[224:227], v[32:35], v[128:143]\n"
    "s_waitcnt lgkmcnt(6)\n"
    "ds_read_b64_tr_b16 a[244:245], v3 offset:256\n"
    "ds_read_b64_tr_b16 a[246:247], v3 offset:2304\n"
    "ds_read_b128 v[40:43], v98 offset:384\n"
    "v_mfma_f32_32x32x16_bf16 v[144:159], a[228:231], v[32:35], v[144:159]\n"
    "s_waitcnt lgkmcnt(7)\n"
    "ds_read_b64_tr_b16 a[248:249], v4 offset:256\n"
    "ds_read_b64_tr_b16 a[250:251], v4 offset:2304\n"
    "buffer_load_dwordx4 v13, s[8:11], s48 offen offset:3072 lds\n"
    "v_add_u32 v12, 0x20000, v12\n"
    "v_add_u32 v13, 0x20000, v13\n"
    "v_mfma_f32_32x32x16_bf16 v[160:175], a[232:235], v[32:35], v[160:175]\n"
    "s_waitcnt lgkmcnt(7)\n"
    "ds_read_b64_tr_b16 a[252:253], v5 offset:256\n"
    "ds_read_b64_tr_b16 a[254:255], v5 offset:2304\n"
    "v_mfma_f32_32x32x16_bf16 v[176:191], a[236:239], v[32:35], v[176:191]\n"
    "s_waitcnt vmcnt(14) lgkmcnt(7)\n"
    "s_barrier\n"
    "s_add_u32 m0, s45, 0x14000\n"
    "ds_read_b64_tr_b16 a[224:225], v2 offset:16384\n"
    "ds_read_b64_tr_b16 a[226:227], v2 offset:18432\n"
    "buffer_load_dwordx4 v12, s[8:11], 0 offen lds\n"
    "v_mfma_f32_32x32x16_bf16 v[192:207], a[240:243], v[32:35], v[192:207]\n"
    "s_waitcnt lgkmcnt(7)\n"
    "ds_read_b64_tr_b16 a[228:229], v3 offset:16384\n"
    "ds_read_b64_tr_b16 a[230:231], v3 offset:18432\n"
    "v_mfma_f32_32x32x16_bf16 v[208:223], a[244:247], v[32:35], v[208:223]\n"
    "s_waitcnt lgkmcnt(6)\n"
    "ds_read_b64_tr_b16 a[232:233], v4 offset:16384\n"
    "ds_read_b64_tr_b16 a[234:235], v4 offset:18432\n"
    "buffer_load_dwordx4 v13, s[8:11], s46 offen offset:1024 lds\n"
    "v_mfma_f32_32x32x16_bf16 v[224:239], a[248:251], v[32:35], v[224:239]\n"
    "s_waitcnt lgkmcnt(6)\n"
    "ds_read_b64_tr_b16 a[236:237], v5 offset:16384\n"
    "ds_read_b64_tr_b16 a[238:239], v5 offset:18432\n"
    "v_mfma_f32_32x32x16_bf16 v[240:255], a[252:255], v[32:35], v[240:255]\n"
    "s_waitcnt lgkmcnt(6)\n"
    "ds_read_b64_tr_b16 a[240:241], v2 offset:16640\n"
    "ds_read_b64_tr_b16 a[242:243], v2 offset:18688\n"
    "buffer_load_dwordx4 v12, s[8:11], s47 offen offset:2048 lds\n"
    "v_mfma_f32_32x32x16_bf16 v[128:143], a[224:227], v[36:39], v[128:143]\n"
    "s_waitcnt lgkmcnt(6)\n"
    "ds_read_b64_tr_b16 a[244:245], v3 offset:16640\n"
    "ds_read_b64_tr_b16 a[246:247], v3 offset:18688\n"
    "ds_read_b128 v[44:47], v99 offset:384\n"
    "v_mfma_f32_32x32x16_bf16 v[144:159], a[228:231], v[36:39], v[144:159]\n"
    "s_waitcnt lgkmcnt(7)\n"
    "ds_read_b64_tr_b16 a[248:249], v4 offset:16640\n"
    "ds_read_b64_tr_b16 a[250:251], v4 offset:18688\n"
    "buffer_load_dwordx4 v13, s[8:11], s48 offen offset:3072 lds\n"
    "v_add_u32 v12, 0x20000, v12\n"
    "v_add_u32 v13, 0x20000, v13\n"
    "v_mfma_f32_32x32x16_bf16 v[160:175], a[232:235], v[36:39], v[160:175]\n"
    "s_waitcnt lgkmcnt(7)\n"
    "ds_read_b64_tr_b16 a[252:253], v5 offset:16640\n"
    "ds_read_b64_tr_b16 a[254:255], v5 offset:18688\n"
    "v_mfma_f32_32x32x16_bf16 v[176:191], a[236:239], v[36:39], v[176:191]\n"
    "s_waitcnt vmcnt(14) lgkmcnt(7)\n"
    "s_barrier\n"
    "s_add_u32 m0, s45, 0\n"
    "ds_read_b64_tr_b16 a[224:225], v2 offset:32768\n"
    "ds_read_b64_tr_b16 a[226:227], v2 offset:34816\n"
    "buffer_load_dwordx4 v12, s[8:11], 0 offen lds\n"
    "v_mfma_f32_32x32x16_bf16 v[192:207], a[240:243], v[36:39], v[192:207]\n"
    "s_waitcnt lgkmcnt(7)\n"
    "ds_read_b64_tr_b16 a[228:229], v3 offset:32768\n"
    "ds_read_b64_tr_b16 a[230:231], v3 offset:34816\n"
    "v_mfma_f32_32x32x16_bf16 v[208:223], a[244:247], v[36:39], v[208:223]\n"
    "s_waitcnt lgkmcnt(6)\n"
    "ds_read_b64_tr_b16 a[232:233], v4 offset:32768\n"
    "ds_read_b64_tr_b16 a[234:235], v4 offset:34816\n"
    "buffer_load_dwordx4 v13, s[8:11], s46 offen offset:1024 lds\n"
    "v_mfma_f32_32x32x16_bf16 v[224:239], a[248:251], v[36:39], v[224:239]\n"
    "s_waitcnt lgkmcnt(6)\n"
    "ds_read_b64_tr_b16 a[236:237], v5 offset:32768\n"
    "ds_read_b64_tr_b16 a[238:239], v5 offset:34816\n"
    "v_mfma_f32_32x32x16_bf16 v[240:255], a[252:255], v[36:39], v[240:255]\n"
    "s_waitcnt lgkmcnt(6)\n"
    "ds_read_b64_tr_b16 a[240:241], v2 offset:33024\n"
    "ds_read_b64_tr_b16 a[242:243], v2 offset:35072\n"
    "buffer_load_dwordx4 v12, s[8:11], s47 offen offset:2048 lds\n"
    "v_mfma_f32_32x32x16_bf16 v[128:143], a[224:227], v[40:43], v[128:143]\n"
    "s_waitcnt lgkmcnt(6)\n"
    "ds_read_b64_tr_b16 a[244:245], v3 offset:33024\n"
    "ds_read_b64_tr_b16 a[246:247], v3 offset:35072\n"
    "ds_read_b128 v[32:35], v96 offset:512\n"
    "v_mfma_f32_32x32x16_bf16 v[144:159], a[228:231], v[40:43], v[144:159]\n"
    "s_waitcnt lgkmcnt(7)\n"
    "ds_read_b64_tr_b16 a[248:249], v4 offset:33024\n"
    "ds_read_b64_tr_b16 a[250:251], v4 offset:35072\n"
    "buffer_load_dwordx4 v13, s[8:11], s48 offen offset:3072 lds\n"
    "v_add_u32 v12, 0x20000, v12\n"
    "v_add_u32 v13, 0x20000, v13\n"
    "v_mfma_f32_32x32x16_bf16 v[160:175], a[232:235], v[40:43], v[160:175]\n"
    "s_waitcnt lgkmcnt(7)\n"
    "ds_read_b64_tr_b16 a[252:253], v5 offset:33024\n"
    "ds_read_b64_tr_b16 a[254:255], v5 offset:35072\n"
    "v_mfma_f32_32x32x16_bf16 v[176:191], a[236:239], v[40:43], v[176:191]\n"
    "s_waitcnt vmcnt(14) lgkmcnt(7)\n"
    "s_barrier\n"
    "s_add_u32 m0, s45, 0x4000\n"
    "ds_read_b64_tr_b16 a[224:225], v2 offset:49152\n"
    "ds_read_b64_tr_b16 a[226:227], v2 offset:51200\n"
    "buffer_load_dwordx4 v12, s[8:11], 0 offen lds\n"
    "v_mfma_f32_32x32x16_bf16 v[192:207], a[240:243], v[40:43], v[192:207]\n"
    "s_waitcnt lgkmcnt(7)\n"
    "ds_read_b64_tr_b16 a[228:229], v3 offset:49152\n"
    "ds_read_b64_tr_b16 a[230:231], v3 offset:51200\n"
    "v_mfma_f32_32x32x16_bf16 v[208:223], a[244:247], v[40:43], v[208:223]\n"
    "s_waitcnt lgkmcnt(6)\n"
    "ds_read_b64_tr_b16 a[232:233], v4 offset:49152\n"
    "ds_read_b64_tr_b16 a[234:235], v4 offset:51200\n"
    "buffer_load_dwordx4 v13, s[8:11], s46 offen offset:1024 lds\n"
    "v_mfma_f32_32x32x16_bf16 v[224:239], a[248:251], v[40:43], v[224:239]\n"
    "s_waitcnt lgkmcnt(6)\n"
    "ds_read_b64_tr_b16 a[236:237], v5 offset:49152\n"
    "ds_read_b64_tr_b16 a[238:239], v5 offset:51200\n"
    "v_mfma_f32_32x32x16_bf16 v[240:255], a[252:255], v[40:43], v[240:255]\n"
    "s_waitcnt lgkmcnt(6)\n"
    "ds_read_b64_tr_b16 a[240:241], v2 offset:49408\n"
    "ds_read_b64_tr_b16 a[242:243], v2 offset:51456\n"
    "buffer_load_dwordx4 v12, s[8:11], s47 offen offset:2048 lds\n"
    "buffer_load_dword a205, v49, s[20:23], 0 offen\n"
    "v_mfma_f32_32x32x16_bf16 v[128:143], a[224:227], v[44:47], v[128:143]\n"
    "s_waitcnt lgkmcnt(6)\n"
    "ds_read_b64_tr_b16 a[244:245], v3 offset:49408\n"
    "ds_read_b64_tr_b16 a[246:247], v3 offset:51456\n"
    "ds_read_b128 v[36:39], v97 offset:512\n"
    "v_mfma_f32_32x32x16_bf16 v[144:159], a[228:231], v[44:47], v[144:159]\n"
    "s_waitcnt lgkmcnt(7)\n"
    "ds_read_b64_tr_b16 a[248:249], v4 offset:49408\n"
    "ds_read_b64_tr_b16 a[250:251], v4 offset:51456\n"
    "buffer_load_dwordx4 v13, s[8:11], s48 offen offset:3072 lds\n"
    "v_add_u32 v12, 0x20000, v12\n"
    "v_add_u32 v13, 0x20000, v13\n"
    "v_mfma_f32_32x32x16_bf16 v[160:175], a[232:235], v[44:47], v[160:175]\n"
    "s_waitcnt lgkmcnt(7)\n"
    "ds_read_b64_tr_b16 a[252:253], v5 offset:49408\n"
    "ds_read_b64_tr_b16 a[254:255], v5 offset:51456\n"
    "v_mfma_f32_32x32x16_bf16 v[176:191], a[236:239], v[44:47], v[176:191]\n"
    "s_waitcnt vmcnt(13) lgkmcnt(7)\n"
    "s_barrier\n"
    "s_add_u32 m0, s45, 0x8000\n"
    "v_xor_b32 v2, 0x10000, v2\n"
    "ds_read_b64_tr_b16 a[224:225], v2\n"
    "ds_read_b64_tr_b16 a[226:227], v2 offset:2048\n"
    "buffer_load_dwordx4 v12, s[8:11], 0 offen lds\n"
    "v_mfma_f32_32x32x16_bf16 v[192:207], a[240:243], v[44:47], v[192:207]\n"
    "s_waitcnt lgkmcnt(7)\n"
    "v_xor_b32 v3, 0x10000, v3\n"
    "ds_read_b64_tr_b16 a[228:229], v3\n"
    "ds_read_b64_tr_b16 a[230:231], v3 offset:2048\n"
    "v_mfma_f32_32x32x16_bf16 v[208:223], a[244:247], v[44:47], v[208:223]\n"
    "s_waitcnt lgkmcnt(6)\n"
    "v_xor_b32 v4, 0x10000, v4\n"
    "ds_read_b64_tr_b16 a[232:233], v4\n"
    "ds_read_b64_tr_b16 a[234:235], v4 offset:2048\n"
    "buffer_load_dwordx4 v13, s[8:11], s46 offen offset:1024 lds\n"
    "v_mfma_f32_32x32x16_bf16 v[224:239], a[248:251], v[44:47], v[224:239]\n"
    "s_waitcnt lgkmcnt(6)\n"
    "v_xor_b32 v5, 0x10000, v5\n"
    "ds_read_b64_tr_b16 a[236:237], v5\n"
    "ds_read_b64_tr_b16 a[238:239], v5 offset:2048\n"
    "v_mfma_f32_32x32x16_bf16 v[240:255], a[252:255], v[44:47], v[240:255]\n"
    "s_waitcnt lgkmcnt(6)\n"
    "ds_read_b64_tr_b16 a[240:241], v2 offset:256\n"
    "ds_read_b64_tr_b16 a[242:243], v2 offset:2304\n"
    "buffer_load_dwordx4 v12, s[8:11], s47 offen offset:2048 lds\n"
    "v_mfma_f32_32x32x16_bf16 v[128:143], a[224:227], v[32:35], v[128:143]\n"
    "s_waitcnt lgkmcnt(6)\n"
    "ds_read_b64_tr_b16 a[244:245], v3 offset:256\n"
    "ds_read_b64_tr_b16 a[246:247], v3 offset:2304\n"
    "ds_read_b128 v[40:43], v98 offset:512\n"
    "v_mfma_f32_32x32x16_bf16 v[144:159], a[228:231], v[32:35], v[144:159]\n"
    "s_waitcnt lgkmcnt(7)\n"
    "ds_read_b64_tr_b16 a[248:249], v4 offset:256\n"
    "ds_read_b64_tr_b16 a[250:251], v4 offset:2304\n"
    "buffer_load_dwordx4 v13, s[8:11], s48 offen offset:3072 lds\n"
    "v_add_u32 v12, 0x20000, v12\n"
    "v_add_u32 v13, 0x20000, v13\n"
    "v_mfma_f32_32x32x16_bf16 v[160:175], a[232:235], v[32:35], v[160:175]\n"
    "s_waitcnt lgkmcnt(7)\n"
    "ds_read_b64_tr_b16 a[252:253], v5 offset:256\n"
    "ds_read_b64_tr_b16 a[254:255], v5 offset:2304\n"
    "v_mfma_f32_32x32x16_bf16 v[176:191], a[236:239], v[32:35], v[176:191]\n"
    "s_waitcnt vmcnt(13) lgkmcnt(7)\n"
    "s_barrier\n"
    "s_add_u32 m0, s45, 0xc000\n"
    "ds_read_b64_tr_b16 a[224:225], v2 offset:16384\n"
    "ds_read_b64_tr_b16 a[226:227], v2 offset:18432\n"
    "buffer_load_dwordx4 v12, s[8:11], 0 offen lds\n"
    "v_mfma_f32_32x32x16_bf16 v[192:207], a[240:243], v[32:35], v[192:207]\n"
    "s_waitcnt lgkmcnt(7)\n"
    "ds_read_b64_tr_b16 a[228:229], v3 offset:16384\n"
    "ds_read_b64_tr_b16 a[230:231], v3 offset:18432\n"
    "v_mfma_f32_32x32x16_bf16 v[208:223], a[244:247], v[32:35], v[208:223]\n"
    "s_waitcnt lgkmcnt(6)\n"
    "ds_read_b64_tr_b16 a[232:233], v4 offset:16384\n"
    "ds_read_b64_tr_b16 a[234:235], v4 offset:18432\n"
    "buffer_load_dwordx4 v13, s[8:11], s46 offen offset:1024 lds\n"
    "v_mfma_f32_32x32x16_bf16 v[224:239], a[248:251], v[32:35], v[224:239]\n"
    "s_waitcnt lgkmcnt(6)\n"
    "ds_read_b64_tr_b16 a[236:237], v5 offset:16384\n"
    "ds_read_b64_tr_b16 a[238:239], v5 offset:18432\n"
    "v_mfma_f32_32x32x16_bf16 v[240:255], a[252:255], v[32:35], v[240:255]\n"
    "s_waitcnt lgkmcnt(6)\n"
    "ds_read_b64_tr_b16 a[240:241], v2 offset:16640\n"
    "ds_read_b64_tr_b16 a[242:243], v2 offset:18688\n"
    "buffer_load_dwordx4 v12, s[8:11], s47 offen offset:2048 lds\n"
    "v_mfma_f32_32x32x16_bf16 v[128:143], a[224:227], v[36:39], v[128:143]\n"
    "s_waitcnt lgkmcnt(6)\n"
    "ds_read_b64_tr_b16 a[244:245], v3 offset:16640\n"
    "ds_read_b64_tr_b16 a[246:247], v3 offset:18688\n"
    "ds_read_b128 v[44:47], v99 offset:512\n"
    "v_mfma_f32_32x32x16_bf16 v[144:159], a[228:231], v[36:39], v[144:159]\n"
    "s_waitcnt lgkmcnt(7)\n"
    "ds_read_b64_tr_b16 a[248:249], v4 offset:16640\n"
    "ds_read_b64_tr_b16 a[250:251], v4 offset:18688\n"
    "buffer_load_dwordx4 v13, s[8:11], s48 offen offset:3072 lds\n"
    "v_add_u32 v12, 0x20000, v12\n"
    "v_add_u32 v13, 0x20000, v13\n"
    "v_mfma_f32_32x32x16_bf16 v[160:175], a[232:235], v[36:39], v[160:175]\n"
    "s_waitcnt lgkmcnt(7)\n"
    "ds_read_b64_tr_b16 a[252:253], v5 offset:16640\n"
    "ds_read_b64_tr_b16 a[254:255], v5 offset:18688\n"
    "v_mfma_f32_32x32x16_bf16 v[176:191], a[236:239], v[36:39], v[176:191]\n"
    "s_waitcnt vmcnt(13) lgkmcnt(7)\n"
    "s_barrier\n"
    "s_add_u32 m0, s45, 0x10000\n"
    "v_xor_b32 v2, 0x10000, v2\n"
    "ds_read_b64_tr_b16 a[224:225], v2\n"
    "ds_read_b64_tr_b16 a[226:227], v2 offset:2048\n"
    "buffer_load_dwordx4 v12, s[8:11], 0 offen lds\n"
    "s_lshl_b32 s58, s42, 2\n"
    "s_add_u32 s58, s58, s40\n"
    "s_mul_i32 s58, s58, 0x60000\n"
    "v_min_u32 v55, 5, v1\n"
    "v_lshlrev_b32 v55, 16, v55\n"
    "buffer_load_dword a214, v55, s[28:31], s58 offen\n"
    "v_mfma_f32_32x32x16_bf16 v[192:207], a[240:243], v[36:39], v[192:207]\n"
    "s_waitcnt lgkmcnt(7)\n"
    "v_xor_b32 v3, 0x10000, v3\n"
    "ds_read_b64_tr_b16 a[228:229], v3\n"
    "ds_read_b64_tr_b16 a[230:231], v3 offset:2048\n"
    "v_mfma_f32_32x32x16_bf16 v[208:223], a[244:247], v[36:39], v[208:223]\n"
    "s_waitcnt lgkmcnt(6)\n"
    "v_xor_b32 v4, 0x10000, v4\n"
    "ds_read_b64_tr_b16 a[232:233], v4\n"
    "ds_read_b64_tr_b16 a[234:235], v4 offset:2048\n"
    "buffer_load_dwordx4 v13, s[8:11], s46 offen offset:1024 lds\n"
    "v_mfma_f32_32x32x16_bf16 v[224:239], a[248:251], v[36:39], v[224:239]\n"
    "s_waitcnt lgkmcnt(6)\n"
    "v_xor_b32 v5, 0x10000, v5\n"
    "ds_read_b64_tr_b16 a[236:237], v5\n"
    "ds_read_b64_tr_b16 a[238:239], v5 offset:2048\n"
    "v_mfma_f32_32x32x16_bf16 v[240:255], a[252:255], v[36:39], v[240:255]\n"
    "s_waitcnt lgkmcnt(6)\n"
    "ds_read_b64_tr_b16 a[240:241], v2 offset:256\n"
    "ds_read_b64_tr_b16 a[242:243], v2 offset:2304\n"
    "buffer_load_dwordx4 v12, s[8:11], s47 offen offset:2048 lds\n"
    "v_mfma_f32_32x32x16_bf16 v[128:143], a[224:227], v[40:43], v[128:143]\n"
    "s_waitcnt lgkmcnt(6)\n"
    "ds_read_b64_tr_b16 a[244:245], v3 offset:256\n"
    "ds_read_b64_tr_b16 a[246:247], v3 offset:2304\n"
    "ds_read_b128 v[32:35], v96 offset:640\n"
    "v_mfma_f32_32x32x16_bf16 v[144:159], a[228:231], v[40:43], v[144:159]\n"
    "s_waitcnt lgkmcnt(7)\n"
    "ds_read_b64_tr_b16 a[248:249], v4 offset:256\n"
    "ds_read_b64_tr_b16 a[250:251], v4 offset:2304\n"
    "buffer_load_dwordx4 v13, s[8:11], s48 offen offset:3072 lds\n"
    "v_add_u32 v12, 0x20000, v12\n"
    "v_add_u32 v13, 0x20000, v13\n"
    "v_mfma_f32_32x32x16_bf16 v[160:175], a[232:235], v[40:43], v[160:175]\n"
    "s_waitcnt lgkmcnt(7)\n"
    "ds_read_b64_tr_b16 a[252:253], v5 offset:256\n"
    "ds_read_b64_tr_b16 a[254:255], v5 offset:2304\n"
    "v_mfma_f32_32x32x16_bf16 v[176:191], a[236:239], v[40:43], v[176:191]\n"
    "s_waitcnt vmcnt(13) lgkmcnt(7)\n"
    "s_barrier\n"
    "s_add_u32 m0, s45, 0x14000\n"
    "ds_read_b64_tr_b16 a[224:225], v2 offset:16384\n"
    "ds_read_b64_tr_b16 a[226:227], v2 offset:18432\n"
    "buffer_load_dwordx4 v12, s[8:11], 0 offen lds\n"
    "v_mfma_f32_32x32x16_bf16 v[192:207], a[240:243], v[40:43], v[192:207]\n"
    "s_waitcnt lgkmcnt(7)\n"
    "ds_read_b64_tr_b16 a[228:229], v3 offset:16384\n"
    "ds_read_b64_tr_b16 a[230:231], v3 offset:18432\n"
    "v_mfma_f32_32x32x16_bf16 v[208:223], a[244:247], v[40:43], v[208:223]\n"
    "s_waitcnt lgkmcnt(6)\n"
    "ds_read_b64_tr_b16 a[232:233], v4 offset:16384\n"
    "ds_read_b64_tr_b16 a[234:235], v4 offset:18432\n"
    "buffer_load_dwordx4 v13, s[8:11], s46 offen offset:1024 lds\n"
    "v_mfma_f32_32x32x16_bf16 v[224:239], a[248:251], v[40:43], v[224:239]\n"
    "s_waitcnt lgkmcnt(6)\n"
    "ds_read_b64_tr_b16 a[236:237], v5 offset:16384\n"
    "ds_read_b64_tr_b16 a[238:239], v5 offset:18432\n"
    "v_mfma_f32_32x32x16_bf16 v[240:255], a[252:255], v[40:43], v[240:255]\n"
    "s_waitcnt lgkmcnt(6)\n"
    "ds_read_b64_tr_b16 a[240:241], v2 offset:16640\n"
    "ds_read_b64_tr_b16 a[242:243], v2 offset:18688\n"
    "buffer_load_dwordx4 v12, s[8:11], s47 offen offset:2048 lds\n"
    "v_mfma_f32_32x32x16_bf16 v[128:143], a[224:227], v[44:47], v[128:143]\n"
    "s_waitcnt lgkmcnt(6)\n"
    "ds_read_b64_tr_b16 a[244:245], v3 offset:16640\n"
    "ds_read_b64_tr_b16 a[246:247], v3 offset:18688\n"
    "ds_read_b128 v[36:39], v97 offset:640\n"
    "v_mfma_f32_32x32x16_bf16 v[144:159], a[228:231], v[44:47], v[144:159]\n"
    "s_waitcnt lgkmcnt(7)\n"
    "ds_read_b64_tr_b16 a[248:249], v4 offset:16640\n"
    "ds_read_b64_tr_b16 a[250:251], v4 offset:18688\n"
    "buffer_load_dwordx4 v13, s[8:11], s48 offen offset:3072 lds\n"
    "v_add_u32 v12, 0x20000, v12\n"
    "v_add_u32 v13, 0x20000, v13\n"
    "v_mfma_f32_32x32x16_bf16 v[160:175], a[232:235], v[44:47], v[160:175]\n"
    "s_waitcnt lgkmcnt(7)\n"
    "ds_read_b64_tr_b16 a[252:253], v5 offset:16640\n"
    "ds_read_b64_tr_b16 a[254:255], v5 offset:18688\n"
    "v_mfma_f32_32x32x16_bf16 v[176:191], a[236:239], v[44:47], v[176:191]\n"
    "s_waitcnt vmcnt(13) lgkmcnt(7)\n"
    "s_barrier\n"
    "s_add_u32 m0, s45, 0\n"
    "ds_read_b64_tr_b16 a[224:225], v2 offset:32768\n"
    "ds_read_b64_tr_b16 a[226:227], v2 offset:34816\n"
    "buffer_load_dwordx4 v12, s[8:11], 0 offen lds\n"
    "s_lshl_b32 s58, s42, 2\n"
    "s_add_u32 s58, s58, s40\n"
    "s_lshl_b32 s58, s58, 16\n"
    "v_mov_b32 v55, s58\n"
    "s_mov_b32 s32, s76\n"
    "s_and_b32 s33, s77, 0xffff\n"
    "s_mov_b32 s34, 0x800000\n"
    "s_mov_b32 s35, 0x20000\n"
    "buffer_load_dword a215, v55, s[32:35], 0 offen\n"
    "v_mfma_f32_32x32x16_bf16 v[192:207], a[240:243], v[44:47], v[192:207]\n"
    "s_waitcnt lgkmcnt(7)\n"
    "ds_read_b64_tr_b16 a[228:229], v3 offset:32768\n"
    "ds_read_b64_tr_b16 a[230:231], v3 offset:34816\n"
    "v_mfma_f32_32x32x16_bf16 v[208:223], a[244:247], v[44:47], v[208:223]\n"
    "s_waitcnt lgkmcnt(6)\n"
    "ds_read_b64_tr_b16 a[232:233], v4 offset:32768\n"
    "ds_read_b64_tr_b16 a[234:235], v4 offset:34816\n"
    "buffer_load_dwordx4 v13, s[8:11], s46 offen offset:1024 lds\n"
    "v_mfma_f32_32x32x16_bf16 v[224:239], a[248:251], v[44:47], v[224:239]\n"
    "s_waitcnt lgkmcnt(6)\n"
    "ds_read_b64_tr_b16 a[236:237], v5 offset:32768\n"
    "ds_read_b64_tr_b16 a[238:239], v5 offset:34816\n"
    "v_mfma_f32_32x32x16_bf16 v[240:255], a[252:255], v[44:47], v[240:255]\n"
    "s_waitcnt lgkmcnt(6)\n"
    "ds_read_b64_tr_b16 a[240:241], v2 offset:33024\n"
    "ds_read_b64_tr_b16 a[242:243], v2 offset:35072\n"
    "buffer_load_dwordx4 v12, s[8:11], s47 offen offset:2048 lds\n"
    "v_mfma_f32_32x32x16_bf16 v[128:143], a[224:227], v[32:35], v[128:143]\n"
    "s_waitcnt lgkmcnt(6)\n"
    "ds_read_b64_tr_b16 a[244:245], v3 offset:33024\n"
    "ds_read_b64_tr_b16 a[246:247], v3 offset:35072\n"
    "ds_read_b128 v[40:43], v98 offset:640\n"
    "v_mfma_f32_32x32x16_bf16 v[144:159], a[228:231], v[32:35], v[144:159]\n"
    "s_waitcnt lgkmcnt(7)\n"
    "ds_read_b64_tr_b16 a[248:249], v4 offset:33024\n"
    "ds_read_b64_tr_b16 a[250:251], v4 offset:35072\n"
    "buffer_load_dwordx4 v13, s[8:11], s48 offen offset:3072 lds\n"
    "v_add_u32 v12, 0x20000, v12\n"
    "v_add_u32 v13, 0x20000, v13\n"
    "v_mfma_f32_32x32x16_bf16 v[160:175], a[232:235], v[32:35], v[160:175]\n"
    "s_waitcnt lgkmcnt(7)\n"
    "ds_read_b64_tr_b16 a[252:253], v5 offset:33024\n"
    "ds_read_b64_tr_b16 a[254:255], v5 offset:35072\n"
    "v_mfma_f32_32x32x16_bf16 v[176:191], a[236:239], v[32:35], v[176:191]\n"
    "s_waitcnt vmcnt(14) lgkmcnt(7)\n"
    "s_barrier\n"
    "s_add_u32 m0, s45, 0x4000\n"
    "ds_read_b64_tr_b16 a[224:225], v2 offset:49152\n"
    "ds_read_b64_tr_b16 a[226:227], v2 offset:51200\n"
    "buffer_load_dwordx4 v12, s[8:11], 0 offen lds\n"
    "v_mfma_f32_32x32x16_bf16 v[192:207], a[240:243], v[32:35], v[192:207]\n"
    "s_waitcnt lgkmcnt(7)\n"
    "ds_read_b64_tr_b16 a[228:229], v3 offset:49152\n"
    "ds_read_b64_tr_b16 a[230:231], v3 offset:51200\n"
    "v_mfma_f32_32x32x16_bf16 v[208:223], a[244:247], v[32:35], v[208:223]\n"
    "s_waitcnt lgkmcnt(6)\n"
    "ds_read_b64_tr_b16 a[232:233], v4 offset:49152\n"
    "ds_read_b64_tr_b16 a[234:235], v4 offset:51200\n"
    "buffer_load_dwordx4 v13, s[8:11], s46 offen offset:1024 lds\n"
    "v_mfma_f32_32x32x16_bf16 v[224:239], a[248:251], v[32:35], v[224:239]\n"
    "s_waitcnt lgkmcnt(6)\n"
    "ds_read_b64_tr_b16 a[236:237], v5 offset:49152\n"
    "ds_read_b64_tr_b16 a[238:239], v5 offset:51200\n"
    "v_mfma_f32_32x32x16_bf16 v[240:255], a[252:255], v[32:35], v[240:255]\n"
    "s_waitcnt lgkmcnt(6)\n"
    "ds_read_b64_tr_b16 a[240:241], v2 offset:49408\n"
    "ds_read_b64_tr_b16 a[242:243], v2 offset:51456\n"
    "buffer_load_dwordx4 v12, s[8:11], s47 offen offset:2048 lds\n"
    "v_mfma_f32_32x32x16_bf16 v[128:143], a[224:227], v[36:39], v[128:143]\n"
    "s_waitcnt lgkmcnt(6)\n"
    "ds_read_b64_tr_b16 a[244:245], v3 offset:49408\n"
    "ds_read_b64_tr_b16 a[246:247], v3 offset:51456\n"
    "ds_read_b128 v[44:47], v99 offset:640\n"
    "v_mfma_f32_32x32x16_bf16 v[144:159], a[228:231], v[36:39], v[144:159]\n"
    "s_waitcnt lgkmcnt(7)\n"
    "ds_read_b64_tr_b16 a[248:249], v4 offset:49408\n"
    "ds_read_b64_tr_b16 a[250:251], v4 offset:51456\n"
    "buffer_load_dwordx4 v13, s[8:11], s48 offen offset:3072 lds\n"
    "v_add_u32 v12, 0x20000, v12\n"
    "v_add_u32 v13, 0x20000, v13\n"
    "v_mfma_f32_32x32x16_bf16 v[160:175], a[232:235], v[36:39], v[160:175]\n"
    "s_waitcnt lgkmcnt(7)\n"
    "ds_read_b64_tr_b16 a[252:253], v5 offset:49408\n"
    "ds_read_b64_tr_b16 a[254:255], v5 offset:51456\n"
    "v_mfma_f32_32x32x16_bf16 v[176:191], a[236:239], v[36:39], v[176:191]\n"
    "s_waitcnt vmcnt(13) lgkmcnt(7)\n"
    "s_barrier\n"
    "s_add_u32 m0, s45, 0x8000\n"
    "v_xor_b32 v2, 0x10000, v2\n"
    "ds_read_b64_tr_b16 a[224:225], v2\n"
    "ds_read_b64_tr_b16 a[226:227], v2 offset:2048\n"
    "buffer_load_dwordx4 v12, s[8:11], 0 offen lds\n"
    "s_lshl_b32 s58, s42, 2\n"
    "s_add_u32 s58, s58, s40\n"
    "s_lshl_b32 s58, s58, 16\n"
    "v_mov_b32 v55, s58\n"
    "s_mov_b32 s32, s74\n"
    "s_and_b32 s33, s75, 0xffff\n"
    "s_mov_b32 s34, 0x188000\n"
    "s_mov_b32 s35, 0x20000\n"
    "buffer_load_dword a216, v55, s[32:35], 0 offen\n"
    "s_mov_b32 s32, s80\n"
    "s_and_b32 s33, s81, 0xffff\n"
    "s_mov_b32 s34, 0xc4000\n"
    "s_mov_b32 s35, 0x20000\n"
    "buffer_load_dword a217, v55, s[32:35], 0 offen\n"
    "s_mov_b32 s32, s86\n"
    "s_and_b32 s33, s87, 0xffff\n"
    "s_mov_b32 s34, 0x1c0000\n"
    "s_mov_b32 s35, 0x20000\n"
    "buffer_load_dword a218, v55, s[32:35], 0 offen\n"
    "v_mfma_f32_32x32x16_bf16 v[192:207], a[240:243], v[36:39], v[192:207]\n"
    "s_waitcnt lgkmcnt(7)\n"
    "v_xor_b32 v3, 0x10000, v3\n"
    "ds_read_b64_tr_b16 a[228:229], v3\n"
    "ds_read_b64_tr_b16 a[230:231], v3 offset:2048\n"
    "v_mfma_f32_32x32x16_bf16 v[208:223], a[244:247], v[36:39], v[208:223]\n"
    "s_waitcnt lgkmcnt(6)\n"
    "v_xor_b32 v4, 0x10000, v4\n"
    "ds_read_b64_tr_b16 a[232:233], v4\n"
    "ds_read_b64_tr_b16 a[234:235], v4 offset:2048\n"
    "buffer_load_dwordx4 v13, s[8:11], s46 offen offset:1024 lds\n"
    "v_mfma_f32_32x32x16_bf16 v[224:239], a[248:251], v[36:39], v[224:239]\n"
    "s_waitcnt lgkmcnt(6)\n"
    "v_xor_b32 v5, 0x10000, v5\n"
    "ds_read_b64_tr_b16 a[236:237], v5\n"
    "ds_read_b64_tr_b16 a[238:239], v5 offset:2048\n"
    "v_mfma_f32_32x32x16_bf16 v[240:255], a[252:255], v[36:39], v[240:255]\n"
    "s_waitcnt lgkmcnt(6)\n"
    "ds_read_b64_tr_b16 a[240:241], v2 offset:256\n"
    "ds_read_b64_tr_b16 a[242:243], v2 offset:2304\n"
    "buffer_load_dwordx4 v12, s[8:11], s47 offen offset:2048 lds\n"
    "v_mfma_f32_32x32x16_bf16 v[128:143], a[224:227], v[40:43], v[128:143]\n"
    "s_waitcnt lgkmcnt(6)\n"
    "ds_read_b64_tr_b16 a[244:245], v3 offset:256\n"
    "ds_read_b64_tr_b16 a[246:247], v3 offset:2304\n"
    "ds_read_b128 v[32:35], v96 offset:768\n"
    "v_mfma_f32_32x32x16_bf16 v[144:159], a[228:231], v[40:43], v[144:159]\n"
    "s_waitcnt lgkmcnt(7)\n"
    "ds_read_b64_tr_b16 a[248:249], v4 offset:256\n"
    "ds_read_b64_tr_b16 a[250:251], v4 offset:2304\n"
    "buffer_load_dwordx4 v13, s[8:11], s48 offen offset:3072 lds\n"
    "v_add_u32 v12, 0x20000, v12\n"
    "v_add_u32 v13, 0x20000, v13\n"
    "v_mfma_f32_32x32x16_bf16 v[160:175], a[232:235], v[40:43], v[160:175]\n"
    "s_waitcnt lgkmcnt(7)\n"
    "ds_read_b64_tr_b16 a[252:253], v5 offset:256\n"
    "ds_read_b64_tr_b16 a[254:255], v5 offset:2304\n"
    "v_mfma_f32_32x32x16_bf16 v[176:191], a[236:239], v[40:43], v[176:191]\n"
    "s_waitcnt vmcnt(16) lgkmcnt(7)\n"
    "s_barrier\n"
    "s_add_u32 m0, s45, 0xc000\n"
    "ds_read_b64_tr_b16 a[224:225], v2 offset:16384\n"
    "ds_read_b64_tr_b16 a[226:227], v2 offset:18432\n"
    "buffer_load_dwordx4 v12, s[8:11], 0 offen lds\n"
    "v_mfma_f32_32x32x16_bf16 v[192:207], a[240:243], v[40:43], v[192:207]\n"
    "s_waitcnt lgkmcnt(7)\n"
    "ds_read_b64_tr_b16 a[228:229], v3 offset:16384\n"
    "ds_read_b64_tr_b16 a[230:231], v3 offset:18432\n"
    "v_mfma_f32_32x32x16_bf16 v[208:223], a[244:247], v[40:43], v[208:223]\n"
    "s_waitcnt lgkmcnt(6)\n"
    "ds_read_b64_tr_b16 a[232:233], v4 offset:16384\n"
    "ds_read_b64_tr_b16 a[234:235], v4 offset:18432\n"
    "buffer_load_dwordx4 v13, s[8:11], s46 offen offset:1024 lds\n"
    "v_mfma_f32_32x32x16_bf16 v[224:239], a[248:251], v[40:43], v[224:239]\n"
    "s_waitcnt lgkmcnt(6)\n"
    "ds_read_b64_tr_b16 a[236:237], v5 offset:16384\n"
    "ds_read_b64_tr_b16 a[238:239], v5 offset:18432\n"
    "v_mfma_f32_32x32x16_bf16 v[240:255], a[252:255], v[40:43], v[240:255]\n"
    "s_waitcnt lgkmcnt(6)\n"
    "ds_read_b64_tr_b16 a[240:241], v2 offset:16640\n"
    "ds_read_b64_tr_b16 a[242:243], v2 offset:18688\n"
    "buffer_load_dwordx4 v12, s[8:11], s47 offen offset:2048 lds\n"
    "v_mfma_f32_32x32x16_bf16 v[128:143], a[224:227], v[44:47], v[128:143]\n"
    "s_waitcnt lgkmcnt(6)\n"
    "ds_read_b64_tr_b16 a[244:245], v3 offset:16640\n"
    "ds_read_b64_tr_b16 a[246:247], v3 offset:18688\n"
    "ds_read_b128 v[36:39], v97 offset:768\n"
    "v_mfma_f32_32x32x16_bf16 v[144:159], a[228:231], v[44:47], v[144:159]\n"
    "s_waitcnt lgkmcnt(7)\n"
    "ds_read_b64_tr_b16 a[248:249], v4 offset:16640\n"
    "ds_read_b64_tr_b16 a[250:251], v4 offset:18688\n"
    "buffer_load_dwordx4 v13, s[8:11], s48 offen offset:3072 lds\n"
    "v_add_u32 v12, 0x20000, v12\n"
    "v_add_u32 v13, 0x20000, v13\n"
    "v_mfma_f32_32x32x16_bf16 v[160:175], a[232:235], v[44:47], v[160:175]\n"
    "s_waitcnt lgkmcnt(7)\n"
    "ds_read_b64_tr_b16 a[252:253], v5 offset:16640\n"
    "ds_read_b64_tr_b16 a[254:255], v5 offset:18688\n"
    "v_mfma_f32_32x32x16_bf16 v[176:191], a[236:239], v[44:47], v[176:191]\n"
    "s_waitcnt vmcnt(15) lgkmcnt(7)\n"
    "s_barrier\n"
    "s_add_u32 m0, s45, 0x10000\n"
    "v_xor_b32 v2, 0x10000, v2\n"
    "ds_read_b64_tr_b16 a[224:225], v2\n"
    "ds_read_b64_tr_b16 a[226:227], v2 offset:2048\n"
    "buffer_load_dwordx4 v14, s[12:15], s49 offen lds\n"
    "v_mfma_f32_32x32x16_bf16 v[192:207], a[240:243], v[44:47], v[192:207]\n"
    "s_waitcnt lgkmcnt(7)\n"
    "v_xor_b32 v3, 0x10000, v3\n"
    "ds_read_b64_tr_b16 a[228:229], v3\n"
    "ds_read_b64_tr_b16 a[230:231], v3 offset:2048\n"
    "v_mfma_f32_32x32x16_bf16 v[208:223], a[244:247], v[44:47], v[208:223]\n"
    "s_waitcnt lgkmcnt(6)\n"
    "v_xor_b32 v4, 0x10000, v4\n"
    "ds_read_b64_tr_b16 a[232:233], v4\n"
    "ds_read_b64_tr_b16 a[234:235], v4 offset:2048\n"
    "buffer_load_dwordx4 v14, s[12:15], s50 offen offset:1024 lds\n"
    "v_mfma_f32_32x32x16_bf16 v[224:239], a[248:251], v[44:47], v[224:239]\n"
    "s_waitcnt lgkmcnt(6)\n"
    "v_xor_b32 v5, 0x10000, v5\n"
    "ds_read_b64_tr_b16 a[236:237], v5\n"
    "ds_read_b64_tr_b16 a[238:239], v5 offset:2048\n"
    "v_mfma_f32_32x32x16_bf16 v[240:255], a[252:255], v[44:47], v[240:255]\n"
    "s_waitcnt lgkmcnt(6)\n"
    "ds_read_b64_tr_b16 a[240:241], v2 offset:256\n"
    "ds_read_b64_tr_b16 a[242:243], v2 offset:2304\n"
    "buffer_load_dwordx4 v14, s[12:15], s51 offen offset:2048 lds\n"
    "v_mfma_f32_32x32x16_bf16 v[128:143], a[224:227], v[32:35], v[128:143]\n"
    "s_waitcnt lgkmcnt(6)\n"
    "ds_read_b64_tr_b16 a[244:245], v3 offset:256\n"
    "ds_read_b64_tr_b16 a[246:247], v3 offset:2304\n"
    "ds_read_b128 v[40:43], v98 offset:768\n"
    "v_mfma_f32_32x32x16_bf16 v[144:159], a[228:231], v[32:35], v[144:159]\n"
    "s_waitcnt lgkmcnt(7)\n"
    "ds_read_b64_tr_b16 a[248:249], v4 offset:256\n"
    "ds_read_b64_tr_b16 a[250:251], v4 offset:2304\n"
    "buffer_load_dwordx4 v14, s[12:15], s52 offen offset:3072 lds\n"
    "v_add_u32 v14, 0x4000, v14\n"
    "v_mfma_f32_32x32x16_bf16 v[160:175], a[232:235], v[32:35], v[160:175]\n"
    "s_waitcnt lgkmcnt(7)\n"
    "ds_read_b64_tr_b16 a[252:253], v5 offset:256\n"
    "ds_read_b64_tr_b16 a[254:255], v5 offset:2304\n"
    "v_mfma_f32_32x32x16_bf16 v[176:191], a[236:239], v[32:35], v[176:191]\n"
    "s_waitcnt vmcnt(15) lgkmcnt(7)\n"
    "s_barrier\n"
    "s_add_u32 m0, s45, 0x14000\n"
    "ds_read_b64_tr_b16 a[224:225], v2 offset:16384\n"
    "ds_read_b64_tr_b16 a[226:227], v2 offset:18432\n"
    "buffer_load_dwordx4 v14, s[12:15], s49 offen lds\n"
    "v_mfma_f32_32x32x16_bf16 v[192:207], a[240:243], v[32:35], v[192:207]\n"
    "s_waitcnt lgkmcnt(7)\n"
    "ds_read_b64_tr_b16 a[228:229], v3 offset:16384\n"
    "ds_read_b64_tr_b16 a[230:231], v3 offset:18432\n"
    "v_mfma_f32_32x32x16_bf16 v[208:223], a[244:247], v[32:35], v[208:223]\n"
    "s_waitcnt lgkmcnt(6)\n"
    "ds_read_b64_tr_b16 a[232:233], v4 offset:16384\n"
    "ds_read_b64_tr_b16 a[234:235], v4 offset:18432\n"
    "buffer_load_dwordx4 v14, s[12:15], s50 offen offset:1024 lds\n"
    "v_mfma_f32_32x32x16_bf16 v[224:239], a[248:251], v[32:35], v[224:239]\n"
    "s_waitcnt lgkmcnt(6)\n"
    "ds_read_b64_tr_b16 a[236:237], v5 offset:16384\n"
    "ds_read_b64_tr_b16 a[238:239], v5 offset:18432\n"
    "v_mfma_f32_32x32x16_bf16 v[240:255], a[252:255], v[32:35], v[240:255]\n"
    "s_waitcnt lgkmcnt(6)\n"
    "ds_read_b64_tr_b16 a[240:241], v2 offset:16640\n"
    "ds_read_b64_tr_b16 a[242:243], v2 offset:18688\n"
    "buffer_load_dwordx4 v14, s[12:15], s51 offen offset:2048 lds\n"
    "v_mfma_f32_32x32x16_bf16 v[128:143], a[224:227], v[36:39], v[128:143]\n"
    "s_waitcnt lgkmcnt(6)\n"
    "ds_read_b64_tr_b16 a[244:245], v3 offset:16640\n"
    "ds_read_b64_tr_b16 a[246:247], v3 offset:18688\n"
    "ds_read_b128 v[44:47], v99 offset:768\n"
    "v_mfma_f32_32x32x16_bf16 v[144:159], a[228:231], v[36:39], v[144:159]\n"
    "s_waitcnt lgkmcnt(7)\n"
    "ds_read_b64_tr_b16 a[248:249], v4 offset:16640\n"
    "ds_read_b64_tr_b16 a[250:251], v4 offset:18688\n"
    "buffer_load_dwordx4 v14, s[12:15], s52 offen offset:3072 lds\n"
    "v_add_u32 v14, 0x4000, v14\n"
    "v_mfma_f32_32x32x16_bf16 v[160:175], a[232:235], v[36:39], v[160:175]\n"
    "s_waitcnt lgkmcnt(7)\n"
    "ds_read_b64_tr_b16 a[252:253], v5 offset:16640\n"
    "ds_read_b64_tr_b16 a[254:255], v5 offset:18688\n"
    "v_mfma_f32_32x32x16_bf16 v[176:191], a[236:239], v[36:39], v[176:191]\n"
    "s_waitcnt vmcnt(12) lgkmcnt(7)\n"
    "s_barrier\n"
    "s_add_u32 m0, s45, 0\n"
    "ds_read_b64_tr_b16 a[224:225], v2 offset:32768\n"
    "ds_read_b64_tr_b16 a[226:227], v2 offset:34816\n"
    "buffer_load_dwordx4 v14, s[12:15], s49 offen lds\n"
    "v_mfma_f32_32x32x16_bf16 v[192:207], a[240:243], v[36:39], v[192:207]\n"
    "s_waitcnt vmcnt(9) lgkmcnt(7)\n"
    "s_barrier\n"
    "ds_read_b64_tr_b16 a[228:229], v2 offset:49152\n"
    "ds_read_b64_tr_b16 a[230:231], v2 offset:51200\n"
    "v_mfma_f32_32x32x16_bf16 v[208:223], a[244:247], v[36:39], v[208:223]\n"
    "s_waitcnt lgkmcnt(6)\n"
    "ds_read_b64_tr_b16 a[232:233], v3 offset:32768\n"
    "ds_read_b64_tr_b16 a[234:235], v3 offset:34816\n"
    "v_mfma_f32_32x32x16_bf16 v[224:239], a[248:251], v[36:39], v[224:239]\n"
    "s_waitcnt lgkmcnt(6)\n"
    "ds_read_b64_tr_b16 a[236:237], v3 offset:49152\n"
    "ds_read_b64_tr_b16 a[238:239], v3 offset:51200\n"
    "v_mfma_f32_32x32x16_bf16 v[240:255], a[252:255], v[36:39], v[240:255]\n"
    "s_waitcnt lgkmcnt(6)\n"
    "ds_read_b64_tr_b16 a[240:241], v4 offset:32768\n"
    "ds_read_b64_tr_b16 a[242:243], v4 offset:34816\n"
    "buffer_load_dwordx4 v14, s[12:15], s50 offen offset:1024 lds\n"
    "v_mfma_f32_32x32x16_bf16 v[128:143], a[224:227], v[40:43], v[128:143]\n"
    "s_waitcnt lgkmcnt(6)\n"
    "ds_read_b64_tr_b16 a[244:245], v4 offset:49152\n"
    "ds_read_b64_tr_b16 a[246:247], v4 offset:51200\n"
    "v_mfma_f32_32x32x16_bf16 v[128:143], a[228:231], v[44:47], v[128:143]\n"
    "s_waitcnt lgkmcnt(6)\n"
    "ds_read_b64_tr_b16 a[248:249], v5 offset:32768\n"
    "ds_read_b64_tr_b16 a[250:251], v5 offset:34816\n"
    "v_mfma_f32_32x32x16_bf16 v[144:159], a[232:235], v[40:43], v[144:159]\n"
    "s_waitcnt lgkmcnt(6)\n"
    "ds_read_b64_tr_b16 a[252:253], v5 offset:49152\n"
    "ds_read_b64_tr_b16 a[254:255], v5 offset:51200\n"
    "v_mfma_f32_32x32x16_bf16 v[144:159], a[236:239], v[44:47], v[144:159]\n"
    "s_waitcnt lgkmcnt(6)\n"
    "ds_read_b64_tr_b16 a[224:225], v2 offset:33024\n"
    "ds_read_b64_tr_b16 a[226:227], v2 offset:35072\n"
    "buffer_load_dwordx4 v14, s[12:15], s51 offen offset:2048 lds\n"
    "v_cvt_pk_bf16_f32 v128, v128, v129\n"
    "v_cvt_pk_bf16_f32 v129, v130, v131\n"
    "v_mfma_f32_32x32x16_bf16 v[160:175], a[240:243], v[40:43], v[160:175]\n"
    "s_waitcnt lgkmcnt(6)\n"
    "ds_read_b64_tr_b16 a[228:229], v2 offset:49408\n"
    "ds_read_b64_tr_b16 a[230:231], v2 offset:51456\n"
    "v_cvt_pk_bf16_f32 v130, v132, v133\n"
    "v_cvt_pk_bf16_f32 v131, v134, v135\n"
    "v_cvt_pk_bf16_f32 v132, v136, v137\n"
    "v_cvt_pk_bf16_f32 v133, v138, v139\n"
    "v_mfma_f32_32x32x16_bf16 v[160:175], a[244:247], v[44:47], v[160:175]\n"
    "s_waitcnt lgkmcnt(6)\n"
    "ds_read_b64_tr_b16 a[232:233], v3 offset:33024\n"
    "ds_read_b64_tr_b16 a[234:235], v3 offset:35072\n"
    "v_cvt_pk_bf16_f32 v134, v140, v141\n"
    "v_cvt_pk_bf16_f32 v135, v142, v143\n"
    "v_cvt_pk_bf16_f32 v144, v144, v145\n"
    "v_cvt_pk_bf16_f32 v145, v146, v147\n"
    "v_mfma_f32_32x32x16_bf16 v[176:191], a[248:251], v[40:43], v[176:191]\n"
    "s_waitcnt lgkmcnt(6)\n"
    "ds_read_b64_tr_b16 a[236:237], v3 offset:49408\n"
    "ds_read_b64_tr_b16 a[238:239], v3 offset:51456\n"
    "v_cvt_pk_bf16_f32 v146, v148, v149\n"
    "v_cvt_pk_bf16_f32 v147, v150, v151\n"
    "v_cvt_pk_bf16_f32 v148, v152, v153\n"
    "v_cvt_pk_bf16_f32 v149, v154, v155\n"
    "v_mfma_f32_32x32x16_bf16 v[176:191], a[252:255], v[44:47], v[176:191]\n"
    "s_waitcnt lgkmcnt(6)\n"
    "ds_read_b64_tr_b16 a[240:241], v4 offset:33024\n"
    "ds_read_b64_tr_b16 a[242:243], v4 offset:35072\n"
    "buffer_load_dwordx4 v14, s[12:15], s52 offen offset:3072 lds\n"
    "v_add_u32 v14, 0x4000, v14\n"
    "v_cvt_pk_bf16_f32 v150, v156, v157\n"
    "v_mfma_f32_32x32x16_bf16 v[192:207], a[224:227], v[40:43], v[192:207]\n"
    "s_waitcnt lgkmcnt(6)\n"
    "ds_read_b64_tr_b16 a[244:245], v4 offset:49408\n"
    "ds_read_b64_tr_b16 a[246:247], v4 offset:51456\n"
    "v_cvt_pk_bf16_f32 v151, v158, v159\n"
    "v_cvt_pk_bf16_f32 v160, v160, v161\n"
    "v_cvt_pk_bf16_f32 v161, v162, v163\n"
    "v_cvt_pk_bf16_f32 v162, v164, v165\n"
    "v_mfma_f32_32x32x16_bf16 v[192:207], a[228:231], v[44:47], v[192:207]\n"
    "s_waitcnt lgkmcnt(6)\n"
    "ds_read_b64_tr_b16 a[248:249], v5 offset:33024\n"
    "ds_read_b64_tr_b16 a[250:251], v5 offset:35072\n"
    "v_cvt_pk_bf16_f32 v163, v166, v167\n"
    "v_cvt_pk_bf16_f32 v164, v168, v169\n"
    "v_cvt_pk_bf16_f32 v165, v170, v171\n"
    "v_cvt_pk_bf16_f32 v166, v172, v173\n"
    "v_mfma_f32_32x32x16_bf16 v[208:223], a[232:235], v[40:43], v[208:223]\n"
    "s_waitcnt lgkmcnt(6)\n"
    "ds_read_b64_tr_b16 a[252:253], v5 offset:49408\n"
    "ds_read_b64_tr_b16 a[254:255], v5 offset:51456\n"
    "v_cvt_pk_bf16_f32 v167, v174, v175\n"
    "v_cvt_pk_bf16_f32 v176, v176, v177\n"
    "v_cvt_pk_bf16_f32 v177, v178, v179\n"
    "v_cvt_pk_bf16_f32 v178, v180, v181\n"
    "v_mfma_f32_32x32x16_bf16 v[208:223], a[236:239], v[44:47], v[208:223]\n"
    "s_waitcnt vmcnt(8) lgkmcnt(6)\n"
    "s_barrier\n"
    "s_add_u32 m0, s45, 0x4000\n"
    "v_xor_b32 v6, 0x10000, v6\n"
    "ds_read_b128 a[224:227], v6\n"
    "buffer_load_dwordx4 v14, s[12:15], s49 offen lds\n"
    "v_mfma_f32_32x32x16_bf16 v[224:239], a[240:243], v[40:43], v[224:239]\n"
    "s_waitcnt lgkmcnt(5)\n"
    "v_xor_b32 v7, 0x10000, v7\n"
    "ds_read_b128 a[228:231], v7\n"
    "v_cvt_pk_bf16_f32 v179, v182, v183\n"
    "v_cvt_pk_bf16_f32 v180, v184, v185\n"
    "v_cvt_pk_bf16_f32 v181, v186, v187\n"
    "v_cvt_pk_bf16_f32 v182, v188, v189\n"
    "v_mfma_f32_32x32x16_bf16 v[224:239], a[244:247], v[44:47], v[224:239]\n"
    "s_waitcnt lgkmcnt(4)\n"
    "v_xor_b32 v8, 0x10000, v8\n"
    "ds_read_b128 a[232:235], v8\n"
    "v_cvt_pk_bf16_f32 v183, v190, v191\n"
    "v_cvt_pk_bf16_f32 v192, v192, v193\n"
    "v_cvt_pk_bf16_f32 v193, v194, v195\n"
    "v_cvt_pk_bf16_f32 v194, v196, v197\n"
    "v_mfma_f32_32x32x16_bf16 v[240:255], a[248:251], v[40:43], v[240:255]\n"
    "s_waitcnt lgkmcnt(3)\n"
    "v_xor_b32 v9, 0x10000, v9\n"
    "ds_read_b128 a[236:239], v9\n"
    "v_cvt_pk_bf16_f32 v195, v198, v199\n"
    "v_cvt_pk_bf16_f32 v196, v200, v201\n"
    "v_cvt_pk_bf16_f32 v197, v202, v203\n"
    "v_cvt_pk_bf16_f32 v198, v204, v205\n"
    "v_mfma_f32_32x32x16_bf16 v[240:255], a[252:255], v[44:47], v[240:255]\n"
    "s_waitcnt lgkmcnt(3)\n"
    "ds_read_b128 a[240:243], v6 offset:1024\n"
    "buffer_load_dwordx4 v14, s[12:15], s50 offen offset:1024 lds\n"
    "v_mov_b32 v18, 0xff800000\n"
    "v_cvt_pk_bf16_f32 v199, v206, v207\n"
    "v_cvt_pk_bf16_f32 v208, v208, v209\n"
    "v_mfma_f32_32x32x16_bf16 v[32:47], a[224:227], v[128:131], 0\n"
    "s_waitcnt lgkmcnt(3)\n"
    "ds_read_b128 a[244:247], v7 offset:1024\n"
    "v_cvt_pk_bf16_f32 v209, v210, v211\n"
    "v_cvt_pk_bf16_f32 v210, v212, v213\n"
    "v_cvt_pk_bf16_f32 v211, v214, v215\n"
    "v_cvt_pk_bf16_f32 v212, v216, v217\n"
    "v_cvt_pk_bf16_f32 v213, v218, v219\n"
    "v_mfma_f32_32x32x16_bf16 v[32:47], a[228:231], v[132:135], v[32:47]\n"
    "s_waitcnt lgkmcnt(3)\n"
    "ds_read_b128 a[248:251], v8 offset:1024\n"
    "v_cvt_pk_bf16_f32 v214, v220, v221\n"
    "v_cvt_pk_bf16_f32 v215, v222, v223\n"
    "v_cvt_pk_bf16_f32 v224, v224, v225\n"
    "v_cvt_pk_bf16_f32 v225, v226, v227\n"
    "v_cvt_pk_bf16_f32 v226, v228, v229\n"
    "v_mfma_f32_32x32x16_bf16 v[32:47], a[232:235], v[144:147], v[32:47]\n"
    "s_waitcnt lgkmcnt(3)\n"
    "ds_read_b128 a[252:255], v9 offset:1024\n"
    "v_cvt_pk_bf16_f32 v227, v230, v231\n"
    "v_cvt_pk_bf16_f32 v228, v232, v233\n"
    "v_cvt_pk_bf16_f32 v229, v234, v235\n"
    "v_cvt_pk_bf16_f32 v230, v236, v237\n"
    "v_cvt_pk_bf16_f32 v231, v238, v239\n"
    "v_mfma_f32_32x32x16_bf16 v[32:47], a[236:239], v[148:151], v[32:47]\n"
    "s_waitcnt lgkmcnt(3)\n"
    "ds_read_b128 a[224:227], v6 offset:2048\n"
    "buffer_load_dwordx4 v14, s[12:15], s51 offen offset:2048 lds\n"
    "v_cvt_pk_bf16_f32 v240, v240, v241\n"
    "v_cvt_pk_bf16_f32 v241, v242, v243\n"
    "v_cvt_pk_bf16_f32 v242, v244, v245\n"
    "v_mfma_f32_32x32x16_bf16 v[32:47], a[240:243], v[160:163], v[32:47]\n"
    "s_waitcnt lgkmcnt(3)\n"
    "ds_read_b128 a[228:231], v7 offset:2048\n"
    "v_cvt_pk_bf16_f32 v243, v246, v247\n"
    "v_cvt_pk_bf16_f32 v244, v248, v249\n"
    "v_cvt_pk_bf16_f32 v245, v250, v251\n"
    "v_cvt_pk_bf16_f32 v246, v252, v253\n"
    "v_cvt_pk_bf16_f32 v247, v254, v255\n"
    "v_mfma_f32_32x32x16_bf16 v[32:47], a[244:247], v[164:167], v[32:47]\n"
    "s_waitcnt lgkmcnt(3)\n"
    "ds_read_b128 a[232:235], v8 offset:2048\n"
    "v_mfma_f32_32x32x16_bf16 v[32:47], a[248:251], v[176:179], v[32:47]\n"
    "s_waitcnt lgkmcnt(3)\n"
    "ds_read_b128 a[236:239], v9 offset:2048\n"
    "v_mfma_f32_32x32x16_bf16 v[32:47], a[252:255], v[180:183], v[32:47]\n"
    "s_waitcnt lgkmcnt(3)\n"
    "ds_read_b128 a[240:243], v6 offset:3072\n"
    "buffer_load_dwordx4 v14, s[12:15], s52 offen offset:3072 lds\n"
    "v_add_u32 v14, 0x4000, v14\n"
    "v_mfma_f32_32x32x16_bf16 v[32:47], a[224:227], v[192:195], v[32:47]\n"
    "s_waitcnt lgkmcnt(3)\n"
    "ds_read_b128 a[244:247], v7 offset:3072\n"
    "v_mfma_f32_32x32x16_bf16 v[32:47], a[228:231], v[196:199], v[32:47]\n"
    "s_waitcnt lgkmcnt(3)\n"
    "ds_read_b128 a[248:251], v8 offset:3072\n"
    "v_mfma_f32_32x32x16_bf16 v[32:47], a[232:235], v[208:211], v[32:47]\n"
    "s_waitcnt lgkmcnt(3)\n"
    "ds_read_b128 a[252:255], v9 offset:3072\n"
    "v_mfma_f32_32x32x16_bf16 v[32:47], a[236:239], v[212:215], v[32:47]\n"
    "s_waitcnt vmcnt(8) lgkmcnt(3)\n"
    "s_barrier\n"
    "s_add_u32 m0, s45, 0x8000\n"
    "ds_read_b128 a[224:227], v6 offset:16384\n"
    "buffer_load_dwordx4 v14, s[12:15], s49 offen lds\n"
    "v_mfma_f32_32x32x16_bf16 v[32:47], a[240:243], v[224:227], v[32:47]\n"
    "s_waitcnt lgkmcnt(3)\n"
    "ds_read_b128 a[228:231], v7 offset:16384\n"
    "buffer_load_dwordx4 v14, s[12:15], s50 offen offset:1024 lds\n"
    "v_mfma_f32_32x32x16_bf16 v[32:47], a[244:247], v[228:231], v[32:47]\n"
    "s_waitcnt lgkmcnt(3)\n"
    "ds_read_b128 a[232:235], v8 offset:16384\n"
    "buffer_load_dwordx4 v14, s[12:15], s51 offen offset:2048 lds\n"
    "v_mfma_f32_32x32x16_bf16 v[32:47], a[248:251], v[240:243], v[32:47]\n"
    "s_waitcnt lgkmcnt(3)\n"
    "ds_read_b128 a[236:239], v9 offset:16384\n"
    "buffer_load_dwordx4 v14, s[12:15], s52 offen offset:3072 lds\n"
    "v_add_u32 v14, 0x4000, v14\n"
    "v_mfma_f32_32x32x16_bf16 v[32:47], a[252:255], v[244:247], v[32:47]\n"
    "s_waitcnt lgkmcnt(3)\n"
    "s_add_u32 m0, s45, 0xc000\n"
    "ds_read_b128 a[240:243], v6 offset:17408\n"
    "buffer_load_dwordx4 v14, s[12:15], s49 offen lds\n"
    "s_lshl_b32 s58, s43, 13\n"
    "v_subrev_u32 v2, s58, v2\n"
    "v_mfma_f32_32x32x16_bf16 v[48:63], a[224:227], v[128:131], 0\n"
    "s_waitcnt lgkmcnt(3)\n"
    "ds_read_b128 a[244:247], v7 offset:17408\n"
    "v_subrev_u32 v3, s58, v3\n"
    "v_subrev_u32 v4, s58, v4\n"
    "v_subrev_u32 v5, s58, v5\n"
    "v_accvgpr_write_b32 a0, v128\n"
    "v_accvgpr_write_b32 a1, v129\n"
    "v_mfma_f32_32x32x16_bf16 v[48:63], a[228:231], v[132:135], v[48:63]\n"
    "s_waitcnt lgkmcnt(3)\n"
    "ds_read_b128 a[248:251], v8 offset:17408\n"
    "v_max3_f32 v18, v18, v32, v33\n"
    "v_max3_f32 v18, v18, v34, v35\n"
    "v_max3_f32 v18, v18, v36, v37\n"
    "v_max3_f32 v18, v18, v38, v39\n"
    "v_max3_f32 v18, v18, v40, v41\n"
    "v_mfma_f32_32x32x16_bf16 v[48:63], a[232:235], v[144:147], v[48:63]\n"
    "s_waitcnt lgkmcnt(3)\n"
    "ds_read_b128 a[252:255], v9 offset:17408\n"
    "buffer_load_dwordx4 v14, s[12:15], s50 offen offset:1024 lds\n"
    "v_max3_f32 v18, v18, v42, v43\n"
    "v_max3_f32 v18, v18, v44, v45\n"
    "v_max3_f32 v18, v18, v46, v47\n"
    "v_mfma_f32_32x32x16_bf16 v[48:63], a[236:239], v[148:151], v[48:63]\n"
    "s_waitcnt lgkmcnt(3)\n"
    "ds_read_b128 a[224:227], v6 offset:18432\n"
    "v_mul_f32 v19, s57, v18\n"
    "v_accvgpr_write_b32 a160, v19\n"
    "v_fma_f32 v32, v32, s56, v19\n"
    "v_fma_f32 v33, v33, s56, v19\n"
    "v_accvgpr_write_b32 a2, v130\n"
    "v_mfma_f32_32x32x16_bf16 v[48:63], a[240:243], v[160:163], v[48:63]\n"
    "s_waitcnt lgkmcnt(3)\n"
    "ds_read_b128 a[228:231], v7 offset:18432\n"
    "v_exp_f32 v32, v32\n"
    "v_fma_f32 v34, v34, s56, v19\n"
    "v_exp_f32 v33, v33\n"
    "v_mfma_f32_32x32x16_bf16 v[48:63], a[244:247], v[164:167], v[48:63]\n"
    "s_waitcnt lgkmcnt(3)\n"
    "ds_read_b128 a[232:235], v8 offset:18432\n"
    "buffer_load_dwordx4 v14, s[12:15], s51 offen offset:2048 lds\n"
    "v_fma_f32 v35, v35, s56, v19\n"
    "v_exp_f32 v34, v34\n"
    "v_mfma_f32_32x32x16_bf16 v[48:63], a[248:251], v[176:179], v[48:63]\n"
    "s_waitcnt lgkmcnt(3)\n"
    "ds_read_b128 a[236:239], v9 offset:18432\n"
    "v_fma_f32 v36, v36, s56, v19\n"
    "v_exp_f32 v35, v35\n"
    "v_fma_f32 v37, v37, s56, v19\n"
    "v_accvgpr_write_b32 a3, v131\n"
    "v_mfma_f32_32x32x16_bf16 v[48:63], a[252:255], v[180:183], v[48:63]\n"
    "s_waitcnt lgkmcnt(3)\n"
    "ds_read_b128 a[240:243], v6 offset:19456\n"
    "s_mov_b32 s32, s76\n"
    "s_and_b32 s33, s77, 0xffff\n"
    "s_mov_b32 s34, 0x800000\n"
    "s_mov_b32 s35, 0x20000\n"
    "buffer_load_dword a200, v101, s[32:35], 0 offen\n"
    "v_mfma_f32_32x32x16_bf16 v[48:63], a[224:227], v[192:195], v[48:63]\n"
    "s_waitcnt lgkmcnt(3)\n"
    "ds_read_b128 a[244:247], v7 offset:19456\n"
    "buffer_load_dwordx4 v14, s[12:15], s52 offen offset:3072 lds\n"
    "v_add_u32 v14, 0x4000, v14\n"
    "v_exp_f32 v36, v36\n"
    "v_mfma_f32_32x32x16_bf16 v[48:63], a[228:231], v[196:199], v[48:63]\n"
    "s_waitcnt lgkmcnt(3)\n"
    "ds_read_b128 a[248:251], v8 offset:19456\n"
    "v_fma_f32 v38, v38, s56, v19\n"
    "v_exp_f32 v37, v37\n"
    "v_fma_f32 v39, v39, s56, v19\n"
    "v_accvgpr_write_b32 a4, v132\n"
    "v_mfma_f32_32x32x16_bf16 v[48:63], a[232:235], v[208:211], v[48:63]\n"
    "s_waitcnt lgkmcnt(3)\n"
    "ds_read_b128 a[252:255], v9 offset:19456\n"
    "v_exp_f32 v38, v38\n"
    "v_fma_f32 v40, v40, s56, v19\n"
    "v_accvgpr_write_b32 a5, v133\n"
    "v_accvgpr_write_b32 a6, v134\n"
    "v_mfma_f32_32x32x16_bf16 v[48:63], a[236:239], v[212:215], v[48:63]\n"
    "s_waitcnt vmcnt(13) lgkmcnt(3)\n"
    "s_barrier\n"
    "s_add_u32 m0, s45, 0x10000\n"
    "v_xor_b32 v6, 0x10000, v6\n"
    "ds_read_b128 a[224:227], v6\n"
    "buffer_load_dwordx4 v14, s[12:15], s49 offen lds\n"
    "v_mfma_f32_32x32x16_bf16 v[48:63], a[240:243], v[224:227], v[48:63]\n"
    "s_waitcnt lgkmcnt(3)\n"
    "v_xor_b32 v7, 0x10000, v7\n"
    "ds_read_b128 a[228:231], v7\n"
    "v_accvgpr_write_b32 a7, v135\n"
    "v_accvgpr_write_b32 a8, v144\n"
    "v_accvgpr_write_b32 a9, v145\n"
    "v_accvgpr_write_b32 a10, v146\n"
    "v_mfma_f32_32x32x16_bf16 v[48:63], a[244:247], v[228:231], v[48:63]\n"
    "s_waitcnt lgkmcnt(3)\n"
    "v_xor_b32 v8, 0x10000, v8\n"
    "ds_read_b128 a[232:235], v8\n"
    "v_accvgpr_write_b32 a11, v147\n"
    "v_accvgpr_write_b32 a12, v148\n"
    "v_accvgpr_write_b32 a13, v149\n"
    "v_accvgpr_write_b32 a14, v150\n"
    "v_mfma_f32_32x32x16_bf16 v[48:63], a[248:251], v[240:243], v[48:63]\n"
    "s_waitcnt lgkmcnt(3)\n"
    "v_xor_b32 v9, 0x10000, v9\n"
    "ds_read_b128 a[236:239], v9\n"
    "v_accvgpr_write_b32 a15, v151\n"
    "v_accvgpr_write_b32 a16, v160\n"
    "v_accvgpr_write_b32 a17, v161\n"
    "v_accvgpr_write_b32 a18, v162\n"
    "v_mfma_f32_32x32x16_bf16 v[48:63], a[252:255], v[244:247], v[48:63]\n"
    "s_waitcnt lgkmcnt(3)\n"
    "ds_read_b128 a[240:243], v6 offset:1024\n"
    "buffer_load_dwordx4 v14, s[12:15], s50 offen offset:1024 lds\n"
    "v_accvgpr_write_b32 a19, v163\n"
    "v_accvgpr_write_b32 a20, v164\n"
    "v_accvgpr_write_b32 a21, v165\n"
    "v_mfma_f32_32x32x16_bf16 v[64:79], a[224:227], v[128:131], 0\n"
    "s_waitcnt lgkmcnt(3)\n"
    "ds_read_b128 a[244:247], v7 offset:1024\n"
    "v_accvgpr_write_b32 a22, v166\n"
    "v_accvgpr_write_b32 a23, v167\n"
    "v_accvgpr_write_b32 a24, v176\n"
    "v_accvgpr_write_b32 a25, v177\n"
    "v_accvgpr_write_b32 a26, v178\n"
    "v_mfma_f32_32x32x16_bf16 v[64:79], a[228:231], v[132:135], v[64:79]\n"
    "s_waitcnt lgkmcnt(3)\n"
    "ds_read_b128 a[248:251], v8 offset:1024\n"
    "v_max3_f32 v18, v18, v48, v49\n"
    "v_exp_f32 v39, v39\n"
    "v_fma_f32 v41, v41, s56, v19\n"
    "v_max3_f32 v18, v18, v50, v51\n"
    "v_mfma_f32_32x32x16_bf16 v[64:79], a[232:235], v[144:147], v[64:79]\n"
    "s_waitcnt lgkmcnt(3)\n"
    "ds_read_b128 a[252:255], v9 offset:1024\n"
    "v_exp_f32 v40, v40\n"
    "v_fma_f32 v42, v42, s56, v19\n"
    "v_max3_f32 v18, v18, v52, v53\n"
    "v_accvgpr_write_b32 a27, v179\n"
    "v_mfma_f32_32x32x16_bf16 v[64:79], a[236:239], v[148:151], v[64:79]\n"
    "s_waitcnt lgkmcnt(3)\n"
    "ds_read_b128 a[224:227], v6 offset:2048\n"
    "buffer_load_dwordx4 v14, s[12:15], s51 offen offset:2048 lds\n"
    "s_mov_b32 s32, s74\n"
    "s_and_b32 s33, s75, 0xffff\n"
    "s_mov_b32 s34, 0x188000\n"
    "s_mov_b32 s35, 0x20000\n"
    "buffer_load_dword a201, v102, s[32:35], 0 offen\n"
    "v_mfma_f32_32x32x16_bf16 v[64:79], a[240:243], v[160:163], v[64:79]\n"
    "s_waitcnt lgkmcnt(3)\n"
    "ds_read_b128 a[228:231], v7 offset:2048\n"
    "v_exp_f32 v41, v41\n"
    "v_fma_f32 v43, v43, s56, v19\n"
    "v_max3_f32 v18, v18, v54, v55\n"
    "v_accvgpr_write_b32 a28, v180\n"
    "v_mfma_f32_32x32x16_bf16 v[64:79], a[244:247], v[164:167], v[64:79]\n"
    "s_waitcnt lgkmcnt(3)\n"
    "ds_read_b128 a[232:235], v8 offset:2048\n"
    "v_exp_f32 v42, v42\n"
    "v_fma_f32 v44, v44, s56, v19\n"
    "v_max3_f32 v18, v18, v56, v57\n"
    "v_accvgpr_write_b32 a29, v181\n"
    "v_mfma_f32_32x32x16_bf16 v[64:79], a[248:251], v[176:179], v[64:79]\n"
    "s_waitcnt lgkmcnt(3)\n"
    "ds_read_b128 a[236:239], v9 offset:2048\n"
    "v_exp_f32 v43, v43\n"
    "v_fma_f32 v45, v45, s56, v19\n"
    "v_max3_f32 v18, v18, v58, v59\n"
    "v_accvgpr_write_b32 a30, v182\n"
    "v_mfma_f32_32x32x16_bf16 v[64:79], a[252:255], v[180:183], v[64:79]\n"
    "s_waitcnt lgkmcnt(3)\n"
    "ds_read_b128 a[240:243], v6 offset:3072\n"
    "buffer_load_dwordx4 v14, s[12:15], s52 offen offset:3072 lds\n"
    "v_add_u32 v14, 0x4000, v14\n"
    "v_exp_f32 v44, v44\n"
    "v_mfma_f32_32x32x16_bf16 v[64:79], a[224:227], v[192:195], v[64:79]\n"
    "s_waitcnt lgkmcnt(3)\n"
    "ds_read_b128 a[244:247], v7 offset:3072\n"
    "v_fma_f32 v46, v46, s56, v19\n"
    "v_max3_f32 v18, v18, v60, v61\n"
    "v_exp_f32 v45, v45\n"
    "v_fma_f32 v47, v47, s56, v19\n"
    "v_mfma_f32_32x32x16_bf16 v[64:79], a[228:231], v[196:199], v[64:79]\n"
    "s_waitcnt lgkmcnt(3)\n"
    "ds_read_b128 a[248:251], v8 offset:3072\n"
    "v_max3_f32 v18, v18, v62, v63\n"
    "v_exp_f32 v46, v46\n"
    "v_mul_f32 v20, s57, v18\n"
    "v_accvgpr_write_b32 a161, v20\n"
    "v_mfma_f32_32x32x16_bf16 v[64:79], a[232:235], v[208:211], v[64:79]\n"
    "s_waitcnt lgkmcnt(3)\n"
    "ds_read_b128 a[252:255], v9 offset:3072\n"
    "v_fma_f32 v48, v48, s56, v20\n"
    "v_exp_f32 v47, v47\n"
    "v_fma_f32 v49, v49, s56, v20\n"
    "v_accvgpr_write_b32 a31, v183\n"
    "v_mfma_f32_32x32x16_bf16 v[64:79], a[236:239], v[212:215], v[64:79]\n"
    "s_waitcnt vmcnt(14) lgkmcnt(3)\n"
    "s_barrier\n"
    "s_add_u32 m0, s45, 0x14000\n"
    "ds_read_b128 a[224:227], v6 offset:16384\n"
    "buffer_load_dwordx4 v14, s[12:15], s49 offen lds\n"
    "v_accvgpr_write_b32 a32, v192\n"
    "v_mfma_f32_32x32x16_bf16 v[64:79], a[240:243], v[224:227], v[64:79]\n"
    "s_waitcnt lgkmcnt(3)\n"
    "ds_read_b128 a[228:231], v7 offset:16384\n"
    "v_exp_f32 v48, v48\n"
    "v_fma_f32 v50, v50, s56, v20\n"
    "v_exp_f32 v49, v49\n"
    "v_mfma_f32_32x32x16_bf16 v[64:79], a[244:247], v[228:231], v[64:79]\n"
    "s_waitcnt lgkmcnt(3)\n"
    "ds_read_b128 a[232:235], v8 offset:16384\n"
    "v_fma_f32 v51, v51, s56, v20\n"
    "v_exp_f32 v50, v50\n"
    "v_fma_f32 v52, v52, s56, v20\n"
    "v_accvgpr_write_b32 a33, v193\n"
    "v_mfma_f32_32x32x16_bf16 v[64:79], a[248:251], v[240:243], v[64:79]\n"
    "s_waitcnt lgkmcnt(3)\n"
    "ds_read_b128 a[236:239], v9 offset:16384\n"
    "v_exp_f32 v51, v51\n"
    "v_fma_f32 v53, v53, s56, v20\n"
    "v_exp_f32 v52, v52\n"
    "v_mfma_f32_32x32x16_bf16 v[64:79], a[252:255], v[244:247], v[64:79]\n"
    "s_waitcnt lgkmcnt(3)\n"
    "ds_read_b128 a[240:243], v6 offset:17408\n"
    "buffer_load_dwordx4 v14, s[12:15], s50 offen offset:1024 lds\n"
    "s_mov_b32 s32, s80\n"
    "s_and_b32 s33, s81, 0xffff\n"
    "s_mov_b32 s34, 0xc4000\n"
    "s_mov_b32 s35, 0x20000\n"
    "buffer_load_dword a202, v103, s[32:35], 0 offen\n"
    "v_mfma_f32_32x32x16_bf16 v[80:95], a[224:227], v[128:131], 0\n"
    "s_waitcnt lgkmcnt(3)\n"
    "ds_read_b128 a[244:247], v7 offset:17408\n"
    "v_fma_f32 v54, v54, s56, v20\n"
    "v_exp_f32 v53, v53\n"
    "v_fma_f32 v55, v55, s56, v20\n"
    "v_accvgpr_write_b32 a34, v194\n"
    "v_mfma_f32_32x32x16_bf16 v[80:95], a[228:231], v[132:135], v[80:95]\n"
    "s_waitcnt lgkmcnt(3)\n"
    "ds_read_b128 a[248:251], v8 offset:17408\n"
    "v_exp_f32 v54, v54\n"
    "v_fma_f32 v56, v56, s56, v20\n"
    "v_max3_f32 v18, v18, v64, v65\n"
    "v_accvgpr_write_b32 a35, v195\n"
    "v_mfma_f32_32x32x16_bf16 v[80:95], a[232:235], v[144:147], v[80:95]\n"
    "s_waitcnt lgkmcnt(3)\n"
    "ds_read_b128 a[252:255], v9 offset:17408\n"
    "v_exp_f32 v55, v55\n"
    "v_fma_f32 v57, v57, s56, v20\n"
    "v_max3_f32 v18, v18, v66, v67\n"
    "v_accvgpr_write_b32 a36, v196\n"
    "v_mfma_f32_32x32x16_bf16 v[80:95], a[236:239], v[148:151], v[80:95]\n"
    "s_waitcnt lgkmcnt(3)\n"
    "ds_read_b128 a[224:227], v6 offset:18432\n"
    "buffer_load_dwordx4 v14, s[12:15], s51 offen offset:2048 lds\n"
    "v_exp_f32 v56, v56\n"
    "v_fma_f32 v58, v58, s56, v20\n"
    "v_mfma_f32_32x32x16_bf16 v[80:95], a[240:243], v[160:163], v[80:95]\n"
    "s_waitcnt lgkmcnt(3)\n"
    "ds_read_b128 a[228:231], v7 offset:18432\n"
    "v_max3_f32 v18, v18, v68, v69\n"
    "v_exp_f32 v57, v57\n"
    "v_fma_f32 v59, v59, s56, v20\n"
    "v_max3_f32 v18, v18, v70, v71\n"
    "v_mfma_f32_32x32x16_bf16 v[80:95], a[244:247], v[164:167], v[80:95]\n"
    "s_waitcnt lgkmcnt(3)\n"
    "ds_read_b128 a[232:235], v8 offset:18432\n"
    "v_exp_f32 v58, v58\n"
    "v_fma_f32 v60, v60, s56, v20\n"
    "v_max3_f32 v18, v18, v72, v73\n"
    "v_accvgpr_write_b32 a37, v197\n"
    "v_mfma_f32_32x32x16_bf16 v[80:95], a[248:251], v[176:179], v[80:95]\n"
    "s_waitcnt lgkmcnt(3)\n"
    "ds_read_b128 a[236:239], v9 offset:18432\n"
    "v_exp_f32 v59, v59\n"
    "v_fma_f32 v61, v61, s56, v20\n"
    "v_max3_f32 v18, v18, v74, v75\n"
    "v_accvgpr_write_b32 a38, v198\n"
    "v_mfma_f32_32x32x16_bf16 v[80:95], a[252:255], v[180:183], v[80:95]\n"
    "s_waitcnt lgkmcnt(3)\n"
    "ds_read_b128 a[240:243], v6 offset:19456\n"
    "buffer_load_dwordx4 v14, s[12:15], s52 offen offset:3072 lds\n"
    "v_add_u32 v14, 0x4000, v14\n"
    "v_exp_f32 v60, v60\n"
    "v_mfma_f32_32x32x16_bf16 v[80:95], a[224:227], v[192:195], v[80:95]\n"
    "s_waitcnt lgkmcnt(3)\n"
    "ds_read_b128 a[244:247], v7 offset:19456\n"
    "v_fma_f32 v62, v62, s56, v20\n"
    "v_max3_f32 v18, v18, v76, v77\n"
    "v_exp_f32 v61, v61\n"
    "v_fma_f32 v63, v63, s56, v20\n"
    "v_mfma_f32_32x32x16_bf16 v[80:95], a[228:231], v[196:199], v[80:95]\n"
    "s_waitcnt lgkmcnt(3)\n"
    "ds_read_b128 a[248:251], v8 offset:19456\n"
    "v_max3_f32 v18, v18, v78, v79\n"
    "v_exp_f32 v62, v62\n"
    "v_mul_f32 v19, s57, v18\n"
    "v_accvgpr_write_b32 a162, v19\n"
    "v_mfma_f32_32x32x16_bf16 v[80:95], a[232:235], v[208:211], v[80:95]\n"
    "s_waitcnt lgkmcnt(3)\n"
    "ds_read_b128 a[252:255], v9 offset:19456\n"
    "v_fma_f32 v64, v64, s56, v19\n"
    "v_exp_f32 v63, v63\n"
    "v_fma_f32 v65, v65, s56, v19\n"
    "v_accvgpr_write_b32 a39, v199\n"
    "v_mfma_f32_32x32x16_bf16 v[80:95], a[236:239], v[212:215], v[80:95]\n"
    "s_waitcnt vmcnt(15) lgkmcnt(3)\n"
    "s_barrier\n"
    "s_add_u32 m0, s45, 0\n"
    "ds_read_b128 a[224:227], v6 offset:32768\n"
    "buffer_load_dwordx4 v14, s[12:15], s49 offen lds\n"
    "v_accvgpr_write_b32 a40, v208\n"
    "v_mfma_f32_32x32x16_bf16 v[80:95], a[240:243], v[224:227], v[80:95]\n"
    "s_waitcnt lgkmcnt(3)\n"
    "ds_read_b128 a[228:231], v7 offset:32768\n"
    "v_exp_f32 v64, v64\n"
    "v_fma_f32 v66, v66, s56, v19\n"
    "v_exp_f32 v65, v65\n"
    "v_mfma_f32_32x32x16_bf16 v[80:95], a[244:247], v[228:231], v[80:95]\n"
    "s_waitcnt lgkmcnt(3)\n"
    "ds_read_b128 a[232:235], v8 offset:32768\n"
    "v_fma_f32 v67, v67, s56, v19\n"
    "v_exp_f32 v66, v66\n"
    "v_fma_f32 v68, v68, s56, v19\n"
    "v_accvgpr_write_b32 a41, v209\n"
    "v_mfma_f32_32x32x16_bf16 v[80:95], a[248:251], v[240:243], v[80:95]\n"
    "s_waitcnt lgkmcnt(3)\n"
    "ds_read_b128 a[236:239], v9 offset:32768\n"
    "v_exp_f32 v67, v67\n"
    "v_fma_f32 v69, v69, s56, v19\n"
    "v_exp_f32 v68, v68\n"
    "v_mfma_f32_32x32x16_bf16 v[80:95], a[252:255], v[244:247], v[80:95]\n"
    "s_waitcnt lgkmcnt(3)\n"
    "ds_read_b128 a[240:243], v6 offset:33792\n"
    "buffer_load_dwordx4 v14, s[12:15], s50 offen offset:1024 lds\n"
    "v_fma_f32 v70, v70, s56, v19\n"
    "v_exp_f32 v69, v69\n"
    "v_mfma_f32_32x32x16_bf16 v[96:111], a[224:227], v[128:131], 0\n"
    "s_waitcnt lgkmcnt(3)\n"
    "ds_read_b128 a[244:247], v7 offset:33792\n"
    "v_fma_f32 v71, v71, s56, v19\n"
    "v_exp_f32 v70, v70\n"
    "v_fma_f32 v72, v72, s56, v19\n"
    "v_accvgpr_write_b32 a42, v210\n"
    "v_mfma_f32_32x32x16_bf16 v[96:111], a[228:231], v[132:135], v[96:111]\n"
    "s_waitcnt lgkmcnt(3)\n"
    "ds_read_b128 a[248:251], v8 offset:33792\n"
    "v_max3_f32 v18, v18, v80, v81\n"
    "v_exp_f32 v71, v71\n"
    "v_fma_f32 v73, v73, s56, v19\n"
    "v_max3_f32 v18, v18, v82, v83\n"
    "v_mfma_f32_32x32x16_bf16 v[96:111], a[232:235], v[144:147], v[96:111]\n"
    "s_waitcnt lgkmcnt(3)\n"
    "ds_read_b128 a[252:255], v9 offset:33792\n"
    "v_exp_f32 v72, v72\n"
    "v_fma_f32 v74, v74, s56, v19\n"
    "v_max3_f32 v18, v18, v84, v85\n"
    "v_accvgpr_write_b32 a43, v211\n"
    "v_mfma_f32_32x32x16_bf16 v[96:111], a[236:239], v[148:151], v[96:111]\n"
    "s_waitcnt lgkmcnt(3)\n"
    "ds_read_b128 a[224:227], v6 offset:34816\n"
    "buffer_load_dwordx4 v14, s[12:15], s51 offen offset:2048 lds\n"
    "v_exp_f32 v73, v73\n"
    "v_fma_f32 v75, v75, s56, v19\n"
    "v_mfma_f32_32x32x16_bf16 v[96:111], a[240:243], v[160:163], v[96:111]\n"
    "s_waitcnt lgkmcnt(3)\n"
    "ds_read_b128 a[228:231], v7 offset:34816\n"
    "v_max3_f32 v18, v18, v86, v87\n"
    "v_exp_f32 v74, v74\n"
    "v_fma_f32 v76, v76, s56, v19\n"
    "v_max3_f32 v18, v18, v88, v89\n"
    "v_mfma_f32_32x32x16_bf16 v[96:111], a[244:247], v[164:167], v[96:111]\n"
    "s_waitcnt lgkmcnt(3)\n"
    "ds_read_b128 a[232:235], v8 offset:34816\n"
    "v_exp_f32 v75, v75\n"
    "v_fma_f32 v77, v77, s56, v19\n"
    "v_max3_f32 v18, v18, v90, v91\n"
    "v_accvgpr_write_b32 a44, v212\n"
    "v_mfma_f32_32x32x16_bf16 v[96:111], a[248:251], v[176:179], v[96:111]\n"
    "s_waitcnt lgkmcnt(3)\n"
    "ds_read_b128 a[236:239], v9 offset:34816\n"
    "v_exp_f32 v76, v76\n"
    "v_fma_f32 v78, v78, s56, v19\n"
    "v_max3_f32 v18, v18, v92, v93\n"
    "v_accvgpr_write_b32 a45, v213\n"
    "v_mfma_f32_32x32x16_bf16 v[96:111], a[252:255], v[180:183], v[96:111]\n"
    "s_waitcnt lgkmcnt(3)\n"
    "ds_read_b128 a[240:243], v6 offset:35840\n"
    "buffer_load_dwordx4 v14, s[12:15], s52 offen offset:3072 lds\n"
    "v_add_u32 v14, 0x4000, v14\n"
    "v_exp_f32 v77, v77\n"
    "v_mfma_f32_32x32x16_bf16 v[96:111], a[224:227], v[192:195], v[96:111]\n"
    "s_waitcnt lgkmcnt(3)\n"
    "ds_read_b128 a[244:247], v7 offset:35840\n"
    "v_fma_f32 v79, v79, s56, v19\n"
    "v_max3_f32 v18, v18, v94, v95\n"
    "v_exp_f32 v78, v78\n"
    "v_mul_f32 v20, s57, v18\n"
    "v_mfma_f32_32x32x16_bf16 v[96:111], a[228:231], v[196:199], v[96:111]\n"
    "s_waitcnt lgkmcnt(3)\n"
    "ds_read_b128 a[248:251], v8 offset:35840\n"
    "v_accvgpr_write_b32 a163, v20\n"
    "v_fma_f32 v80, v80, s56, v20\n"
    "v_exp_f32 v79, v79\n"
    "v_fma_f32 v81, v81, s56, v20\n"
    "v_mfma_f32_32x32x16_bf16 v[96:111], a[232:235], v[208:211], v[96:111]\n"
    "s_waitcnt lgkmcnt(3)\n"
    "ds_read_b128 a[252:255], v9 offset:35840\n"
    "v_exp_f32 v80, v80\n"
    "v_fma_f32 v82, v82, s56, v20\n"
    "v_exp_f32 v81, v81\n"
    "v_mfma_f32_32x32x16_bf16 v[96:111], a[236:239], v[212:215], v[96:111]\n"
    "s_waitcnt vmcnt(14) lgkmcnt(3)\n"
    "s_barrier\n"
    "s_add_u32 m0, s45, 0x4000\n"
    "ds_read_b128 a[224:227], v6 offset:49152\n"
    "buffer_load_dwordx4 v14, s[12:15], s49 offen lds\n"
    "v_fma_f32 v83, v83, s56, v20\n"
    "v_mfma_f32_32x32x16_bf16 v[96:111], a[240:243], v[224:227], v[96:111]\n"
    "s_waitcnt lgkmcnt(3)\n"
    "ds_read_b128 a[228:231], v7 offset:49152\n"
    "v_exp_f32 v82, v82\n"
    "v_fma_f32 v84, v84, s56, v20\n"
    "v_exp_f32 v83, v83\n"
    "v_mfma_f32_32x32x16_bf16 v[96:111], a[244:247], v[228:231], v[96:111]\n"
    "s_waitcnt lgkmcnt(3)\n"
    "ds_read_b128 a[232:235], v8 offset:49152\n"
    "v_fma_f32 v85, v85, s56, v20\n"
    "v_exp_f32 v84, v84\n"
    "v_fma_f32 v86, v86, s56, v20\n"
    "v_accvgpr_write_b32 a46, v214\n"
    "v_mfma_f32_32x32x16_bf16 v[96:111], a[248:251], v[240:243], v[96:111]\n"
    "s_waitcnt lgkmcnt(3)\n"
    "ds_read_b128 a[236:239], v9 offset:49152\n"
    "v_accvgpr_write_b32 a47, v215\n"
    "v_accvgpr_write_b32 a48, v224\n"
    "v_accvgpr_write_b32 a49, v225\n"
    "v_accvgpr_write_b32 a50, v226\n"
    "v_accvgpr_write_b32 a51, v227\n"
    "v_accvgpr_write_b32 a52, v228\n"
    "v_accvgpr_write_b32 a53, v229\n"
    "v_accvgpr_write_b32 a54, v230\n"
    "v_accvgpr_write_b32 a55, v231\n"
    "v_accvgpr_write_b32 a56, v240\n"
    "v_accvgpr_write_b32 a57, v241\n"
    "v_accvgpr_write_b32 a58, v242\n"
    "v_accvgpr_write_b32 a59, v243\n"
    "v_accvgpr_write_b32 a60, v244\n"
    "v_accvgpr_write_b32 a61, v245\n"
    "v_accvgpr_write_b32 a62, v246\n"
    "v_accvgpr_write_b32 a63, v247\n"
    "v_mfma_f32_32x32x16_bf16 v[96:111], a[252:255], v[244:247], v[96:111]\n"
    "s_waitcnt lgkmcnt(3)\n"
    "ds_read_b128 a[240:243], v6 offset:50176\n"
    "buffer_load_dwordx4 v14, s[12:15], s50 offen offset:1024 lds\n"
    "v_exp_f32 v85, v85\n"
    "v_fma_f32 v87, v87, s56, v20\n"
    "v_mfma_f32_32x32x16_bf16 v[112:127], a[224:227], a[0:3], 0\n"
    "s_waitcnt lgkmcnt(3)\n"
    "ds_read_b128 a[244:247], v7 offset:50176\n"
    "v_exp_f32 v86, v86\n"
    "v_fma_f32 v88, v88, s56, v20\n"
    "v_mfma_f32_32x32x16_bf16 v[112:127], a[228:231], a[4:7], v[112:127]\n"
    "s_waitcnt lgkmcnt(3)\n"
    "ds_read_b128 a[248:251], v8 offset:50176\n"
    "v_max3_f32 v18, v18, v96, v97\n"
    "v_exp_f32 v87, v87\n"
    "v_fma_f32 v89, v89, s56, v20\n"
    "v_max3_f32 v18, v18, v98, v99\n"
    "v_mfma_f32_32x32x16_bf16 v[112:127], a[232:235], a[8:11], v[112:127]\n"
    "s_waitcnt lgkmcnt(3)\n"
    "ds_read_b128 a[252:255], v9 offset:50176\n"
    "v_exp_f32 v88, v88\n"
    "v_fma_f32 v90, v90, s56, v20\n"
    "v_max3_f32 v18, v18, v100, v101\n"
    "v_mfma_f32_32x32x16_bf16 v[112:127], a[236:239], a[12:15], v[112:127]\n"
    "s_waitcnt lgkmcnt(3)\n"
    "ds_read_b128 a[224:227], v6 offset:51200\n"
    "buffer_load_dwordx4 v14, s[12:15], s51 offen offset:2048 lds\n"
    "v_exp_f32 v89, v89\n"
    "v_fma_f32 v91, v91, s56, v20\n"
    "v_mfma_f32_32x32x16_bf16 v[112:127], a[240:243], a[16:19], v[112:127]\n"
    "s_waitcnt lgkmcnt(3)\n"
    "ds_read_b128 a[228:231], v7 offset:51200\n"
    "v_max3_f32 v18, v18, v102, v103\n"
    "v_exp_f32 v90, v90\n"
    "v_fma_f32 v92, v92, s56, v20\n"
    "v_max3_f32 v18, v18, v104, v105\n"
    "v_mfma_f32_32x32x16_bf16 v[112:127], a[244:247], a[20:23], v[112:127]\n"
    "s_waitcnt lgkmcnt(3)\n"
    "ds_read_b128 a[232:235], v8 offset:51200\n"
    "v_exp_f32 v91, v91\n"
    "v_fma_f32 v93, v93, s56, v20\n"
    "v_max3_f32 v18, v18, v106, v107\n"
    "v_mfma_f32_32x32x16_bf16 v[112:127], a[248:251], a[24:27], v[112:127]\n"
    "s_waitcnt lgkmcnt(3)\n"
    "ds_read_b128 a[236:239], v9 offset:51200\n"
    "v_exp_f32 v92, v92\n"
    "v_fma_f32 v94, v94, s56, v20\n"
    "v_max3_f32 v18, v18, v108, v109\n"
    "v_mfma_f32_32x32x16_bf16 v[112:127], a[252:255], a[28:31], v[112:127]\n"
    "s_waitcnt lgkmcnt(3)\n"
    "ds_read_b128 a[240:243], v6 offset:52224\n"
    "buffer_load_dwordx4 v14, s[12:15], s52 offen offset:3072 lds\n"
    "v_add_u32 v14, 0x4000, v14\n"
    "v_exp_f32 v93, v93\n"
    "v_mfma_f32_32x32x16_bf16 v[112:127], a[224:227], a[32:35], v[112:127]\n"
    "s_waitcnt lgkmcnt(3)\n"
    "ds_read_b128 a[244:247], v7 offset:52224\n"
    "v_fma_f32 v95, v95, s56, v20\n"
    "v_max3_f32 v18, v18, v110, v111\n"
    "v_exp_f32 v94, v94\n"
    "v_mul_f32 v19, s57, v18\n"
    "v_mfma_f32_32x32x16_bf16 v[112:127], a[228:231], a[36:39], v[112:127]\n"
    "s_waitcnt lgkmcnt(3)\n"
    "ds_read_b128 a[248:251], v8 offset:52224\n"
    "v_accvgpr_write_b32 a164, v19\n"
    "v_fma_f32 v96, v96, s56, v19\n"
    "v_exp_f32 v95, v95\n"
    "v_fma_f32 v97, v97, s56, v19\n"
    "v_mfma_f32_32x32x16_bf16 v[112:127], a[232:235], a[40:43], v[112:127]\n"
    "s_waitcnt lgkmcnt(3)\n"
    "ds_read_b128 a[252:255], v9 offset:52224\n"
    "v_exp_f32 v96, v96\n"
    "v_fma_f32 v98, v98, s56, v19\n"
    "v_exp_f32 v97, v97\n"
    "v_mfma_f32_32x32x16_bf16 v[112:127], a[236:239], a[44:47], v[112:127]\n"
    "s_waitcnt vmcnt(13) lgkmcnt(3)\n"
    "s_barrier\n"
    "s_add_u32 m0, s45, 0x8000\n"
    "v_xor_b32 v6, 0x10000, v6\n"
    "ds_read_b128 a[224:227], v6\n"
    "buffer_load_dwordx4 v14, s[12:15], s49 offen lds\n"
    "v_mfma_f32_32x32x16_bf16 v[112:127], a[240:243], a[48:51], v[112:127]\n"
    "s_waitcnt lgkmcnt(3)\n"
    "v_xor_b32 v7, 0x10000, v7\n"
    "ds_read_b128 a[228:231], v7\n"
    "v_fma_f32 v99, v99, s56, v19\n"
    "v_exp_f32 v98, v98\n"
    "v_fma_f32 v100, v100, s56, v19\n"
    "v_mfma_f32_32x32x16_bf16 v[112:127], a[244:247], a[52:55], v[112:127]\n"
    "s_waitcnt lgkmcnt(3)\n"
    "v_xor_b32 v8, 0x10000, v8\n"
    "ds_read_b128 a[232:235], v8\n"
    "v_exp_f32 v99, v99\n"
    "v_fma_f32 v101, v101, s56, v19\n"
    "v_mfma_f32_32x32x16_bf16 v[112:127], a[248:251], a[56:59], v[112:127]\n"
    "s_waitcnt lgkmcnt(3)\n"
    "v_xor_b32 v9, 0x10000, v9\n"
    "ds_read_b128 a[236:239], v9\n"
    "v_exp_f32 v100, v100\n"
    "v_fma_f32 v102, v102, s56, v19\n"
    "v_mfma_f32_32x32x16_bf16 v[112:127], a[252:255], a[60:63], v[112:127]\n"
    "s_waitcnt lgkmcnt(3)\n"
    "ds_read_b128 a[240:243], v6 offset:1024\n"
    "buffer_load_dwordx4 v14, s[12:15], s50 offen offset:1024 lds\n"
    "v_exp_f32 v101, v101\n"
    "v_fma_f32 v103, v103, s56, v19\n"
    "v_mfma_f32_32x32x16_bf16 v[128:143], a[224:227], a[0:3], 0\n"
    "s_waitcnt lgkmcnt(3)\n"
    "ds_read_b128 a[244:247], v7 offset:1024\n"
    "v_exp_f32 v102, v102\n"
    "v_fma_f32 v104, v104, s56, v19\n"
    "v_mfma_f32_32x32x16_bf16 v[128:143], a[228:231], a[4:7], v[128:143]\n"
    "s_waitcnt lgkmcnt(3)\n"
    "ds_read_b128 a[248:251], v8 offset:1024\n"
    "v_max3_f32 v18, v18, v112, v113\n"
    "v_exp_f32 v103, v103\n"
    "v_fma_f32 v105, v105, s56, v19\n"
    "v_max3_f32 v18, v18, v114, v115\n"
    "v_mfma_f32_32x32x16_bf16 v[128:143], a[232:235], a[8:11], v[128:143]\n"
    "s_waitcnt lgkmcnt(3)\n"
    "ds_read_b128 a[252:255], v9 offset:1024\n"
    "v_exp_f32 v104, v104\n"
    "v_fma_f32 v106, v106, s56, v19\n"
    "v_max3_f32 v18, v18, v116, v117\n"
    "v_mfma_f32_32x32x16_bf16 v[128:143], a[236:239], a[12:15], v[128:143]\n"
    "s_waitcnt lgkmcnt(3)\n"
    "ds_read_b128 a[224:227], v6 offset:2048\n"
    "buffer_load_dwordx4 v14, s[12:15], s51 offen offset:2048 lds\n"
    "v_exp_f32 v105, v105\n"
    "v_fma_f32 v107, v107, s56, v19\n"
    "v_mfma_f32_32x32x16_bf16 v[128:143], a[240:243], a[16:19], v[128:143]\n"
    "s_waitcnt lgkmcnt(3)\n"
    "ds_read_b128 a[228:231], v7 offset:2048\n"
    "v_max3_f32 v18, v18, v118, v119\n"
    "v_exp_f32 v106, v106\n"
    "v_fma_f32 v108, v108, s56, v19\n"
    "v_max3_f32 v18, v18, v120, v121\n"
    "v_mfma_f32_32x32x16_bf16 v[128:143], a[244:247], a[20:23], v[128:143]\n"
    "s_waitcnt lgkmcnt(3)\n"
    "ds_read_b128 a[232:235], v8 offset:2048\n"
    "v_exp_f32 v107, v107\n"
    "v_fma_f32 v109, v109, s56, v19\n"
    "v_max3_f32 v18, v18, v122, v123\n"
    "v_mfma_f32_32x32x16_bf16 v[128:143], a[248:251], a[24:27], v[128:143]\n"
    "s_waitcnt lgkmcnt(3)\n"
    "ds_read_b128 a[236:239], v9 offset:2048\n"
    "v_exp_f32 v108, v108\n"
    "v_fma_f32 v110, v110, s56, v19\n"
    "v_max3_f32 v18, v18, v124, v125\n"
    "v_mfma_f32_32x32x16_bf16 v[128:143], a[252:255], a[28:31], v[128:143]\n"
    "s_waitcnt lgkmcnt(3)\n"
    "ds_read_b128 a[240:243], v6 offset:3072\n"
    "buffer_load_dwordx4 v14, s[12:15], s52 offen offset:3072 lds\n"
    "v_add_u32 v14, 0x4000, v14\n"
    "v_exp_f32 v109, v109\n"
    "v_mfma_f32_32x32x16_bf16 v[128:143], a[224:227], a[32:35], v[128:143]\n"
    "s_waitcnt lgkmcnt(3)\n"
    "ds_read_b128 a[244:247], v7 offset:3072\n"
    "v_fma_f32 v111, v111, s56, v19\n"
    "v_max3_f32 v18, v18, v126, v127\n"
    "v_exp_f32 v110, v110\n"
    "v_mul_f32 v20, s57, v18\n"
    "v_mfma_f32_32x32x16_bf16 v[128:143], a[228:231], a[36:39], v[128:143]\n"
    "s_waitcnt lgkmcnt(3)\n"
    "ds_read_b128 a[248:251], v8 offset:3072\n"
    "v_accvgpr_write_b32 a165, v20\n"
    "v_fma_f32 v112, v112, s56, v20\n"
    "v_exp_f32 v111, v111\n"
    "v_fma_f32 v113, v113, s56, v20\n"
    "v_mfma_f32_32x32x16_bf16 v[128:143], a[232:235], a[40:43], v[128:143]\n"
    "s_waitcnt lgkmcnt(3)\n"
    "ds_read_b128 a[252:255], v9 offset:3072\n"
    "v_exp_f32 v112, v112\n"
    "v_fma_f32 v114, v114, s56, v20\n"
    "v_exp_f32 v113, v113\n"
    "v_mfma_f32_32x32x16_bf16 v[128:143], a[236:239], a[44:47], v[128:143]\n"
    "s_waitcnt vmcnt(12) lgkmcnt(3)\n"
    "s_barrier\n"
    "s_add_u32 m0, s45, 0xc000\n"
    "ds_read_b128 a[224:227], v6 offset:16384\n"
    "buffer_load_dwordx4 v14, s[12:15], s49 offen lds\n"
    "v_fma_f32 v115, v115, s56, v20\n"
    "v_mfma_f32_32x32x16_bf16 v[128:143], a[240:243], a[48:51], v[128:143]\n"
    "s_waitcnt lgkmcnt(3)\n"
    "ds_read_b128 a[228:231], v7 offset:16384\n"
    "v_exp_f32 v114, v114\n"
    "v_fma_f32 v116, v116, s56, v20\n"
    "v_exp_f32 v115, v115\n"
    "v_mfma_f32_32x32x16_bf16 v[128:143], a[244:247], a[52:55], v[128:143]\n"
    "s_waitcnt lgkmcnt(3)\n"
    "ds_read_b128 a[232:235], v8 offset:16384\n"
    "v_fma_f32 v117, v117, s56, v20\n"
    "v_exp_f32 v116, v116\n"
    "v_fma_f32 v118, v118, s56, v20\n"
    "v_mfma_f32_32x32x16_bf16 v[128:143], a[248:251], a[56:59], v[128:143]\n"
    "s_waitcnt lgkmcnt(3)\n"
    "ds_read_b128 a[236:239], v9 offset:16384\n"
    "v_exp_f32 v117, v117\n"
    "v_fma_f32 v119, v119, s56, v20\n"
    "v_exp_f32 v118, v118\n"
    "v_mfma_f32_32x32x16_bf16 v[128:143], a[252:255], a[60:63], v[128:143]\n"
    "s_waitcnt lgkmcnt(3)\n"
    "ds_read_b128 a[240:243], v6 offset:17408\n"
    "buffer_load_dwordx4 v14, s[12:15], s50 offen offset:1024 lds\n"
    "v_fma_f32 v120, v120, s56, v20\n"
    "v_mfma_f32_32x32x16_bf16 v[144:159], a[224:227], a[0:3], 0\n"
    "s_waitcnt lgkmcnt(3)\n"
    "ds_read_b128 a[244:247], v7 offset:17408\n"
    "v_mfma_f32_32x32x16_bf16 v[144:159], a[228:231], a[4:7], v[144:159]\n"
    "s_waitcnt lgkmcnt(3)\n"
    "ds_read_b128 a[248:251], v8 offset:17408\n"
    "s_nop 1\n"
    "v_max3_f32 v18, v18, v128, v129\n"
    "v_exp_f32 v119, v119\n"
    "v_fma_f32 v121, v121, s56, v20\n"
    "v_max3_f32 v18, v18, v130, v131\n"
    "v_mfma_f32_32x32x16_bf16 v[144:159], a[232:235], a[8:11], v[144:159]\n"
    "s_waitcnt lgkmcnt(3)\n"
    "ds_read_b128 a[252:255], v9 offset:17408\n"
    "v_exp_f32 v120, v120\n"
    "v_fma_f32 v122, v122, s56, v20\n"
    "v_max3_f32 v18, v18, v132, v133\n"
    "v_mfma_f32_32x32x16_bf16 v[144:159], a[236:239], a[12:15], v[144:159]\n"
    "s_waitcnt lgkmcnt(3)\n"
    "ds_read_b128 a[224:227], v6 offset:18432\n"
    "buffer_load_dwordx4 v14, s[12:15], s51 offen offset:2048 lds\n"
    "v_exp_f32 v121, v121\n"
    "v_fma_f32 v123, v123, s56, v20\n"
    "v_mfma_f32_32x32x16_bf16 v[144:159], a[240:243], a[16:19], v[144:159]\n"
    "s_waitcnt lgkmcnt(3)\n"
    "ds_read_b128 a[228:231], v7 offset:18432\n"
    "v_max3_f32 v18, v18, v134, v135\n"
    "v_exp_f32 v122, v122\n"
    "v_fma_f32 v124, v124, s56, v20\n"
    "v_max3_f32 v18, v18, v136, v137\n"
    "v_mfma_f32_32x32x16_bf16 v[144:159], a[244:247], a[20:23], v[144:159]\n"
    "s_waitcnt lgkmcnt(3)\n"
    "ds_read_b128 a[232:235], v8 offset:18432\n"
    "v_exp_f32 v123, v123\n"
    "v_fma_f32 v125, v125, s56, v20\n"
    "v_max3_f32 v18, v18, v138, v139\n"
    "v_mfma_f32_32x32x16_bf16 v[144:159], a[248:251], a[24:27], v[144:159]\n"
    "s_waitcnt lgkmcnt(3)\n"
    "ds_read_b128 a[236:239], v9 offset:18432\n"
    "v_exp_f32 v124, v124\n"
    "v_fma_f32 v126, v126, s56, v20\n"
    "v_max3_f32 v18, v18, v140, v141\n"
    "v_mfma_f32_32x32x16_bf16 v[144:159], a[252:255], a[28:31], v[144:159]\n"
    "s_waitcnt lgkmcnt(3)\n"
    "ds_read_b128 a[240:243], v6 offset:19456\n"
    "buffer_load_dwordx4 v14, s[12:15], s52 offen offset:3072 lds\n"
    "v_add_u32 v14, 0x4000, v14\n"
    "v_exp_f32 v125, v125\n"
    "v_mfma_f32_32x32x16_bf16 v[144:159], a[224:227], a[32:35], v[144:159]\n"
    "s_waitcnt lgkmcnt(3)\n"
    "ds_read_b128 a[244:247], v7 offset:19456\n"
    "v_fma_f32 v127, v127, s56, v20\n"
    "v_max3_f32 v18, v18, v142, v143\n"
    "v_exp_f32 v126, v126\n"
    "v_mul_f32 v19, s57, v18\n"
    "v_mfma_f32_32x32x16_bf16 v[144:159], a[228:231], a[36:39], v[144:159]\n"
    "s_waitcnt lgkmcnt(3)\n"
    "ds_read_b128 a[248:251], v8 offset:19456\n"
    "v_accvgpr_write_b32 a166, v19\n"
    "v_fma_f32 v128, v128, s56, v19\n"
    "v_exp_f32 v127, v127\n"
    "v_fma_f32 v129, v129, s56, v19\n"
    "v_mfma_f32_32x32x16_bf16 v[144:159], a[232:235], a[40:43], v[144:159]\n"
    "s_waitcnt lgkmcnt(3)\n"
    "ds_read_b128 a[252:255], v9 offset:19456\n"
    "v_exp_f32 v128, v128\n"
    "v_fma_f32 v130, v130, s56, v19\n"
    "v_exp_f32 v129, v129\n"
    "v_mfma_f32_32x32x16_bf16 v[144:159], a[236:239], a[44:47], v[144:159]\n"
    "s_waitcnt vmcnt(12) lgkmcnt(3)\n"
    "s_barrier\n"
    "s_add_u32 m0, s45, 0x10000\n"
    "v_xor_b32 v6, 0x10000, v6\n"
    "ds_read_b128 a[224:227], v6\n"
    "buffer_load_dwordx4 v14, s[12:15], s49 offen lds\n"
    "v_mfma_f32_32x32x16_bf16 v[144:159], a[240:243], a[48:51], v[144:159]\n"
    "s_waitcnt lgkmcnt(3)\n"
    "v_xor_b32 v7, 0x10000, v7\n"
    "ds_read_b128 a[228:231], v7\n"
    "v_fma_f32 v131, v131, s56, v19\n"
    "v_exp_f32 v130, v130\n"
    "v_fma_f32 v132, v132, s56, v19\n"
    "v_mfma_f32_32x32x16_bf16 v[144:159], a[244:247], a[52:55], v[144:159]\n"
    "s_waitcnt lgkmcnt(3)\n"
    "v_xor_b32 v8, 0x10000, v8\n"
    "ds_read_b128 a[232:235], v8\n"
    "v_exp_f32 v131, v131\n"
    "v_fma_f32 v133, v133, s56, v19\n"
    "v_mfma_f32_32x32x16_bf16 v[144:159], a[248:251], a[56:59], v[144:159]\n"
    "s_waitcnt lgkmcnt(3)\n"
    "v_xor_b32 v9, 0x10000, v9\n"
    "ds_read_b128 a[236:239], v9\n"
    "v_exp_f32 v132, v132\n"
    "v_fma_f32 v134, v134, s56, v19\n"
    "v_mfma_f32_32x32x16_bf16 v[144:159], a[252:255], a[60:63], v[144:159]\n"
    "s_waitcnt lgkmcnt(3)\n"
    "ds_read_b128 a[240:243], v6 offset:1024\n"
    "buffer_load_dwordx4 v14, s[12:15], s50 offen offset:1024 lds\n"
    "v_exp_f32 v133, v133\n"
    "v_fma_f32 v135, v135, s56, v19\n"
    "v_mfma_f32_32x32x16_bf16 v[160:175], a[224:227], a[0:3], 0\n"
    "s_waitcnt lgkmcnt(3)\n"
    "ds_read_b128 a[244:247], v7 offset:1024\n"
    "v_exp_f32 v134, v134\n"
    "v_fma_f32 v136, v136, s56, v19\n"
    "v_mfma_f32_32x32x16_bf16 v[160:175], a[228:231], a[4:7], v[160:175]\n"
    "s_waitcnt lgkmcnt(3)\n"
    "ds_read_b128 a[248:251], v8 offset:1024\n"
    "v_max3_f32 v18, v18, v144, v145\n"
    "v_exp_f32 v135, v135\n"
    "v_fma_f32 v137, v137, s56, v19\n"
    "v_max3_f32 v18, v18, v146, v147\n"
    "v_mfma_f32_32x32x16_bf16 v[160:175], a[232:235], a[8:11], v[160:175]\n"
    "s_waitcnt lgkmcnt(3)\n"
    "ds_read_b128 a[252:255], v9 offset:1024\n"
    "v_exp_f32 v136, v136\n"
    "v_fma_f32 v138, v138, s56, v19\n"
    "v_max3_f32 v18, v18, v148, v149\n"
    "v_mfma_f32_32x32x16_bf16 v[160:175], a[236:239], a[12:15], v[160:175]\n"
    "s_waitcnt lgkmcnt(3)\n"
    "ds_read_b128 a[224:227], v6 offset:2048\n"
    "buffer_load_dwordx4 v14, s[12:15], s51 offen offset:2048 lds\n"
    "v_exp_f32 v137, v137\n"
    "v_fma_f32 v139, v139, s56, v19\n"
    "v_mfma_f32_32x32x16_bf16 v[160:175], a[240:243], a[16:19], v[160:175]\n"
    "s_waitcnt lgkmcnt(3)\n"
    "ds_read_b128 a[228:231], v7 offset:2048\n"
    "v_max3_f32 v18, v18, v150, v151\n"
    "v_exp_f32 v138, v138\n"
    "v_fma_f32 v140, v140, s56, v19\n"
    "v_max3_f32 v18, v18, v152, v153\n"
    "v_mfma_f32_32x32x16_bf16 v[160:175], a[244:247], a[20:23], v[160:175]\n"
    "s_waitcnt lgkmcnt(3)\n"
    "ds_read_b128 a[232:235], v8 offset:2048\n"
    "v_exp_f32 v139, v139\n"
    "v_fma_f32 v141, v141, s56, v19\n"
    "v_max3_f32 v18, v18, v154, v155\n"
    "v_mfma_f32_32x32x16_bf16 v[160:175], a[248:251], a[24:27], v[160:175]\n"
    "s_waitcnt lgkmcnt(3)\n"
    "ds_read_b128 a[236:239], v9 offset:2048\n"
    "v_exp_f32 v140, v140\n"
    "v_fma_f32 v142, v142, s56, v19\n"
    "v_max3_f32 v18, v18, v156, v157\n"
    "v_mfma_f32_32x32x16_bf16 v[160:175], a[252:255], a[28:31], v[160:175]\n"
    "s_waitcnt lgkmcnt(3)\n"
    "ds_read_b128 a[240:243], v6 offset:3072\n"
    "buffer_load_dwordx4 v14, s[12:15], s52 offen offset:3072 lds\n"
    "v_add_u32 v14, 0x4000, v14\n"
    "v_exp_f32 v141, v141\n"
    "v_mfma_f32_32x32x16_bf16 v[160:175], a[224:227], a[32:35], v[160:175]\n"
    "s_waitcnt lgkmcnt(3)\n"
    "ds_read_b128 a[244:247], v7 offset:3072\n"
    "v_fma_f32 v143, v143, s56, v19\n"
    "v_max3_f32 v18, v18, v158, v159\n"
    "v_exp_f32 v142, v142\n"
    "v_mul_f32 v20, s57, v18\n"
    "v_mfma_f32_32x32x16_bf16 v[160:175], a[228:231], a[36:39], v[160:175]\n"
    "s_waitcnt lgkmcnt(3)\n"
    "ds_read_b128 a[248:251], v8 offset:3072\n"
    "v_accvgpr_write_b32 a167, v20\n"
    "v_fma_f32 v144, v144, s56, v20\n"
    "v_exp_f32 v143, v143\n"
    "v_fma_f32 v145, v145, s56, v20\n"
    "v_mfma_f32_32x32x16_bf16 v[160:175], a[232:235], a[40:43], v[160:175]\n"
    "s_waitcnt lgkmcnt(3)\n"
    "ds_read_b128 a[252:255], v9 offset:3072\n"
    "v_exp_f32 v144, v144\n"
    "v_fma_f32 v146, v146, s56, v20\n"
    "v_exp_f32 v145, v145\n"
    "v_mfma_f32_32x32x16_bf16 v[160:175], a[236:239], a[44:47], v[160:175]\n"
    "s_waitcnt vmcnt(12) lgkmcnt(3)\n"
    "s_barrier\n"
    "s_add_u32 m0, s45, 0x14000\n"
    "ds_read_b128 a[224:227], v6 offset:16384\n"
    "buffer_load_dwordx4 v14, s[12:15], s49 offen lds\n"
    "v_fma_f32 v147, v147, s56, v20\n"
    "v_mfma_f32_32x32x16_bf16 v[160:175], a[240:243], a[48:51], v[160:175]\n"
    "s_waitcnt lgkmcnt(3)\n"
    "ds_read_b128 a[228:231], v7 offset:16384\n"
    "v_exp_f32 v146, v146\n"
    "v_fma_f32 v148, v148, s56, v20\n"
    "v_exp_f32 v147, v147\n"
    "v_mfma_f32_32x32x16_bf16 v[160:175], a[244:247], a[52:55], v[160:175]\n"
    "s_waitcnt lgkmcnt(3)\n"
    "ds_read_b128 a[232:235], v8 offset:16384\n"
    "v_fma_f32 v149, v149, s56, v20\n"
    "v_exp_f32 v148, v148\n"
    "v_fma_f32 v150, v150, s56, v20\n"
    "v_mfma_f32_32x32x16_bf16 v[160:175], a[248:251], a[56:59], v[160:175]\n"
    "s_waitcnt lgkmcnt(3)\n"
    "ds_read_b128 a[236:239], v9 offset:16384\n"
    "v_exp_f32 v149, v149\n"
    "v_fma_f32 v151, v151, s56, v20\n"
    "v_exp_f32 v150, v150\n"
    "v_mfma_f32_32x32x16_bf16 v[160:175], a[252:255], a[60:63], v[160:175]\n"
    "s_waitcnt lgkmcnt(3)\n"
    "ds_read_b128 a[240:243], v6 offset:17408\n"
    "buffer_load_dwordx4 v14, s[12:15], s50 offen offset:1024 lds\n"
    "v_fma_f32 v152, v152, s56, v20\n"
    "v_mfma_f32_32x32x16_bf16 v[176:191], a[224:227], a[0:3], 0\n"
    "s_waitcnt lgkmcnt(3)\n"
    "ds_read_b128 a[244:247], v7 offset:17408\n"
    "v_mfma_f32_32x32x16_bf16 v[176:191], a[228:231], a[4:7], v[176:191]\n"
    "s_waitcnt lgkmcnt(3)\n"
    "ds_read_b128 a[248:251], v8 offset:17408\n"
    "s_nop 1\n"
    "v_max3_f32 v18, v18, v160, v161\n"
    "v_exp_f32 v151, v151\n"
    "v_fma_f32 v153, v153, s56, v20\n"
    "v_max3_f32 v18, v18, v162, v163\n"
    "v_mfma_f32_32x32x16_bf16 v[176:191], a[232:235], a[8:11], v[176:191]\n"
    "s_waitcnt lgkmcnt(3)\n"
    "ds_read_b128 a[252:255], v9 offset:17408\n"
    "v_exp_f32 v152, v152\n"
    "v_fma_f32 v154, v154, s56, v20\n"
    "v_max3_f32 v18, v18, v164, v165\n"
    "v_mfma_f32_32x32x16_bf16 v[176:191], a[236:239], a[12:15], v[176:191]\n"
    "s_waitcnt lgkmcnt(3)\n"
    "ds_read_b128 a[224:227], v6 offset:18432\n"
    "buffer_load_dwordx4 v14, s[12:15], s51 offen offset:2048 lds\n"
    "v_exp_f32 v153, v153\n"
    "v_fma_f32 v155, v155, s56, v20\n"
    "v_mfma_f32_32x32x16_bf16 v[176:191], a[240:243], a[16:19], v[176:191]\n"
    "s_waitcnt lgkmcnt(3)\n"
    "ds_read_b128 a[228:231], v7 offset:18432\n"
    "v_max3_f32 v18, v18, v166, v167\n"
    "v_exp_f32 v154, v154\n"
    "v_fma_f32 v156, v156, s56, v20\n"
    "v_max3_f32 v18, v18, v168, v169\n"
    "v_mfma_f32_32x32x16_bf16 v[176:191], a[244:247], a[20:23], v[176:191]\n"
    "s_waitcnt lgkmcnt(3)\n"
    "ds_read_b128 a[232:235], v8 offset:18432\n"
    "v_exp_f32 v155, v155\n"
    "v_fma_f32 v157, v157, s56, v20\n"
    "v_max3_f32 v18, v18, v170, v171\n"
    "v_mfma_f32_32x32x16_bf16 v[176:191], a[248:251], a[24:27], v[176:191]\n"
    "s_waitcnt lgkmcnt(3)\n"
    "ds_read_b128 a[236:239], v9 offset:18432\n"
    "v_exp_f32 v156, v156\n"
    "v_fma_f32 v158, v158, s56, v20\n"
    "v_max3_f32 v18, v18, v172, v173\n"
    "v_mfma_f32_32x32x16_bf16 v[176:191], a[252:255], a[28:31], v[176:191]\n"
    "s_waitcnt lgkmcnt(3)\n"
    "ds_read_b128 a[240:243], v6 offset:19456\n"
    "buffer_load_dwordx4 v14, s[12:15], s52 offen offset:3072 lds\n"
    "v_add_u32 v14, 0x4000, v14\n"
    "v_exp_f32 v157, v157\n"
    "v_mfma_f32_32x32x16_bf16 v[176:191], a[224:227], a[32:35], v[176:191]\n"
    "s_waitcnt lgkmcnt(3)\n"
    "ds_read_b128 a[244:247], v7 offset:19456\n"
    "v_fma_f32 v159, v159, s56, v20\n"
    "v_max3_f32 v18, v18, v174, v175\n"
    "v_exp_f32 v158, v158\n"
    "v_mul_f32 v19, s57, v18\n"
    "v_mfma_f32_32x32x16_bf16 v[176:191], a[228:231], a[36:39], v[176:191]\n"
    "s_waitcnt lgkmcnt(3)\n"
    "ds_read_b128 a[248:251], v8 offset:19456\n"
    "v_accvgpr_write_b32 a168, v19\n"
    "v_fma_f32 v160, v160, s56, v19\n"
    "v_exp_f32 v159, v159\n"
    "v_fma_f32 v161, v161, s56, v19\n"
    "v_mfma_f32_32x32x16_bf16 v[176:191], a[232:235], a[40:43], v[176:191]\n"
    "s_waitcnt lgkmcnt(3)\n"
    "ds_read_b128 a[252:255], v9 offset:19456\n"
    "v_exp_f32 v160, v160\n"
    "v_fma_f32 v162, v162, s56, v19\n"
    "v_exp_f32 v161, v161\n"
    "v_mfma_f32_32x32x16_bf16 v[176:191], a[236:239], a[44:47], v[176:191]\n"
    "s_waitcnt vmcnt(12) lgkmcnt(3)\n"
    "s_barrier\n"
    "s_add_u32 m0, s45, 0\n"
    "ds_read_b128 a[224:227], v6 offset:32768\n"
    "buffer_load_dwordx4 v14, s[12:15], s49 offen lds\n"
    "v_fma_f32 v163, v163, s56, v19\n"
    "v_mfma_f32_32x32x16_bf16 v[176:191], a[240:243], a[48:51], v[176:191]\n"
    "s_waitcnt lgkmcnt(3)\n"
    "ds_read_b128 a[228:231], v7 offset:32768\n"
    "v_exp_f32 v162, v162\n"
    "v_fma_f32 v164, v164, s56, v19\n"
    "v_exp_f32 v163, v163\n"
    "v_mfma_f32_32x32x16_bf16 v[176:191], a[244:247], a[52:55], v[176:191]\n"
    "s_waitcnt lgkmcnt(3)\n"
    "ds_read_b128 a[232:235], v8 offset:32768\n"
    "v_fma_f32 v165, v165, s56, v19\n"
    "v_exp_f32 v164, v164\n"
    "v_fma_f32 v166, v166, s56, v19\n"
    "v_mfma_f32_32x32x16_bf16 v[176:191], a[248:251], a[56:59], v[176:191]\n"
    "s_waitcnt lgkmcnt(3)\n"
    "ds_read_b128 a[236:239], v9 offset:32768\n"
    "v_exp_f32 v165, v165\n"
    "v_fma_f32 v167, v167, s56, v19\n"
    "v_exp_f32 v166, v166\n"
    "v_mfma_f32_32x32x16_bf16 v[176:191], a[252:255], a[60:63], v[176:191]\n"
    "s_waitcnt lgkmcnt(3)\n"
    "ds_read_b128 a[240:243], v6 offset:33792\n"
    "buffer_load_dwordx4 v14, s[12:15], s50 offen offset:1024 lds\n"
    "v_fma_f32 v168, v168, s56, v19\n"
    "v_mfma_f32_32x32x16_bf16 v[192:207], a[224:227], a[0:3], 0\n"
    "s_waitcnt lgkmcnt(3)\n"
    "ds_read_b128 a[244:247], v7 offset:33792\n"
    "v_mfma_f32_32x32x16_bf16 v[192:207], a[228:231], a[4:7], v[192:207]\n"
    "s_waitcnt lgkmcnt(3)\n"
    "ds_read_b128 a[248:251], v8 offset:33792\n"
    "s_nop 1\n"
    "v_max3_f32 v18, v18, v176, v177\n"
    "v_exp_f32 v167, v167\n"
    "v_fma_f32 v169, v169, s56, v19\n"
    "v_max3_f32 v18, v18, v178, v179\n"
    "v_mfma_f32_32x32x16_bf16 v[192:207], a[232:235], a[8:11], v[192:207]\n"
    "s_waitcnt lgkmcnt(3)\n"
    "ds_read_b128 a[252:255], v9 offset:33792\n"
    "v_exp_f32 v168, v168\n"
    "v_fma_f32 v170, v170, s56, v19\n"
    "v_max3_f32 v18, v18, v180, v181\n"
    "v_mfma_f32_32x32x16_bf16 v[192:207], a[236:239], a[12:15], v[192:207]\n"
    "s_waitcnt lgkmcnt(3)\n"
    "ds_read_b128 a[224:227], v6 offset:34816\n"
    "buffer_load_dwordx4 v14, s[12:15], s51 offen offset:2048 lds\n"
    "v_exp_f32 v169, v169\n"
    "v_fma_f32 v171, v171, s56, v19\n"
    "v_mfma_f32_32x32x16_bf16 v[192:207], a[240:243], a[16:19], v[192:207]\n"
    "s_waitcnt lgkmcnt(3)\n"
    "ds_read_b128 a[228:231], v7 offset:34816\n"
    "v_max3_f32 v18, v18, v182, v183\n"
    "v_exp_f32 v170, v170\n"
    "v_fma_f32 v172, v172, s56, v19\n"
    "v_max3_f32 v18, v18, v184, v185\n"
    "v_mfma_f32_32x32x16_bf16 v[192:207], a[244:247], a[20:23], v[192:207]\n"
    "s_waitcnt lgkmcnt(3)\n"
    "ds_read_b128 a[232:235], v8 offset:34816\n"
    "v_exp_f32 v171, v171\n"
    "v_fma_f32 v173, v173, s56, v19\n"
    "v_max3_f32 v18, v18, v186, v187\n"
    "v_mfma_f32_32x32x16_bf16 v[192:207], a[248:251], a[24:27], v[192:207]\n"
    "s_waitcnt lgkmcnt(3)\n"
    "ds_read_b128 a[236:239], v9 offset:34816\n"
    "v_exp_f32 v172, v172\n"
    "v_fma_f32 v174, v174, s56, v19\n"
    "v_max3_f32 v18, v18, v188, v189\n"
    "v_mfma_f32_32x32x16_bf16 v[192:207], a[252:255], a[28:31], v[192:207]\n"
    "s_waitcnt lgkmcnt(3)\n"
    "ds_read_b128 a[240:243], v6 offset:35840\n"
    "buffer_load_dwordx4 v14, s[12:15], s52 offen offset:3072 lds\n"
    "v_add_u32 v14, 0x4000, v14\n"
    "v_exp_f32 v173, v173\n"
    "v_mfma_f32_32x32x16_bf16 v[192:207], a[224:227], a[32:35], v[192:207]\n"
    "s_waitcnt lgkmcnt(3)\n"
    "ds_read_b128 a[244:247], v7 offset:35840\n"
    "v_fma_f32 v175, v175, s56, v19\n"
    "v_max3_f32 v18, v18, v190, v191\n"
    "v_exp_f32 v174, v174\n"
    "v_mul_f32 v20, s57, v18\n"
    "v_mfma_f32_32x32x16_bf16 v[192:207], a[228:231], a[36:39], v[192:207]\n"
    "s_waitcnt lgkmcnt(3)\n"
    "ds_read_b128 a[248:251], v8 offset:35840\n"
    "v_accvgpr_write_b32 a169, v20\n"
    "v_fma_f32 v176, v176, s56, v20\n"
    "v_exp_f32 v175, v175\n"
    "v_fma_f32 v177, v177, s56, v20\n"
    "v_mfma_f32_32x32x16_bf16 v[192:207], a[232:235], a[40:43], v[192:207]\n"
    "s_waitcnt lgkmcnt(3)\n"
    "ds_read_b128 a[252:255], v9 offset:35840\n"
    "v_exp_f32 v176, v176\n"
    "v_fma_f32 v178, v178, s56, v20\n"
    "v_exp_f32 v177, v177\n"
    "v_mfma_f32_32x32x16_bf16 v[192:207], a[236:239], a[44:47], v[192:207]\n"
    "s_waitcnt vmcnt(12) lgkmcnt(3)\n"
    "s_barrier\n"
    "s_add_u32 m0, s45, 0x4000\n"
    "ds_read_b128 a[224:227], v6 offset:49152\n"
    "buffer_load_dwordx4 v14, s[12:15], s49 offen lds\n"
    "v_fma_f32 v179, v179, s56, v20\n"
    "v_mfma_f32_32x32x16_bf16 v[192:207], a[240:243], a[48:51], v[192:207]\n"
    "s_waitcnt lgkmcnt(3)\n"
    "ds_read_b128 a[228:231], v7 offset:49152\n"
    "v_exp_f32 v178, v178\n"
    "v_fma_f32 v180, v180, s56, v20\n"
    "v_exp_f32 v179, v179\n"
    "v_mfma_f32_32x32x16_bf16 v[192:207], a[244:247], a[52:55], v[192:207]\n"
    "s_waitcnt lgkmcnt(3)\n"
    "ds_read_b128 a[232:235], v8 offset:49152\n"
    "v_fma_f32 v181, v181, s56, v20\n"
    "v_exp_f32 v180, v180\n"
    "v_fma_f32 v182, v182, s56, v20\n"
    "v_mfma_f32_32x32x16_bf16 v[192:207], a[248:251], a[56:59], v[192:207]\n"
    "s_waitcnt lgkmcnt(3)\n"
    "ds_read_b128 a[236:239], v9 offset:49152\n"
    "v_exp_f32 v181, v181\n"
    "v_fma_f32 v183, v183, s56, v20\n"
    "v_exp_f32 v182, v182\n"
    "v_mfma_f32_32x32x16_bf16 v[192:207], a[252:255], a[60:63], v[192:207]\n"
    "s_waitcnt lgkmcnt(3)\n"
    "ds_read_b128 a[240:243], v6 offset:50176\n"
    "buffer_load_dwordx4 v14, s[12:15], s50 offen offset:1024 lds\n"
    "v_fma_f32 v184, v184, s56, v20\n"
    "v_mfma_f32_32x32x16_bf16 v[208:223], a[224:227], a[0:3], 0\n"
    "s_waitcnt lgkmcnt(3)\n"
    "ds_read_b128 a[244:247], v7 offset:50176\n"
    "v_mfma_f32_32x32x16_bf16 v[208:223], a[228:231], a[4:7], v[208:223]\n"
    "s_waitcnt lgkmcnt(3)\n"
    "ds_read_b128 a[248:251], v8 offset:50176\n"
    "s_nop 1\n"
    "v_max3_f32 v18, v18, v192, v193\n"
    "v_exp_f32 v183, v183\n"
    "v_fma_f32 v185, v185, s56, v20\n"
    "v_max3_f32 v18, v18, v194, v195\n"
    "v_mfma_f32_32x32x16_bf16 v[208:223], a[232:235], a[8:11], v[208:223]\n"
    "s_waitcnt lgkmcnt(3)\n"
    "ds_read_b128 a[252:255], v9 offset:50176\n"
    "v_exp_f32 v184, v184\n"
    "v_fma_f32 v186, v186, s56, v20\n"
    "v_max3_f32 v18, v18, v196, v197\n"
    "v_mfma_f32_32x32x16_bf16 v[208:223], a[236:239], a[12:15], v[208:223]\n"
    "s_waitcnt lgkmcnt(3)\n"
    "ds_read_b128 a[224:227], v6 offset:51200\n"
    "buffer_load_dwordx4 v14, s[12:15], s51 offen offset:2048 lds\n"
    "v_exp_f32 v185, v185\n"
    "v_fma_f32 v187, v187, s56, v20\n"
    "v_mfma_f32_32x32x16_bf16 v[208:223], a[240:243], a[16:19], v[208:223]\n"
    "s_waitcnt lgkmcnt(3)\n"
    "ds_read_b128 a[228:231], v7 offset:51200\n"
    "v_max3_f32 v18, v18, v198, v199\n"
    "v_exp_f32 v186, v186\n"
    "v_fma_f32 v188, v188, s56, v20\n"
    "v_max3_f32 v18, v18, v200, v201\n"
    "v_mfma_f32_32x32x16_bf16 v[208:223], a[244:247], a[20:23], v[208:223]\n"
    "s_waitcnt lgkmcnt(3)\n"
    "ds_read_b128 a[232:235], v8 offset:51200\n"
    "v_exp_f32 v187, v187\n"
    "v_fma_f32 v189, v189, s56, v20\n"
    "v_max3_f32 v18, v18, v202, v203\n"
    "v_mfma_f32_32x32x16_bf16 v[208:223], a[248:251], a[24:27], v[208:223]\n"
    "s_waitcnt lgkmcnt(3)\n"
    "ds_read_b128 a[236:239], v9 offset:51200\n"
    "v_exp_f32 v188, v188\n"
    "v_fma_f32 v190, v190, s56, v20\n"
    "v_max3_f32 v18, v18, v204, v205\n"
    "v_mfma_f32_32x32x16_bf16 v[208:223], a[252:255], a[28:31], v[208:223]\n"
    "s_waitcnt lgkmcnt(3)\n"
    "ds_read_b128 a[240:243], v6 offset:52224\n"
    "buffer_load_dwordx4 v14, s[12:15], s52 offen offset:3072 lds\n"
    "v_add_u32 v14, 0x4000, v14\n"
    "v_exp_f32 v189, v189\n"
    "v_mfma_f32_32x32x16_bf16 v[208:223], a[224:227], a[32:35], v[208:223]\n"
    "s_waitcnt lgkmcnt(3)\n"
    "ds_read_b128 a[244:247], v7 offset:52224\n"
    "v_fma_f32 v191, v191, s56, v20\n"
    "v_max3_f32 v18, v18, v206, v207\n"
    "v_exp_f32 v190, v190\n"
    "v_mul_f32 v19, s57, v18\n"
    "v_mfma_f32_32x32x16_bf16 v[208:223], a[228:231], a[36:39], v[208:223]\n"
    "s_waitcnt lgkmcnt(3)\n"
    "ds_read_b128 a[248:251], v8 offset:52224\n"
    "v_accvgpr_write_b32 a170, v19\n"
    "v_fma_f32 v192, v192, s56, v19\n"
    "v_exp_f32 v191, v191\n"
    "v_fma_f32 v193, v193, s56, v19\n"
    "v_mfma_f32_32x32x16_bf16 v[208:223], a[232:235], a[40:43], v[208:223]\n"
    "s_waitcnt lgkmcnt(3)\n"
    "ds_read_b128 a[252:255], v9 offset:52224\n"
    "v_exp_f32 v192, v192\n"
    "v_fma_f32 v194, v194, s56, v19\n"
    "v_exp_f32 v193, v193\n"
    "v_mfma_f32_32x32x16_bf16 v[208:223], a[236:239], a[44:47], v[208:223]\n"
    "s_waitcnt vmcnt(12) lgkmcnt(3)\n"
    "s_barrier\n"
    "s_add_u32 m0, s45, 0x8000\n"
    "v_xor_b32 v6, 0x10000, v6\n"
    "ds_read_b128 a[224:227], v6\n"
    "buffer_load_dwordx4 v15, s[16:19], 0 offen lds\n"
    "v_mfma_f32_32x32x16_bf16 v[208:223], a[240:243], a[48:51], v[208:223]\n"
    "s_waitcnt lgkmcnt(3)\n"
    "v_xor_b32 v7, 0x10000, v7\n"
    "ds_read_b128 a[228:231], v7\n"
    "v_fma_f32 v195, v195, s56, v19\n"
    "v_exp_f32 v194, v194\n"
    "v_fma_f32 v196, v196, s56, v19\n"
    "v_mfma_f32_32x32x16_bf16 v[208:223], a[244:247], a[52:55], v[208:223]\n"
    "s_waitcnt lgkmcnt(3)\n"
    "v_xor_b32 v8, 0x10000, v8\n"
    "ds_read_b128 a[232:235], v8\n"
    "v_exp_f32 v195, v195\n"
    "v_fma_f32 v197, v197, s56, v19\n"
    "v_mfma_f32_32x32x16_bf16 v[208:223], a[248:251], a[56:59], v[208:223]\n"
    "s_waitcnt lgkmcnt(3)\n"
    "v_xor_b32 v9, 0x10000, v9\n"
    "ds_read_b128 a[236:239], v9\n"
    "v_exp_f32 v196, v196\n"
    "v_fma_f32 v198, v198, s56, v19\n"
    "v_mfma_f32_32x32x16_bf16 v[208:223], a[252:255], a[60:63], v[208:223]\n"
    "s_waitcnt lgkmcnt(3)\n"
    "ds_read_b128 a[240:243], v6 offset:1024\n"
    "buffer_load_dwordx4 v16, s[16:19], 0 offen offset:1024 lds\n"
    "v_exp_f32 v197, v197\n"
    "v_fma_f32 v199, v199, s56, v19\n"
    "v_mfma_f32_32x32x16_bf16 v[224:239], a[224:227], a[0:3], 0\n"
    "s_waitcnt lgkmcnt(3)\n"
    "ds_read_b128 a[244:247], v7 offset:1024\n"
    "v_exp_f32 v198, v198\n"
    "v_fma_f32 v200, v200, s56, v19\n"
    "v_mfma_f32_32x32x16_bf16 v[224:239], a[228:231], a[4:7], v[224:239]\n"
    "s_waitcnt lgkmcnt(3)\n"
    "ds_read_b128 a[248:251], v8 offset:1024\n"
    "v_max3_f32 v18, v18, v208, v209\n"
    "v_exp_f32 v199, v199\n"
    "v_fma_f32 v201, v201, s56, v19\n"
    "v_max3_f32 v18, v18, v210, v211\n"
    "v_mfma_f32_32x32x16_bf16 v[224:239], a[232:235], a[8:11], v[224:239]\n"
    "s_waitcnt lgkmcnt(3)\n"
    "ds_read_b128 a[252:255], v9 offset:1024\n"
    "v_exp_f32 v200, v200\n"
    "v_fma_f32 v202, v202, s56, v19\n"
    "v_max3_f32 v18, v18, v212, v213\n"
    "v_mfma_f32_32x32x16_bf16 v[224:239], a[236:239], a[12:15], v[224:239]\n"
    "s_waitcnt lgkmcnt(3)\n"
    "ds_read_b128 a[224:227], v6 offset:2048\n"
    "buffer_load_dwordx4 v15, s[16:19], 0 offen offset:2048 lds\n"
    "v_exp_f32 v201, v201\n"
    "v_fma_f32 v203, v203, s56, v19\n"
    "v_mfma_f32_32x32x16_bf16 v[224:239], a[240:243], a[16:19], v[224:239]\n"
    "s_waitcnt lgkmcnt(3)\n"
    "ds_read_b128 a[228:231], v7 offset:2048\n"
    "v_max3_f32 v18, v18, v214, v215\n"
    "v_exp_f32 v202, v202\n"
    "v_fma_f32 v204, v204, s56, v19\n"
    "v_max3_f32 v18, v18, v216, v217\n"
    "v_mfma_f32_32x32x16_bf16 v[224:239], a[244:247], a[20:23], v[224:239]\n"
    "s_waitcnt lgkmcnt(3)\n"
    "ds_read_b128 a[232:235], v8 offset:2048\n"
    "v_exp_f32 v203, v203\n"
    "v_fma_f32 v205, v205, s56, v19\n"
    "v_max3_f32 v18, v18, v218, v219\n"
    "v_mfma_f32_32x32x16_bf16 v[224:239], a[248:251], a[24:27], v[224:239]\n"
    "s_waitcnt lgkmcnt(3)\n"
    "ds_read_b128 a[236:239], v9 offset:2048\n"
    "v_exp_f32 v204, v204\n"
    "v_fma_f32 v206, v206, s56, v19\n"
    "v_max3_f32 v18, v18, v220, v221\n"
    "v_mfma_f32_32x32x16_bf16 v[224:239], a[252:255], a[28:31], v[224:239]\n"
    "s_waitcnt lgkmcnt(3)\n"
    "ds_read_b128 a[240:243], v6 offset:3072\n"
    "buffer_load_dwordx4 v16, s[16:19], 0 offen offset:3072 lds\n"
    "v_add_u32 v15, 0x4000, v15\n"
    "v_add_u32 v16, 0x4000, v16\n"
    "v_mfma_f32_32x32x16_bf16 v[224:239], a[224:227], a[32:35], v[224:239]\n"
    "s_waitcnt lgkmcnt(3)\n"
    "ds_read_b128 a[244:247], v7 offset:3072\n"
    "v_exp_f32 v205, v205\n"
    "v_fma_f32 v207, v207, s56, v19\n"
    "v_max3_f32 v18, v18, v222, v223\n"
    "v_mfma_f32_32x32x16_bf16 v[224:239], a[228:231], a[36:39], v[224:239]\n"
    "s_waitcnt lgkmcnt(3)\n"
    "ds_read_b128 a[248:251], v8 offset:3072\n"
    "v_exp_f32 v206, v206\n"
    "v_mul_f32 v20, s57, v18\n"
    "v_accvgpr_write_b32 a171, v20\n"
    "v_fma_f32 v208, v208, s56, v20\n"
    "v_mfma_f32_32x32x16_bf16 v[224:239], a[232:235], a[40:43], v[224:239]\n"
    "s_waitcnt lgkmcnt(3)\n"
    "ds_read_b128 a[252:255], v9 offset:3072\n"
    "v_exp_f32 v207, v207\n"
    "v_fma_f32 v209, v209, s56, v20\n"
    "v_exp_f32 v208, v208\n"
    "v_mfma_f32_32x32x16_bf16 v[224:239], a[236:239], a[44:47], v[224:239]\n"
    "s_waitcnt vmcnt(12) lgkmcnt(3)\n"
    "s_barrier\n"
    "s_add_u32 m0, s45, 0xc000\n"
    "ds_read_b128 a[224:227], v6 offset:16384\n"
    "buffer_load_dwordx4 v15, s[16:19], 0 offen lds\n"
    "v_fma_f32 v210, v210, s56, v20\n"
    "v_mfma_f32_32x32x16_bf16 v[224:239], a[240:243], a[48:51], v[224:239]\n"
    "s_waitcnt lgkmcnt(3)\n"
    "ds_read_b128 a[228:231], v7 offset:16384\n"
    "v_exp_f32 v209, v209\n"
    "v_fma_f32 v211, v211, s56, v20\n"
    "v_exp_f32 v210, v210\n"
    "v_mfma_f32_32x32x16_bf16 v[224:239], a[244:247], a[52:55], v[224:239]\n"
    "s_waitcnt lgkmcnt(3)\n"
    "ds_read_b128 a[232:235], v8 offset:16384\n"
    "v_fma_f32 v212, v212, s56, v20\n"
    "v_exp_f32 v211, v211\n"
    "v_fma_f32 v213, v213, s56, v20\n"
    "v_mfma_f32_32x32x16_bf16 v[224:239], a[248:251], a[56:59], v[224:239]\n"
    "s_waitcnt lgkmcnt(3)\n"
    "ds_read_b128 a[236:239], v9 offset:16384\n"
    "v_exp_f32 v212, v212\n"
    "v_fma_f32 v214, v214, s56, v20\n"
    "v_exp_f32 v213, v213\n"
    "v_mfma_f32_32x32x16_bf16 v[224:239], a[252:255], a[60:63], v[224:239]\n"
    "s_waitcnt lgkmcnt(3)\n"
    "ds_read_b128 a[240:243], v6 offset:17408\n"
    "buffer_load_dwordx4 v16, s[16:19], 0 offen offset:1024 lds\n"
    "v_fma_f32 v215, v215, s56, v20\n"
    "v_exp_f32 v214, v214\n"
    "v_mfma_f32_32x32x16_bf16 v[240:255], a[224:227], a[0:3], 0\n"
    "s_waitcnt lgkmcnt(3)\n"
    "ds_read_b128 a[244:247], v7 offset:17408\n"
    "v_fma_f32 v216, v216, s56, v20\n"
    "v_mfma_f32_32x32x16_bf16 v[240:255], a[228:231], a[4:7], v[240:255]\n"
    "s_waitcnt lgkmcnt(3)\n"
    "ds_read_b128 a[248:251], v8 offset:17408\n"
    "v_max3_f32 v18, v18, v224, v225\n"
    "v_exp_f32 v215, v215\n"
    "v_fma_f32 v217, v217, s56, v20\n"
    "v_max3_f32 v18, v18, v226, v227\n"
    "v_mfma_f32_32x32x16_bf16 v[240:255], a[232:235], a[8:11], v[240:255]\n"
    "s_waitcnt lgkmcnt(3)\n"
    "ds_read_b128 a[252:255], v9 offset:17408\n"
    "v_exp_f32 v216, v216\n"
    "v_fma_f32 v218, v218, s56, v20\n"
    "v_max3_f32 v18, v18, v228, v229\n"
    "v_mfma_f32_32x32x16_bf16 v[240:255], a[236:239], a[12:15], v[240:255]\n"
    "s_waitcnt lgkmcnt(3)\n"
    "ds_read_b128 a[224:227], v6 offset:18432\n"
    "buffer_load_dwordx4 v15, s[16:19], 0 offen offset:2048 lds\n"
    "v_exp_f32 v217, v217\n"
    "v_fma_f32 v219, v219, s56, v20\n"
    "v_mfma_f32_32x32x16_bf16 v[240:255], a[240:243], a[16:19], v[240:255]\n"
    "s_waitcnt lgkmcnt(3)\n"
    "ds_read_b128 a[228:231], v7 offset:18432\n"
    "v_max3_f32 v18, v18, v230, v231\n"
    "v_exp_f32 v218, v218\n"
    "v_fma_f32 v220, v220, s56, v20\n"
    "v_max3_f32 v18, v18, v232, v233\n"
    "v_mfma_f32_32x32x16_bf16 v[240:255], a[244:247], a[20:23], v[240:255]\n"
    "s_waitcnt lgkmcnt(3)\n"
    "ds_read_b128 a[232:235], v8 offset:18432\n"
    "v_exp_f32 v219, v219\n"
    "v_fma_f32 v221, v221, s56, v20\n"
    "v_max3_f32 v18, v18, v234, v235\n"
    "v_mfma_f32_32x32x16_bf16 v[240:255], a[248:251], a[24:27], v[240:255]\n"
    "s_waitcnt lgkmcnt(3)\n"
    "ds_read_b128 a[236:239], v9 offset:18432\n"
    "v_exp_f32 v220, v220\n"
    "v_fma_f32 v222, v222, s56, v20\n"
    "v_max3_f32 v18, v18, v236, v237\n"
    "v_mfma_f32_32x32x16_bf16 v[240:255], a[252:255], a[28:31], v[240:255]\n"
    "s_waitcnt lgkmcnt(3)\n"
    "ds_read_b128 a[240:243], v6 offset:19456\n"
    "buffer_load_dwordx4 v16, s[16:19], 0 offen offset:3072 lds\n"
    "v_add_u32 v15, 0x4000, v15\n"
    "v_add_u32 v16, 0x4000, v16\n"
    "v_mfma_f32_32x32x16_bf16 v[240:255], a[224:227], a[32:35], v[240:255]\n"
    "s_waitcnt lgkmcnt(3)\n"
    "ds_read_b128 a[244:247], v7 offset:19456\n"
    "v_exp_f32 v221, v221\n"
    "v_fma_f32 v223, v223, s56, v20\n"
    "v_max3_f32 v18, v18, v238, v239\n"
    "v_mfma_f32_32x32x16_bf16 v[240:255], a[228:231], a[36:39], v[240:255]\n"
    "s_waitcnt lgkmcnt(3)\n"
    "ds_read_b128 a[248:251], v8 offset:19456\n"
    "v_exp_f32 v222, v222\n"
    "v_mul_f32 v19, s57, v18\n"
    "v_accvgpr_write_b32 a172, v19\n"
    "v_fma_f32 v224, v224, s56, v19\n"
    "v_mfma_f32_32x32x16_bf16 v[240:255], a[232:235], a[40:43], v[240:255]\n"
    "s_waitcnt lgkmcnt(3)\n"
    "ds_read_b128 a[252:255], v9 offset:19456\n"
    "v_exp_f32 v223, v223\n"
    "v_fma_f32 v225, v225, s56, v19\n"
    "v_exp_f32 v224, v224\n"
    "v_mfma_f32_32x32x16_bf16 v[240:255], a[236:239], a[44:47], v[240:255]\n"
    "s_waitcnt vmcnt(12) lgkmcnt(3)\n"
    "s_barrier\n"
    "s_add_u32 m0, s45, 0x10000\n"
    "v_xor_b32 v6, 0x10000, v6\n"
    "ds_read_b128 a[224:227], v6\n"
    "buffer_load_dwordx4 v15, s[16:19], 0 offen lds\n"
    "v_mfma_f32_32x32x16_bf16 v[240:255], a[240:243], a[48:51], v[240:255]\n"
    "s_waitcnt lgkmcnt(3)\n"
    "v_xor_b32 v7, 0x10000, v7\n"
    "ds_read_b128 a[228:231], v7\n"
    "v_fma_f32 v226, v226, s56, v19\n"
    "v_exp_f32 v225, v225\n"
    "v_fma_f32 v227, v227, s56, v19\n"
    "v_mfma_f32_32x32x16_bf16 v[240:255], a[244:247], a[52:55], v[240:255]\n"
    "s_waitcnt lgkmcnt(3)\n"
    "v_xor_b32 v8, 0x10000, v8\n"
    "ds_read_b128 a[232:235], v8\n"
    "v_exp_f32 v226, v226\n"
    "v_fma_f32 v228, v228, s56, v19\n"
    "v_mfma_f32_32x32x16_bf16 v[240:255], a[248:251], a[56:59], v[240:255]\n"
    "s_waitcnt lgkmcnt(3)\n"
    "v_xor_b32 v9, 0x10000, v9\n"
    "ds_read_b128 a[236:239], v9\n"
    "v_exp_f32 v227, v227\n"
    "v_fma_f32 v229, v229, s56, v19\n"
    "v_mfma_f32_32x32x16_bf16 v[240:255], a[252:255], a[60:63], v[240:255]\n"
    "s_waitcnt lgkmcnt(3)\n"
    "ds_read_b128 a[240:243], v6 offset:1024\n"
    "buffer_load_dwordx4 v16, s[16:19], 0 offen offset:1024 lds\n"
    "v_exp_f32 v228, v228\n"
    "v_fma_f32 v230, v230, s56, v19\n"
    "v_mfma_f32_32x32x16_bf16 a[128:143], a[224:227], a[0:3], 0\n"
    "s_waitcnt lgkmcnt(3)\n"
    "ds_read_b128 a[244:247], v7 offset:1024\n"
    "v_exp_f32 v229, v229\n"
    "v_fma_f32 v231, v231, s56, v19\n"
    "v_exp_f32 v230, v230\n"
    "v_mfma_f32_32x32x16_bf16 a[128:143], a[228:231], a[4:7], a[128:143]\n"
    "s_waitcnt lgkmcnt(3)\n"
    "ds_read_b128 a[248:251], v8 offset:1024\n"
    "v_fma_f32 v232, v232, s56, v19\n"
    "v_max3_f32 v18, v18, v240, v241\n"
    "v_exp_f32 v231, v231\n"
    "v_fma_f32 v233, v233, s56, v19\n"
    "v_mfma_f32_32x32x16_bf16 a[128:143], a[232:235], a[8:11], a[128:143]\n"
    "s_waitcnt lgkmcnt(3)\n"
    "ds_read_b128 a[252:255], v9 offset:1024\n"
    "v_max3_f32 v18, v18, v242, v243\n"
    "v_exp_f32 v232, v232\n"
    "v_fma_f32 v234, v234, s56, v19\n"
    "v_max3_f32 v18, v18, v244, v245\n"
    "v_mfma_f32_32x32x16_bf16 a[128:143], a[236:239], a[12:15], a[128:143]\n"
    "s_waitcnt lgkmcnt(3)\n"
    "ds_read_b128 a[224:227], v6 offset:2048\n"
    "buffer_load_dwordx4 v15, s[16:19], 0 offen offset:2048 lds\n"
    "v_exp_f32 v233, v233\n"
    "v_fma_f32 v235, v235, s56, v19\n"
    "v_mfma_f32_32x32x16_bf16 a[128:143], a[240:243], a[16:19], a[128:143]\n"
    "s_waitcnt lgkmcnt(3)\n"
    "ds_read_b128 a[228:231], v7 offset:2048\n"
    "v_max3_f32 v18, v18, v246, v247\n"
    "v_exp_f32 v234, v234\n"
    "v_fma_f32 v236, v236, s56, v19\n"
    "v_max3_f32 v18, v18, v248, v249\n"
    "v_mfma_f32_32x32x16_bf16 a[128:143], a[244:247], a[20:23], a[128:143]\n"
    "s_waitcnt lgkmcnt(3)\n"
    "ds_read_b128 a[232:235], v8 offset:2048\n"
    "v_exp_f32 v235, v235\n"
    "v_fma_f32 v237, v237, s56, v19\n"
    "v_max3_f32 v18, v18, v250, v251\n"
    "v_mfma_f32_32x32x16_bf16 a[128:143], a[248:251], a[24:27], a[128:143]\n"
    "s_waitcnt lgkmcnt(3)\n"
    "ds_read_b128 a[236:239], v9 offset:2048\n"
    "v_exp_f32 v236, v236\n"
    "v_fma_f32 v238, v238, s56, v19\n"
    "v_max3_f32 v18, v18, v252, v253\n"
    "v_mfma_f32_32x32x16_bf16 a[128:143], a[252:255], a[28:31], a[128:143]\n"
    "s_waitcnt lgkmcnt(3)\n"
    "ds_read_b128 a[240:243], v6 offset:3072\n"
    "buffer_load_dwordx4 v16, s[16:19], 0 offen offset:3072 lds\n"
    "v_add_u32 v15, 0x4000, v15\n"
    "v_add_u32 v16, 0x4000, v16\n"
    "v_mfma_f32_32x32x16_bf16 a[128:143], a[224:227], a[32:35], a[128:143]\n"
    "s_waitcnt lgkmcnt(3)\n"
    "ds_read_b128 a[244:247], v7 offset:3072\n"
    "v_exp_f32 v237, v237\n"
    "v_fma_f32 v239, v239, s56, v19\n"
    "v_max3_f32 v18, v18, v254, v255\n"
    "v_mfma_f32_32x32x16_bf16 a[128:143], a[228:231], a[36:39], a[128:143]\n"
    "s_waitcnt lgkmcnt(3)\n"
    "ds_read_b128 a[248:251], v8 offset:3072\n"
    "v_exp_f32 v238, v238\n"
    "v_mul_f32 v20, s57, v18\n"
    "v_accvgpr_write_b32 a173, v20\n"
    "v_fma_f32 v240, v240, s56, v20\n"
    "v_mfma_f32_32x32x16_bf16 a[128:143], a[232:235], a[40:43], a[128:143]\n"
    "s_waitcnt lgkmcnt(3)\n"
    "ds_read_b128 a[252:255], v9 offset:3072\n"
    "v_exp_f32 v239, v239\n"
    "v_fma_f32 v241, v241, s56, v20\n"
    "v_exp_f32 v240, v240\n"
    "v_mfma_f32_32x32x16_bf16 a[128:143], a[236:239], a[44:47], a[128:143]\n"
    "s_waitcnt vmcnt(12) lgkmcnt(3)\n"
    "s_barrier\n"
    "s_add_u32 m0, s45, 0x14000\n"
    "ds_read_b128 a[224:227], v6 offset:16384\n"
    "buffer_load_dwordx4 v15, s[16:19], 0 offen lds\n"
    "v_fma_f32 v242, v242, s56, v20\n"
    "v_mfma_f32_32x32x16_bf16 a[128:143], a[240:243], a[48:51], a[128:143]\n"
    "s_waitcnt lgkmcnt(3)\n"
    "ds_read_b128 a[228:231], v7 offset:16384\n"
    "v_exp_f32 v241, v241\n"
    "v_fma_f32 v243, v243, s56, v20\n"
    "v_exp_f32 v242, v242\n"
    "v_mfma_f32_32x32x16_bf16 a[128:143], a[244:247], a[52:55], a[128:143]\n"
    "s_waitcnt lgkmcnt(3)\n"
    "ds_read_b128 a[232:235], v8 offset:16384\n"
    "v_fma_f32 v244, v244, s56, v20\n"
    "v_exp_f32 v243, v243\n"
    "v_fma_f32 v245, v245, s56, v20\n"
    "v_mfma_f32_32x32x16_bf16 a[128:143], a[248:251], a[56:59], a[128:143]\n"
    "s_waitcnt lgkmcnt(3)\n"
    "ds_read_b128 a[236:239], v9 offset:16384\n"
    "v_exp_f32 v244, v244\n"
    "v_fma_f32 v246, v246, s56, v20\n"
    "v_exp_f32 v245, v245\n"
    "v_mfma_f32_32x32x16_bf16 a[128:143], a[252:255], a[60:63], a[128:143]\n"
    "s_waitcnt lgkmcnt(3)\n"
    "ds_read_b128 a[240:243], v6 offset:17408\n"
    "buffer_load_dwordx4 v16, s[16:19], 0 offen offset:1024 lds\n"
    "v_fma_f32 v247, v247, s56, v20\n"
    "v_exp_f32 v246, v246\n"
    "v_mfma_f32_32x32x16_bf16 a[144:159], a[224:227], a[0:3], 0\n"
    "s_waitcnt lgkmcnt(3)\n"
    "ds_read_b128 a[244:247], v7 offset:17408\n"
    "v_fma_f32 v248, v248, s56, v20\n"
    "v_mfma_f32_32x32x16_bf16 a[144:159], a[228:231], a[4:7], a[144:159]\n"
    "s_waitcnt lgkmcnt(3)\n"
    "ds_read_b128 a[248:251], v8 offset:17408\n"
    "v_accvgpr_read_b32 v25, a128\n"
    "v_exp_f32 v247, v247\n"
    "v_accvgpr_read_b32 v26, a129\n"
    "v_max3_f32 v18, v18, v25, v26\n"
    "v_mfma_f32_32x32x16_bf16 a[144:159], a[232:235], a[8:11], a[144:159]\n"
    "s_waitcnt lgkmcnt(3)\n"
    "ds_read_b128 a[252:255], v9 offset:17408\n"
    "v_fma_f32 v249, v249, s56, v20\n"
    "v_exp_f32 v248, v248\n"
    "v_accvgpr_read_b32 v25, a130\n"
    "v_accvgpr_read_b32 v26, a131\n"
    "v_mfma_f32_32x32x16_bf16 a[144:159], a[236:239], a[12:15], a[144:159]\n"
    "s_waitcnt lgkmcnt(3)\n"
    "ds_read_b128 a[224:227], v6 offset:18432\n"
    "buffer_load_dwordx4 v15, s[16:19], 0 offen offset:2048 lds\n"
    "v_fma_f32 v250, v250, s56, v20\n"
    "v_exp_f32 v249, v249\n"
    "v_mfma_f32_32x32x16_bf16 a[144:159], a[240:243], a[16:19], a[144:159]\n"
    "s_waitcnt lgkmcnt(3)\n"
    "ds_read_b128 a[228:231], v7 offset:18432\n"
    "v_max3_f32 v18, v18, v25, v26\n"
    "v_fma_f32 v251, v251, s56, v20\n"
    "v_accvgpr_read_b32 v25, a132\n"
    "v_exp_f32 v250, v250\n"
    "v_mfma_f32_32x32x16_bf16 a[144:159], a[244:247], a[20:23], a[144:159]\n"
    "s_waitcnt lgkmcnt(3)\n"
    "ds_read_b128 a[232:235], v8 offset:18432\n"
    "v_accvgpr_read_b32 v26, a133\n"
    "v_max3_f32 v18, v18, v25, v26\n"
    "v_fma_f32 v252, v252, s56, v20\n"
    "v_exp_f32 v251, v251\n"
    "v_mfma_f32_32x32x16_bf16 a[144:159], a[248:251], a[24:27], a[144:159]\n"
    "s_waitcnt lgkmcnt(3)\n"
    "ds_read_b128 a[236:239], v9 offset:18432\n"
    "v_accvgpr_read_b32 v25, a134\n"
    "v_accvgpr_read_b32 v26, a135\n"
    "v_fma_f32 v253, v253, s56, v20\n"
    "v_exp_f32 v252, v252\n"
    "v_mfma_f32_32x32x16_bf16 a[144:159], a[252:255], a[28:31], a[144:159]\n"
    "s_waitcnt lgkmcnt(3)\n"
    "ds_read_b128 a[240:243], v6 offset:19456\n"
    "buffer_load_dwordx4 v16, s[16:19], 0 offen offset:3072 lds\n"
    "v_add_u32 v15, 0x4000, v15\n"
    "v_add_u32 v16, 0x4000, v16\n"
    "v_max3_f32 v18, v18, v25, v26\n"
    "v_mfma_f32_32x32x16_bf16 a[144:159], a[224:227], a[32:35], a[144:159]\n"
    "s_waitcnt lgkmcnt(3)\n"
    "ds_read_b128 a[244:247], v7 offset:19456\n"
    "v_fma_f32 v254, v254, s56, v20\n"
    "v_accvgpr_read_b32 v25, a136\n"
    "v_exp_f32 v253, v253\n"
    "v_accvgpr_read_b32 v26, a137\n"
    "v_mfma_f32_32x32x16_bf16 a[144:159], a[228:231], a[36:39], a[144:159]\n"
    "s_waitcnt lgkmcnt(3)\n"
    "ds_read_b128 a[248:251], v8 offset:19456\n"
    "v_max3_f32 v18, v18, v25, v26\n"
    "v_fma_f32 v255, v255, s56, v20\n"
    "v_exp_f32 v254, v254\n"
    "v_accvgpr_read_b32 v25, a138\n"
    "v_mfma_f32_32x32x16_bf16 a[144:159], a[232:235], a[40:43], a[144:159]\n"
    "s_waitcnt lgkmcnt(3)\n"
    "ds_read_b128 a[252:255], v9 offset:19456\n"
    "v_accvgpr_read_b32 v26, a139\n"
    "v_exp_f32 v255, v255\n"
    "v_max3_f32 v18, v18, v25, v26\n"
    "v_accvgpr_read_b32 v25, a140\n"
    "v_mfma_f32_32x32x16_bf16 a[144:159], a[236:239], a[44:47], a[144:159]\n"
    "s_waitcnt vmcnt(12) lgkmcnt(3)\n"
    "s_barrier\n"
    "s_add_u32 m0, s45, 0\n"
    "ds_read_b64_tr_b16 a[224:225], v2 offset:32768\n"
    "ds_read_b64_tr_b16 a[226:227], v2 offset:34816\n"
    "buffer_load_dwordx4 v15, s[16:19], 0 offen lds\n"
    "v_mfma_f32_32x32x16_bf16 a[144:159], a[240:243], a[48:51], a[144:159]\n"
    "s_waitcnt lgkmcnt(4)\n"
    "ds_read_b64_tr_b16 a[228:229], v3 offset:32768\n"
    "ds_read_b64_tr_b16 a[230:231], v3 offset:34816\n"
    "v_accvgpr_read_b32 v26, a141\n"
    "v_max3_f32 v18, v18, v25, v26\n"
    "v_accvgpr_read_b32 v25, a142\n"
    "v_accvgpr_read_b32 v26, a143\n"
    "v_mfma_f32_32x32x16_bf16 a[144:159], a[244:247], a[52:55], a[144:159]\n"
    "s_waitcnt lgkmcnt(5)\n"
    "ds_read_b64_tr_b16 a[232:233], v4 offset:32768\n"
    "ds_read_b64_tr_b16 a[234:235], v4 offset:34816\n"
    "v_max3_f32 v18, v18, v25, v26\n"
    "v_mfma_f32_32x32x16_bf16 a[144:159], a[248:251], a[56:59], a[144:159]\n"
    "s_waitcnt lgkmcnt(6)\n"
    "ds_read_b64_tr_b16 a[236:237], v5 offset:32768\n"
    "ds_read_b64_tr_b16 a[238:239], v5 offset:34816\n"
    "v_mfma_f32_32x32x16_bf16 a[144:159], a[252:255], a[60:63], a[144:159]\n"
    "s_waitcnt lgkmcnt(6)\n"
    "ds_read_b64_tr_b16 a[240:241], v2 offset:33024\n"
    "ds_read_b64_tr_b16 a[242:243], v2 offset:35072\n"
    "buffer_load_dwordx4 v16, s[16:19], 0 offen offset:1024 lds\n"
    "s_nop 7\n"
    "v_accvgpr_read_b32 v25, a144\n"
    "v_accvgpr_read_b32 v26, a145\n"
    "v_max3_f32 v18, v18, v25, v26\n"
    "v_accvgpr_read_b32 v27, a146\n"
    "v_accvgpr_read_b32 v28, a147\n"
    "v_max3_f32 v18, v18, v27, v28\n"
    "v_accvgpr_read_b32 v25, a148\n"
    "v_accvgpr_read_b32 v26, a149\n"
    "v_max3_f32 v18, v18, v25, v26\n"
    "v_accvgpr_read_b32 v27, a150\n"
    "v_accvgpr_read_b32 v28, a151\n"
    "v_max3_f32 v18, v18, v27, v28\n"
    "v_accvgpr_read_b32 v25, a152\n"
    "v_accvgpr_read_b32 v26, a153\n"
    "v_max3_f32 v18, v18, v25, v26\n"
    "v_accvgpr_read_b32 v27, a154\n"
    "v_accvgpr_read_b32 v28, a155\n"
    "v_max3_f32 v18, v18, v27, v28\n"
    "v_accvgpr_read_b32 v25, a156\n"
    "v_accvgpr_read_b32 v26, a157\n"
    "v_max3_f32 v18, v18, v25, v26\n"
    "v_accvgpr_read_b32 v27, a158\n"
    "v_accvgpr_read_b32 v28, a159\n"
    "v_max3_f32 v18, v18, v27, v28\n"
    "v_mov_b32 v25, v18\n"
    "v_mov_b32 v21, 0\n"
    "v_mov_b32 v22, 0\n"
    "v_permlane32_swap_b32 v25, v18\n"
    "v_max_f32 v18, v18, v25\n"
    "v_mul_f32 v24, s57, v18\n"
    "v_accvgpr_read_b32 v23, a160\n"
    "v_sub_f32 v23, v24, v23\n"
    "v_exp_f32 v23, v23\n"
    "s_nop 0\n"
    "v_mul_f32 v32, v32, v23\n"
    "v_mul_f32 v33, v33, v23\n"
    "v_mul_f32 v34, v34, v23\n"
    "v_mul_f32 v35, v35, v23\n"
    "v_mul_f32 v36, v36, v23\n"
    "v_mul_f32 v37, v37, v23\n"
    "v_mul_f32 v38, v38, v23\n"
    "v_mul_f32 v39, v39, v23\n"
    "v_add_f32 v21, v21, v32\n"
    "v_add_f32 v22, v22, v33\n"
    "v_cvt_pk_bf16_f32 v32, v32, v33\n"
    "v_add_f32 v21, v21, v34\n"
    "v_add_f32 v22, v22, v35\n"
    "v_cvt_pk_bf16_f32 v33, v34, v35\n"
    "v_add_f32 v21, v21, v36\n"
    "v_add_f32 v22, v22, v37\n"
    "v_cvt_pk_bf16_f32 v34, v36, v37\n"
    "v_add_f32 v21, v21, v38\n"
    "v_add_f32 v22, v22, v39\n"
    "v_cvt_pk_bf16_f32 v35, v38, v39\n"
    "s_nop 1\n"
    "v_mfma_f32_32x32x16_bf16 a[0:15], a[224:227], v[32:35], 0\n"
    "s_waitcnt lgkmcnt(6)\n"
    "ds_read_b64_tr_b16 a[244:245], v3 offset:33024\n"
    "ds_read_b64_tr_b16 a[246:247], v3 offset:35072\n"
    "v_mul_f32 v40, v40, v23\n"
    "v_mul_f32 v41, v41, v23\n"
    "v_mul_f32 v42, v42, v23\n"
    "v_mul_f32 v43, v43, v23\n"
    "v_mfma_f32_32x32x16_bf16 a[16:31], a[228:231], v[32:35], 0\n"
    "s_waitcnt lgkmcnt(6)\n"
    "ds_read_b64_tr_b16 a[248:249], v4 offset:33024\n"
    "ds_read_b64_tr_b16 a[250:251], v4 offset:35072\n"
    "v_mul_f32 v44, v44, v23\n"
    "v_mul_f32 v45, v45, v23\n"
    "v_mul_f32 v46, v46, v23\n"
    "v_mul_f32 v47, v47, v23\n"
    "v_mfma_f32_32x32x16_bf16 a[32:47], a[232:235], v[32:35], 0\n"
    "s_waitcnt lgkmcnt(6)\n"
    "ds_read_b64_tr_b16 a[252:253], v5 offset:33024\n"
    "ds_read_b64_tr_b16 a[254:255], v5 offset:35072\n"
    "v_add_f32 v21, v21, v40\n"
    "v_add_f32 v22, v22, v41\n"
    "v_cvt_pk_bf16_f32 v40, v40, v41\n"
    "v_add_f32 v21, v21, v42\n"
    "v_mfma_f32_32x32x16_bf16 a[48:63], a[236:239], v[32:35], 0\n"
    "s_waitcnt lgkmcnt(6)\n"
    "ds_read_b64_tr_b16 a[224:225], v2 offset:40960\n"
    "ds_read_b64_tr_b16 a[226:227], v2 offset:43008\n"
    "buffer_load_dwordx4 v15, s[16:19], 0 offen offset:2048 lds\n"
    "v_add_f32 v22, v22, v43\n"
    "v_cvt_pk_bf16_f32 v41, v42, v43\n"
    "v_mfma_f32_32x32x16_bf16 a[64:79], a[240:243], v[32:35], 0\n"
    "s_waitcnt lgkmcnt(6)\n"
    "ds_read_b64_tr_b16 a[228:229], v3 offset:40960\n"
    "ds_read_b64_tr_b16 a[230:231], v3 offset:43008\n"
    "v_add_f32 v21, v21, v44\n"
    "v_add_f32 v22, v22, v45\n"
    "v_cvt_pk_bf16_f32 v42, v44, v45\n"
    "v_add_f32 v21, v21, v46\n"
    "v_mfma_f32_32x32x16_bf16 a[80:95], a[244:247], v[32:35], 0\n"
    "s_waitcnt lgkmcnt(6)\n"
    "ds_read_b64_tr_b16 a[232:233], v4 offset:40960\n"
    "ds_read_b64_tr_b16 a[234:235], v4 offset:43008\n"
    "v_add_f32 v22, v22, v47\n"
    "v_cvt_pk_bf16_f32 v43, v46, v47\n"
    "v_accvgpr_read_b32 v23, a161\n"
    "v_sub_f32 v23, v24, v23\n"
    "v_mfma_f32_32x32x16_bf16 a[96:111], a[248:251], v[32:35], 0\n"
    "s_waitcnt lgkmcnt(6)\n"
    "ds_read_b64_tr_b16 a[236:237], v5 offset:40960\n"
    "ds_read_b64_tr_b16 a[238:239], v5 offset:43008\n"
    "v_exp_f32 v23, v23\n"
    "s_nop 0\n"
    "v_mul_f32 v48, v48, v23\n"
    "v_mul_f32 v49, v49, v23\n"
    "v_mfma_f32_32x32x16_bf16 a[112:127], a[252:255], v[32:35], 0\n"
    "s_waitcnt lgkmcnt(6)\n"
    "ds_read_b64_tr_b16 a[240:241], v2 offset:41216\n"
    "ds_read_b64_tr_b16 a[242:243], v2 offset:43264\n"
    "buffer_load_dwordx4 v16, s[16:19], 0 offen offset:3072 lds\n"
    "v_add_u32 v15, 0x4000, v15\n"
    "v_add_u32 v16, 0x4000, v16\n"
    "v_mfma_f32_32x32x16_bf16 a[0:15], a[224:227], v[40:43], a[0:15]\n"
    "s_waitcnt lgkmcnt(6)\n"
    "ds_read_b64_tr_b16 a[244:245], v3 offset:41216\n"
    "ds_read_b64_tr_b16 a[246:247], v3 offset:43264\n"
    "v_mul_f32 v50, v50, v23\n"
    "v_mul_f32 v51, v51, v23\n"
    "v_mul_f32 v52, v52, v23\n"
    "v_mul_f32 v53, v53, v23\n"
    "v_mfma_f32_32x32x16_bf16 a[16:31], a[228:231], v[40:43], a[16:31]\n"
    "s_waitcnt lgkmcnt(6)\n"
    "ds_read_b64_tr_b16 a[248:249], v4 offset:41216\n"
    "ds_read_b64_tr_b16 a[250:251], v4 offset:43264\n"
    "v_mul_f32 v54, v54, v23\n"
    "v_mul_f32 v55, v55, v23\n"
    "v_add_f32 v21, v21, v48\n"
    "v_add_f32 v22, v22, v49\n"
    "v_mfma_f32_32x32x16_bf16 a[32:47], a[232:235], v[40:43], a[32:47]\n"
    "s_waitcnt lgkmcnt(6)\n"
    "ds_read_b64_tr_b16 a[252:253], v5 offset:41216\n"
    "ds_read_b64_tr_b16 a[254:255], v5 offset:43264\n"
    "v_cvt_pk_bf16_f32 v48, v48, v49\n"
    "v_add_f32 v21, v21, v50\n"
    "v_add_f32 v22, v22, v51\n"
    "v_cvt_pk_bf16_f32 v49, v50, v51\n"
    "v_mfma_f32_32x32x16_bf16 a[48:63], a[236:239], v[40:43], a[48:63]\n"
    "s_waitcnt vmcnt(12) lgkmcnt(6)\n"
    "s_barrier\n"
    "s_add_u32 m0, s45, 0x4000\n"
    "ds_read_b64_tr_b16 a[224:225], v2 offset:49152\n"
    "ds_read_b64_tr_b16 a[226:227], v2 offset:51200\n"
    "buffer_load_dwordx4 v15, s[16:19], 0 offen lds\n"
    "v_mfma_f32_32x32x16_bf16 a[64:79], a[240:243], v[40:43], a[64:79]\n"
    "s_waitcnt lgkmcnt(6)\n"
    "ds_read_b64_tr_b16 a[228:229], v3 offset:49152\n"
    "ds_read_b64_tr_b16 a[230:231], v3 offset:51200\n"
    "v_add_f32 v21, v21, v52\n"
    "v_add_f32 v22, v22, v53\n"
    "v_cvt_pk_bf16_f32 v50, v52, v53\n"
    "v_add_f32 v21, v21, v54\n"
    "v_mfma_f32_32x32x16_bf16 a[80:95], a[244:247], v[40:43], a[80:95]\n"
    "s_waitcnt lgkmcnt(6)\n"
    "ds_read_b64_tr_b16 a[232:233], v4 offset:49152\n"
    "ds_read_b64_tr_b16 a[234:235], v4 offset:51200\n"
    "v_add_f32 v22, v22, v55\n"
    "v_cvt_pk_bf16_f32 v51, v54, v55\n"
    "v_mul_f32 v56, v56, v23\n"
    "v_mul_f32 v57, v57, v23\n"
    "v_mfma_f32_32x32x16_bf16 a[96:111], a[248:251], v[40:43], a[96:111]\n"
    "s_waitcnt lgkmcnt(6)\n"
    "ds_read_b64_tr_b16 a[236:237], v5 offset:49152\n"
    "ds_read_b64_tr_b16 a[238:239], v5 offset:51200\n"
    "v_mul_f32 v58, v58, v23\n"
    "v_mul_f32 v59, v59, v23\n"
    "v_mul_f32 v60, v60, v23\n"
    "v_mul_f32 v61, v61, v23\n"
    "v_mfma_f32_32x32x16_bf16 a[112:127], a[252:255], v[40:43], a[112:127]\n"
    "s_waitcnt lgkmcnt(6)\n"
    "ds_read_b64_tr_b16 a[240:241], v2 offset:49408\n"
    "ds_read_b64_tr_b16 a[242:243], v2 offset:51456\n"
    "buffer_load_dwordx4 v16, s[16:19], 0 offen offset:1024 lds\n"
    "v_mul_f32 v62, v62, v23\n"
    "v_mul_f32 v63, v63, v23\n"
    "v_mfma_f32_32x32x16_bf16 a[0:15], a[224:227], v[48:51], a[0:15]\n"
    "s_waitcnt lgkmcnt(6)\n"
    "ds_read_b64_tr_b16 a[244:245], v3 offset:49408\n"
    "ds_read_b64_tr_b16 a[246:247], v3 offset:51456\n"
    "v_add_f32 v21, v21, v56\n"
    "v_add_f32 v22, v22, v57\n"
    "v_cvt_pk_bf16_f32 v56, v56, v57\n"
    "v_add_f32 v21, v21, v58\n"
    "v_mfma_f32_32x32x16_bf16 a[16:31], a[228:231], v[48:51], a[16:31]\n"
    "s_waitcnt lgkmcnt(6)\n"
    "ds_read_b64_tr_b16 a[248:249], v4 offset:49408\n"
    "ds_read_b64_tr_b16 a[250:251], v4 offset:51456\n"
    "v_add_f32 v22, v22, v59\n"
    "v_cvt_pk_bf16_f32 v57, v58, v59\n"
    "v_add_f32 v21, v21, v60\n"
    "v_add_f32 v22, v22, v61\n"
    "v_mfma_f32_32x32x16_bf16 a[32:47], a[232:235], v[48:51], a[32:47]\n"
    "s_waitcnt lgkmcnt(6)\n"
    "ds_read_b64_tr_b16 a[252:253], v5 offset:49408\n"
    "ds_read_b64_tr_b16 a[254:255], v5 offset:51456\n"
    "v_cvt_pk_bf16_f32 v58, v60, v61\n"
    "v_add_f32 v21, v21, v62\n"
    "v_add_f32 v22, v22, v63\n"
    "v_cvt_pk_bf16_f32 v59, v62, v63\n"
    "v_mfma_f32_32x32x16_bf16 a[48:63], a[236:239], v[48:51], a[48:63]\n"
    "s_waitcnt lgkmcnt(6)\n"
    "ds_read_b64_tr_b16 a[224:225], v2 offset:57344\n"
    "ds_read_b64_tr_b16 a[226:227], v2 offset:59392\n"
    "buffer_load_dwordx4 v15, s[16:19], 0 offen offset:2048 lds\n"
    "v_accvgpr_read_b32 v23, a162\n"
    "v_sub_f32 v23, v24, v23\n"
    "v_mfma_f32_32x32x16_bf16 a[64:79], a[240:243], v[48:51], a[64:79]\n"
    "s_waitcnt lgkmcnt(6)\n"
    "ds_read_b64_tr_b16 a[228:229], v3 offset:57344\n"
    "ds_read_b64_tr_b16 a[230:231], v3 offset:59392\n"
    "v_exp_f32 v23, v23\n"
    "s_nop 0\n"
    "v_mul_f32 v64, v64, v23\n"
    "v_mul_f32 v65, v65, v23\n"
    "v_mfma_f32_32x32x16_bf16 a[80:95], a[244:247], v[48:51], a[80:95]\n"
    "s_waitcnt lgkmcnt(6)\n"
    "ds_read_b64_tr_b16 a[232:233], v4 offset:57344\n"
    "ds_read_b64_tr_b16 a[234:235], v4 offset:59392\n"
    "v_mul_f32 v66, v66, v23\n"
    "v_mul_f32 v67, v67, v23\n"
    "v_mul_f32 v68, v68, v23\n"
    "v_mul_f32 v69, v69, v23\n"
    "v_mfma_f32_32x32x16_bf16 a[96:111], a[248:251], v[48:51], a[96:111]\n"
    "s_waitcnt lgkmcnt(6)\n"
    "ds_read_b64_tr_b16 a[236:237], v5 offset:57344\n"
    "ds_read_b64_tr_b16 a[238:239], v5 offset:59392\n"
    "v_mul_f32 v70, v70, v23\n"
    "v_mul_f32 v71, v71, v23\n"
    "v_add_f32 v21, v21, v64\n"
    "v_add_f32 v22, v22, v65\n"
    "v_mfma_f32_32x32x16_bf16 a[112:127], a[252:255], v[48:51], a[112:127]\n"
    "s_waitcnt lgkmcnt(6)\n"
    "ds_read_b64_tr_b16 a[240:241], v2 offset:57600\n"
    "ds_read_b64_tr_b16 a[242:243], v2 offset:59648\n"
    "buffer_load_dwordx4 v16, s[16:19], 0 offen offset:3072 lds\n"
    "v_add_u32 v15, 0x4000, v15\n"
    "v_add_u32 v16, 0x4000, v16\n"
    "v_mfma_f32_32x32x16_bf16 a[0:15], a[224:227], v[56:59], a[0:15]\n"
    "s_waitcnt lgkmcnt(6)\n"
    "ds_read_b64_tr_b16 a[244:245], v3 offset:57600\n"
    "ds_read_b64_tr_b16 a[246:247], v3 offset:59648\n"
    "v_cvt_pk_bf16_f32 v64, v64, v65\n"
    "v_add_f32 v21, v21, v66\n"
    "v_add_f32 v22, v22, v67\n"
    "v_cvt_pk_bf16_f32 v65, v66, v67\n"
    "v_mfma_f32_32x32x16_bf16 a[16:31], a[228:231], v[56:59], a[16:31]\n"
    "s_waitcnt lgkmcnt(6)\n"
    "ds_read_b64_tr_b16 a[248:249], v4 offset:57600\n"
    "ds_read_b64_tr_b16 a[250:251], v4 offset:59648\n"
    "v_add_f32 v21, v21, v68\n"
    "v_add_f32 v22, v22, v69\n"
    "v_cvt_pk_bf16_f32 v66, v68, v69\n"
    "v_add_f32 v21, v21, v70\n"
    "v_mfma_f32_32x32x16_bf16 a[32:47], a[232:235], v[56:59], a[32:47]\n"
    "s_waitcnt lgkmcnt(6)\n"
    "ds_read_b64_tr_b16 a[252:253], v5 offset:57600\n"
    "ds_read_b64_tr_b16 a[254:255], v5 offset:59648\n"
    "v_add_f32 v22, v22, v71\n"
    "v_cvt_pk_bf16_f32 v67, v70, v71\n"
    "v_mul_f32 v72, v72, v23\n"
    "v_mul_f32 v73, v73, v23\n"
    "v_mfma_f32_32x32x16_bf16 a[48:63], a[236:239], v[56:59], a[48:63]\n"
    "s_waitcnt vmcnt(12) lgkmcnt(6)\n"
    "s_barrier\n"
    "s_add_u32 m0, s45, 0x8000\n"
    "v_xor_b32 v2, 0x10000, v2\n"
    "ds_read_b64_tr_b16 a[224:225], v2\n"
    "ds_read_b64_tr_b16 a[226:227], v2 offset:2048\n"
    "buffer_load_dwordx4 v15, s[16:19], 0 offen lds\n"
    "v_mfma_f32_32x32x16_bf16 a[64:79], a[240:243], v[56:59], a[64:79]\n"
    "s_waitcnt lgkmcnt(6)\n"
    "v_xor_b32 v3, 0x10000, v3\n"
    "ds_read_b64_tr_b16 a[228:229], v3\n"
    "ds_read_b64_tr_b16 a[230:231], v3 offset:2048\n"
    "v_mul_f32 v74, v74, v23\n"
    "v_mul_f32 v75, v75, v23\n"
    "v_mul_f32 v76, v76, v23\n"
    "v_mfma_f32_32x32x16_bf16 a[80:95], a[244:247], v[56:59], a[80:95]\n"
    "s_waitcnt lgkmcnt(6)\n"
    "v_xor_b32 v4, 0x10000, v4\n"
    "ds_read_b64_tr_b16 a[232:233], v4\n"
    "ds_read_b64_tr_b16 a[234:235], v4 offset:2048\n"
    "v_mul_f32 v77, v77, v23\n"
    "v_mul_f32 v78, v78, v23\n"
    "v_mul_f32 v79, v79, v23\n"
    "v_mfma_f32_32x32x16_bf16 a[96:111], a[248:251], v[56:59], a[96:111]\n"
    "s_waitcnt lgkmcnt(6)\n"
    "v_xor_b32 v5, 0x10000, v5\n"
    "ds_read_b64_tr_b16 a[236:237], v5\n"
    "ds_read_b64_tr_b16 a[238:239], v5 offset:2048\n"
    "v_add_f32 v21, v21, v72\n"
    "v_add_f32 v22, v22, v73\n"
    "v_cvt_pk_bf16_f32 v72, v72, v73\n"
    "v_mfma_f32_32x32x16_bf16 a[112:127], a[252:255], v[56:59], a[112:127]\n"
    "s_waitcnt lgkmcnt(6)\n"
    "ds_read_b64_tr_b16 a[240:241], v2 offset:256\n"
    "ds_read_b64_tr_b16 a[242:243], v2 offset:2304\n"
    "buffer_load_dwordx4 v16, s[16:19], 0 offen offset:1024 lds\n"
    "v_add_f32 v21, v21, v74\n"
    "v_add_f32 v22, v22, v75\n"
    "v_mfma_f32_32x32x16_bf16 a[0:15], a[224:227], v[64:67], a[0:15]\n"
    "s_waitcnt lgkmcnt(6)\n"
    "ds_read_b64_tr_b16 a[244:245], v3 offset:256\n"
    "ds_read_b64_tr_b16 a[246:247], v3 offset:2304\n"
    "v_cvt_pk_bf16_f32 v73, v74, v75\n"
    "v_add_f32 v21, v21, v76\n"
    "v_add_f32 v22, v22, v77\n"
    "v_cvt_pk_bf16_f32 v74, v76, v77\n"
    "v_mfma_f32_32x32x16_bf16 a[16:31], a[228:231], v[64:67], a[16:31]\n"
    "s_waitcnt lgkmcnt(6)\n"
    "ds_read_b64_tr_b16 a[248:249], v4 offset:256\n"
    "ds_read_b64_tr_b16 a[250:251], v4 offset:2304\n"
    "v_add_f32 v21, v21, v78\n"
    "v_add_f32 v22, v22, v79\n"
    "v_accvgpr_read_b32 v32, a128\n"
    "v_cvt_pk_bf16_f32 v75, v78, v79\n"
    "v_mfma_f32_32x32x16_bf16 a[32:47], a[232:235], v[64:67], a[32:47]\n"
    "s_waitcnt lgkmcnt(6)\n"
    "ds_read_b64_tr_b16 a[252:253], v5 offset:256\n"
    "ds_read_b64_tr_b16 a[254:255], v5 offset:2304\n"
    "v_accvgpr_read_b32 v23, a163\n"
    "v_sub_f32 v23, v24, v23\n"
    "v_accvgpr_read_b32 v33, a129\n"
    "v_accvgpr_read_b32 v34, a130\n"
    "v_mfma_f32_32x32x16_bf16 a[48:63], a[236:239], v[64:67], a[48:63]\n"
    "s_waitcnt lgkmcnt(6)\n"
    "ds_read_b64_tr_b16 a[224:225], v2 offset:8192\n"
    "ds_read_b64_tr_b16 a[226:227], v2 offset:10240\n"
    "buffer_load_dwordx4 v15, s[16:19], 0 offen offset:2048 lds\n"
    "v_exp_f32 v23, v23\n"
    "v_mfma_f32_32x32x16_bf16 a[64:79], a[240:243], v[64:67], a[64:79]\n"
    "s_waitcnt lgkmcnt(6)\n"
    "ds_read_b64_tr_b16 a[228:229], v3 offset:8192\n"
    "ds_read_b64_tr_b16 a[230:231], v3 offset:10240\n"
    "v_mul_f32 v80, v80, v23\n"
    "v_mul_f32 v81, v81, v23\n"
    "v_mul_f32 v82, v82, v23\n"
    "v_mul_f32 v83, v83, v23\n"
    "v_mfma_f32_32x32x16_bf16 a[80:95], a[244:247], v[64:67], a[80:95]\n"
    "s_waitcnt lgkmcnt(6)\n"
    "ds_read_b64_tr_b16 a[232:233], v4 offset:8192\n"
    "ds_read_b64_tr_b16 a[234:235], v4 offset:10240\n"
    "v_accvgpr_read_b32 v35, a131\n"
    "v_mul_f32 v84, v84, v23\n"
    "v_mul_f32 v85, v85, v23\n"
    "v_mul_f32 v86, v86, v23\n"
    "v_mfma_f32_32x32x16_bf16 a[96:111], a[248:251], v[64:67], a[96:111]\n"
    "s_waitcnt lgkmcnt(6)\n"
    "ds_read_b64_tr_b16 a[236:237], v5 offset:8192\n"
    "ds_read_b64_tr_b16 a[238:239], v5 offset:10240\n"
    "v_fma_f32 v32, v32, s56, v24\n"
    "v_mul_f32 v87, v87, v23\n"
    "v_add_f32 v21, v21, v80\n"
    "v_add_f32 v22, v22, v81\n"
    "v_mfma_f32_32x32x16_bf16 a[112:127], a[252:255], v[64:67], a[112:127]\n"
    "s_waitcnt lgkmcnt(6)\n"
    "ds_read_b64_tr_b16 a[240:241], v2 offset:8448\n"
    "ds_read_b64_tr_b16 a[242:243], v2 offset:10496\n"
    "buffer_load_dwordx4 v16, s[16:19], 0 offen offset:3072 lds\n"
    "v_add_u32 v15, 0x4000, v15\n"
    "v_add_u32 v16, 0x4000, v16\n"
    "v_mfma_f32_32x32x16_bf16 a[0:15], a[224:227], v[72:75], a[0:15]\n"
    "s_waitcnt lgkmcnt(6)\n"
    "ds_read_b64_tr_b16 a[244:245], v3 offset:8448\n"
    "ds_read_b64_tr_b16 a[246:247], v3 offset:10496\n"
    "v_cvt_pk_bf16_f32 v80, v80, v81\n"
    "v_fma_f32 v33, v33, s56, v24\n"
    "v_add_f32 v21, v21, v82\n"
    "v_add_f32 v22, v22, v83\n"
    "v_mfma_f32_32x32x16_bf16 a[16:31], a[228:231], v[72:75], a[16:31]\n"
    "s_waitcnt lgkmcnt(6)\n"
    "ds_read_b64_tr_b16 a[248:249], v4 offset:8448\n"
    "ds_read_b64_tr_b16 a[250:251], v4 offset:10496\n"
    "v_cvt_pk_bf16_f32 v81, v82, v83\n"
    "v_fma_f32 v34, v34, s56, v24\n"
    "v_add_f32 v21, v21, v84\n"
    "v_add_f32 v22, v22, v85\n"
    "v_mfma_f32_32x32x16_bf16 a[32:47], a[232:235], v[72:75], a[32:47]\n"
    "s_waitcnt lgkmcnt(6)\n"
    "ds_read_b64_tr_b16 a[252:253], v5 offset:8448\n"
    "ds_read_b64_tr_b16 a[254:255], v5 offset:10496\n"
    "v_cvt_pk_bf16_f32 v82, v84, v85\n"
    "v_fma_f32 v35, v35, s56, v24\n"
    "v_add_f32 v21, v21, v86\n"
    "v_add_f32 v22, v22, v87\n"
    "v_mfma_f32_32x32x16_bf16 a[48:63], a[236:239], v[72:75], a[48:63]\n"
    "s_waitcnt vmcnt(12) lgkmcnt(6)\n"
    "s_barrier\n"
    "s_add_u32 m0, s45, 0xc000\n"
    "ds_read_b64_tr_b16 a[224:225], v2 offset:16384\n"
    "ds_read_b64_tr_b16 a[226:227], v2 offset:18432\n"
    "buffer_load_dwordx4 v15, s[16:19], 0 offen lds\n"
    "v_mfma_f32_32x32x16_bf16 a[64:79], a[240:243], v[72:75], a[64:79]\n"
    "s_waitcnt lgkmcnt(6)\n"
    "ds_read_b64_tr_b16 a[228:229], v3 offset:16384\n"
    "ds_read_b64_tr_b16 a[230:231], v3 offset:18432\n"
    "v_cvt_pk_bf16_f32 v83, v86, v87\n"
    "v_exp_f32 v32, v32\n"
    "v_mul_f32 v88, v88, v23\n"
    "v_mfma_f32_32x32x16_bf16 a[80:95], a[244:247], v[72:75], a[80:95]\n"
    "s_waitcnt lgkmcnt(6)\n"
    "ds_read_b64_tr_b16 a[232:233], v4 offset:16384\n"
    "ds_read_b64_tr_b16 a[234:235], v4 offset:18432\n"
    "v_mul_f32 v89, v89, v23\n"
    "v_exp_f32 v33, v33\n"
    "v_mul_f32 v90, v90, v23\n"
    "v_mfma_f32_32x32x16_bf16 a[96:111], a[248:251], v[72:75], a[96:111]\n"
    "s_waitcnt lgkmcnt(6)\n"
    "ds_read_b64_tr_b16 a[236:237], v5 offset:16384\n"
    "ds_read_b64_tr_b16 a[238:239], v5 offset:18432\n"
    "v_mul_f32 v91, v91, v23\n"
    "v_mul_f32 v92, v92, v23\n"
    "v_mul_f32 v93, v93, v23\n"
    "v_mul_f32 v94, v94, v23\n"
    "v_mfma_f32_32x32x16_bf16 a[112:127], a[252:255], v[72:75], a[112:127]\n"
    "s_waitcnt lgkmcnt(6)\n"
    "ds_read_b64_tr_b16 a[240:241], v2 offset:16640\n"
    "ds_read_b64_tr_b16 a[242:243], v2 offset:18688\n"
    "buffer_load_dwordx4 v16, s[16:19], 0 offen offset:1024 lds\n"
    "v_mul_f32 v95, v95, v23\n"
    "v_add_f32 v21, v21, v88\n"
    "v_mfma_f32_32x32x16_bf16 a[0:15], a[224:227], v[80:83], a[0:15]\n"
    "s_waitcnt lgkmcnt(6)\n"
    "ds_read_b64_tr_b16 a[244:245], v3 offset:16640\n"
    "ds_read_b64_tr_b16 a[246:247], v3 offset:18688\n"
    "v_exp_f32 v34, v34\n"
    "v_add_f32 v22, v22, v89\n"
    "v_cvt_pk_bf16_f32 v88, v88, v89\n"
    "v_mfma_f32_32x32x16_bf16 a[16:31], a[228:231], v[80:83], a[16:31]\n"
    "s_waitcnt lgkmcnt(6)\n"
    "ds_read_b64_tr_b16 a[248:249], v4 offset:16640\n"
    "ds_read_b64_tr_b16 a[250:251], v4 offset:18688\n"
    "v_add_f32 v21, v21, v90\n"
    "v_add_f32 v22, v22, v91\n"
    "v_cvt_pk_bf16_f32 v89, v90, v91\n"
    "v_add_f32 v21, v21, v92\n"
    "v_mfma_f32_32x32x16_bf16 a[32:47], a[232:235], v[80:83], a[32:47]\n"
    "s_waitcnt lgkmcnt(6)\n"
    "ds_read_b64_tr_b16 a[252:253], v5 offset:16640\n"
    "ds_read_b64_tr_b16 a[254:255], v5 offset:18688\n"
    "v_exp_f32 v35, v35\n"
    "v_add_f32 v22, v22, v93\n"
    "v_cvt_pk_bf16_f32 v90, v92, v93\n"
    "v_mfma_f32_32x32x16_bf16 a[48:63], a[236:239], v[80:83], a[48:63]\n"
    "s_waitcnt lgkmcnt(6)\n"
    "ds_read_b64_tr_b16 a[224:225], v2 offset:24576\n"
    "ds_read_b64_tr_b16 a[226:227], v2 offset:26624\n"
    "buffer_load_dwordx4 v15, s[16:19], 0 offen offset:2048 lds\n"
    "v_add_f32 v21, v21, v94\n"
    "v_add_f32 v22, v22, v95\n"
    "v_mfma_f32_32x32x16_bf16 a[64:79], a[240:243], v[80:83], a[64:79]\n"
    "s_waitcnt lgkmcnt(6)\n"
    "ds_read_b64_tr_b16 a[228:229], v3 offset:24576\n"
    "ds_read_b64_tr_b16 a[230:231], v3 offset:26624\n"
    "v_cvt_pk_bf16_f32 v91, v94, v95\n"
    "v_accvgpr_read_b32 v23, a164\n"
    "v_add_f32 v21, v21, v32\n"
    "v_sub_f32 v23, v24, v23\n"
    "v_mfma_f32_32x32x16_bf16 a[80:95], a[244:247], v[80:83], a[80:95]\n"
    "s_waitcnt lgkmcnt(6)\n"
    "ds_read_b64_tr_b16 a[232:233], v4 offset:24576\n"
    "ds_read_b64_tr_b16 a[234:235], v4 offset:26624\n"
    "v_exp_f32 v23, v23\n"
    "s_nop 0\n"
    "v_mul_f32 v96, v96, v23\n"
    "v_add_f32 v22, v22, v33\n"
    "v_mfma_f32_32x32x16_bf16 a[96:111], a[248:251], v[80:83], a[96:111]\n"
    "s_waitcnt lgkmcnt(6)\n"
    "ds_read_b64_tr_b16 a[236:237], v5 offset:24576\n"
    "ds_read_b64_tr_b16 a[238:239], v5 offset:26624\n"
    "v_mul_f32 v97, v97, v23\n"
    "v_mul_f32 v98, v98, v23\n"
    "v_mul_f32 v99, v99, v23\n"
    "v_add_f32 v21, v21, v34\n"
    "v_mfma_f32_32x32x16_bf16 a[112:127], a[252:255], v[80:83], a[112:127]\n"
    "s_waitcnt lgkmcnt(6)\n"
    "ds_read_b64_tr_b16 a[240:241], v2 offset:24832\n"
    "ds_read_b64_tr_b16 a[242:243], v2 offset:26880\n"
    "buffer_load_dwordx4 v16, s[16:19], 0 offen offset:3072 lds\n"
    "v_add_u32 v15, 0x4000, v15\n"
    "v_add_u32 v16, 0x4000, v16\n"
    "v_mfma_f32_32x32x16_bf16 a[0:15], a[224:227], v[88:91], a[0:15]\n"
    "s_waitcnt lgkmcnt(6)\n"
    "ds_read_b64_tr_b16 a[244:245], v3 offset:24832\n"
    "ds_read_b64_tr_b16 a[246:247], v3 offset:26880\n"
    "v_mul_f32 v100, v100, v23\n"
    "v_mul_f32 v101, v101, v23\n"
    "v_mul_f32 v102, v102, v23\n"
    "v_add_f32 v22, v22, v35\n"
    "v_mfma_f32_32x32x16_bf16 a[16:31], a[228:231], v[88:91], a[16:31]\n"
    "s_waitcnt lgkmcnt(6)\n"
    "ds_read_b64_tr_b16 a[248:249], v4 offset:24832\n"
    "ds_read_b64_tr_b16 a[250:251], v4 offset:26880\n"
    "v_mul_f32 v103, v103, v23\n"
    "v_add_f32 v21, v21, v96\n"
    "v_add_f32 v22, v22, v97\n"
    "v_accvgpr_read_b32 v36, a132\n"
    "v_mfma_f32_32x32x16_bf16 a[32:47], a[232:235], v[88:91], a[32:47]\n"
    "s_waitcnt lgkmcnt(6)\n"
    "ds_read_b64_tr_b16 a[252:253], v5 offset:24832\n"
    "ds_read_b64_tr_b16 a[254:255], v5 offset:26880\n"
    "v_cvt_pk_bf16_f32 v96, v96, v97\n"
    "v_add_f32 v21, v21, v98\n"
    "v_add_f32 v22, v22, v99\n"
    "v_accvgpr_read_b32 v37, a133\n"
    "v_mfma_f32_32x32x16_bf16 a[48:63], a[236:239], v[88:91], a[48:63]\n"
    "s_waitcnt vmcnt(12) lgkmcnt(6)\n"
    "s_barrier\n"
    "s_add_u32 m0, s45, 0x10000\n"
    "v_xor_b32 v2, 0x10000, v2\n"
    "ds_read_b64_tr_b16 a[224:225], v2\n"
    "ds_read_b64_tr_b16 a[226:227], v2 offset:2048\n"
    "buffer_load_dwordx4 v15, s[16:19], 0 offen lds\n"
    "v_mfma_f32_32x32x16_bf16 a[64:79], a[240:243], v[88:91], a[64:79]\n"
    "s_waitcnt lgkmcnt(6)\n"
    "v_xor_b32 v3, 0x10000, v3\n"
    "ds_read_b64_tr_b16 a[228:229], v3\n"
    "ds_read_b64_tr_b16 a[230:231], v3 offset:2048\n"
    "v_cvt_pk_bf16_f32 v97, v98, v99\n"
    "v_add_f32 v21, v21, v100\n"
    "v_add_f32 v22, v22, v101\n"
    "v_mfma_f32_32x32x16_bf16 a[80:95], a[244:247], v[88:91], a[80:95]\n"
    "s_waitcnt lgkmcnt(6)\n"
    "v_xor_b32 v4, 0x10000, v4\n"
    "ds_read_b64_tr_b16 a[232:233], v4\n"
    "ds_read_b64_tr_b16 a[234:235], v4 offset:2048\n"
    "v_accvgpr_read_b32 v38, a134\n"
    "v_cvt_pk_bf16_f32 v98, v100, v101\n"
    "v_add_f32 v21, v21, v102\n"
    "v_mfma_f32_32x32x16_bf16 a[96:111], a[248:251], v[88:91], a[96:111]\n"
    "s_waitcnt lgkmcnt(6)\n"
    "v_xor_b32 v5, 0x10000, v5\n"
    "ds_read_b64_tr_b16 a[236:237], v5\n"
    "ds_read_b64_tr_b16 a[238:239], v5 offset:2048\n"
    "v_add_f32 v22, v22, v103\n"
    "v_cvt_pk_bf16_f32 v99, v102, v103\n"
    "v_accvgpr_read_b32 v39, a135\n"
    "v_mfma_f32_32x32x16_bf16 a[112:127], a[252:255], v[88:91], a[112:127]\n"
    "s_waitcnt lgkmcnt(6)\n"
    "ds_read_b64_tr_b16 a[240:241], v2 offset:256\n"
    "ds_read_b64_tr_b16 a[242:243], v2 offset:2304\n"
    "buffer_load_dwordx4 v16, s[16:19], 0 offen offset:1024 lds\n"
    "v_fma_f32 v36, v36, s56, v24\n"
    "v_mul_f32 v104, v104, v23\n"
    "v_mfma_f32_32x32x16_bf16 a[0:15], a[224:227], v[96:99], a[0:15]\n"
    "s_waitcnt lgkmcnt(6)\n"
    "ds_read_b64_tr_b16 a[244:245], v3 offset:256\n"
    "ds_read_b64_tr_b16 a[246:247], v3 offset:2304\n"
    "v_mul_f32 v105, v105, v23\n"
    "v_fma_f32 v37, v37, s56, v24\n"
    "v_mul_f32 v106, v106, v23\n"
    "v_mul_f32 v107, v107, v23\n"
    "v_mfma_f32_32x32x16_bf16 a[16:31], a[228:231], v[96:99], a[16:31]\n"
    "s_waitcnt lgkmcnt(6)\n"
    "ds_read_b64_tr_b16 a[248:249], v4 offset:256\n"
    "ds_read_b64_tr_b16 a[250:251], v4 offset:2304\n"
    "v_mul_f32 v108, v108, v23\n"
    "v_fma_f32 v38, v38, s56, v24\n"
    "v_mul_f32 v109, v109, v23\n"
    "v_mul_f32 v110, v110, v23\n"
    "v_mfma_f32_32x32x16_bf16 a[32:47], a[232:235], v[96:99], a[32:47]\n"
    "s_waitcnt lgkmcnt(6)\n"
    "ds_read_b64_tr_b16 a[252:253], v5 offset:256\n"
    "ds_read_b64_tr_b16 a[254:255], v5 offset:2304\n"
    "v_mul_f32 v111, v111, v23\n"
    "v_fma_f32 v39, v39, s56, v24\n"
    "v_add_f32 v21, v21, v104\n"
    "v_add_f32 v22, v22, v105\n"
    "v_mfma_f32_32x32x16_bf16 a[48:63], a[236:239], v[96:99], a[48:63]\n"
    "s_waitcnt lgkmcnt(6)\n"
    "ds_read_b64_tr_b16 a[224:225], v2 offset:8192\n"
    "ds_read_b64_tr_b16 a[226:227], v2 offset:10240\n"
    "buffer_load_dwordx4 v15, s[16:19], 0 offen offset:2048 lds\n"
    "v_cvt_pk_bf16_f32 v104, v104, v105\n"
    "v_add_f32 v21, v21, v106\n"
    "v_mfma_f32_32x32x16_bf16 a[64:79], a[240:243], v[96:99], a[64:79]\n"
    "s_waitcnt lgkmcnt(6)\n"
    "ds_read_b64_tr_b16 a[228:229], v3 offset:8192\n"
    "ds_read_b64_tr_b16 a[230:231], v3 offset:10240\n"
    "v_exp_f32 v36, v36\n"
    "v_add_f32 v22, v22, v107\n"
    "v_cvt_pk_bf16_f32 v105, v106, v107\n"
    "v_mfma_f32_32x32x16_bf16 a[80:95], a[244:247], v[96:99], a[80:95]\n"
    "s_waitcnt lgkmcnt(6)\n"
    "ds_read_b64_tr_b16 a[232:233], v4 offset:8192\n"
    "ds_read_b64_tr_b16 a[234:235], v4 offset:10240\n"
    "v_add_f32 v21, v21, v108\n"
    "v_add_f32 v22, v22, v109\n"
    "v_cvt_pk_bf16_f32 v106, v108, v109\n"
    "v_add_f32 v21, v21, v110\n"
    "v_mfma_f32_32x32x16_bf16 a[96:111], a[248:251], v[96:99], a[96:111]\n"
    "s_waitcnt lgkmcnt(6)\n"
    "ds_read_b64_tr_b16 a[236:237], v5 offset:8192\n"
    "ds_read_b64_tr_b16 a[238:239], v5 offset:10240\n"
    "v_add_f32 v22, v22, v111\n"
    "v_cvt_pk_bf16_f32 v107, v110, v111\n"
    "v_exp_f32 v37, v37\n"
    "v_mfma_f32_32x32x16_bf16 a[112:127], a[252:255], v[96:99], a[112:127]\n"
    "s_waitcnt lgkmcnt(6)\n"
    "ds_read_b64_tr_b16 a[240:241], v2 offset:8448\n"
    "ds_read_b64_tr_b16 a[242:243], v2 offset:10496\n"
    "buffer_load_dwordx4 v16, s[16:19], 0 offen offset:3072 lds\n"
    "v_add_u32 v15, 0x4000, v15\n"
    "v_add_u32 v16, 0x4000, v16\n"
    "v_mfma_f32_32x32x16_bf16 a[0:15], a[224:227], v[104:107], a[0:15]\n"
    "s_waitcnt lgkmcnt(6)\n"
    "ds_read_b64_tr_b16 a[244:245], v3 offset:8448\n"
    "ds_read_b64_tr_b16 a[246:247], v3 offset:10496\n"
    "v_accvgpr_read_b32 v23, a165\n"
    "v_sub_f32 v23, v24, v23\n"
    "v_exp_f32 v23, v23\n"
    "v_mfma_f32_32x32x16_bf16 a[16:31], a[228:231], v[104:107], a[16:31]\n"
    "s_waitcnt lgkmcnt(6)\n"
    "ds_read_b64_tr_b16 a[248:249], v4 offset:8448\n"
    "ds_read_b64_tr_b16 a[250:251], v4 offset:10496\n"
    "v_exp_f32 v38, v38\n"
    "v_mul_f32 v112, v112, v23\n"
    "v_mul_f32 v113, v113, v23\n"
    "v_mfma_f32_32x32x16_bf16 a[32:47], a[232:235], v[104:107], a[32:47]\n"
    "s_waitcnt lgkmcnt(6)\n"
    "ds_read_b64_tr_b16 a[252:253], v5 offset:8448\n"
    "ds_read_b64_tr_b16 a[254:255], v5 offset:10496\n"
    "v_mul_f32 v114, v114, v23\n"
    "v_mul_f32 v115, v115, v23\n"
    "v_mul_f32 v116, v116, v23\n"
    "v_mul_f32 v117, v117, v23\n"
    "v_mfma_f32_32x32x16_bf16 a[48:63], a[236:239], v[104:107], a[48:63]\n"
    "s_waitcnt vmcnt(12) lgkmcnt(6)\n"
    "s_barrier\n"
    "s_add_u32 m0, s45, 0x14000\n"
    "ds_read_b64_tr_b16 a[224:225], v2 offset:16384\n"
    "ds_read_b64_tr_b16 a[226:227], v2 offset:18432\n"
    "buffer_load_dwordx4 v15, s[16:19], 0 offen lds\n"
    "v_mfma_f32_32x32x16_bf16 a[64:79], a[240:243], v[104:107], a[64:79]\n"
    "s_waitcnt lgkmcnt(6)\n"
    "ds_read_b64_tr_b16 a[228:229], v3 offset:16384\n"
    "ds_read_b64_tr_b16 a[230:231], v3 offset:18432\n"
    "v_exp_f32 v39, v39\n"
    "v_mul_f32 v118, v118, v23\n"
    "v_mul_f32 v119, v119, v23\n"
    "v_mfma_f32_32x32x16_bf16 a[80:95], a[244:247], v[104:107], a[80:95]\n"
    "s_waitcnt lgkmcnt(6)\n"
    "ds_read_b64_tr_b16 a[232:233], v4 offset:16384\n"
    "ds_read_b64_tr_b16 a[234:235], v4 offset:18432\n"
    "v_add_f32 v21, v21, v112\n"
    "v_add_f32 v22, v22, v113\n"
    "v_cvt_pk_bf16_f32 v112, v112, v113\n"
    "v_add_f32 v21, v21, v114\n"
    "v_mfma_f32_32x32x16_bf16 a[96:111], a[248:251], v[104:107], a[96:111]\n"
    "s_waitcnt lgkmcnt(6)\n"
    "ds_read_b64_tr_b16 a[236:237], v5 offset:16384\n"
    "ds_read_b64_tr_b16 a[238:239], v5 offset:18432\n"
    "v_add_f32 v22, v22, v115\n"
    "v_cvt_pk_bf16_f32 v113, v114, v115\n"
    "v_add_f32 v21, v21, v116\n"
    "v_add_f32 v22, v22, v117\n"
    "v_cvt_pk_bf16_f32 v114, v116, v117\n"
    "v_add_f32 v21, v21, v118\n"
    "v_add_f32 v22, v22, v119\n"
    "v_cvt_pk_bf16_f32 v115, v118, v119\n"
    "v_mfma_f32_32x32x16_bf16 a[112:127], a[252:255], v[104:107], a[112:127]\n"
    "s_waitcnt lgkmcnt(6)\n"
    "ds_read_b64_tr_b16 a[240:241], v2 offset:16640\n"
    "ds_read_b64_tr_b16 a[242:243], v2 offset:18688\n"
    "buffer_load_dwordx4 v16, s[16:19], 0 offen offset:1024 lds\n"
    "v_add_f32 v21, v21, v36\n"
    "v_add_f32 v22, v22, v37\n"
    "v_mfma_f32_32x32x16_bf16 a[0:15], a[224:227], v[112:115], a[0:15]\n"
    "s_waitcnt lgkmcnt(6)\n"
    "ds_read_b64_tr_b16 a[244:245], v3 offset:16640\n"
    "ds_read_b64_tr_b16 a[246:247], v3 offset:18688\n"
    "v_add_f32 v21, v21, v38\n"
    "v_add_f32 v22, v22, v39\n"
    "v_mul_f32 v120, v120, v23\n"
    "v_accvgpr_read_b32 v40, a136\n"
    "v_mfma_f32_32x32x16_bf16 a[16:31], a[228:231], v[112:115], a[16:31]\n"
    "s_waitcnt lgkmcnt(6)\n"
    "ds_read_b64_tr_b16 a[248:249], v4 offset:16640\n"
    "ds_read_b64_tr_b16 a[250:251], v4 offset:18688\n"
    "v_mul_f32 v121, v121, v23\n"
    "v_mul_f32 v122, v122, v23\n"
    "v_mul_f32 v123, v123, v23\n"
    "v_accvgpr_read_b32 v41, a137\n"
    "v_mfma_f32_32x32x16_bf16 a[32:47], a[232:235], v[112:115], a[32:47]\n"
    "s_waitcnt lgkmcnt(6)\n"
    "ds_read_b64_tr_b16 a[252:253], v5 offset:16640\n"
    "ds_read_b64_tr_b16 a[254:255], v5 offset:18688\n"
    "v_mul_f32 v124, v124, v23\n"
    "v_mul_f32 v125, v125, v23\n"
    "v_mul_f32 v126, v126, v23\n"
    "v_mul_f32 v127, v127, v23\n"
    "v_mfma_f32_32x32x16_bf16 a[48:63], a[236:239], v[112:115], a[48:63]\n"
    "s_waitcnt lgkmcnt(6)\n"
    "ds_read_b64_tr_b16 a[224:225], v2 offset:24576\n"
    "ds_read_b64_tr_b16 a[226:227], v2 offset:26624\n"
    "buffer_load_dwordx4 v15, s[16:19], 0 offen offset:2048 lds\n"
    "v_accvgpr_read_b32 v42, a138\n"
    "v_add_f32 v21, v21, v120\n"
    "v_mfma_f32_32x32x16_bf16 a[64:79], a[240:243], v[112:115], a[64:79]\n"
    "s_waitcnt lgkmcnt(6)\n"
    "ds_read_b64_tr_b16 a[228:229], v3 offset:24576\n"
    "ds_read_b64_tr_b16 a[230:231], v3 offset:26624\n"
    "v_add_f32 v22, v22, v121\n"
    "v_cvt_pk_bf16_f32 v120, v120, v121\n"
    "v_accvgpr_read_b32 v43, a139\n"
    "v_add_f32 v21, v21, v122\n"
    "v_mfma_f32_32x32x16_bf16 a[80:95], a[244:247], v[112:115], a[80:95]\n"
    "s_waitcnt lgkmcnt(6)\n"
    "ds_read_b64_tr_b16 a[232:233], v4 offset:24576\n"
    "ds_read_b64_tr_b16 a[234:235], v4 offset:26624\n"
    "v_add_f32 v22, v22, v123\n"
    "v_cvt_pk_bf16_f32 v121, v122, v123\n"
    "v_fma_f32 v40, v40, s56, v24\n"
    "v_add_f32 v21, v21, v124\n"
    "v_mfma_f32_32x32x16_bf16 a[96:111], a[248:251], v[112:115], a[96:111]\n"
    "s_waitcnt lgkmcnt(6)\n"
    "ds_read_b64_tr_b16 a[236:237], v5 offset:24576\n"
    "ds_read_b64_tr_b16 a[238:239], v5 offset:26624\n"
    "v_add_f32 v22, v22, v125\n"
    "v_cvt_pk_bf16_f32 v122, v124, v125\n"
    "v_add_f32 v21, v21, v126\n"
    "v_add_f32 v22, v22, v127\n"
    "v_cvt_pk_bf16_f32 v123, v126, v127\n"
    "v_mfma_f32_32x32x16_bf16 a[112:127], a[252:255], v[112:115], a[112:127]\n"
    "s_waitcnt lgkmcnt(6)\n"
    "ds_read_b64_tr_b16 a[240:241], v2 offset:24832\n"
    "ds_read_b64_tr_b16 a[242:243], v2 offset:26880\n"
    "buffer_load_dwordx4 v16, s[16:19], 0 offen offset:3072 lds\n"
    "v_add_u32 v15, 0x4000, v15\n"
    "v_add_u32 v16, 0x4000, v16\n"
    "v_mfma_f32_32x32x16_bf16 a[0:15], a[224:227], v[120:123], a[0:15]\n"
    "s_waitcnt lgkmcnt(6)\n"
    "ds_read_b64_tr_b16 a[244:245], v3 offset:24832\n"
    "ds_read_b64_tr_b16 a[246:247], v3 offset:26880\n"
    "v_fma_f32 v41, v41, s56, v24\n"
    "v_fma_f32 v42, v42, s56, v24\n"
    "v_accvgpr_read_b32 v23, a166\n"
    "v_sub_f32 v23, v24, v23\n"
    "v_mfma_f32_32x32x16_bf16 a[16:31], a[228:231], v[120:123], a[16:31]\n"
    "s_waitcnt lgkmcnt(6)\n"
    "ds_read_b64_tr_b16 a[248:249], v4 offset:24832\n"
    "ds_read_b64_tr_b16 a[250:251], v4 offset:26880\n"
    "v_exp_f32 v23, v23\n"
    "v_fma_f32 v43, v43, s56, v24\n"
    "v_mul_f32 v128, v128, v23\n"
    "v_mfma_f32_32x32x16_bf16 a[32:47], a[232:235], v[120:123], a[32:47]\n"
    "s_waitcnt lgkmcnt(6)\n"
    "ds_read_b64_tr_b16 a[252:253], v5 offset:24832\n"
    "ds_read_b64_tr_b16 a[254:255], v5 offset:26880\n"
    "v_mul_f32 v129, v129, v23\n"
    "v_exp_f32 v40, v40\n"
    "v_mul_f32 v130, v130, v23\n"
    "v_mfma_f32_32x32x16_bf16 a[48:63], a[236:239], v[120:123], a[48:63]\n"
    "s_waitcnt vmcnt(12) lgkmcnt(6)\n"
    "s_barrier\n"
    "s_add_u32 m0, s45, 0\n"
    "ds_read_b64_tr_b16 a[224:225], v2 offset:32768\n"
    "ds_read_b64_tr_b16 a[226:227], v2 offset:34816\n"
    "buffer_load_dwordx4 v15, s[16:19], 0 offen lds\n"
    "v_mfma_f32_32x32x16_bf16 a[64:79], a[240:243], v[120:123], a[64:79]\n"
    "s_waitcnt lgkmcnt(6)\n"
    "ds_read_b64_tr_b16 a[228:229], v3 offset:32768\n"
    "ds_read_b64_tr_b16 a[230:231], v3 offset:34816\n"
    "v_mul_f32 v131, v131, v23\n"
    "v_mul_f32 v132, v132, v23\n"
    "v_mul_f32 v133, v133, v23\n"
    "v_mul_f32 v134, v134, v23\n"
    "v_mfma_f32_32x32x16_bf16 a[80:95], a[244:247], v[120:123], a[80:95]\n"
    "s_waitcnt lgkmcnt(6)\n"
    "ds_read_b64_tr_b16 a[232:233], v4 offset:32768\n"
    "ds_read_b64_tr_b16 a[234:235], v4 offset:34816\n"
    "v_mul_f32 v135, v135, v23\n"
    "v_add_f32 v21, v21, v128\n"
    "v_exp_f32 v41, v41\n"
    "v_mfma_f32_32x32x16_bf16 a[96:111], a[248:251], v[120:123], a[96:111]\n"
    "s_waitcnt lgkmcnt(6)\n"
    "ds_read_b64_tr_b16 a[236:237], v5 offset:32768\n"
    "ds_read_b64_tr_b16 a[238:239], v5 offset:34816\n"
    "v_add_f32 v22, v22, v129\n"
    "v_cvt_pk_bf16_f32 v128, v128, v129\n"
    "v_add_f32 v21, v21, v130\n"
    "v_add_f32 v22, v22, v131\n"
    "v_cvt_pk_bf16_f32 v129, v130, v131\n"
    "v_add_f32 v21, v21, v132\n"
    "v_add_f32 v22, v22, v133\n"
    "v_cvt_pk_bf16_f32 v130, v132, v133\n"
    "v_add_f32 v21, v21, v134\n"
    "v_add_f32 v22, v22, v135\n"
    "v_cvt_pk_bf16_f32 v131, v134, v135\n"
    "v_mfma_f32_32x32x16_bf16 a[112:127], a[252:255], v[120:123], a[112:127]\n"
    "s_waitcnt lgkmcnt(6)\n"
    "ds_read_b64_tr_b16 a[240:241], v2 offset:33024\n"
    "ds_read_b64_tr_b16 a[242:243], v2 offset:35072\n"
    "buffer_load_dwordx4 v16, s[16:19], 0 offen offset:1024 lds\n"
    "v_exp_f32 v42, v42\n"
    "v_mfma_f32_32x32x16_bf16 a[0:15], a[224:227], v[128:131], a[0:15]\n"
    "s_waitcnt lgkmcnt(6)\n"
    "ds_read_b64_tr_b16 a[244:245], v3 offset:33024\n"
    "ds_read_b64_tr_b16 a[246:247], v3 offset:35072\n"
    "v_exp_f32 v43, v43\n"
    "v_mul_f32 v136, v136, v23\n"
    "v_mul_f32 v137, v137, v23\n"
    "v_mfma_f32_32x32x16_bf16 a[16:31], a[228:231], v[128:131], a[16:31]\n"
    "s_waitcnt lgkmcnt(6)\n"
    "ds_read_b64_tr_b16 a[248:249], v4 offset:33024\n"
    "ds_read_b64_tr_b16 a[250:251], v4 offset:35072\n"
    "v_mul_f32 v138, v138, v23\n"
    "v_mul_f32 v139, v139, v23\n"
    "v_add_f32 v21, v21, v40\n"
    "v_mul_f32 v140, v140, v23\n"
    "v_mfma_f32_32x32x16_bf16 a[32:47], a[232:235], v[128:131], a[32:47]\n"
    "s_waitcnt lgkmcnt(6)\n"
    "ds_read_b64_tr_b16 a[252:253], v5 offset:33024\n"
    "ds_read_b64_tr_b16 a[254:255], v5 offset:35072\n"
    "v_mul_f32 v141, v141, v23\n"
    "v_mul_f32 v142, v142, v23\n"
    "v_add_f32 v22, v22, v41\n"
    "v_mul_f32 v143, v143, v23\n"
    "v_mfma_f32_32x32x16_bf16 a[48:63], a[236:239], v[128:131], a[48:63]\n"
    "s_waitcnt lgkmcnt(6)\n"
    "ds_read_b64_tr_b16 a[224:225], v2 offset:40960\n"
    "ds_read_b64_tr_b16 a[226:227], v2 offset:43008\n"
    "buffer_load_dwordx4 v15, s[16:19], 0 offen offset:2048 lds\n"
    "v_add_f32 v21, v21, v136\n"
    "v_add_f32 v22, v22, v137\n"
    "v_mfma_f32_32x32x16_bf16 a[64:79], a[240:243], v[128:131], a[64:79]\n"
    "s_waitcnt lgkmcnt(6)\n"
    "ds_read_b64_tr_b16 a[228:229], v3 offset:40960\n"
    "ds_read_b64_tr_b16 a[230:231], v3 offset:43008\n"
    "v_add_f32 v21, v21, v42\n"
    "v_cvt_pk_bf16_f32 v136, v136, v137\n"
    "s_lshl_b32 s58, s40, 14\n"
    "v_add_f32 v21, v21, v138\n"
    "v_mfma_f32_32x32x16_bf16 a[80:95], a[244:247], v[128:131], a[80:95]\n"
    "s_waitcnt lgkmcnt(6)\n"
    "ds_read_b64_tr_b16 a[232:233], v4 offset:40960\n"
    "ds_read_b64_tr_b16 a[234:235], v4 offset:43008\n"
    "v_add_f32 v22, v22, v139\n"
    "v_add_f32 v22, v22, v43\n"
    "v_cvt_pk_bf16_f32 v137, v138, v139\n"
    "v_add_f32 v21, v21, v140\n"
    "v_mfma_f32_32x32x16_bf16 a[96:111], a[248:251], v[128:131], a[96:111]\n"
    "s_waitcnt lgkmcnt(6)\n"
    "ds_read_b64_tr_b16 a[236:237], v5 offset:40960\n"
    "ds_read_b64_tr_b16 a[238:239], v5 offset:43008\n"
    "v_add_f32 v22, v22, v141\n"
    "v_cvt_pk_bf16_f32 v138, v140, v141\n"
    "v_add_f32 v21, v21, v142\n"
    "v_add_f32 v22, v22, v143\n"
    "v_cvt_pk_bf16_f32 v139, v142, v143\n"
    "v_mfma_f32_32x32x16_bf16 a[112:127], a[252:255], v[128:131], a[112:127]\n"
    "s_waitcnt lgkmcnt(6)\n"
    "ds_read_b64_tr_b16 a[240:241], v2 offset:41216\n"
    "ds_read_b64_tr_b16 a[242:243], v2 offset:43264\n"
    "buffer_load_dwordx4 v16, s[16:19], 0 offen offset:3072 lds\n"
    "v_add_u32 v15, 0x4000, v15\n"
    "v_add_u32 v16, 0x4000, v16\n"
    "v_mfma_f32_32x32x16_bf16 a[0:15], a[224:227], v[136:139], a[0:15]\n"
    "s_waitcnt lgkmcnt(6)\n"
    "ds_read_b64_tr_b16 a[244:245], v3 offset:41216\n"
    "ds_read_b64_tr_b16 a[246:247], v3 offset:43264\n"
    "v_accvgpr_read_b32 v44, a140\n"
    "v_accvgpr_read_b32 v45, a141\n"
    "v_accvgpr_read_b32 v23, a167\n"
    "v_sub_f32 v23, v24, v23\n"
    "v_mfma_f32_32x32x16_bf16 a[16:31], a[228:231], v[136:139], a[16:31]\n"
    "s_waitcnt lgkmcnt(6)\n"
    "ds_read_b64_tr_b16 a[248:249], v4 offset:41216\n"
    "ds_read_b64_tr_b16 a[250:251], v4 offset:43264\n"
    "v_exp_f32 v23, v23\n"
    "v_accvgpr_read_b32 v46, a142\n"
    "v_mul_f32 v144, v144, v23\n"
    "v_mfma_f32_32x32x16_bf16 a[32:47], a[232:235], v[136:139], a[32:47]\n"
    "s_waitcnt lgkmcnt(6)\n"
    "ds_read_b64_tr_b16 a[252:253], v5 offset:41216\n"
    "ds_read_b64_tr_b16 a[254:255], v5 offset:43264\n"
    "v_mul_f32 v145, v145, v23\n"
    "s_add_u32 s58, s58, 0x18000\n"
    "v_accvgpr_read_b32 v47, a143\n"
    "v_mul_f32 v146, v146, v23\n"
    "v_mfma_f32_32x32x16_bf16 a[48:63], a[236:239], v[136:139], a[48:63]\n"
    "s_waitcnt vmcnt(12) lgkmcnt(6)\n"
    "s_barrier\n"
    "s_add_u32 m0, s45, 0x4000\n"
    "ds_read_b64_tr_b16 a[224:225], v2 offset:49152\n"
    "ds_read_b64_tr_b16 a[226:227], v2 offset:51200\n"
    "buffer_load_dwordx4 v15, s[16:19], 0 offen lds\n"
    "v_mfma_f32_32x32x16_bf16 a[64:79], a[240:243], v[136:139], a[64:79]\n"
    "s_waitcnt lgkmcnt(6)\n"
    "ds_read_b64_tr_b16 a[228:229], v3 offset:49152\n"
    "ds_read_b64_tr_b16 a[230:231], v3 offset:51200\n"
    "v_mul_f32 v147, v147, v23\n"
    "v_mul_f32 v148, v148, v23\n"
    "v_fma_f32 v44, v44, s56, v24\n"
    "v_mul_f32 v149, v149, v23\n"
    "v_mfma_f32_32x32x16_bf16 a[80:95], a[244:247], v[136:139], a[80:95]\n"
    "s_waitcnt lgkmcnt(6)\n"
    "ds_read_b64_tr_b16 a[232:233], v4 offset:49152\n"
    "ds_read_b64_tr_b16 a[234:235], v4 offset:51200\n"
    "v_mul_f32 v150, v150, v23\n"
    "v_mul_f32 v151, v151, v23\n"
    "v_fma_f32 v45, v45, s56, v24\n"
    "v_add_f32 v21, v21, v144\n"
    "v_mfma_f32_32x32x16_bf16 a[96:111], a[248:251], v[136:139], a[96:111]\n"
    "s_waitcnt lgkmcnt(6)\n"
    "ds_read_b64_tr_b16 a[236:237], v5 offset:49152\n"
    "ds_read_b64_tr_b16 a[238:239], v5 offset:51200\n"
    "v_add_f32 v22, v22, v145\n"
    "v_cvt_pk_bf16_f32 v144, v144, v145\n"
    "v_add_f32 v21, v21, v146\n"
    "v_add_f32 v22, v22, v147\n"
    "v_cvt_pk_bf16_f32 v145, v146, v147\n"
    "v_add_f32 v21, v21, v148\n"
    "v_add_f32 v22, v22, v149\n"
    "v_cvt_pk_bf16_f32 v146, v148, v149\n"
    "v_add_f32 v21, v21, v150\n"
    "v_add_f32 v22, v22, v151\n"
    "v_cvt_pk_bf16_f32 v147, v150, v151\n"
    "v_mfma_f32_32x32x16_bf16 a[112:127], a[252:255], v[136:139], a[112:127]\n"
    "s_waitcnt lgkmcnt(6)\n"
    "ds_read_b64_tr_b16 a[240:241], v2 offset:49408\n"
    "ds_read_b64_tr_b16 a[242:243], v2 offset:51456\n"
    "buffer_load_dwordx4 v16, s[16:19], 0 offen offset:1024 lds\n"
    "v_fma_f32 v46, v46, s56, v24\n"
    "v_fma_f32 v47, v47, s56, v24\n"
    "v_mfma_f32_32x32x16_bf16 a[0:15], a[224:227], v[144:147], a[0:15]\n"
    "s_waitcnt lgkmcnt(6)\n"
    "ds_read_b64_tr_b16 a[244:245], v3 offset:49408\n"
    "ds_read_b64_tr_b16 a[246:247], v3 offset:51456\n"
    "v_lshlrev_b32 v27, 4, v1\n"
    "v_exp_f32 v44, v44\n"
    "v_mul_f32 v152, v152, v23\n"
    "v_mfma_f32_32x32x16_bf16 a[16:31], a[228:231], v[144:147], a[16:31]\n"
    "s_waitcnt lgkmcnt(6)\n"
    "ds_read_b64_tr_b16 a[248:249], v4 offset:49408\n"
    "ds_read_b64_tr_b16 a[250:251], v4 offset:51456\n"
    "v_exp_f32 v45, v45\n"
    "v_mul_f32 v153, v153, v23\n"
    "v_mul_f32 v154, v154, v23\n"
    "v_mfma_f32_32x32x16_bf16 a[32:47], a[232:235], v[144:147], a[32:47]\n"
    "s_waitcnt lgkmcnt(6)\n"
    "ds_read_b64_tr_b16 a[252:253], v5 offset:49408\n"
    "ds_read_b64_tr_b16 a[254:255], v5 offset:51456\n"
    "v_mul_f32 v155, v155, v23\n"
    "v_mul_f32 v156, v156, v23\n"
    "v_mul_f32 v157, v157, v23\n"
    "v_mul_f32 v158, v158, v23\n"
    "v_mfma_f32_32x32x16_bf16 a[48:63], a[236:239], v[144:147], a[48:63]\n"
    "s_waitcnt lgkmcnt(6)\n"
    "ds_read_b64_tr_b16 a[224:225], v2 offset:57344\n"
    "ds_read_b64_tr_b16 a[226:227], v2 offset:59392\n"
    "buffer_load_dwordx4 v15, s[16:19], 0 offen offset:2048 lds\n"
    "v_exp_f32 v46, v46\n"
    "v_mfma_f32_32x32x16_bf16 a[64:79], a[240:243], v[144:147], a[64:79]\n"
    "s_waitcnt lgkmcnt(6)\n"
    "ds_read_b64_tr_b16 a[228:229], v3 offset:57344\n"
    "ds_read_b64_tr_b16 a[230:231], v3 offset:59392\n"
    "v_mul_f32 v159, v159, v23\n"
    "v_add_u32 v6, s58, v27\n"
    "v_add_f32 v21, v21, v152\n"
    "v_add_f32 v22, v22, v153\n"
    "v_mfma_f32_32x32x16_bf16 a[80:95], a[244:247], v[144:147], a[80:95]\n"
    "s_waitcnt lgkmcnt(6)\n"
    "ds_read_b64_tr_b16 a[232:233], v4 offset:57344\n"
    "ds_read_b64_tr_b16 a[234:235], v4 offset:59392\n"
    "v_cvt_pk_bf16_f32 v152, v152, v153\n"
    "v_add_f32 v21, v21, v154\n"
    "v_add_f32 v22, v22, v155\n"
    "v_cvt_pk_bf16_f32 v153, v154, v155\n"
    "v_mfma_f32_32x32x16_bf16 a[96:111], a[248:251], v[144:147], a[96:111]\n"
    "s_waitcnt lgkmcnt(6)\n"
    "ds_read_b64_tr_b16 a[236:237], v5 offset:57344\n"
    "ds_read_b64_tr_b16 a[238:239], v5 offset:59392\n"
    "v_add_f32 v21, v21, v156\n"
    "v_add_f32 v22, v22, v157\n"
    "v_cvt_pk_bf16_f32 v154, v156, v157\n"
    "v_add_f32 v21, v21, v158\n"
    "v_add_f32 v22, v22, v159\n"
    "v_cvt_pk_bf16_f32 v155, v158, v159\n"
    "v_mfma_f32_32x32x16_bf16 a[112:127], a[252:255], v[144:147], a[112:127]\n"
    "s_waitcnt lgkmcnt(6)\n"
    "ds_read_b64_tr_b16 a[240:241], v2 offset:57600\n"
    "ds_read_b64_tr_b16 a[242:243], v2 offset:59648\n"
    "buffer_load_dwordx4 v16, s[16:19], 0 offen offset:3072 lds\n"
    "v_add_u32 v15, 0x4000, v15\n"
    "v_add_u32 v16, 0x4000, v16\n"
    "v_mfma_f32_32x32x16_bf16 a[0:15], a[224:227], v[152:155], a[0:15]\n"
    "s_waitcnt lgkmcnt(6)\n"
    "ds_read_b64_tr_b16 a[244:245], v3 offset:57600\n"
    "ds_read_b64_tr_b16 a[246:247], v3 offset:59648\n"
    "v_exp_f32 v47, v47\n"
    "v_add_f32 v21, v21, v44\n"
    "v_accvgpr_read_b32 v23, a168\n"
    "v_mfma_f32_32x32x16_bf16 a[16:31], a[228:231], v[152:155], a[16:31]\n"
    "s_waitcnt lgkmcnt(6)\n"
    "ds_read_b64_tr_b16 a[248:249], v4 offset:57600\n"
    "ds_read_b64_tr_b16 a[250:251], v4 offset:59648\n"
    "v_sub_f32 v23, v24, v23\n"
    "v_add_f32 v22, v22, v45\n"
    "v_exp_f32 v23, v23\n"
    "v_mfma_f32_32x32x16_bf16 a[32:47], a[232:235], v[152:155], a[32:47]\n"
    "s_waitcnt lgkmcnt(6)\n"
    "ds_read_b64_tr_b16 a[252:253], v5 offset:57600\n"
    "ds_read_b64_tr_b16 a[254:255], v5 offset:59648\n"
    "s_xor_b32 s59, s40, 2\n"
    "v_mul_f32 v160, v160, v23\n"
    "v_add_f32 v21, v21, v46\n"
    "v_mul_f32 v161, v161, v23\n"
    "v_mfma_f32_32x32x16_bf16 a[48:63], a[236:239], v[152:155], a[48:63]\n"
    "s_waitcnt vmcnt(12) lgkmcnt(6)\n"
    "s_barrier\n"
    "s_add_u32 m0, s45, 0x8000\n"
    "v_xor_b32 v2, 0x10000, v2\n"
    "ds_read_b64_tr_b16 a[224:225], v2\n"
    "ds_read_b64_tr_b16 a[226:227], v2 offset:2048\n"
    "buffer_load_dwordx4 v15, s[16:19], 0 offen lds\n"
    "v_mfma_f32_32x32x16_bf16 a[64:79], a[240:243], v[152:155], a[64:79]\n"
    "s_waitcnt lgkmcnt(6)\n"
    "v_xor_b32 v3, 0x10000, v3\n"
    "ds_read_b64_tr_b16 a[228:229], v3\n"
    "ds_read_b64_tr_b16 a[230:231], v3 offset:2048\n"
    "v_mul_f32 v162, v162, v23\n"
    "v_mul_f32 v163, v163, v23\n"
    "v_add_f32 v22, v22, v47\n"
    "v_mfma_f32_32x32x16_bf16 a[80:95], a[244:247], v[152:155], a[80:95]\n"
    "s_waitcnt lgkmcnt(6)\n"
    "v_xor_b32 v4, 0x10000, v4\n"
    "ds_read_b64_tr_b16 a[232:233], v4\n"
    "ds_read_b64_tr_b16 a[234:235], v4 offset:2048\n"
    "v_mul_f32 v164, v164, v23\n"
    "v_mul_f32 v165, v165, v23\n"
    "v_mul_f32 v166, v166, v23\n"
    "v_mfma_f32_32x32x16_bf16 a[96:111], a[248:251], v[152:155], a[96:111]\n"
    "s_waitcnt lgkmcnt(6)\n"
    "v_xor_b32 v5, 0x10000, v5\n"
    "ds_read_b64_tr_b16 a[236:237], v5\n"
    "ds_read_b64_tr_b16 a[238:239], v5 offset:2048\n"
    "v_mul_f32 v167, v167, v23\n"
    "v_add_f32 v21, v21, v160\n"
    "v_add_f32 v22, v22, v161\n"
    "v_cvt_pk_bf16_f32 v160, v160, v161\n"
    "v_add_f32 v21, v21, v162\n"
    "v_add_f32 v22, v22, v163\n"
    "v_cvt_pk_bf16_f32 v161, v162, v163\n"
    "v_add_f32 v21, v21, v164\n"
    "v_add_f32 v22, v22, v165\n"
    "v_cvt_pk_bf16_f32 v162, v164, v165\n"
    "v_add_f32 v21, v21, v166\n"
    "v_add_f32 v22, v22, v167\n"
    "v_cvt_pk_bf16_f32 v163, v166, v167\n"
    "v_mfma_f32_32x32x16_bf16 a[112:127], a[252:255], v[152:155], a[112:127]\n"
    "s_waitcnt lgkmcnt(6)\n"
    "ds_read_b64_tr_b16 a[240:241], v2 offset:256\n"
    "ds_read_b64_tr_b16 a[242:243], v2 offset:2304\n"
    "buffer_load_dwordx4 v16, s[16:19], 0 offen offset:1024 lds\n"
    "v_cvt_pk_bf16_f32 v32, v32, v33\n"
    "v_cvt_pk_bf16_f32 v33, v34, v35\n"
    "v_mfma_f32_32x32x16_bf16 a[0:15], a[224:227], v[160:163], a[0:15]\n"
    "s_waitcnt lgkmcnt(6)\n"
    "ds_read_b64_tr_b16 a[244:245], v3 offset:256\n"
    "ds_read_b64_tr_b16 a[246:247], v3 offset:2304\n"
    "s_lshl_b32 s59, s59, 14\n"
    "v_cvt_pk_bf16_f32 v34, v36, v37\n"
    "v_cvt_pk_bf16_f32 v35, v38, v39\n"
    "v_cvt_pk_bf16_f32 v40, v40, v41\n"
    "v_mfma_f32_32x32x16_bf16 a[16:31], a[228:231], v[160:163], a[16:31]\n"
    "s_waitcnt lgkmcnt(6)\n"
    "ds_read_b64_tr_b16 a[248:249], v4 offset:256\n"
    "ds_read_b64_tr_b16 a[250:251], v4 offset:2304\n"
    "v_cvt_pk_bf16_f32 v41, v42, v43\n"
    "v_mul_f32 v168, v168, v23\n"
    "v_mul_f32 v169, v169, v23\n"
    "v_cvt_pk_bf16_f32 v42, v44, v45\n"
    "v_mfma_f32_32x32x16_bf16 a[32:47], a[232:235], v[160:163], a[32:47]\n"
    "s_waitcnt lgkmcnt(6)\n"
    "ds_read_b64_tr_b16 a[252:253], v5 offset:256\n"
    "ds_read_b64_tr_b16 a[254:255], v5 offset:2304\n"
    "v_mul_f32 v170, v170, v23\n"
    "v_mul_f32 v171, v171, v23\n"
    "v_mul_f32 v172, v172, v23\n"
    "s_add_u32 s59, s59, 0x18000\n"
    "v_mfma_f32_32x32x16_bf16 a[48:63], a[236:239], v[160:163], a[48:63]\n"
    "s_waitcnt lgkmcnt(6)\n"
    "ds_read_b64_tr_b16 a[224:225], v2 offset:8192\n"
    "ds_read_b64_tr_b16 a[226:227], v2 offset:10240\n"
    "buffer_load_dwordx4 v15, s[16:19], 0 offen offset:2048 lds\n"
    "v_mul_f32 v173, v173, v23\n"
    "v_cvt_pk_bf16_f32 v43, v46, v47\n"
    "v_mfma_f32_32x32x16_bf16 a[64:79], a[240:243], v[160:163], a[64:79]\n"
    "s_waitcnt lgkmcnt(6)\n"
    "ds_read_b64_tr_b16 a[228:229], v3 offset:8192\n"
    "ds_read_b64_tr_b16 a[230:231], v3 offset:10240\n"
    "v_mul_f32 v174, v174, v23\n"
    "v_mul_f32 v175, v175, v23\n"
    "v_add_f32 v21, v21, v168\n"
    "v_accvgpr_read_b32 v48, a144\n"
    "v_mfma_f32_32x32x16_bf16 a[80:95], a[244:247], v[160:163], a[80:95]\n"
    "s_waitcnt lgkmcnt(6)\n"
    "ds_read_b64_tr_b16 a[232:233], v4 offset:8192\n"
    "ds_read_b64_tr_b16 a[234:235], v4 offset:10240\n"
    "v_add_f32 v22, v22, v169\n"
    "v_cvt_pk_bf16_f32 v168, v168, v169\n"
    "v_add_f32 v21, v21, v170\n"
    "v_accvgpr_read_b32 v49, a145\n"
    "v_mfma_f32_32x32x16_bf16 a[96:111], a[248:251], v[160:163], a[96:111]\n"
    "s_waitcnt lgkmcnt(6)\n"
    "ds_read_b64_tr_b16 a[236:237], v5 offset:8192\n"
    "ds_read_b64_tr_b16 a[238:239], v5 offset:10240\n"
    "v_add_f32 v22, v22, v171\n"
    "v_cvt_pk_bf16_f32 v169, v170, v171\n"
    "v_add_f32 v21, v21, v172\n"
    "v_add_f32 v22, v22, v173\n"
    "v_cvt_pk_bf16_f32 v170, v172, v173\n"
    "v_add_f32 v21, v21, v174\n"
    "v_add_f32 v22, v22, v175\n"
    "v_cvt_pk_bf16_f32 v171, v174, v175\n"
    "v_mfma_f32_32x32x16_bf16 a[112:127], a[252:255], v[160:163], a[112:127]\n"
    "s_waitcnt lgkmcnt(6)\n"
    "ds_read_b64_tr_b16 a[240:241], v2 offset:8448\n"
    "ds_read_b64_tr_b16 a[242:243], v2 offset:10496\n"
    "buffer_load_dwordx4 v16, s[16:19], 0 offen offset:3072 lds\n"
    "v_add_u32 v15, 0x4000, v15\n"
    "v_add_u32 v16, 0x4000, v16\n"
    "v_mfma_f32_32x32x16_bf16 a[0:15], a[224:227], v[168:171], a[0:15]\n"
    "s_waitcnt lgkmcnt(6)\n"
    "ds_read_b64_tr_b16 a[244:245], v3 offset:8448\n"
    "ds_read_b64_tr_b16 a[246:247], v3 offset:10496\n"
    "v_accvgpr_read_b32 v50, a146\n"
    "v_accvgpr_read_b32 v51, a147\n"
    "v_add_u32 v7, s59, v27\n"
    "v_accvgpr_read_b32 v23, a169\n"
    "v_mfma_f32_32x32x16_bf16 a[16:31], a[228:231], v[168:171], a[16:31]\n"
    "s_waitcnt lgkmcnt(6)\n"
    "ds_read_b64_tr_b16 a[248:249], v4 offset:8448\n"
    "ds_read_b64_tr_b16 a[250:251], v4 offset:10496\n"
    "v_fma_f32 v48, v48, s56, v24\n"
    "v_sub_f32 v23, v24, v23\n"
    "v_exp_f32 v23, v23\n"
    "v_mfma_f32_32x32x16_bf16 a[32:47], a[232:235], v[168:171], a[32:47]\n"
    "s_waitcnt lgkmcnt(6)\n"
    "ds_read_b64_tr_b16 a[252:253], v5 offset:8448\n"
    "ds_read_b64_tr_b16 a[254:255], v5 offset:10496\n"
    "v_mul_f32 v176, v176, v23\n"
    "v_fma_f32 v49, v49, s56, v24\n"
    "v_mul_f32 v177, v177, v23\n"
    "v_mul_f32 v178, v178, v23\n"
    "v_mfma_f32_32x32x16_bf16 a[48:63], a[236:239], v[168:171], a[48:63]\n"
    "s_waitcnt vmcnt(12) lgkmcnt(6)\n"
    "s_barrier\n"
    "s_add_u32 m0, s45, 0xc000\n"
    "ds_read_b64_tr_b16 a[224:225], v2 offset:16384\n"
    "ds_read_b64_tr_b16 a[226:227], v2 offset:18432\n"
    "buffer_load_dwordx4 v15, s[16:19], 0 offen lds\n"
    "v_mfma_f32_32x32x16_bf16 a[64:79], a[240:243], v[168:171], a[64:79]\n"
    "s_waitcnt lgkmcnt(6)\n"
    "ds_read_b64_tr_b16 a[228:229], v3 offset:16384\n"
    "ds_read_b64_tr_b16 a[230:231], v3 offset:18432\n"
    "v_mul_f32 v179, v179, v23\n"
    "v_fma_f32 v50, v50, s56, v24\n"
    "v_mul_f32 v180, v180, v23\n"
    "v_mul_f32 v181, v181, v23\n"
    "v_mfma_f32_32x32x16_bf16 a[80:95], a[244:247], v[168:171], a[80:95]\n"
    "s_waitcnt lgkmcnt(6)\n"
    "ds_read_b64_tr_b16 a[232:233], v4 offset:16384\n"
    "ds_read_b64_tr_b16 a[234:235], v4 offset:18432\n"
    "v_mul_f32 v182, v182, v23\n"
    "v_fma_f32 v51, v51, s56, v24\n"
    "v_mul_f32 v183, v183, v23\n"
    "v_add_f32 v21, v21, v176\n"
    "v_mfma_f32_32x32x16_bf16 a[96:111], a[248:251], v[168:171], a[96:111]\n"
    "s_waitcnt lgkmcnt(6)\n"
    "ds_read_b64_tr_b16 a[236:237], v5 offset:16384\n"
    "ds_read_b64_tr_b16 a[238:239], v5 offset:18432\n"
    "v_add_f32 v22, v22, v177\n"
    "v_cvt_pk_bf16_f32 v176, v176, v177\n"
    "v_add_f32 v21, v21, v178\n"
    "v_add_f32 v22, v22, v179\n"
    "v_cvt_pk_bf16_f32 v177, v178, v179\n"
    "v_add_f32 v21, v21, v180\n"
    "v_add_f32 v22, v22, v181\n"
    "v_cvt_pk_bf16_f32 v178, v180, v181\n"
    "v_add_f32 v21, v21, v182\n"
    "v_add_f32 v22, v22, v183\n"
    "v_cvt_pk_bf16_f32 v179, v182, v183\n"
    "v_mfma_f32_32x32x16_bf16 a[112:127], a[252:255], v[168:171], a[112:127]\n"
    "s_waitcnt lgkmcnt(6)\n"
    "ds_read_b64_tr_b16 a[240:241], v2 offset:16640\n"
    "ds_read_b64_tr_b16 a[242:243], v2 offset:18688\n"
    "buffer_load_dwordx4 v16, s[16:19], 0 offen offset:1024 lds\n"
    "v_exp_f32 v48, v48\n"
    "v_mfma_f32_32x32x16_bf16 a[0:15], a[224:227], v[176:179], a[0:15]\n"
    "s_waitcnt lgkmcnt(6)\n"
    "ds_read_b64_tr_b16 a[244:245], v3 offset:16640\n"
    "ds_read_b64_tr_b16 a[246:247], v3 offset:18688\n"
    "s_lshl_b32 s58, s41, 6\n"
    "v_exp_f32 v49, v49\n"
    "v_mul_f32 v184, v184, v23\n"
    "v_mfma_f32_32x32x16_bf16 a[16:31], a[228:231], v[176:179], a[16:31]\n"
    "s_waitcnt lgkmcnt(6)\n"
    "ds_read_b64_tr_b16 a[248:249], v4 offset:16640\n"
    "ds_read_b64_tr_b16 a[250:251], v4 offset:18688\n"
    "v_exp_f32 v50, v50\n"
    "v_mul_f32 v185, v185, v23\n"
    "s_lshl_b32 s59, s42, 1\n"
    "v_mfma_f32_32x32x16_bf16 a[32:47], a[232:235], v[176:179], a[32:47]\n"
    "s_waitcnt lgkmcnt(6)\n"
    "ds_read_b64_tr_b16 a[252:253], v5 offset:16640\n"
    "ds_read_b64_tr_b16 a[254:255], v5 offset:18688\n"
    "v_mul_f32 v186, v186, v23\n"
    "v_mul_f32 v187, v187, v23\n"
    "v_mul_f32 v188, v188, v23\n"
    "v_mul_f32 v189, v189, v23\n"
    "v_mfma_f32_32x32x16_bf16 a[48:63], a[236:239], v[176:179], a[48:63]\n"
    "s_waitcnt lgkmcnt(6)\n"
    "ds_read_b64_tr_b16 a[224:225], v2 offset:24576\n"
    "ds_read_b64_tr_b16 a[226:227], v2 offset:26624\n"
    "buffer_load_dwordx4 v15, s[16:19], 0 offen offset:2048 lds\n"
    "v_exp_f32 v51, v51\n"
    "v_mfma_f32_32x32x16_bf16 a[64:79], a[240:243], v[176:179], a[64:79]\n"
    "s_waitcnt lgkmcnt(6)\n"
    "ds_read_b64_tr_b16 a[228:229], v3 offset:24576\n"
    "ds_read_b64_tr_b16 a[230:231], v3 offset:26624\n"
    "v_mul_f32 v190, v190, v23\n"
    "v_mul_f32 v191, v191, v23\n"
    "v_add_f32 v21, v21, v184\n"
    "v_add_f32 v22, v22, v185\n"
    "v_mfma_f32_32x32x16_bf16 a[80:95], a[244:247], v[176:179], a[80:95]\n"
    "s_waitcnt lgkmcnt(6)\n"
    "ds_read_b64_tr_b16 a[232:233], v4 offset:24576\n"
    "ds_read_b64_tr_b16 a[234:235], v4 offset:26624\n"
    "v_cvt_pk_bf16_f32 v184, v184, v185\n"
    "v_add_f32 v21, v21, v48\n"
    "v_add_f32 v21, v21, v186\n"
    "v_add_f32 v22, v22, v187\n"
    "v_mfma_f32_32x32x16_bf16 a[96:111], a[248:251], v[176:179], a[96:111]\n"
    "s_waitcnt lgkmcnt(6)\n"
    "ds_read_b64_tr_b16 a[236:237], v5 offset:24576\n"
    "ds_read_b64_tr_b16 a[238:239], v5 offset:26624\n"
    "v_cvt_pk_bf16_f32 v185, v186, v187\n"
    "v_add_f32 v21, v21, v188\n"
    "v_add_f32 v22, v22, v189\n"
    "v_cvt_pk_bf16_f32 v186, v188, v189\n"
    "v_add_f32 v21, v21, v190\n"
    "v_add_f32 v22, v22, v191\n"
    "v_cvt_pk_bf16_f32 v187, v190, v191\n"
    "v_mfma_f32_32x32x16_bf16 a[112:127], a[252:255], v[176:179], a[112:127]\n"
    "s_waitcnt lgkmcnt(6)\n"
    "ds_read_b64_tr_b16 a[240:241], v2 offset:24832\n"
    "ds_read_b64_tr_b16 a[242:243], v2 offset:26880\n"
    "buffer_load_dwordx4 v16, s[16:19], 0 offen offset:3072 lds\n"
    "v_add_u32 v15, 0x4000, v15\n"
    "v_add_u32 v16, 0x4000, v16\n"
    "v_mfma_f32_32x32x16_bf16 a[0:15], a[224:227], v[184:187], a[0:15]\n"
    "s_waitcnt lgkmcnt(6)\n"
    "ds_read_b64_tr_b16 a[244:245], v3 offset:24832\n"
    "ds_read_b64_tr_b16 a[246:247], v3 offset:26880\n"
    "v_add_f32 v22, v22, v49\n"
    "s_add_u32 s58, s58, s59\n"
    "v_add_f32 v21, v21, v50\n"
    "v_accvgpr_read_b32 v23, a170\n"
    "v_mfma_f32_32x32x16_bf16 a[16:31], a[228:231], v[184:187], a[16:31]\n"
    "s_waitcnt lgkmcnt(6)\n"
    "ds_read_b64_tr_b16 a[248:249], v4 offset:24832\n"
    "ds_read_b64_tr_b16 a[250:251], v4 offset:26880\n"
    "v_add_f32 v22, v22, v51\n"
    "v_sub_f32 v23, v24, v23\n"
    "v_exp_f32 v23, v23\n"
    "v_mfma_f32_32x32x16_bf16 a[32:47], a[232:235], v[184:187], a[32:47]\n"
    "s_waitcnt lgkmcnt(6)\n"
    "ds_read_b64_tr_b16 a[252:253], v5 offset:24832\n"
    "ds_read_b64_tr_b16 a[254:255], v5 offset:26880\n"
    "v_accvgpr_read_b32 v52, a148\n"
    "v_mul_f32 v192, v192, v23\n"
    "v_mul_f32 v193, v193, v23\n"
    "v_mul_f32 v194, v194, v23\n"
    "v_mfma_f32_32x32x16_bf16 a[48:63], a[236:239], v[184:187], a[48:63]\n"
    "s_waitcnt vmcnt(12) lgkmcnt(6)\n"
    "s_barrier\n"
    "s_add_u32 m0, s45, 0x10000\n"
    "v_xor_b32 v2, 0x10000, v2\n"
    "ds_read_b64_tr_b16 a[224:225], v2\n"
    "ds_read_b64_tr_b16 a[226:227], v2 offset:2048\n"
    "buffer_load_dwordx4 v15, s[16:19], 0 offen lds\n"
    "v_mfma_f32_32x32x16_bf16 a[64:79], a[240:243], v[184:187], a[64:79]\n"
    "s_waitcnt lgkmcnt(6)\n"
    "v_xor_b32 v3, 0x10000, v3\n"
    "ds_read_b64_tr_b16 a[228:229], v3\n"
    "ds_read_b64_tr_b16 a[230:231], v3 offset:2048\n"
    "v_accvgpr_read_b32 v53, a149\n"
    "v_mul_f32 v195, v195, v23\n"
    "v_mul_f32 v196, v196, v23\n"
    "v_mfma_f32_32x32x16_bf16 a[80:95], a[244:247], v[184:187], a[80:95]\n"
    "s_waitcnt lgkmcnt(6)\n"
    "v_xor_b32 v4, 0x10000, v4\n"
    "ds_read_b64_tr_b16 a[232:233], v4\n"
    "ds_read_b64_tr_b16 a[234:235], v4 offset:2048\n"
    "v_mul_f32 v197, v197, v23\n"
    "v_accvgpr_read_b32 v54, a150\n"
    "v_mul_f32 v198, v198, v23\n"
    "v_mfma_f32_32x32x16_bf16 a[96:111], a[248:251], v[184:187], a[96:111]\n"
    "s_waitcnt lgkmcnt(6)\n"
    "v_xor_b32 v5, 0x10000, v5\n"
    "ds_read_b64_tr_b16 a[236:237], v5\n"
    "ds_read_b64_tr_b16 a[238:239], v5 offset:2048\n"
    "v_mul_f32 v199, v199, v23\n"
    "v_add_f32 v21, v21, v192\n"
    "v_add_f32 v22, v22, v193\n"
    "v_cvt_pk_bf16_f32 v192, v192, v193\n"
    "v_add_f32 v21, v21, v194\n"
    "v_add_f32 v22, v22, v195\n"
    "v_cvt_pk_bf16_f32 v193, v194, v195\n"
    "v_add_f32 v21, v21, v196\n"
    "v_add_f32 v22, v22, v197\n"
    "v_cvt_pk_bf16_f32 v194, v196, v197\n"
    "v_add_f32 v21, v21, v198\n"
    "v_add_f32 v22, v22, v199\n"
    "v_cvt_pk_bf16_f32 v195, v198, v199\n"
    "v_mfma_f32_32x32x16_bf16 a[112:127], a[252:255], v[184:187], a[112:127]\n"
    "s_waitcnt lgkmcnt(6)\n"
    "ds_read_b64_tr_b16 a[240:241], v2 offset:256\n"
    "ds_read_b64_tr_b16 a[242:243], v2 offset:2304\n"
    "buffer_load_dwordx4 v16, s[16:19], 0 offen offset:1024 lds\n"
    "s_add_u32 s58, s58, s44\n"
    "v_accvgpr_read_b32 v55, a151\n"
    "v_mfma_f32_32x32x16_bf16 a[0:15], a[224:227], v[192:195], a[0:15]\n"
    "s_waitcnt lgkmcnt(6)\n"
    "ds_read_b64_tr_b16 a[244:245], v3 offset:256\n"
    "ds_read_b64_tr_b16 a[246:247], v3 offset:2304\n"
    "v_fma_f32 v52, v52, s56, v24\n"
    "v_fma_f32 v53, v53, s56, v24\n"
    "v_fma_f32 v54, v54, s56, v24\n"
    "v_fma_f32 v55, v55, s56, v24\n"
    "v_mfma_f32_32x32x16_bf16 a[16:31], a[228:231], v[192:195], a[16:31]\n"
    "s_waitcnt lgkmcnt(6)\n"
    "ds_read_b64_tr_b16 a[248:249], v4 offset:256\n"
    "ds_read_b64_tr_b16 a[250:251], v4 offset:2304\n"
    "s_mul_i32 s58, s58, 0x7000\n"
    "v_mul_f32 v200, v200, v23\n"
    "v_exp_f32 v52, v52\n"
    "v_mfma_f32_32x32x16_bf16 a[32:47], a[232:235], v[192:195], a[32:47]\n"
    "s_waitcnt lgkmcnt(6)\n"
    "ds_read_b64_tr_b16 a[252:253], v5 offset:256\n"
    "ds_read_b64_tr_b16 a[254:255], v5 offset:2304\n"
    "v_mul_f32 v201, v201, v23\n"
    "v_mul_f32 v202, v202, v23\n"
    "v_mul_f32 v203, v203, v23\n"
    "v_mul_f32 v204, v204, v23\n"
    "v_mfma_f32_32x32x16_bf16 a[48:63], a[236:239], v[192:195], a[48:63]\n"
    "s_waitcnt lgkmcnt(6)\n"
    "ds_read_b64_tr_b16 a[224:225], v2 offset:8192\n"
    "ds_read_b64_tr_b16 a[226:227], v2 offset:10240\n"
    "buffer_load_dwordx4 v15, s[16:19], 0 offen offset:2048 lds\n"
    "v_mul_f32 v205, v205, v23\n"
    "v_mul_f32 v206, v206, v23\n"
    "v_mfma_f32_32x32x16_bf16 a[64:79], a[240:243], v[192:195], a[64:79]\n"
    "s_waitcnt lgkmcnt(6)\n"
    "ds_read_b64_tr_b16 a[228:229], v3 offset:8192\n"
    "ds_read_b64_tr_b16 a[230:231], v3 offset:10240\n"
    "v_mul_f32 v207, v207, v23\n"
    "v_exp_f32 v53, v53\n"
    "v_add_f32 v21, v21, v200\n"
    "v_mfma_f32_32x32x16_bf16 a[80:95], a[244:247], v[192:195], a[80:95]\n"
    "s_waitcnt lgkmcnt(6)\n"
    "ds_read_b64_tr_b16 a[232:233], v4 offset:8192\n"
    "ds_read_b64_tr_b16 a[234:235], v4 offset:10240\n"
    "v_add_f32 v22, v22, v201\n"
    "v_cvt_pk_bf16_f32 v200, v200, v201\n"
    "v_add_f32 v21, v21, v202\n"
    "v_add_f32 v22, v22, v203\n"
    "v_mfma_f32_32x32x16_bf16 a[96:111], a[248:251], v[192:195], a[96:111]\n"
    "s_waitcnt lgkmcnt(6)\n"
    "ds_read_b64_tr_b16 a[236:237], v5 offset:8192\n"
    "ds_read_b64_tr_b16 a[238:239], v5 offset:10240\n"
    "v_cvt_pk_bf16_f32 v201, v202, v203\n"
    "v_add_f32 v21, v21, v204\n"
    "v_add_f32 v22, v22, v205\n"
    "v_cvt_pk_bf16_f32 v202, v204, v205\n"
    "v_add_f32 v21, v21, v206\n"
    "v_add_f32 v22, v22, v207\n"
    "v_cvt_pk_bf16_f32 v203, v206, v207\n"
    "v_mfma_f32_32x32x16_bf16 a[112:127], a[252:255], v[192:195], a[112:127]\n"
    "s_waitcnt lgkmcnt(6)\n"
    "ds_read_b64_tr_b16 a[240:241], v2 offset:8448\n"
    "ds_read_b64_tr_b16 a[242:243], v2 offset:10496\n"
    "buffer_load_dwordx4 v16, s[16:19], 0 offen offset:3072 lds\n"
    "v_add_u32 v15, 0x4000, v15\n"
    "v_add_u32 v16, 0x4000, v16\n"
    "v_mfma_f32_32x32x16_bf16 a[0:15], a[224:227], v[200:203], a[0:15]\n"
    "s_waitcnt lgkmcnt(6)\n"
    "ds_read_b64_tr_b16 a[244:245], v3 offset:8448\n"
    "ds_read_b64_tr_b16 a[246:247], v3 offset:10496\n"
    "s_lshl_b32 s59, s43, 11\n"
    "v_exp_f32 v54, v54\n"
    "v_accvgpr_read_b32 v23, a171\n"
    "v_mfma_f32_32x32x16_bf16 a[16:31], a[228:231], v[200:203], a[16:31]\n"
    "s_waitcnt lgkmcnt(6)\n"
    "ds_read_b64_tr_b16 a[248:249], v4 offset:8448\n"
    "ds_read_b64_tr_b16 a[250:251], v4 offset:10496\n"
    "v_exp_f32 v55, v55\n"
    "v_sub_f32 v23, v24, v23\n"
    "v_add_f32 v21, v21, v52\n"
    "v_mfma_f32_32x32x16_bf16 a[32:47], a[232:235], v[200:203], a[32:47]\n"
    "s_waitcnt lgkmcnt(6)\n"
    "ds_read_b64_tr_b16 a[252:253], v5 offset:8448\n"
    "ds_read_b64_tr_b16 a[254:255], v5 offset:10496\n"
    "v_exp_f32 v23, v23\n"
    "s_nop 0\n"
    "v_mul_f32 v208, v208, v23\n"
    "v_mul_f32 v209, v209, v23\n"
    "v_mfma_f32_32x32x16_bf16 a[48:63], a[236:239], v[200:203], a[48:63]\n"
    "s_waitcnt vmcnt(12) lgkmcnt(6)\n"
    "s_barrier\n"
    "s_add_u32 m0, s45, 0x14000\n"
    "ds_read_b64_tr_b16 a[224:225], v2 offset:16384\n"
    "ds_read_b64_tr_b16 a[226:227], v2 offset:18432\n"
    "buffer_load_dwordx4 v15, s[16:19], 0 offen lds\n"
    "v_mfma_f32_32x32x16_bf16 a[64:79], a[240:243], v[200:203], a[64:79]\n"
    "s_waitcnt lgkmcnt(6)\n"
    "ds_read_b64_tr_b16 a[228:229], v3 offset:16384\n"
    "ds_read_b64_tr_b16 a[230:231], v3 offset:18432\n"
    "v_mul_f32 v210, v210, v23\n"
    "v_mul_f32 v211, v211, v23\n"
    "v_mul_f32 v212, v212, v23\n"
    "s_add_u32 s58, s58, s59\n"
    "v_mfma_f32_32x32x16_bf16 a[80:95], a[244:247], v[200:203], a[80:95]\n"
    "s_waitcnt lgkmcnt(6)\n"
    "ds_read_b64_tr_b16 a[232:233], v4 offset:16384\n"
    "ds_read_b64_tr_b16 a[234:235], v4 offset:18432\n"
    "v_mul_f32 v213, v213, v23\n"
    "v_add_f32 v22, v22, v53\n"
    "v_mul_f32 v214, v214, v23\n"
    "v_mul_f32 v215, v215, v23\n"
    "v_mfma_f32_32x32x16_bf16 a[96:111], a[248:251], v[200:203], a[96:111]\n"
    "s_waitcnt lgkmcnt(6)\n"
    "ds_read_b64_tr_b16 a[236:237], v5 offset:16384\n"
    "ds_read_b64_tr_b16 a[238:239], v5 offset:18432\n"
    "v_add_f32 v21, v21, v208\n"
    "v_add_f32 v22, v22, v209\n"
    "v_cvt_pk_bf16_f32 v208, v208, v209\n"
    "v_add_f32 v21, v21, v210\n"
    "v_add_f32 v22, v22, v211\n"
    "v_cvt_pk_bf16_f32 v209, v210, v211\n"
    "v_add_f32 v21, v21, v212\n"
    "v_add_f32 v22, v22, v213\n"
    "v_cvt_pk_bf16_f32 v210, v212, v213\n"
    "v_add_f32 v21, v21, v214\n"
    "v_add_f32 v22, v22, v215\n"
    "v_cvt_pk_bf16_f32 v211, v214, v215\n"
    "v_mfma_f32_32x32x16_bf16 a[112:127], a[252:255], v[200:203], a[112:127]\n"
    "s_waitcnt lgkmcnt(6)\n"
    "ds_read_b64_tr_b16 a[240:241], v2 offset:16640\n"
    "ds_read_b64_tr_b16 a[242:243], v2 offset:18688\n"
    "buffer_load_dwordx4 v16, s[16:19], 0 offen offset:1024 lds\n"
    "v_add_f32 v21, v21, v54\n"
    "v_add_f32 v22, v22, v55\n"
    "v_mfma_f32_32x32x16_bf16 a[0:15], a[224:227], v[208:211], a[0:15]\n"
    "s_waitcnt lgkmcnt(6)\n"
    "ds_read_b64_tr_b16 a[244:245], v3 offset:16640\n"
    "ds_read_b64_tr_b16 a[246:247], v3 offset:18688\n"
    "v_accvgpr_read_b32 v56, a152\n"
    "v_accvgpr_read_b32 v57, a153\n"
    "s_add_u32 s58, s58, 0x80000\n"
    "v_accvgpr_read_b32 v58, a154\n"
    "v_mfma_f32_32x32x16_bf16 a[16:31], a[228:231], v[208:211], a[16:31]\n"
    "s_waitcnt lgkmcnt(6)\n"
    "ds_read_b64_tr_b16 a[248:249], v4 offset:16640\n"
    "ds_read_b64_tr_b16 a[250:251], v4 offset:18688\n"
    "v_mul_f32 v216, v216, v23\n"
    "v_accvgpr_read_b32 v59, a155\n"
    "v_mul_f32 v217, v217, v23\n"
    "v_mul_f32 v218, v218, v23\n"
    "v_mfma_f32_32x32x16_bf16 a[32:47], a[232:235], v[208:211], a[32:47]\n"
    "s_waitcnt lgkmcnt(6)\n"
    "ds_read_b64_tr_b16 a[252:253], v5 offset:16640\n"
    "ds_read_b64_tr_b16 a[254:255], v5 offset:18688\n"
    "v_mul_f32 v219, v219, v23\n"
    "v_fma_f32 v56, v56, s56, v24\n"
    "v_mul_f32 v220, v220, v23\n"
    "v_mul_f32 v221, v221, v23\n"
    "v_mfma_f32_32x32x16_bf16 a[48:63], a[236:239], v[208:211], a[48:63]\n"
    "s_waitcnt lgkmcnt(6)\n"
    "ds_read_b64_tr_b16 a[224:225], v2 offset:24576\n"
    "ds_read_b64_tr_b16 a[226:227], v2 offset:26624\n"
    "buffer_load_dwordx4 v15, s[16:19], 0 offen offset:2048 lds\n"
    "v_mul_f32 v222, v222, v23\n"
    "v_fma_f32 v57, v57, s56, v24\n"
    "v_mfma_f32_32x32x16_bf16 a[64:79], a[240:243], v[208:211], a[64:79]\n"
    "s_waitcnt lgkmcnt(6)\n"
    "ds_read_b64_tr_b16 a[228:229], v3 offset:24576\n"
    "ds_read_b64_tr_b16 a[230:231], v3 offset:26624\n"
    "v_mul_f32 v223, v223, v23\n"
    "v_add_f32 v21, v21, v216\n"
    "v_add_f32 v22, v22, v217\n"
    "v_fma_f32 v58, v58, s56, v24\n"
    "v_mfma_f32_32x32x16_bf16 a[80:95], a[244:247], v[208:211], a[80:95]\n"
    "s_waitcnt lgkmcnt(6)\n"
    "ds_read_b64_tr_b16 a[232:233], v4 offset:24576\n"
    "ds_read_b64_tr_b16 a[234:235], v4 offset:26624\n"
    "v_cvt_pk_bf16_f32 v216, v216, v217\n"
    "v_lshlrev_b32 v27, 4, v1\n"
    "v_add_f32 v21, v21, v218\n"
    "v_add_f32 v22, v22, v219\n"
    "v_mfma_f32_32x32x16_bf16 a[96:111], a[248:251], v[208:211], a[96:111]\n"
    "s_waitcnt lgkmcnt(6)\n"
    "ds_read_b64_tr_b16 a[236:237], v5 offset:24576\n"
    "ds_read_b64_tr_b16 a[238:239], v5 offset:26624\n"
    "v_cvt_pk_bf16_f32 v217, v218, v219\n"
    "v_add_f32 v21, v21, v220\n"
    "v_add_f32 v22, v22, v221\n"
    "v_cvt_pk_bf16_f32 v218, v220, v221\n"
    "v_add_f32 v21, v21, v222\n"
    "v_add_f32 v22, v22, v223\n"
    "v_cvt_pk_bf16_f32 v219, v222, v223\n"
    "v_mfma_f32_32x32x16_bf16 a[112:127], a[252:255], v[208:211], a[112:127]\n"
    "s_waitcnt lgkmcnt(6)\n"
    "ds_read_b64_tr_b16 a[240:241], v2 offset:24832\n"
    "ds_read_b64_tr_b16 a[242:243], v2 offset:26880\n"
    "buffer_load_dwordx4 v16, s[16:19], 0 offen offset:3072 lds\n"
    "v_add_u32 v15, 0x4000, v15\n"
    "v_add_u32 v16, 0x4000, v16\n"
    "v_mfma_f32_32x32x16_bf16 a[0:15], a[224:227], v[216:219], a[0:15]\n"
    "s_waitcnt lgkmcnt(6)\n"
    "ds_read_b64_tr_b16 a[244:245], v3 offset:24832\n"
    "ds_read_b64_tr_b16 a[246:247], v3 offset:26880\n"
    "v_fma_f32 v59, v59, s56, v24\n"
    "v_exp_f32 v56, v56\n"
    "v_accvgpr_read_b32 v23, a172\n"
    "v_mfma_f32_32x32x16_bf16 a[16:31], a[228:231], v[216:219], a[16:31]\n"
    "s_waitcnt lgkmcnt(6)\n"
    "ds_read_b64_tr_b16 a[248:249], v4 offset:24832\n"
    "ds_read_b64_tr_b16 a[250:251], v4 offset:26880\n"
    "v_sub_f32 v23, v24, v23\n"
    "v_exp_f32 v23, v23\n"
    "s_nop 0\n"
    "v_mul_f32 v224, v224, v23\n"
    "v_mfma_f32_32x32x16_bf16 a[32:47], a[232:235], v[216:219], a[32:47]\n"
    "s_waitcnt lgkmcnt(6)\n"
    "ds_read_b64_tr_b16 a[252:253], v5 offset:24832\n"
    "ds_read_b64_tr_b16 a[254:255], v5 offset:26880\n"
    "v_exp_f32 v57, v57\n"
    "v_mul_f32 v225, v225, v23\n"
    "v_add_u32 v13, s58, v27\n"
    "v_mfma_f32_32x32x16_bf16 a[48:63], a[236:239], v[216:219], a[48:63]\n"
    "s_waitcnt vmcnt(12) lgkmcnt(6)\n"
    "s_barrier\n"
    "s_add_u32 m0, s45, 0\n"
    "ds_read_b64_tr_b16 a[224:225], v2 offset:32768\n"
    "ds_read_b64_tr_b16 a[226:227], v2 offset:34816\n"
    "buffer_load_dwordx4 v17, s[20:23], 0 offen lds\n"
    "v_mfma_f32_32x32x16_bf16 a[64:79], a[240:243], v[216:219], a[64:79]\n"
    "s_waitcnt lgkmcnt(6)\n"
    "ds_read_b64_tr_b16 a[228:229], v3 offset:32768\n"
    "ds_read_b64_tr_b16 a[230:231], v3 offset:34816\n"
    "v_mul_f32 v226, v226, v23\n"
    "v_mul_f32 v227, v227, v23\n"
    "v_mul_f32 v228, v228, v23\n"
    "v_mul_f32 v229, v229, v23\n"
    "v_mfma_f32_32x32x16_bf16 a[80:95], a[244:247], v[216:219], a[80:95]\n"
    "s_waitcnt lgkmcnt(6)\n"
    "ds_read_b64_tr_b16 a[232:233], v4 offset:32768\n"
    "ds_read_b64_tr_b16 a[234:235], v4 offset:34816\n"
    "v_exp_f32 v58, v58\n"
    "v_mul_f32 v230, v230, v23\n"
    "v_mul_f32 v231, v231, v23\n"
    "v_mfma_f32_32x32x16_bf16 a[96:111], a[248:251], v[216:219], a[96:111]\n"
    "s_waitcnt lgkmcnt(6)\n"
    "ds_read_b64_tr_b16 a[236:237], v5 offset:32768\n"
    "ds_read_b64_tr_b16 a[238:239], v5 offset:34816\n"
    "v_add_f32 v21, v21, v224\n"
    "v_add_f32 v22, v22, v225\n"
    "v_cvt_pk_bf16_f32 v224, v224, v225\n"
    "v_add_f32 v21, v21, v226\n"
    "v_add_f32 v22, v22, v227\n"
    "v_cvt_pk_bf16_f32 v225, v226, v227\n"
    "v_add_f32 v21, v21, v228\n"
    "v_add_f32 v22, v22, v229\n"
    "v_cvt_pk_bf16_f32 v226, v228, v229\n"
    "v_add_f32 v21, v21, v230\n"
    "v_add_f32 v22, v22, v231\n"
    "v_cvt_pk_bf16_f32 v227, v230, v231\n"
    "v_mfma_f32_32x32x16_bf16 a[112:127], a[252:255], v[216:219], a[112:127]\n"
    "s_waitcnt lgkmcnt(6)\n"
    "ds_read_b64_tr_b16 a[240:241], v2 offset:33024\n"
    "ds_read_b64_tr_b16 a[242:243], v2 offset:35072\n"
    "buffer_load_dwordx4 v17, s[20:23], s53 offen offset:1024 lds\n"
    "v_exp_f32 v59, v59\n"
    "v_mfma_f32_32x32x16_bf16 a[0:15], a[224:227], v[224:227], a[0:15]\n"
    "s_waitcnt lgkmcnt(6)\n"
    "ds_read_b64_tr_b16 a[244:245], v3 offset:33024\n"
    "ds_read_b64_tr_b16 a[246:247], v3 offset:35072\n"
    "s_lshl_b32 s58, s40, 8\n"
    "v_add_f32 v21, v21, v56\n"
    "v_add_f32 v22, v22, v57\n"
    "v_add_f32 v21, v21, v58\n"
    "v_mfma_f32_32x32x16_bf16 a[16:31], a[228:231], v[224:227], a[16:31]\n"
    "s_waitcnt lgkmcnt(6)\n"
    "ds_read_b64_tr_b16 a[248:249], v4 offset:33024\n"
    "ds_read_b64_tr_b16 a[250:251], v4 offset:35072\n"
    "v_mul_f32 v232, v232, v23\n"
    "v_mul_f32 v233, v233, v23\n"
    "v_mul_f32 v234, v234, v23\n"
    "v_add_f32 v22, v22, v59\n"
    "v_mfma_f32_32x32x16_bf16 a[32:47], a[232:235], v[224:227], a[32:47]\n"
    "s_waitcnt lgkmcnt(6)\n"
    "ds_read_b64_tr_b16 a[252:253], v5 offset:33024\n"
    "ds_read_b64_tr_b16 a[254:255], v5 offset:35072\n"
    "v_mul_f32 v235, v235, v23\n"
    "v_mul_f32 v236, v236, v23\n"
    "v_mul_f32 v237, v237, v23\n"
    "v_accvgpr_read_b32 v60, a156\n"
    "v_mfma_f32_32x32x16_bf16 a[48:63], a[236:239], v[224:227], a[48:63]\n"
    "s_waitcnt lgkmcnt(6)\n"
    "ds_read_b64_tr_b16 a[224:225], v2 offset:40960\n"
    "ds_read_b64_tr_b16 a[226:227], v2 offset:43008\n"
    "buffer_load_dwordx4 v17, s[20:23], s54 offen offset:2048 lds\n"
    "v_mul_f32 v238, v238, v23\n"
    "v_mul_f32 v239, v239, v23\n"
    "v_mfma_f32_32x32x16_bf16 a[64:79], a[240:243], v[224:227], a[64:79]\n"
    "s_waitcnt lgkmcnt(6)\n"
    "ds_read_b64_tr_b16 a[228:229], v3 offset:40960\n"
    "ds_read_b64_tr_b16 a[230:231], v3 offset:43008\n"
    "s_add_u32 s58, s58, 0x20000\n"
    "v_add_f32 v21, v21, v232\n"
    "v_accvgpr_read_b32 v61, a157\n"
    "v_add_f32 v22, v22, v233\n"
    "v_mfma_f32_32x32x16_bf16 a[80:95], a[244:247], v[224:227], a[80:95]\n"
    "s_waitcnt lgkmcnt(6)\n"
    "ds_read_b64_tr_b16 a[232:233], v4 offset:40960\n"
    "ds_read_b64_tr_b16 a[234:235], v4 offset:43008\n"
    "v_cvt_pk_bf16_f32 v232, v232, v233\n"
    "v_add_f32 v21, v21, v234\n"
    "v_add_f32 v22, v22, v235\n"
    "v_accvgpr_read_b32 v62, a158\n"
    "v_mfma_f32_32x32x16_bf16 a[96:111], a[248:251], v[224:227], a[96:111]\n"
    "s_waitcnt lgkmcnt(6)\n"
    "ds_read_b64_tr_b16 a[236:237], v5 offset:40960\n"
    "ds_read_b64_tr_b16 a[238:239], v5 offset:43008\n"
    "v_cvt_pk_bf16_f32 v233, v234, v235\n"
    "v_add_f32 v21, v21, v236\n"
    "v_add_f32 v22, v22, v237\n"
    "v_cvt_pk_bf16_f32 v234, v236, v237\n"
    "v_add_f32 v21, v21, v238\n"
    "v_add_f32 v22, v22, v239\n"
    "v_cvt_pk_bf16_f32 v235, v238, v239\n"
    "v_mfma_f32_32x32x16_bf16 a[112:127], a[252:255], v[224:227], a[112:127]\n"
    "s_waitcnt lgkmcnt(6)\n"
    "ds_read_b64_tr_b16 a[240:241], v2 offset:41216\n"
    "ds_read_b64_tr_b16 a[242:243], v2 offset:43264\n"
    "buffer_load_dwordx4 v17, s[20:23], s55 offen offset:3072 lds\n"
    "v_add_u32 v17, 0xe000, v17\n"
    "v_accvgpr_read_b32 v63, a159\n"
    "v_mfma_f32_32x32x16_bf16 a[0:15], a[224:227], v[232:235], a[0:15]\n"
    "s_waitcnt lgkmcnt(6)\n"
    "ds_read_b64_tr_b16 a[244:245], v3 offset:41216\n"
    "ds_read_b64_tr_b16 a[246:247], v3 offset:43264\n"
    "v_fma_f32 v60, v60, s56, v24\n"
    "v_accvgpr_read_b32 v23, a173\n"
    "v_sub_f32 v23, v24, v23\n"
    "v_fma_f32 v61, v61, s56, v24\n"
    "v_mfma_f32_32x32x16_bf16 a[16:31], a[228:231], v[232:235], a[16:31]\n"
    "s_waitcnt lgkmcnt(6)\n"
    "ds_read_b64_tr_b16 a[248:249], v4 offset:41216\n"
    "ds_read_b64_tr_b16 a[250:251], v4 offset:43264\n"
    "v_exp_f32 v23, v23\n"
    "v_lshlrev_b32 v27, 2, v1\n"
    "v_mul_f32 v240, v240, v23\n"
    "v_mfma_f32_32x32x16_bf16 a[32:47], a[232:235], v[232:235], a[32:47]\n"
    "s_waitcnt lgkmcnt(6)\n"
    "ds_read_b64_tr_b16 a[252:253], v5 offset:41216\n"
    "ds_read_b64_tr_b16 a[254:255], v5 offset:43264\n"
    "v_fma_f32 v62, v62, s56, v24\n"
    "v_mul_f32 v241, v241, v23\n"
    "v_mul_f32 v242, v242, v23\n"
    "v_mul_f32 v243, v243, v23\n"
    "v_mfma_f32_32x32x16_bf16 a[48:63], a[236:239], v[232:235], a[48:63]\n"
    "s_waitcnt vmcnt(12) lgkmcnt(6)\n"
    "s_barrier\n"
    "s_add_u32 m0, s45, 0x4000\n"
    "ds_read_b64_tr_b16 a[224:225], v2 offset:49152\n"
    "ds_read_b64_tr_b16 a[226:227], v2 offset:51200\n"
    "buffer_load_dwordx4 v17, s[20:23], 0 offen lds\n"
    "v_mfma_f32_32x32x16_bf16 a[64:79], a[240:243], v[232:235], a[64:79]\n"
    "s_waitcnt lgkmcnt(6)\n"
    "ds_read_b64_tr_b16 a[228:229], v3 offset:49152\n"
    "ds_read_b64_tr_b16 a[230:231], v3 offset:51200\n"
    "v_fma_f32 v63, v63, s56, v24\n"
    "v_mul_f32 v244, v244, v23\n"
    "v_mul_f32 v245, v245, v23\n"
    "v_mul_f32 v246, v246, v23\n"
    "v_mfma_f32_32x32x16_bf16 a[80:95], a[244:247], v[232:235], a[80:95]\n"
    "s_waitcnt lgkmcnt(6)\n"
    "ds_read_b64_tr_b16 a[232:233], v4 offset:49152\n"
    "ds_read_b64_tr_b16 a[234:235], v4 offset:51200\n"
    "v_mul_f32 v247, v247, v23\n"
    "v_exp_f32 v60, v60\n"
    "v_add_f32 v21, v21, v240\n"
    "v_mfma_f32_32x32x16_bf16 a[96:111], a[248:251], v[232:235], a[96:111]\n"
    "s_waitcnt lgkmcnt(6)\n"
    "ds_read_b64_tr_b16 a[236:237], v5 offset:49152\n"
    "ds_read_b64_tr_b16 a[238:239], v5 offset:51200\n"
    "v_add_f32 v22, v22, v241\n"
    "v_cvt_pk_bf16_f32 v240, v240, v241\n"
    "v_add_f32 v21, v21, v242\n"
    "v_add_f32 v22, v22, v243\n"
    "v_cvt_pk_bf16_f32 v241, v242, v243\n"
    "v_add_f32 v21, v21, v244\n"
    "v_add_f32 v22, v22, v245\n"
    "v_cvt_pk_bf16_f32 v242, v244, v245\n"
    "v_add_f32 v21, v21, v246\n"
    "v_add_f32 v22, v22, v247\n"
    "v_cvt_pk_bf16_f32 v243, v246, v247\n"
    "v_mfma_f32_32x32x16_bf16 a[112:127], a[252:255], v[232:235], a[112:127]\n"
    "s_waitcnt lgkmcnt(6)\n"
    "ds_read_b64_tr_b16 a[240:241], v2 offset:49408\n"
    "ds_read_b64_tr_b16 a[242:243], v2 offset:51456\n"
    "buffer_load_dwordx4 v17, s[20:23], s53 offen offset:1024 lds\n"
    "v_add_u32 v19, s58, v27\n"
    "v_mul_f32 v248, v248, v23\n"
    "v_mfma_f32_32x32x16_bf16 a[0:15], a[224:227], v[240:243], a[0:15]\n"
    "s_waitcnt lgkmcnt(6)\n"
    "ds_read_b64_tr_b16 a[244:245], v3 offset:49408\n"
    "ds_read_b64_tr_b16 a[246:247], v3 offset:51456\n"
    "v_exp_f32 v61, v61\n"
    "v_exp_f32 v62, v62\n"
    "v_mfma_f32_32x32x16_bf16 a[16:31], a[228:231], v[240:243], a[16:31]\n"
    "s_waitcnt lgkmcnt(6)\n"
    "ds_read_b64_tr_b16 a[248:249], v4 offset:49408\n"
    "ds_read_b64_tr_b16 a[250:251], v4 offset:51456\n"
    "v_mul_f32 v249, v249, v23\n"
    "v_exp_f32 v63, v63\n"
    "v_mul_f32 v250, v250, v23\n"
    "v_mfma_f32_32x32x16_bf16 a[32:47], a[232:235], v[240:243], a[32:47]\n"
    "s_waitcnt lgkmcnt(6)\n"
    "ds_read_b64_tr_b16 a[252:253], v5 offset:49408\n"
    "ds_read_b64_tr_b16 a[254:255], v5 offset:51456\n"
    "v_mul_f32 v251, v251, v23\n"
    "v_mul_f32 v252, v252, v23\n"
    "s_xor_b32 s58, s58, 0x200\n"
    "v_mul_f32 v253, v253, v23\n"
    "v_mfma_f32_32x32x16_bf16 a[48:63], a[236:239], v[240:243], a[48:63]\n"
    "s_waitcnt lgkmcnt(6)\n"
    "ds_read_b64_tr_b16 a[224:225], v2 offset:57344\n"
    "ds_read_b64_tr_b16 a[226:227], v2 offset:59392\n"
    "buffer_load_dwordx4 v17, s[20:23], s54 offen offset:2048 lds\n"
    "v_mul_f32 v254, v254, v23\n"
    "v_mul_f32 v255, v255, v23\n"
    "v_mfma_f32_32x32x16_bf16 a[64:79], a[240:243], v[240:243], a[64:79]\n"
    "s_waitcnt lgkmcnt(6)\n"
    "ds_read_b64_tr_b16 a[228:229], v3 offset:57344\n"
    "ds_read_b64_tr_b16 a[230:231], v3 offset:59392\n"
    "v_add_f32 v21, v21, v248\n"
    "v_add_f32 v21, v21, v60\n"
    "v_add_f32 v22, v22, v249\n"
    "v_cvt_pk_bf16_f32 v248, v248, v249\n"
    "v_mfma_f32_32x32x16_bf16 a[80:95], a[244:247], v[240:243], a[80:95]\n"
    "s_waitcnt lgkmcnt(6)\n"
    "ds_read_b64_tr_b16 a[232:233], v4 offset:57344\n"
    "ds_read_b64_tr_b16 a[234:235], v4 offset:59392\n"
    "v_add_f32 v21, v21, v250\n"
    "v_add_f32 v22, v22, v61\n"
    "v_add_f32 v22, v22, v251\n"
    "v_cvt_pk_bf16_f32 v249, v250, v251\n"
    "v_mfma_f32_32x32x16_bf16 a[96:111], a[248:251], v[240:243], a[96:111]\n"
    "s_waitcnt lgkmcnt(6)\n"
    "ds_read_b64_tr_b16 a[236:237], v5 offset:57344\n"
    "ds_read_b64_tr_b16 a[238:239], v5 offset:59392\n"
    "v_add_f32 v21, v21, v252\n"
    "v_add_f32 v22, v22, v253\n"
    "v_cvt_pk_bf16_f32 v250, v252, v253\n"
    "v_add_f32 v21, v21, v254\n"
    "v_add_f32 v22, v22, v255\n"
    "v_cvt_pk_bf16_f32 v251, v254, v255\n"
    "v_add_u32 v20, s58, v27\n"
    "v_mfma_f32_32x32x16_bf16 a[112:127], a[252:255], v[240:243], a[112:127]\n"
    "s_waitcnt lgkmcnt(6)\n"
    "ds_read_b64_tr_b16 a[240:241], v2 offset:57600\n"
    "ds_read_b64_tr_b16 a[242:243], v2 offset:59648\n"
    "buffer_load_dwordx4 v17, s[20:23], s55 offen offset:3072 lds\n"
    "v_add_u32 v17, 0xe000, v17\n"
    "v_add_f32 v21, v21, v62\n"
    "v_mfma_f32_32x32x16_bf16 a[0:15], a[224:227], v[248:251], a[0:15]\n"
    "s_waitcnt lgkmcnt(6)\n"
    "ds_read_b64_tr_b16 a[244:245], v3 offset:57600\n"
    "ds_read_b64_tr_b16 a[246:247], v3 offset:59648\n"
    "v_add_f32 v22, v22, v63\n"
    "v_cvt_pk_bf16_f32 v48, v48, v49\n"
    "v_cvt_pk_bf16_f32 v49, v50, v51\n"
    "v_cvt_pk_bf16_f32 v50, v52, v53\n"
    "v_mfma_f32_32x32x16_bf16 a[16:31], a[228:231], v[248:251], a[16:31]\n"
    "s_waitcnt lgkmcnt(6)\n"
    "ds_read_b64_tr_b16 a[248:249], v4 offset:57600\n"
    "ds_read_b64_tr_b16 a[250:251], v4 offset:59648\n"
    "v_cvt_pk_bf16_f32 v51, v54, v55\n"
    "v_cvt_pk_bf16_f32 v56, v56, v57\n"
    "v_cvt_pk_bf16_f32 v57, v58, v59\n"
    "v_cvt_pk_bf16_f32 v58, v60, v61\n"
    "v_mfma_f32_32x32x16_bf16 a[32:47], a[232:235], v[248:251], a[32:47]\n"
    "s_waitcnt lgkmcnt(6)\n"
    "ds_read_b64_tr_b16 a[252:253], v5 offset:57600\n"
    "ds_read_b64_tr_b16 a[254:255], v5 offset:59648\n"
    "v_cvt_pk_bf16_f32 v59, v62, v63\n"
    "v_mfma_f32_32x32x16_bf16 a[48:63], a[236:239], v[248:251], a[48:63]\n"
    "s_waitcnt vmcnt(12) lgkmcnt(6)\n"
    "s_barrier\n"
    "s_add_u32 m0, s45, 0x8000\n"
    "v_xor_b32 v2, 0x10000, v2\n"
    "ds_read_b64_tr_b16 a[224:225], v2\n"
    "ds_read_b64_tr_b16 a[226:227], v2 offset:2048\n"
    "buffer_load_dwordx4 v17, s[20:23], 0 offen lds\n"
    "v_mfma_f32_32x32x16_bf16 a[64:79], a[240:243], v[248:251], a[64:79]\n"
    "s_waitcnt lgkmcnt(6)\n"
    "ds_read_b64_tr_b16 a[228:229], v2 offset:8192\n"
    "ds_read_b64_tr_b16 a[230:231], v2 offset:10240\n"
    "v_mfma_f32_32x32x16_bf16 a[80:95], a[244:247], v[248:251], a[80:95]\n"
    "s_waitcnt vmcnt(9) lgkmcnt(6)\n"
    "s_barrier\n"
    "ds_read_b64_tr_b16 a[232:233], v2 offset:16384\n"
    "ds_read_b64_tr_b16 a[234:235], v2 offset:18432\n"
    "v_mfma_f32_32x32x16_bf16 a[96:111], a[248:251], v[248:251], a[96:111]\n"
    "s_waitcnt lgkmcnt(6)\n"
    "ds_read_b64_tr_b16 a[236:237], v2 offset:24576\n"
    "ds_read_b64_tr_b16 a[238:239], v2 offset:26624\n"
    "v_mfma_f32_32x32x16_bf16 a[112:127], a[252:255], v[248:251], a[112:127]\n"
    "s_waitcnt lgkmcnt(6)\n"
    "v_xor_b32 v3, 0x10000, v3\n"
    "ds_read_b64_tr_b16 a[240:241], v3\n"
    "ds_read_b64_tr_b16 a[242:243], v3 offset:2048\n"
    "buffer_load_dwordx4 v17, s[20:23], s53 offen offset:1024 lds\n"
    "v_mfma_f32_32x32x16_bf16 a[0:15], a[224:227], v[32:35], a[0:15]\n"
    "s_waitcnt lgkmcnt(6)\n"
    "ds_read_b64_tr_b16 a[244:245], v3 offset:8192\n"
    "ds_read_b64_tr_b16 a[246:247], v3 offset:10240\n"
    "v_add_f32 v21, v21, v22\n"
    "v_mov_b32 v22, v21\n"
    "v_mfma_f32_32x32x16_bf16 a[0:15], a[228:231], v[40:43], a[0:15]\n"
    "s_waitcnt lgkmcnt(6)\n"
    "ds_read_b64_tr_b16 a[248:249], v3 offset:16384\n"
    "ds_read_b64_tr_b16 a[250:251], v3 offset:18432\n"
    "v_mfma_f32_32x32x16_bf16 a[0:15], a[232:235], v[48:51], a[0:15]\n"
    "s_waitcnt lgkmcnt(6)\n"
    "ds_read_b64_tr_b16 a[252:253], v3 offset:24576\n"
    "ds_read_b64_tr_b16 a[254:255], v3 offset:26624\n"
    "v_permlane32_swap_b32 v22, v21\n"
    "v_add_f32 v21, v21, v22\n"
    "v_mfma_f32_32x32x16_bf16 a[0:15], a[236:239], v[56:59], a[0:15]\n"
    "s_waitcnt lgkmcnt(6)\n"
    "v_xor_b32 v4, 0x10000, v4\n"
    "ds_read_b64_tr_b16 a[224:225], v4\n"
    "ds_read_b64_tr_b16 a[226:227], v4 offset:2048\n"
    "buffer_load_dwordx4 v17, s[20:23], s54 offen offset:2048 lds\n"
    "v_rcp_f32 v14, v21\n"
    "v_mfma_f32_32x32x16_bf16 a[16:31], a[240:243], v[32:35], a[16:31]\n"
    "s_waitcnt lgkmcnt(6)\n"
    "ds_read_b64_tr_b16 a[228:229], v4 offset:8192\n"
    "ds_read_b64_tr_b16 a[230:231], v4 offset:10240\n"
    "v_mfma_f32_32x32x16_bf16 a[16:31], a[244:247], v[40:43], a[16:31]\n"
    "s_waitcnt lgkmcnt(6)\n"
    "ds_read_b64_tr_b16 a[232:233], v4 offset:16384\n"
    "ds_read_b64_tr_b16 a[234:235], v4 offset:18432\n"
    "v_accvgpr_read_b32 v27, a0\n"
    "v_accvgpr_read_b32 v28, a1\n"
    "v_cvt_pk_bf16_f32 v64, v27, v28\n"
    "v_accvgpr_read_b32 v29, a2\n"
    "v_mfma_f32_32x32x16_bf16 a[16:31], a[248:251], v[48:51], a[16:31]\n"
    "s_waitcnt lgkmcnt(6)\n"
    "ds_read_b64_tr_b16 a[236:237], v4 offset:24576\n"
    "ds_read_b64_tr_b16 a[238:239], v4 offset:26624\n"
    "v_accvgpr_read_b32 v30, a3\n"
    "v_cvt_pk_bf16_f32 v65, v29, v30\n"
    "v_accvgpr_read_b32 v27, a4\n"
    "v_accvgpr_read_b32 v28, a5\n"
    "v_mfma_f32_32x32x16_bf16 a[16:31], a[252:255], v[56:59], a[16:31]\n"
    "s_waitcnt lgkmcnt(6)\n"
    "v_xor_b32 v5, 0x10000, v5\n"
    "ds_read_b64_tr_b16 a[240:241], v5\n"
    "ds_read_b64_tr_b16 a[242:243], v5 offset:2048\n"
    "buffer_load_dwordx4 v17, s[20:23], s55 offen offset:3072 lds\n"
    "v_add_u32 v17, 0xe000, v17\n"
    "v_mfma_f32_32x32x16_bf16 a[32:47], a[224:227], v[32:35], a[32:47]\n"
    "s_waitcnt lgkmcnt(6)\n"
    "ds_read_b64_tr_b16 a[244:245], v5 offset:8192\n"
    "ds_read_b64_tr_b16 a[246:247], v5 offset:10240\n"
    "v_cvt_pk_bf16_f32 v66, v27, v28\n"
    "v_accvgpr_read_b32 v29, a6\n"
    "v_accvgpr_read_b32 v30, a7\n"
    "v_cvt_pk_bf16_f32 v67, v29, v30\n"
    "v_mfma_f32_32x32x16_bf16 a[32:47], a[228:231], v[40:43], a[32:47]\n"
    "s_waitcnt lgkmcnt(6)\n"
    "ds_read_b64_tr_b16 a[248:249], v5 offset:16384\n"
    "ds_read_b64_tr_b16 a[250:251], v5 offset:18432\n"
    "ds_write_b128 v6, v[64:67]\n"
    "v_accvgpr_read_b32 v27, a8\n"
    "v_accvgpr_read_b32 v28, a9\n"
    "v_cvt_pk_bf16_f32 v68, v27, v28\n"
    "v_mfma_f32_32x32x16_bf16 a[32:47], a[232:235], v[48:51], a[32:47]\n"
    "s_waitcnt lgkmcnt(7)\n"
    "ds_read_b64_tr_b16 a[252:253], v5 offset:24576\n"
    "ds_read_b64_tr_b16 a[254:255], v5 offset:26624\n"
    "v_accvgpr_read_b32 v29, a10\n"
    "v_accvgpr_read_b32 v30, a11\n"
    "v_cvt_pk_bf16_f32 v69, v29, v30\n"
    "v_accvgpr_read_b32 v27, a12\n"
    "v_mfma_f32_32x32x16_bf16 a[32:47], a[236:239], v[56:59], a[32:47]\n"
    "s_waitcnt lgkmcnt(7)\n"
    "ds_read_b64_tr_b16 a[224:225], v2 offset:256\n"
    "ds_read_b64_tr_b16 a[226:227], v2 offset:2304\n"
    "v_accvgpr_read_b32 v28, a13\n"
    "v_cvt_pk_bf16_f32 v70, v27, v28\n"
    "v_accvgpr_read_b32 v29, a14\n"
    "v_accvgpr_read_b32 v30, a15\n"
    "v_mfma_f32_32x32x16_bf16 a[48:63], a[240:243], v[32:35], a[48:63]\n"
    "s_waitcnt lgkmcnt(7)\n"
    "ds_read_b64_tr_b16 a[228:229], v2 offset:8448\n"
    "ds_read_b64_tr_b16 a[230:231], v2 offset:10496\n"
    "v_cvt_pk_bf16_f32 v71, v29, v30\n"
    "ds_write_b128 v6, v[68:71] offset:1024\n"
    "v_accvgpr_read_b32 v27, a16\n"
    "v_accvgpr_read_b32 v28, a17\n"
    "v_mfma_f32_32x32x16_bf16 a[48:63], a[244:247], v[40:43], a[48:63]\n"
    "s_waitcnt lgkmcnt(8)\n"
    "ds_read_b64_tr_b16 a[232:233], v2 offset:16640\n"
    "ds_read_b64_tr_b16 a[234:235], v2 offset:18688\n"
    "v_cvt_pk_bf16_f32 v72, v27, v28\n"
    "v_accvgpr_read_b32 v29, a18\n"
    "v_accvgpr_read_b32 v30, a19\n"
    "v_cvt_pk_bf16_f32 v73, v29, v30\n"
    "v_mfma_f32_32x32x16_bf16 a[48:63], a[248:251], v[48:51], a[48:63]\n"
    "s_waitcnt lgkmcnt(7)\n"
    "ds_read_b64_tr_b16 a[236:237], v2 offset:24832\n"
    "ds_read_b64_tr_b16 a[238:239], v2 offset:26880\n"
    "v_accvgpr_read_b32 v27, a20\n"
    "v_accvgpr_read_b32 v28, a21\n"
    "v_cvt_pk_bf16_f32 v74, v27, v28\n"
    "v_accvgpr_read_b32 v29, a22\n"
    "v_mfma_f32_32x32x16_bf16 a[48:63], a[252:255], v[56:59], a[48:63]\n"
    "s_waitcnt lgkmcnt(7)\n"
    "ds_read_b64_tr_b16 a[240:241], v3 offset:256\n"
    "ds_read_b64_tr_b16 a[242:243], v3 offset:2304\n"
    "v_accvgpr_read_b32 v30, a23\n"
    "v_cvt_pk_bf16_f32 v75, v29, v30\n"
    "ds_write_b128 v6, v[72:75] offset:2048\n"
    "v_accvgpr_read_b32 v27, a24\n"
    "v_mfma_f32_32x32x16_bf16 a[64:79], a[224:227], v[32:35], a[64:79]\n"
    "s_waitcnt lgkmcnt(8)\n"
    "ds_read_b64_tr_b16 a[244:245], v3 offset:8448\n"
    "ds_read_b64_tr_b16 a[246:247], v3 offset:10496\n"
    "v_accvgpr_read_b32 v28, a25\n"
    "v_cvt_pk_bf16_f32 v76, v27, v28\n"
    "v_accvgpr_read_b32 v29, a26\n"
    "v_accvgpr_read_b32 v30, a27\n"
    "v_mfma_f32_32x32x16_bf16 a[64:79], a[228:231], v[40:43], a[64:79]\n"
    "s_waitcnt lgkmcnt(7)\n"
    "ds_read_b64_tr_b16 a[248:249], v3 offset:16640\n"
    "ds_read_b64_tr_b16 a[250:251], v3 offset:18688\n"
    "v_cvt_pk_bf16_f32 v77, v29, v30\n"
    "v_accvgpr_read_b32 v27, a28\n"
    "v_accvgpr_read_b32 v28, a29\n"
    "v_cvt_pk_bf16_f32 v78, v27, v28\n"
    "v_mfma_f32_32x32x16_bf16 a[64:79], a[232:235], v[48:51], a[64:79]\n"
    "s_waitcnt lgkmcnt(7)\n"
    "ds_read_b64_tr_b16 a[252:253], v3 offset:24832\n"
    "ds_read_b64_tr_b16 a[254:255], v3 offset:26880\n"
    "v_accvgpr_read_b32 v29, a30\n"
    "v_accvgpr_read_b32 v30, a31\n"
    "v_cvt_pk_bf16_f32 v79, v29, v30\n"
    "ds_write_b128 v6, v[76:79] offset:3072\n"
    "v_mfma_f32_32x32x16_bf16 a[64:79], a[236:239], v[56:59], a[64:79]\n"
    "s_waitcnt lgkmcnt(8)\n"
    "ds_read_b64_tr_b16 a[224:225], v4 offset:256\n"
    "ds_read_b64_tr_b16 a[226:227], v4 offset:2304\n"
    "v_accvgpr_read_b32 v27, a32\n"
    "v_accvgpr_read_b32 v28, a33\n"
    "v_cvt_pk_bf16_f32 v80, v27, v28\n"
    "v_accvgpr_read_b32 v29, a34\n"
    "v_mfma_f32_32x32x16_bf16 a[80:95], a[240:243], v[32:35], a[80:95]\n"
    "s_waitcnt lgkmcnt(7)\n"
    "ds_read_b64_tr_b16 a[228:229], v4 offset:8448\n"
    "ds_read_b64_tr_b16 a[230:231], v4 offset:10496\n"
    "v_accvgpr_read_b32 v30, a35\n"
    "v_cvt_pk_bf16_f32 v81, v29, v30\n"
    "v_accvgpr_read_b32 v27, a36\n"
    "v_accvgpr_read_b32 v28, a37\n"
    "v_mfma_f32_32x32x16_bf16 a[80:95], a[244:247], v[40:43], a[80:95]\n"
    "s_waitcnt lgkmcnt(7)\n"
    "ds_read_b64_tr_b16 a[232:233], v4 offset:16640\n"
    "ds_read_b64_tr_b16 a[234:235], v4 offset:18688\n"
    "v_cvt_pk_bf16_f32 v82, v27, v28\n"
    "v_accvgpr_read_b32 v29, a38\n"
    "v_accvgpr_read_b32 v30, a39\n"
    "v_cvt_pk_bf16_f32 v83, v29, v30\n"
    "v_mfma_f32_32x32x16_bf16 a[80:95], a[248:251], v[48:51], a[80:95]\n"
    "s_waitcnt lgkmcnt(7)\n"
    "ds_read_b64_tr_b16 a[236:237], v4 offset:24832\n"
    "ds_read_b64_tr_b16 a[238:239], v4 offset:26880\n"
    "ds_write_b128 v6, v[80:83] offset:4096\n"
    "v_accvgpr_read_b32 v27, a40\n"
    "v_accvgpr_read_b32 v28, a41\n"
    "v_cvt_pk_bf16_f32 v84, v27, v28\n"
    "v_mfma_f32_32x32x16_bf16 a[80:95], a[252:255], v[56:59], a[80:95]\n"
    "s_waitcnt lgkmcnt(7)\n"
    "ds_read_b64_tr_b16 a[240:241], v5 offset:256\n"
    "ds_read_b64_tr_b16 a[242:243], v5 offset:2304\n"
    "v_accvgpr_read_b32 v29, a42\n"
    "v_accvgpr_read_b32 v30, a43\n"
    "v_cvt_pk_bf16_f32 v85, v29, v30\n"
    "v_accvgpr_read_b32 v27, a44\n"
    "v_mfma_f32_32x32x16_bf16 a[96:111], a[224:227], v[32:35], a[96:111]\n"
    "s_waitcnt lgkmcnt(7)\n"
    "ds_read_b64_tr_b16 a[244:245], v5 offset:8448\n"
    "ds_read_b64_tr_b16 a[246:247], v5 offset:10496\n"
    "v_accvgpr_read_b32 v28, a45\n"
    "v_cvt_pk_bf16_f32 v86, v27, v28\n"
    "v_accvgpr_read_b32 v29, a46\n"
    "v_accvgpr_read_b32 v30, a47\n"
    "v_mfma_f32_32x32x16_bf16 a[96:111], a[228:231], v[40:43], a[96:111]\n"
    "s_waitcnt lgkmcnt(7)\n"
    "ds_read_b64_tr_b16 a[248:249], v5 offset:16640\n"
    "ds_read_b64_tr_b16 a[250:251], v5 offset:18688\n"
    "v_cvt_pk_bf16_f32 v87, v29, v30\n"
    "ds_write_b128 v6, v[84:87] offset:5120\n"
    "v_accvgpr_read_b32 v27, a48\n"
    "v_accvgpr_read_b32 v28, a49\n"
    "v_mfma_f32_32x32x16_bf16 a[96:111], a[232:235], v[48:51], a[96:111]\n"
    "s_waitcnt lgkmcnt(8)\n"
    "ds_read_b64_tr_b16 a[252:253], v5 offset:24832\n"
    "ds_read_b64_tr_b16 a[254:255], v5 offset:26880\n"
    "v_cvt_pk_bf16_f32 v88, v27, v28\n"
    "v_accvgpr_read_b32 v29, a50\n"
    "v_accvgpr_read_b32 v30, a51\n"
    "v_cvt_pk_bf16_f32 v89, v29, v30\n"
    "v_mfma_f32_32x32x16_bf16 a[96:111], a[236:239], v[56:59], a[96:111]\n"
    "s_waitcnt vmcnt(8) lgkmcnt(7)\n"
    "s_barrier\n"
    "s_add_u32 m0, s45, 0xc000\n"
    "ds_read_b64_tr_b16 a[224:225], v10\n"
    "ds_read_b64_tr_b16 a[226:227], v10 offset:512\n"
    "buffer_load_dwordx4 v17, s[20:23], 0 offen lds\n"
    "v_mfma_f32_32x32x16_bf16 a[112:127], a[240:243], v[32:35], a[112:127]\n"
    "s_waitcnt lgkmcnt(7)\n"
    "ds_read_b64_tr_b16 a[228:229], v10 offset:2048\n"
    "ds_read_b64_tr_b16 a[230:231], v10 offset:2560\n"
    "buffer_load_dwordx4 v17, s[20:23], s53 offen offset:1024 lds\n"
    "v_accvgpr_read_b32 v27, a52\n"
    "v_accvgpr_read_b32 v28, a53\n"
    "v_mfma_f32_32x32x16_bf16 a[112:127], a[244:247], v[40:43], a[112:127]\n"
    "s_waitcnt lgkmcnt(7)\n"
    "ds_read_b64_tr_b16 a[232:233], v10 offset:4096\n"
    "ds_read_b64_tr_b16 a[234:235], v10 offset:4608\n"
    "buffer_load_dwordx4 v17, s[20:23], s54 offen offset:2048 lds\n"
    "v_cvt_pk_bf16_f32 v90, v27, v28\n"
    "v_accvgpr_read_b32 v29, a54\n"
    "v_mfma_f32_32x32x16_bf16 a[112:127], a[248:251], v[48:51], a[112:127]\n"
    "s_waitcnt lgkmcnt(6)\n"
    "ds_read_b64_tr_b16 a[236:237], v10 offset:6144\n"
    "ds_read_b64_tr_b16 a[238:239], v10 offset:6656\n"
    "buffer_load_dwordx4 v17, s[20:23], s55 offen offset:3072 lds\n"
    "v_add_u32 v17, 0xfffd6080, v17\n"
    "v_accvgpr_read_b32 v30, a55\n"
    "v_mfma_f32_32x32x16_bf16 a[112:127], a[252:255], v[56:59], a[112:127]\n"
    "s_waitcnt vmcnt(8) lgkmcnt(6)\n"
    "s_barrier\n"
    "s_add_u32 m0, s45, 0x10000\n"
    "ds_read_b64_tr_b16 a[240:241], v10 offset:16384\n"
    "ds_read_b64_tr_b16 a[242:243], v10 offset:16896\n"
    "buffer_load_dwordx4 v17, s[20:23], 0 offen lds\n"
    "v_mfma_f32_32x32x16_bf16 v[128:143], a[224:227], v[64:67], 0\n"
    "s_waitcnt lgkmcnt(6)\n"
    "ds_read_b64_tr_b16 a[244:245], v10 offset:18432\n"
    "ds_read_b64_tr_b16 a[246:247], v10 offset:18944\n"
    "buffer_load_dwordx4 v17, s[20:23], s53 offen offset:1024 lds\n"
    "v_cvt_pk_bf16_f32 v91, v29, v30\n"
    "ds_write_b128 v6, v[88:91] offset:6144\n"
    "v_mfma_f32_32x32x16_bf16 v[128:143], a[228:231], v[68:71], v[128:143]\n"
    "s_waitcnt lgkmcnt(7)\n"
    "ds_read_b64_tr_b16 a[248:249], v10 offset:20480\n"
    "ds_read_b64_tr_b16 a[250:251], v10 offset:20992\n"
    "buffer_load_dwordx4 v17, s[20:23], s54 offen offset:2048 lds\n"
    "v_accvgpr_read_b32 v27, a56\n"
    "v_accvgpr_read_b32 v28, a57\n"
    "v_mfma_f32_32x32x16_bf16 v[128:143], a[232:235], v[72:75], v[128:143]\n"
    "s_waitcnt lgkmcnt(7)\n"
    "ds_read_b64_tr_b16 a[252:253], v10 offset:22528\n"
    "ds_read_b64_tr_b16 a[254:255], v10 offset:23040\n"
    "buffer_load_dwordx4 v17, s[20:23], s55 offen offset:3072 lds\n"
    "v_add_u32 v17, 0xe000, v17\n"
    "v_cvt_pk_bf16_f32 v92, v27, v28\n"
    "v_mfma_f32_32x32x16_bf16 v[128:143], a[236:239], v[76:79], v[128:143]\n"
    "s_waitcnt vmcnt(8) lgkmcnt(7)\n"
    "s_barrier\n"
    "s_add_u32 m0, s45, 0x14000\n"
    "ds_read_b64_tr_b16 a[224:225], v10 offset:32768\n"
    "ds_read_b64_tr_b16 a[226:227], v10 offset:33280\n"
    "buffer_load_dwordx4 v17, s[20:23], 0 offen lds\n"
    "v_mfma_f32_32x32x16_bf16 v[128:143], a[240:243], v[80:83], v[128:143]\n"
    "s_waitcnt lgkmcnt(7)\n"
    "ds_read_b64_tr_b16 a[228:229], v10 offset:34816\n"
    "ds_read_b64_tr_b16 a[230:231], v10 offset:35328\n"
    "v_accvgpr_read_b32 v29, a58\n"
    "v_accvgpr_read_b32 v30, a59\n"
    "v_cvt_pk_bf16_f32 v93, v29, v30\n"
    "v_accvgpr_read_b32 v27, a60\n"
    "v_accvgpr_read_b32 v28, a61\n"
    "v_cvt_pk_bf16_f32 v94, v27, v28\n"
    "v_accvgpr_read_b32 v29, a62\n"
    "v_accvgpr_read_b32 v30, a63\n"
    "v_cvt_pk_bf16_f32 v95, v29, v30\n"
    "ds_write_b128 v6, v[92:95] offset:7168\n"
    "v_mfma_f32_32x32x16_bf16 v[128:143], a[244:247], v[84:87], v[128:143]\n"
    "s_waitcnt lgkmcnt(7)\n"
    "ds_read_b64_tr_b16 a[232:233], v10 offset:36864\n"
    "ds_read_b64_tr_b16 a[234:235], v10 offset:37376\n"
    "v_accvgpr_read_b32 v27, a64\n"
    "v_accvgpr_read_b32 v28, a65\n"
    "v_cvt_pk_bf16_f32 v96, v27, v28\n"
    "v_accvgpr_read_b32 v29, a66\n"
    "v_accvgpr_read_b32 v30, a67\n"
    "v_cvt_pk_bf16_f32 v97, v29, v30\n"
    "v_accvgpr_read_b32 v27, a68\n"
    "v_accvgpr_read_b32 v28, a69\n"
    "v_cvt_pk_bf16_f32 v98, v27, v28\n"
    "v_accvgpr_read_b32 v29, a70\n"
    "v_accvgpr_read_b32 v30, a71\n"
    "v_cvt_pk_bf16_f32 v99, v29, v30\n"
    "ds_write_b128 v6, v[96:99] offset:8192\n"
    "v_accvgpr_read_b32 v27, a72\n"
    "v_accvgpr_read_b32 v28, a73\n"
    "v_cvt_pk_bf16_f32 v100, v27, v28\n"
    "v_accvgpr_read_b32 v29, a74\n"
    "v_accvgpr_read_b32 v30, a75\n"
    "v_cvt_pk_bf16_f32 v101, v29, v30\n"
    "v_accvgpr_read_b32 v27, a76\n"
    "v_accvgpr_read_b32 v28, a77\n"
    "v_cvt_pk_bf16_f32 v102, v27, v28\n"
    "v_accvgpr_read_b32 v29, a78\n"
    "v_accvgpr_read_b32 v30, a79\n"
    "v_cvt_pk_bf16_f32 v103, v29, v30\n"
    "ds_write_b128 v6, v[100:103] offset:9216\n"
    "v_accvgpr_read_b32 v27, a80\n"
    "v_accvgpr_read_b32 v28, a81\n"
    "v_cvt_pk_bf16_f32 v104, v27, v28\n"
    "v_accvgpr_read_b32 v29, a82\n"
    "v_accvgpr_read_b32 v30, a83\n"
    "v_cvt_pk_bf16_f32 v105, v29, v30\n"
    "v_accvgpr_read_b32 v27, a84\n"
    "v_accvgpr_read_b32 v28, a85\n"
    "v_cvt_pk_bf16_f32 v106, v27, v28\n"
    "v_accvgpr_read_b32 v29, a86\n"
    "v_accvgpr_read_b32 v30, a87\n"
    "v_cvt_pk_bf16_f32 v107, v29, v30\n"
    "ds_write_b128 v6, v[104:107] offset:10240\n"
    "v_accvgpr_read_b32 v27, a88\n"
    "v_accvgpr_read_b32 v28, a89\n"
    "v_cvt_pk_bf16_f32 v108, v27, v28\n"
    "v_accvgpr_read_b32 v29, a90\n"
    "v_accvgpr_read_b32 v30, a91\n"
    "v_cvt_pk_bf16_f32 v109, v29, v30\n"
    "v_accvgpr_read_b32 v27, a92\n"
    "v_accvgpr_read_b32 v28, a93\n"
    "v_cvt_pk_bf16_f32 v110, v27, v28\n"
    "v_accvgpr_read_b32 v29, a94\n"
    "v_accvgpr_read_b32 v30, a95\n"
    "v_cvt_pk_bf16_f32 v111, v29, v30\n"
    "ds_write_b128 v6, v[108:111] offset:11264\n"
    "v_accvgpr_read_b32 v27, a96\n"
    "v_accvgpr_read_b32 v28, a97\n"
    "v_cvt_pk_bf16_f32 v112, v27, v28\n"
    "v_accvgpr_read_b32 v29, a98\n"
    "v_accvgpr_read_b32 v30, a99\n"
    "v_cvt_pk_bf16_f32 v113, v29, v30\n"
    "v_accvgpr_read_b32 v27, a100\n"
    "v_accvgpr_read_b32 v28, a101\n"
    "v_cvt_pk_bf16_f32 v114, v27, v28\n"
    "v_accvgpr_read_b32 v29, a102\n"
    "v_accvgpr_read_b32 v30, a103\n"
    "v_cvt_pk_bf16_f32 v115, v29, v30\n"
    "ds_write_b128 v6, v[112:115] offset:12288\n"
    "v_accvgpr_read_b32 v27, a104\n"
    "v_accvgpr_read_b32 v28, a105\n"
    "v_cvt_pk_bf16_f32 v116, v27, v28\n"
    "v_accvgpr_read_b32 v29, a106\n"
    "v_accvgpr_read_b32 v30, a107\n"
    "v_cvt_pk_bf16_f32 v117, v29, v30\n"
    "v_accvgpr_read_b32 v27, a108\n"
    "v_accvgpr_read_b32 v28, a109\n"
    "v_cvt_pk_bf16_f32 v118, v27, v28\n"
    "v_accvgpr_read_b32 v29, a110\n"
    "v_accvgpr_read_b32 v30, a111\n"
    "v_cvt_pk_bf16_f32 v119, v29, v30\n"
    "ds_write_b128 v6, v[116:119] offset:13312\n"
    "v_accvgpr_read_b32 v27, a112\n"
    "v_accvgpr_read_b32 v28, a113\n"
    "v_cvt_pk_bf16_f32 v120, v27, v28\n"
    "v_accvgpr_read_b32 v29, a114\n"
    "v_accvgpr_read_b32 v30, a115\n"
    "v_cvt_pk_bf16_f32 v121, v29, v30\n"
    "v_accvgpr_read_b32 v27, a116\n"
    "v_accvgpr_read_b32 v28, a117\n"
    "v_cvt_pk_bf16_f32 v122, v27, v28\n"
    "v_accvgpr_read_b32 v29, a118\n"
    "v_accvgpr_read_b32 v30, a119\n"
    "v_cvt_pk_bf16_f32 v123, v29, v30\n"
    "ds_write_b128 v6, v[120:123] offset:14336\n"
    "v_accvgpr_read_b32 v27, a120\n"
    "v_accvgpr_read_b32 v28, a121\n"
    "v_cvt_pk_bf16_f32 v124, v27, v28\n"
    "v_accvgpr_read_b32 v29, a122\n"
    "v_accvgpr_read_b32 v30, a123\n"
    "v_cvt_pk_bf16_f32 v125, v29, v30\n"
    "v_accvgpr_read_b32 v27, a124\n"
    "v_accvgpr_read_b32 v28, a125\n"
    "v_cvt_pk_bf16_f32 v126, v27, v28\n"
    "v_accvgpr_read_b32 v29, a126\n"
    "v_accvgpr_read_b32 v30, a127\n"
    "v_cvt_pk_bf16_f32 v127, v29, v30\n"
    "ds_write_b128 v6, v[124:127] offset:15360\n"
    "v_mfma_f32_32x32x16_bf16 v[128:143], a[248:251], v[88:91], v[128:143]\n"
    "s_waitcnt lgkmcnt(0)\n"
    "s_barrier\n"
    "ds_read_b64_tr_b16 a[236:237], v10 offset:38912\n"
    "ds_read_b64_tr_b16 a[238:239], v10 offset:39424\n"
    "ds_read_b128 a[128:131], v7\n"
    "ds_read_b128 a[132:135], v7 offset:1024\n"
    "v_mfma_f32_32x32x16_bf16 v[128:143], a[252:255], v[92:95], v[128:143]\n"
    "s_waitcnt vmcnt(5)\n"
    "s_barrier\n"
    "ds_read_b64_tr_b16 a[240:241], v10 offset:49152\n"
    "ds_read_b64_tr_b16 a[242:243], v10 offset:49664\n"
    "buffer_load_dwordx4 v17, s[20:23], s53 offen offset:1024 lds\n"
    "ds_read_b128 a[136:139], v7 offset:2048\n"
    "ds_read_b128 a[140:143], v7 offset:3072\n"
    "v_mfma_f32_32x32x16_bf16 v[128:143], a[224:227], v[96:99], v[128:143]\n"
    "ds_read_b64_tr_b16 a[244:245], v10 offset:51200\n"
    "ds_read_b64_tr_b16 a[246:247], v10 offset:51712\n"
    "ds_read_b128 a[144:147], v7 offset:4096\n"
    "ds_read_b128 a[148:151], v7 offset:5120\n"
    "v_mfma_f32_32x32x16_bf16 v[128:143], a[228:231], v[100:103], v[128:143]\n"
    "ds_read_b64_tr_b16 a[248:249], v10 offset:53248\n"
    "ds_read_b64_tr_b16 a[250:251], v10 offset:53760\n"
    "ds_read_b128 a[152:155], v7 offset:6144\n"
    "ds_read_b128 a[156:159], v7 offset:7168\n"
    "v_mfma_f32_32x32x16_bf16 v[128:143], a[232:235], v[104:107], v[128:143]\n"
    "s_waitcnt lgkmcnt(14)\n"
    "ds_read_b64_tr_b16 a[252:253], v10 offset:55296\n"
    "ds_read_b64_tr_b16 a[254:255], v10 offset:55808\n"
    "ds_read_b128 a[160:163], v7 offset:8192\n"
    "ds_read_b128 a[164:167], v7 offset:9216\n"
    "v_mfma_f32_32x32x16_bf16 v[128:143], a[236:239], v[108:111], v[128:143]\n"
    "s_waitcnt lgkmcnt(14)\n"
    "ds_read_b64_tr_b16 a[224:225], v11\n"
    "ds_read_b64_tr_b16 a[226:227], v11 offset:512\n"
    "buffer_load_dwordx4 v17, s[20:23], s54 offen offset:2048 lds\n"
    "ds_read_b128 a[168:171], v7 offset:10240\n"
    "ds_read_b128 a[172:175], v7 offset:11264\n"
    "v_mfma_f32_32x32x16_bf16 v[128:143], a[240:243], v[112:115], v[128:143]\n"
    "s_waitcnt lgkmcnt(14)\n"
    "ds_read_b64_tr_b16 a[228:229], v11 offset:2048\n"
    "ds_read_b64_tr_b16 a[230:231], v11 offset:2560\n"
    "ds_read_b128 a[176:179], v7 offset:12288\n"
    "ds_read_b128 a[180:183], v7 offset:13312\n"
    "v_mfma_f32_32x32x16_bf16 v[128:143], a[244:247], v[116:119], v[128:143]\n"
    "s_waitcnt lgkmcnt(14)\n"
    "ds_read_b64_tr_b16 a[232:233], v11 offset:4096\n"
    "ds_read_b64_tr_b16 a[234:235], v11 offset:4608\n"
    "ds_read_b128 a[184:187], v7 offset:14336\n"
    "ds_read_b128 a[188:191], v7 offset:15360\n"
    "v_mfma_f32_32x32x16_bf16 v[128:143], a[248:251], v[120:123], v[128:143]\n"
    "s_waitcnt lgkmcnt(14)\n"
    "ds_read_b64_tr_b16 a[236:237], v11 offset:6144\n"
    "ds_read_b64_tr_b16 a[238:239], v11 offset:6656\n"
    "v_mfma_f32_32x32x16_bf16 v[128:143], a[252:255], v[124:127], v[128:143]\n"
    "s_waitcnt lgkmcnt(12)\n"
    "ds_read_b64_tr_b16 a[240:241], v11 offset:16384\n"
    "ds_read_b64_tr_b16 a[242:243], v11 offset:16896\n"
    "buffer_load_dwordx4 v17, s[20:23], s55 offen offset:3072 lds\n"
    "v_add_u32 v17, 0xe000, v17\n"
    "v_mfma_f32_32x32x16_bf16 v[144:159], a[224:227], a[128:131], 0\n"
    "s_waitcnt lgkmcnt(10)\n"
    "ds_read_b64_tr_b16 a[244:245], v11 offset:18432\n"
    "ds_read_b64_tr_b16 a[246:247], v11 offset:18944\n"
    "v_mfma_f32_32x32x16_bf16 v[144:159], a[228:231], a[132:135], v[144:159]\n"
    "s_waitcnt lgkmcnt(8)\n"
    "ds_read_b64_tr_b16 a[248:249], v11 offset:20480\n"
    "ds_read_b64_tr_b16 a[250:251], v11 offset:20992\n"
    "v_mfma_f32_32x32x16_bf16 v[144:159], a[232:235], a[136:139], v[144:159]\n"
    "s_waitcnt lgkmcnt(6)\n"
    "ds_read_b64_tr_b16 a[252:253], v11 offset:22528\n"
    "ds_read_b64_tr_b16 a[254:255], v11 offset:23040\n"
    "v_mfma_f32_32x32x16_bf16 v[144:159], a[236:239], a[140:143], v[144:159]\n"
    "s_waitcnt lgkmcnt(6)\n"
    "ds_read_b64_tr_b16 a[224:225], v11 offset:32768\n"
    "ds_read_b64_tr_b16 a[226:227], v11 offset:33280\n"
    "v_mfma_f32_32x32x16_bf16 v[144:159], a[240:243], a[144:147], v[144:159]\n"
    "s_waitcnt lgkmcnt(6)\n"
    "ds_read_b64_tr_b16 a[228:229], v11 offset:34816\n"
    "ds_read_b64_tr_b16 a[230:231], v11 offset:35328\n"
    "v_mfma_f32_32x32x16_bf16 v[144:159], a[244:247], a[148:151], v[144:159]\n"
    "s_waitcnt lgkmcnt(6)\n"
    "ds_read_b64_tr_b16 a[232:233], v11 offset:36864\n"
    "ds_read_b64_tr_b16 a[234:235], v11 offset:37376\n"
    "v_mfma_f32_32x32x16_bf16 v[144:159], a[248:251], a[152:155], v[144:159]\n"
    "s_waitcnt lgkmcnt(6)\n"
    "ds_read_b64_tr_b16 a[236:237], v11 offset:38912\n"
    "ds_read_b64_tr_b16 a[238:239], v11 offset:39424\n"
    "v_mfma_f32_32x32x16_bf16 v[144:159], a[252:255], a[156:159], v[144:159]\n"
    "s_waitcnt lgkmcnt(6)\n"
    "ds_read_b64_tr_b16 a[240:241], v11 offset:49152\n"
    "ds_read_b64_tr_b16 a[242:243], v11 offset:49664\n"
    "v_mfma_f32_32x32x16_bf16 v[144:159], a[224:227], a[160:163], v[144:159]\n"
    "s_waitcnt lgkmcnt(6)\n"
    "ds_read_b64_tr_b16 a[244:245], v11 offset:51200\n"
    "ds_read_b64_tr_b16 a[246:247], v11 offset:51712\n"
    "v_mfma_f32_32x32x16_bf16 v[144:159], a[228:231], a[164:167], v[144:159]\n"
    "s_waitcnt lgkmcnt(6)\n"
    "ds_read_b64_tr_b16 a[248:249], v11 offset:53248\n"
    "ds_read_b64_tr_b16 a[250:251], v11 offset:53760\n"
    "v_mfma_f32_32x32x16_bf16 v[144:159], a[232:235], a[168:171], v[144:159]\n"
    "s_waitcnt lgkmcnt(6)\n"
    "ds_read_b64_tr_b16 a[252:253], v11 offset:55296\n"
    "ds_read_b64_tr_b16 a[254:255], v11 offset:55808\n"
    "v_mfma_f32_32x32x16_bf16 v[144:159], a[236:239], a[172:175], v[144:159]\n"
    "s_waitcnt vmcnt(4) lgkmcnt(6)\n"
    "s_barrier\n"
    "s_add_u32 m0, s45, 0\n"
    "v_xor_b32 v10, 0x10000, v10\n"
    "ds_read_b64_tr_b16 a[224:225], v10\n"
    "ds_read_b64_tr_b16 a[226:227], v10 offset:512\n"
    "buffer_load_dwordx4 v17, s[20:23], 0 offen lds\n"
    "v_mfma_f32_32x32x16_bf16 v[144:159], a[240:243], a[176:179], v[144:159]\n"
    "s_waitcnt lgkmcnt(6)\n"
    "ds_read_b64_tr_b16 a[228:229], v10 offset:2048\n"
    "ds_read_b64_tr_b16 a[230:231], v10 offset:2560\n"
    "buffer_load_dwordx4 v17, s[20:23], s53 offen offset:1024 lds\n"
    "v_mfma_f32_32x32x16_bf16 v[144:159], a[244:247], a[180:183], v[144:159]\n"
    "s_waitcnt lgkmcnt(6)\n"
    "ds_read_b64_tr_b16 a[232:233], v10 offset:4096\n"
    "ds_read_b64_tr_b16 a[234:235], v10 offset:4608\n"
    "buffer_load_dwordx4 v17, s[20:23], s54 offen offset:2048 lds\n"
    "v_mfma_f32_32x32x16_bf16 v[144:159], a[248:251], a[184:187], v[144:159]\n"
    "s_waitcnt lgkmcnt(6)\n"
    "ds_read_b64_tr_b16 a[236:237], v10 offset:6144\n"
    "ds_read_b64_tr_b16 a[238:239], v10 offset:6656\n"
    "buffer_load_dwordx4 v17, s[20:23], s55 offen offset:3072 lds\n"
    "v_add_u32 v17, 0xe000, v17\n"
    "v_mfma_f32_32x32x16_bf16 v[144:159], a[252:255], a[188:191], v[144:159]\n"
    "s_waitcnt lgkmcnt(6)\n"
    "s_add_u32 m0, s45, 0x4000\n"
    "v_xor_b32 v11, 0x10000, v11\n"
    "ds_read_b64_tr_b16 a[240:241], v11\n"
    "ds_read_b64_tr_b16 a[242:243], v11 offset:512\n"
    "buffer_load_dwordx4 v17, s[20:23], 0 offen lds\n"
    "v_mfma_f32_32x32x16_bf16 v[160:175], a[224:227], v[64:67], 0\n"
    "s_waitcnt lgkmcnt(6)\n"
    "ds_read_b64_tr_b16 a[244:245], v11 offset:2048\n"
    "ds_read_b64_tr_b16 a[246:247], v11 offset:2560\n"
    "buffer_load_dwordx4 v17, s[20:23], s53 offen offset:1024 lds\n"
    "v_mfma_f32_32x32x16_bf16 v[160:175], a[228:231], v[68:71], v[160:175]\n"
    "s_waitcnt lgkmcnt(6)\n"
    "ds_read_b64_tr_b16 a[248:249], v11 offset:4096\n"
    "ds_read_b64_tr_b16 a[250:251], v11 offset:4608\n"
    "buffer_load_dwordx4 v17, s[20:23], s54 offen offset:2048 lds\n"
    "v_mfma_f32_32x32x16_bf16 v[160:175], a[232:235], v[72:75], v[160:175]\n"
    "s_waitcnt lgkmcnt(6)\n"
    "ds_read_b64_tr_b16 a[252:253], v11 offset:6144\n"
    "ds_read_b64_tr_b16 a[254:255], v11 offset:6656\n"
    "buffer_load_dwordx4 v17, s[20:23], s55 offen offset:3072 lds\n"
    "v_add_u32 v17, 0xfffd6080, v17\n"
    "v_mfma_f32_32x32x16_bf16 v[160:175], a[236:239], v[76:79], v[160:175]\n"
    "s_waitcnt vmcnt(8) lgkmcnt(6)\n"
    "s_barrier\n"
    "s_add_u32 m0, s45, 0x8000\n"
    "ds_read_b64_tr_b16 a[224:225], v10 offset:16384\n"
    "ds_read_b64_tr_b16 a[226:227], v10 offset:16896\n"
    "buffer_load_dwordx4 v17, s[20:23], 0 offen lds\n"
    "ds_write_b32 v19, v14\n"
    "v_mfma_f32_32x32x16_bf16 v[176:191], a[240:243], a[128:131], 0\n"
    "s_waitcnt lgkmcnt(7)\n"
    "ds_read_b64_tr_b16 a[228:229], v10 offset:18432\n"
    "ds_read_b64_tr_b16 a[230:231], v10 offset:18944\n"
    "buffer_load_dwordx4 v17, s[20:23], s53 offen offset:1024 lds\n"
    "v_mfma_f32_32x32x16_bf16 v[176:191], a[244:247], a[132:135], v[176:191]\n"
    "s_waitcnt lgkmcnt(7)\n"
    "ds_read_b64_tr_b16 a[232:233], v10 offset:20480\n"
    "ds_read_b64_tr_b16 a[234:235], v10 offset:20992\n"
    "buffer_load_dwordx4 v17, s[20:23], s54 offen offset:2048 lds\n"
    "v_mfma_f32_32x32x16_bf16 v[176:191], a[248:251], a[136:139], v[176:191]\n"
    "s_waitcnt lgkmcnt(7)\n"
    "ds_read_b64_tr_b16 a[236:237], v10 offset:22528\n"
    "ds_read_b64_tr_b16 a[238:239], v10 offset:23040\n"
    "buffer_load_dwordx4 v17, s[20:23], s55 offen offset:3072 lds\n"
    "v_add_u32 v17, 0xe000, v17\n"
    "v_mfma_f32_32x32x16_bf16 v[176:191], a[252:255], a[140:143], v[176:191]\n"
    "s_waitcnt lgkmcnt(7)\n"
    "s_add_u32 m0, s45, 0xc000\n"
    "ds_read_b64_tr_b16 a[240:241], v11 offset:16384\n"
    "ds_read_b64_tr_b16 a[242:243], v11 offset:16896\n"
    "buffer_load_dwordx4 v17, s[20:23], 0 offen lds\n"
    "v_mfma_f32_32x32x16_bf16 v[160:175], a[224:227], v[80:83], v[160:175]\n"
    "s_waitcnt lgkmcnt(6)\n"
    "ds_read_b64_tr_b16 a[244:245], v11 offset:18432\n"
    "ds_read_b64_tr_b16 a[246:247], v11 offset:18944\n"
    "buffer_load_dwordx4 v17, s[20:23], s53 offen offset:1024 lds\n"
    "v_mfma_f32_32x32x16_bf16 v[160:175], a[228:231], v[84:87], v[160:175]\n"
    "s_waitcnt lgkmcnt(6)\n"
    "ds_read_b64_tr_b16 a[248:249], v11 offset:20480\n"
    "ds_read_b64_tr_b16 a[250:251], v11 offset:20992\n"
    "buffer_load_dwordx4 v17, s[20:23], s54 offen offset:2048 lds\n"
    "v_mfma_f32_32x32x16_bf16 v[160:175], a[232:235], v[88:91], v[160:175]\n"
    "s_waitcnt lgkmcnt(6)\n"
    "ds_read_b64_tr_b16 a[252:253], v11 offset:22528\n"
    "ds_read_b64_tr_b16 a[254:255], v11 offset:23040\n"
    "buffer_load_dwordx4 v17, s[20:23], s55 offen offset:3072 lds\n"
    "v_add_u32 v17, 0xe000, v17\n"
    "v_mfma_f32_32x32x16_bf16 v[160:175], a[236:239], v[92:95], v[160:175]\n"
    "s_waitcnt vmcnt(12) lgkmcnt(6)\n"
    "s_barrier\n"
    "s_add_u32 m0, s45, 0x10000\n"
    "v_xor_b32 v10, 0x10000, v10\n"
    "ds_read_b32 v18, v20\n"
    "ds_read_b64_tr_b16 a[224:225], v10\n"
    "ds_read_b64_tr_b16 a[226:227], v10 offset:512\n"
    "buffer_load_dwordx4 v17, s[20:23], 0 offen lds\n"
    "v_mfma_f32_32x32x16_bf16 v[176:191], a[240:243], a[144:147], v[176:191]\n"
    "s_waitcnt lgkmcnt(7)\n"
    "ds_read_b64_tr_b16 a[228:229], v10 offset:2048\n"
    "ds_read_b64_tr_b16 a[230:231], v10 offset:2560\n"
    "v_mfma_f32_32x32x16_bf16 v[176:191], a[244:247], a[148:151], v[176:191]\n"
    "s_waitcnt lgkmcnt(7)\n"
    "ds_read_b64_tr_b16 a[232:233], v10 offset:4096\n"
    "ds_read_b64_tr_b16 a[234:235], v10 offset:4608\n"
    "buffer_load_dwordx4 v17, s[20:23], s53 offen offset:1024 lds\n"
    "v_mfma_f32_32x32x16_bf16 v[176:191], a[248:251], a[152:155], v[176:191]\n"
    "s_waitcnt lgkmcnt(7)\n"
    "ds_read_b64_tr_b16 a[236:237], v10 offset:6144\n"
    "ds_read_b64_tr_b16 a[238:239], v10 offset:6656\n"
    "v_mul_f32 v128, v128, v14\n"
    "v_mul_f32 v129, v129, v14\n"
    "v_mul_f32 v130, v130, v14\n"
    "v_mul_f32 v131, v131, v14\n"
    "v_mfma_f32_32x32x16_bf16 v[176:191], a[252:255], a[156:159], v[176:191]\n"
    "s_waitcnt lgkmcnt(6)\n"
    "v_xor_b32 v11, 0x10000, v11\n"
    "ds_read_b64_tr_b16 a[240:241], v11\n"
    "ds_read_b64_tr_b16 a[242:243], v11 offset:512\n"
    "buffer_load_dwordx4 v17, s[20:23], s54 offen offset:2048 lds\n"
    "v_fma_f32 v128, v144, v18, v128\n"
    "v_mfma_f32_32x32x16_bf16 v[160:175], a[224:227], v[96:99], v[160:175]\n"
    "s_waitcnt lgkmcnt(6)\n"
    "ds_read_b64_tr_b16 a[244:245], v11 offset:2048\n"
    "ds_read_b64_tr_b16 a[246:247], v11 offset:2560\n"
    "v_fma_f32 v129, v145, v18, v129\n"
    "v_fma_f32 v130, v146, v18, v130\n"
    "v_fma_f32 v131, v147, v18, v131\n"
    "v_cvt_pk_bf16_f32 v192, v128, v129\n"
    "v_mfma_f32_32x32x16_bf16 v[160:175], a[228:231], v[100:103], v[160:175]\n"
    "s_waitcnt lgkmcnt(6)\n"
    "ds_read_b64_tr_b16 a[248:249], v11 offset:4096\n"
    "ds_read_b64_tr_b16 a[250:251], v11 offset:4608\n"
    "buffer_load_dwordx4 v17, s[20:23], s55 offen offset:3072 lds\n"
    "v_add_u32 v17, 0xe000, v17\n"
    "v_cvt_pk_bf16_f32 v193, v130, v131\n"
    "v_mfma_f32_32x32x16_bf16 v[160:175], a[232:235], v[104:107], v[160:175]\n"
    "s_waitcnt lgkmcnt(6)\n"
    "ds_read_b64_tr_b16 a[252:253], v11 offset:6144\n"
    "ds_read_b64_tr_b16 a[254:255], v11 offset:6656\n"
    "v_mul_f32 v132, v132, v14\n"
    "v_mul_f32 v133, v133, v14\n"
    "v_mul_f32 v134, v134, v14\n"
    "v_mul_f32 v135, v135, v14\n"
    "v_mfma_f32_32x32x16_bf16 v[160:175], a[236:239], v[108:111], v[160:175]\n"
    "s_waitcnt vmcnt(12) lgkmcnt(6)\n"
    "s_barrier\n"
    "s_add_u32 m0, s45, 0x14000\n"
    "ds_read_b64_tr_b16 a[224:225], v10 offset:16384\n"
    "ds_read_b64_tr_b16 a[226:227], v10 offset:16896\n"
    "buffer_load_dwordx4 v17, s[20:23], 0 offen lds\n"
    "v_mfma_f32_32x32x16_bf16 v[176:191], a[240:243], a[160:163], v[176:191]\n"
    "s_waitcnt lgkmcnt(6)\n"
    "ds_read_b64_tr_b16 a[228:229], v10 offset:18432\n"
    "ds_read_b64_tr_b16 a[230:231], v10 offset:18944\n"
    "v_fma_f32 v132, v148, v18, v132\n"
    "v_fma_f32 v133, v149, v18, v133\n"
    "v_fma_f32 v134, v150, v18, v134\n"
    "v_fma_f32 v135, v151, v18, v135\n"
    "v_mfma_f32_32x32x16_bf16 v[176:191], a[244:247], a[164:167], v[176:191]\n"
    "s_waitcnt lgkmcnt(6)\n"
    "ds_read_b64_tr_b16 a[232:233], v10 offset:20480\n"
    "ds_read_b64_tr_b16 a[234:235], v10 offset:20992\n"
    "buffer_load_dwordx4 v17, s[20:23], s53 offen offset:1024 lds\n"
    "v_cvt_pk_bf16_f32 v194, v132, v133\n"
    "v_cvt_pk_bf16_f32 v195, v134, v135\n"
    "v_mfma_f32_32x32x16_bf16 v[176:191], a[248:251], a[168:171], v[176:191]\n"
    "s_waitcnt lgkmcnt(6)\n"
    "ds_read_b64_tr_b16 a[236:237], v10 offset:22528\n"
    "ds_read_b64_tr_b16 a[238:239], v10 offset:23040\n"
    "s_mov_b32 s63, 0\n"
    "buffer_store_dwordx4 v[192:195], v13, s[28:31], s63 offen sc1\n"
    "v_mul_f32 v136, v136, v14\n"
    "v_mfma_f32_32x32x16_bf16 v[176:191], a[252:255], a[172:175], v[176:191]\n"
    "s_waitcnt lgkmcnt(6)\n"
    "ds_read_b64_tr_b16 a[240:241], v11 offset:16384\n"
    "ds_read_b64_tr_b16 a[242:243], v11 offset:16896\n"
    "buffer_load_dwordx4 v17, s[20:23], s54 offen offset:2048 lds\n"
    "v_mul_f32 v137, v137, v14\n"
    "v_mul_f32 v138, v138, v14\n"
    "v_mfma_f32_32x32x16_bf16 v[160:175], a[224:227], v[112:115], v[160:175]\n"
    "s_waitcnt lgkmcnt(6)\n"
    "ds_read_b64_tr_b16 a[244:245], v11 offset:18432\n"
    "ds_read_b64_tr_b16 a[246:247], v11 offset:18944\n"
    "v_mul_f32 v139, v139, v14\n"
    "v_fma_f32 v136, v152, v18, v136\n"
    "v_fma_f32 v137, v153, v18, v137\n"
    "v_fma_f32 v138, v154, v18, v138\n"
    "v_mfma_f32_32x32x16_bf16 v[160:175], a[228:231], v[116:119], v[160:175]\n"
    "s_waitcnt lgkmcnt(6)\n"
    "ds_read_b64_tr_b16 a[248:249], v11 offset:20480\n"
    "ds_read_b64_tr_b16 a[250:251], v11 offset:20992\n"
    "buffer_load_dwordx4 v17, s[20:23], s55 offen offset:3072 lds\n"
    "v_add_u32 v17, 0xfffd6080, v17\n"
    "v_fma_f32 v139, v155, v18, v139\n"
    "v_mfma_f32_32x32x16_bf16 v[160:175], a[232:235], v[120:123], v[160:175]\n"
    "s_waitcnt lgkmcnt(6)\n"
    "ds_read_b64_tr_b16 a[252:253], v11 offset:22528\n"
    "ds_read_b64_tr_b16 a[254:255], v11 offset:23040\n"
    "v_cvt_pk_bf16_f32 v196, v136, v137\n"
    "v_cvt_pk_bf16_f32 v197, v138, v139\n"
    "v_mul_f32 v140, v140, v14\n"
    "v_mul_f32 v141, v141, v14\n"
    "v_mfma_f32_32x32x16_bf16 v[160:175], a[236:239], v[124:127], v[160:175]\n"
    "s_waitcnt vmcnt(13) lgkmcnt(6)\n"
    "s_barrier\n"
    "s_add_u32 m0, s45, 0\n"
    "ds_read_b64_tr_b16 a[224:225], v10 offset:32768\n"
    "ds_read_b64_tr_b16 a[226:227], v10 offset:33280\n"
    "buffer_load_dwordx4 v17, s[20:23], 0 offen lds\n"
    "v_mfma_f32_32x32x16_bf16 v[176:191], a[240:243], a[176:179], v[176:191]\n"
    "s_waitcnt lgkmcnt(6)\n"
    "ds_read_b64_tr_b16 a[228:229], v10 offset:34816\n"
    "ds_read_b64_tr_b16 a[230:231], v10 offset:35328\n"
    "v_mul_f32 v142, v142, v14\n"
    "v_mul_f32 v143, v143, v14\n"
    "v_fma_f32 v140, v156, v18, v140\n"
    "v_fma_f32 v141, v157, v18, v141\n"
    "v_mfma_f32_32x32x16_bf16 v[176:191], a[244:247], a[180:183], v[176:191]\n"
    "s_waitcnt lgkmcnt(6)\n"
    "ds_read_b64_tr_b16 a[232:233], v10 offset:36864\n"
    "ds_read_b64_tr_b16 a[234:235], v10 offset:37376\n"
    "buffer_load_dwordx4 v17, s[20:23], s53 offen offset:1024 lds\n"
    "v_fma_f32 v142, v158, v18, v142\n"
    "v_fma_f32 v143, v159, v18, v143\n"
    "v_cvt_pk_bf16_f32 v198, v140, v141\n"
    "v_cvt_pk_bf16_f32 v199, v142, v143\n"
    "buffer_store_dwordx4 v[196:199], v13, s[28:31], s63 offen offset:1024 sc1\n"
    "v_mfma_f32_32x32x16_bf16 v[176:191], a[248:251], a[184:187], v[176:191]\n"
    "s_waitcnt lgkmcnt(6)\n"
    "ds_read_b64_tr_b16 a[236:237], v10 offset:38912\n"
    "ds_read_b64_tr_b16 a[238:239], v10 offset:39424\n"
    "v_mfma_f32_32x32x16_bf16 v[176:191], a[252:255], a[188:191], v[176:191]\n"
    "s_waitcnt lgkmcnt(6)\n"
    "ds_read_b64_tr_b16 a[240:241], v11 offset:32768\n"
    "ds_read_b64_tr_b16 a[242:243], v11 offset:33280\n"
    "buffer_load_dwordx4 v17, s[20:23], s54 offen offset:2048 lds\n"
    "v_mfma_f32_32x32x16_bf16 v[128:143], a[224:227], v[64:67], 0\n"
    "s_waitcnt lgkmcnt(6)\n"
    "ds_read_b64_tr_b16 a[244:245], v11 offset:34816\n"
    "ds_read_b64_tr_b16 a[246:247], v11 offset:35328\n"
    "v_mfma_f32_32x32x16_bf16 v[128:143], a[228:231], v[68:71], v[128:143]\n"
    "s_waitcnt lgkmcnt(6)\n"
    "ds_read_b64_tr_b16 a[248:249], v11 offset:36864\n"
    "ds_read_b64_tr_b16 a[250:251], v11 offset:37376\n"
    "buffer_load_dwordx4 v17, s[20:23], s55 offen offset:3072 lds\n"
    "v_add_u32 v17, 0xe000, v17\n"
    "v_mul_f32 v160, v160, v14\n"
    "v_mfma_f32_32x32x16_bf16 v[128:143], a[232:235], v[72:75], v[128:143]\n"
    "s_waitcnt lgkmcnt(6)\n"
    "ds_read_b64_tr_b16 a[252:253], v11 offset:38912\n"
    "ds_read_b64_tr_b16 a[254:255], v11 offset:39424\n"
    "v_mul_f32 v161, v161, v14\n"
    "v_mul_f32 v162, v162, v14\n"
    "v_mul_f32 v163, v163, v14\n"
    "v_fma_f32 v160, v176, v18, v160\n"
    "v_mfma_f32_32x32x16_bf16 v[128:143], a[236:239], v[76:79], v[128:143]\n"
    "s_waitcnt vmcnt(14) lgkmcnt(6)\n"
    "s_barrier\n"
    "s_add_u32 m0, s45, 0x4000\n"
    "ds_read_b64_tr_b16 a[224:225], v10 offset:49152\n"
    "ds_read_b64_tr_b16 a[226:227], v10 offset:49664\n"
    "buffer_load_dwordx4 v17, s[20:23], 0 offen lds\n"
    "v_mfma_f32_32x32x16_bf16 v[144:159], a[240:243], a[128:131], 0\n"
    "s_waitcnt lgkmcnt(6)\n"
    "ds_read_b64_tr_b16 a[228:229], v10 offset:51200\n"
    "ds_read_b64_tr_b16 a[230:231], v10 offset:51712\n"
    "v_fma_f32 v161, v177, v18, v161\n"
    "v_fma_f32 v162, v178, v18, v162\n"
    "v_fma_f32 v163, v179, v18, v163\n"
    "v_cvt_pk_bf16_f32 v200, v160, v161\n"
    "v_mfma_f32_32x32x16_bf16 v[144:159], a[244:247], a[132:135], v[144:159]\n"
    "s_waitcnt lgkmcnt(6)\n"
    "ds_read_b64_tr_b16 a[232:233], v10 offset:53248\n"
    "ds_read_b64_tr_b16 a[234:235], v10 offset:53760\n"
    "buffer_load_dwordx4 v17, s[20:23], s53 offen offset:1024 lds\n"
    "v_cvt_pk_bf16_f32 v201, v162, v163\n"
    "v_mul_f32 v164, v164, v14\n"
    "v_mfma_f32_32x32x16_bf16 v[144:159], a[248:251], a[136:139], v[144:159]\n"
    "s_waitcnt lgkmcnt(6)\n"
    "ds_read_b64_tr_b16 a[236:237], v10 offset:55296\n"
    "ds_read_b64_tr_b16 a[238:239], v10 offset:55808\n"
    "v_mul_f32 v165, v165, v14\n"
    "v_mul_f32 v166, v166, v14\n"
    "v_mul_f32 v167, v167, v14\n"
    "v_fma_f32 v164, v180, v18, v164\n"
    "v_mfma_f32_32x32x16_bf16 v[144:159], a[252:255], a[140:143], v[144:159]\n"
    "s_waitcnt lgkmcnt(6)\n"
    "ds_read_b64_tr_b16 a[240:241], v11 offset:49152\n"
    "ds_read_b64_tr_b16 a[242:243], v11 offset:49664\n"
    "buffer_load_dwordx4 v17, s[20:23], s54 offen offset:2048 lds\n"
    "v_fma_f32 v165, v181, v18, v165\n"
    "v_fma_f32 v166, v182, v18, v166\n"
    "v_mfma_f32_32x32x16_bf16 v[128:143], a[224:227], v[80:83], v[128:143]\n"
    "s_waitcnt lgkmcnt(6)\n"
    "ds_read_b64_tr_b16 a[244:245], v11 offset:51200\n"
    "ds_read_b64_tr_b16 a[246:247], v11 offset:51712\n"
    "v_fma_f32 v167, v183, v18, v167\n"
    "v_cvt_pk_bf16_f32 v202, v164, v165\n"
    "v_cvt_pk_bf16_f32 v203, v166, v167\n"
    "s_mov_b32 s63, 0x1000\n"
    "v_mfma_f32_32x32x16_bf16 v[128:143], a[228:231], v[84:87], v[128:143]\n"
    "s_waitcnt lgkmcnt(6)\n"
    "ds_read_b64_tr_b16 a[248:249], v11 offset:53248\n"
    "ds_read_b64_tr_b16 a[250:251], v11 offset:53760\n"
    "buffer_load_dwordx4 v17, s[20:23], s55 offen offset:3072 lds\n"
    "v_add_u32 v17, 0xe000, v17\n"
    "v_mfma_f32_32x32x16_bf16 v[128:143], a[232:235], v[88:91], v[128:143]\n"
    "s_waitcnt lgkmcnt(6)\n"
    "ds_read_b64_tr_b16 a[252:253], v11 offset:55296\n"
    "ds_read_b64_tr_b16 a[254:255], v11 offset:55808\n"
    "buffer_store_dwordx4 v[200:203], v13, s[28:31], s63 offen sc1\n"
    "v_mul_f32 v168, v168, v14\n"
    "v_mul_f32 v169, v169, v14\n"
    "v_mfma_f32_32x32x16_bf16 v[128:143], a[236:239], v[92:95], v[128:143]\n"
    "s_waitcnt vmcnt(15) lgkmcnt(6)\n"
    "s_barrier\n"
    "s_add_u32 m0, s45, 0x8000\n"
    "v_xor_b32 v10, 0x10000, v10\n"
    "ds_read_b64_tr_b16 a[224:225], v10\n"
    "ds_read_b64_tr_b16 a[226:227], v10 offset:512\n"
    "buffer_load_dwordx4 v17, s[20:23], 0 offen lds\n"
    "v_mfma_f32_32x32x16_bf16 v[144:159], a[240:243], a[144:147], v[144:159]\n"
    "s_waitcnt lgkmcnt(6)\n"
    "ds_read_b64_tr_b16 a[228:229], v10 offset:2048\n"
    "ds_read_b64_tr_b16 a[230:231], v10 offset:2560\n"
    "v_mul_f32 v170, v170, v14\n"
    "v_mul_f32 v171, v171, v14\n"
    "v_fma_f32 v168, v184, v18, v168\n"
    "v_fma_f32 v169, v185, v18, v169\n"
    "v_mfma_f32_32x32x16_bf16 v[144:159], a[244:247], a[148:151], v[144:159]\n"
    "s_waitcnt lgkmcnt(6)\n"
    "ds_read_b64_tr_b16 a[232:233], v10 offset:4096\n"
    "ds_read_b64_tr_b16 a[234:235], v10 offset:4608\n"
    "buffer_load_dwordx4 v17, s[20:23], s53 offen offset:1024 lds\n"
    "v_fma_f32 v170, v186, v18, v170\n"
    "v_fma_f32 v171, v187, v18, v171\n"
    "v_mfma_f32_32x32x16_bf16 v[144:159], a[248:251], a[152:155], v[144:159]\n"
    "s_waitcnt lgkmcnt(6)\n"
    "ds_read_b64_tr_b16 a[236:237], v10 offset:6144\n"
    "ds_read_b64_tr_b16 a[238:239], v10 offset:6656\n"
    "v_cvt_pk_bf16_f32 v204, v168, v169\n"
    "v_cvt_pk_bf16_f32 v205, v170, v171\n"
    "v_mul_f32 v172, v172, v14\n"
    "v_mul_f32 v173, v173, v14\n"
    "v_mfma_f32_32x32x16_bf16 v[144:159], a[252:255], a[156:159], v[144:159]\n"
    "s_waitcnt lgkmcnt(6)\n"
    "v_xor_b32 v11, 0x10000, v11\n"
    "ds_read_b64_tr_b16 a[240:241], v11\n"
    "ds_read_b64_tr_b16 a[242:243], v11 offset:512\n"
    "buffer_load_dwordx4 v17, s[20:23], s54 offen offset:2048 lds\n"
    "v_mul_f32 v174, v174, v14\n"
    "v_mfma_f32_32x32x16_bf16 v[128:143], a[224:227], v[96:99], v[128:143]\n"
    "s_waitcnt lgkmcnt(6)\n"
    "ds_read_b64_tr_b16 a[244:245], v11 offset:2048\n"
    "ds_read_b64_tr_b16 a[246:247], v11 offset:2560\n"
    "v_mul_f32 v175, v175, v14\n"
    "v_fma_f32 v172, v188, v18, v172\n"
    "v_fma_f32 v173, v189, v18, v173\n"
    "v_fma_f32 v174, v190, v18, v174\n"
    "v_mfma_f32_32x32x16_bf16 v[128:143], a[228:231], v[100:103], v[128:143]\n"
    "s_waitcnt lgkmcnt(6)\n"
    "ds_read_b64_tr_b16 a[248:249], v11 offset:4096\n"
    "ds_read_b64_tr_b16 a[250:251], v11 offset:4608\n"
    "buffer_load_dwordx4 v17, s[20:23], s55 offen offset:3072 lds\n"
    "v_add_u32 v17, 0xe000, v17\n"
    "v_fma_f32 v175, v191, v18, v175\n"
    "v_mfma_f32_32x32x16_bf16 v[128:143], a[232:235], v[104:107], v[128:143]\n"
    "s_waitcnt lgkmcnt(6)\n"
    "ds_read_b64_tr_b16 a[252:253], v11 offset:6144\n"
    "ds_read_b64_tr_b16 a[254:255], v11 offset:6656\n"
    "v_cvt_pk_bf16_f32 v206, v172, v173\n"
    "v_cvt_pk_bf16_f32 v207, v174, v175\n"
    "buffer_store_dwordx4 v[204:207], v13, s[28:31], s63 offen offset:1024 sc1\n"
    "v_mfma_f32_32x32x16_bf16 v[128:143], a[236:239], v[108:111], v[128:143]\n"
    "s_waitcnt vmcnt(15) lgkmcnt(6)\n"
    "s_barrier\n"
    "s_add_u32 m0, s45, 0xc000\n"
    "ds_read_b64_tr_b16 a[224:225], v10 offset:16384\n"
    "ds_read_b64_tr_b16 a[226:227], v10 offset:16896\n"
    "buffer_load_dwordx4 v17, s[20:23], 0 offen lds\n"
    "v_mfma_f32_32x32x16_bf16 v[144:159], a[240:243], a[160:163], v[144:159]\n"
    "s_waitcnt lgkmcnt(6)\n"
    "ds_read_b64_tr_b16 a[228:229], v10 offset:18432\n"
    "ds_read_b64_tr_b16 a[230:231], v10 offset:18944\n"
    "v_mfma_f32_32x32x16_bf16 v[144:159], a[244:247], a[164:167], v[144:159]\n"
    "s_waitcnt lgkmcnt(6)\n"
    "ds_read_b64_tr_b16 a[232:233], v10 offset:20480\n"
    "ds_read_b64_tr_b16 a[234:235], v10 offset:20992\n"
    "buffer_load_dwordx4 v17, s[20:23], s53 offen offset:1024 lds\n"
    "v_mfma_f32_32x32x16_bf16 v[144:159], a[248:251], a[168:171], v[144:159]\n"
    "s_waitcnt lgkmcnt(6)\n"
    "ds_read_b64_tr_b16 a[236:237], v10 offset:22528\n"
    "ds_read_b64_tr_b16 a[238:239], v10 offset:23040\n"
    "v_mfma_f32_32x32x16_bf16 v[144:159], a[252:255], a[172:175], v[144:159]\n"
    "s_waitcnt lgkmcnt(6)\n"
    "ds_read_b64_tr_b16 a[240:241], v11 offset:16384\n"
    "ds_read_b64_tr_b16 a[242:243], v11 offset:16896\n"
    "buffer_load_dwordx4 v17, s[20:23], s54 offen offset:2048 lds\n"
    "v_mfma_f32_32x32x16_bf16 v[128:143], a[224:227], v[112:115], v[128:143]\n"
    "s_waitcnt lgkmcnt(6)\n"
    "ds_read_b64_tr_b16 a[244:245], v11 offset:18432\n"
    "ds_read_b64_tr_b16 a[246:247], v11 offset:18944\n"
    "v_mfma_f32_32x32x16_bf16 v[128:143], a[228:231], v[116:119], v[128:143]\n"
    "s_waitcnt lgkmcnt(6)\n"
    "ds_read_b64_tr_b16 a[248:249], v11 offset:20480\n"
    "ds_read_b64_tr_b16 a[250:251], v11 offset:20992\n"
    "buffer_load_dwordx4 v17, s[20:23], s55 offen offset:3072 lds\n"
    "v_add_u32 v17, 0xfffd6080, v17\n"
    "v_mfma_f32_32x32x16_bf16 v[128:143], a[232:235], v[120:123], v[128:143]\n"
    "s_waitcnt lgkmcnt(6)\n"
    "ds_read_b64_tr_b16 a[252:253], v11 offset:22528\n"
    "ds_read_b64_tr_b16 a[254:255], v11 offset:23040\n"
    "v_mfma_f32_32x32x16_bf16 v[128:143], a[236:239], v[124:127], v[128:143]\n"
    "s_waitcnt vmcnt(14) lgkmcnt(6)\n"
    "s_barrier\n"
    "s_add_u32 m0, s45, 0x10000\n"
    "v_xor_b32 v10, 0x10000, v10\n"
    "ds_read_b64_tr_b16 a[224:225], v10\n"
    "ds_read_b64_tr_b16 a[226:227], v10 offset:512\n"
    "buffer_load_dwordx4 v17, s[20:23], 0 offen lds\n"
    "v_mfma_f32_32x32x16_bf16 v[144:159], a[240:243], a[176:179], v[144:159]\n"
    "s_waitcnt lgkmcnt(6)\n"
    "ds_read_b64_tr_b16 a[228:229], v10 offset:2048\n"
    "ds_read_b64_tr_b16 a[230:231], v10 offset:2560\n"
    "v_mfma_f32_32x32x16_bf16 v[144:159], a[244:247], a[180:183], v[144:159]\n"
    "s_waitcnt lgkmcnt(6)\n"
    "ds_read_b64_tr_b16 a[232:233], v10 offset:4096\n"
    "ds_read_b64_tr_b16 a[234:235], v10 offset:4608\n"
    "buffer_load_dwordx4 v17, s[20:23], s53 offen offset:1024 lds\n"
    "v_mfma_f32_32x32x16_bf16 v[144:159], a[248:251], a[184:187], v[144:159]\n"
    "s_waitcnt lgkmcnt(6)\n"
    "ds_read_b64_tr_b16 a[236:237], v10 offset:6144\n"
    "ds_read_b64_tr_b16 a[238:239], v10 offset:6656\n"
    "v_mfma_f32_32x32x16_bf16 v[144:159], a[252:255], a[188:191], v[144:159]\n"
    "s_waitcnt lgkmcnt(6)\n"
    "v_xor_b32 v11, 0x10000, v11\n"
    "ds_read_b64_tr_b16 a[240:241], v11\n"
    "ds_read_b64_tr_b16 a[242:243], v11 offset:512\n"
    "buffer_load_dwordx4 v17, s[20:23], s54 offen offset:2048 lds\n"
    "v_mfma_f32_32x32x16_bf16 v[160:175], a[224:227], v[64:67], 0\n"
    "s_waitcnt lgkmcnt(6)\n"
    "ds_read_b64_tr_b16 a[244:245], v11 offset:2048\n"
    "ds_read_b64_tr_b16 a[246:247], v11 offset:2560\n"
    "v_mfma_f32_32x32x16_bf16 v[160:175], a[228:231], v[68:71], v[160:175]\n"
    "s_waitcnt lgkmcnt(6)\n"
    "ds_read_b64_tr_b16 a[248:249], v11 offset:4096\n"
    "ds_read_b64_tr_b16 a[250:251], v11 offset:4608\n"
    "buffer_load_dwordx4 v17, s[20:23], s55 offen offset:3072 lds\n"
    "v_add_u32 v17, 0xe000, v17\n"
    "v_mul_f32 v128, v128, v14\n"
    "v_mfma_f32_32x32x16_bf16 v[160:175], a[232:235], v[72:75], v[160:175]\n"
    "s_waitcnt lgkmcnt(6)\n"
    "ds_read_b64_tr_b16 a[252:253], v11 offset:6144\n"
    "ds_read_b64_tr_b16 a[254:255], v11 offset:6656\n"
    "v_mul_f32 v129, v129, v14\n"
    "v_mul_f32 v130, v130, v14\n"
    "v_mul_f32 v131, v131, v14\n"
    "v_fma_f32 v128, v144, v18, v128\n"
    "v_mfma_f32_32x32x16_bf16 v[160:175], a[236:239], v[76:79], v[160:175]\n"
    "s_waitcnt vmcnt(14) lgkmcnt(6)\n"
    "s_barrier\n"
    "s_add_u32 m0, s45, 0x14000\n"
    "ds_read_b64_tr_b16 a[224:225], v10 offset:16384\n"
    "ds_read_b64_tr_b16 a[226:227], v10 offset:16896\n"
    "buffer_load_dwordx4 v17, s[20:23], 0 offen lds\n"
    "v_mfma_f32_32x32x16_bf16 v[176:191], a[240:243], a[128:131], 0\n"
    "s_waitcnt lgkmcnt(6)\n"
    "ds_read_b64_tr_b16 a[228:229], v10 offset:18432\n"
    "ds_read_b64_tr_b16 a[230:231], v10 offset:18944\n"
    "v_fma_f32 v129, v145, v18, v129\n"
    "v_fma_f32 v130, v146, v18, v130\n"
    "v_fma_f32 v131, v147, v18, v131\n"
    "v_cvt_pk_bf16_f32 v192, v128, v129\n"
    "v_mfma_f32_32x32x16_bf16 v[176:191], a[244:247], a[132:135], v[176:191]\n"
    "s_waitcnt lgkmcnt(6)\n"
    "ds_read_b64_tr_b16 a[232:233], v10 offset:20480\n"
    "ds_read_b64_tr_b16 a[234:235], v10 offset:20992\n"
    "buffer_load_dwordx4 v17, s[20:23], s53 offen offset:1024 lds\n"
    "v_cvt_pk_bf16_f32 v193, v130, v131\n"
    "v_mul_f32 v132, v132, v14\n"
    "v_mfma_f32_32x32x16_bf16 v[176:191], a[248:251], a[136:139], v[176:191]\n"
    "s_waitcnt lgkmcnt(6)\n"
    "ds_read_b64_tr_b16 a[236:237], v10 offset:22528\n"
    "ds_read_b64_tr_b16 a[238:239], v10 offset:23040\n"
    "v_mul_f32 v133, v133, v14\n"
    "v_mul_f32 v134, v134, v14\n"
    "v_mul_f32 v135, v135, v14\n"
    "v_fma_f32 v132, v148, v18, v132\n"
    "v_mfma_f32_32x32x16_bf16 v[176:191], a[252:255], a[140:143], v[176:191]\n"
    "s_waitcnt lgkmcnt(6)\n"
    "ds_read_b64_tr_b16 a[240:241], v11 offset:16384\n"
    "ds_read_b64_tr_b16 a[242:243], v11 offset:16896\n"
    "buffer_load_dwordx4 v17, s[20:23], s54 offen offset:2048 lds\n"
    "v_fma_f32 v133, v149, v18, v133\n"
    "v_fma_f32 v134, v150, v18, v134\n"
    "v_mfma_f32_32x32x16_bf16 v[160:175], a[224:227], v[80:83], v[160:175]\n"
    "s_waitcnt lgkmcnt(6)\n"
    "ds_read_b64_tr_b16 a[244:245], v11 offset:18432\n"
    "ds_read_b64_tr_b16 a[246:247], v11 offset:18944\n"
    "v_fma_f32 v135, v151, v18, v135\n"
    "v_cvt_pk_bf16_f32 v194, v132, v133\n"
    "v_cvt_pk_bf16_f32 v195, v134, v135\n"
    "s_mov_b32 s63, 0x2000\n"
    "v_mfma_f32_32x32x16_bf16 v[160:175], a[228:231], v[84:87], v[160:175]\n"
    "s_waitcnt lgkmcnt(6)\n"
    "ds_read_b64_tr_b16 a[248:249], v11 offset:20480\n"
    "ds_read_b64_tr_b16 a[250:251], v11 offset:20992\n"
    "buffer_load_dwordx4 v17, s[20:23], s55 offen offset:3072 lds\n"
    "v_add_u32 v17, 0xe000, v17\n"
    "v_mfma_f32_32x32x16_bf16 v[160:175], a[232:235], v[88:91], v[160:175]\n"
    "s_waitcnt lgkmcnt(6)\n"
    "ds_read_b64_tr_b16 a[252:253], v11 offset:22528\n"
    "ds_read_b64_tr_b16 a[254:255], v11 offset:23040\n"
    "buffer_store_dwordx4 v[192:195], v13, s[28:31], s63 offen sc1\n"
    "v_mul_f32 v136, v136, v14\n"
    "v_mul_f32 v137, v137, v14\n"
    "v_mfma_f32_32x32x16_bf16 v[160:175], a[236:239], v[92:95], v[160:175]\n"
    "s_waitcnt vmcnt(14) lgkmcnt(6)\n"
    "s_barrier\n"
    "s_add_u32 m0, s45, 0\n"
    "ds_read_b64_tr_b16 a[224:225], v10 offset:32768\n"
    "ds_read_b64_tr_b16 a[226:227], v10 offset:33280\n"
    "buffer_load_dwordx4 v17, s[20:23], 0 offen lds\n"
    "v_mfma_f32_32x32x16_bf16 v[176:191], a[240:243], a[144:147], v[176:191]\n"
    "s_waitcnt lgkmcnt(6)\n"
    "ds_read_b64_tr_b16 a[228:229], v10 offset:34816\n"
    "ds_read_b64_tr_b16 a[230:231], v10 offset:35328\n"
    "v_mul_f32 v138, v138, v14\n"
    "v_mul_f32 v139, v139, v14\n"
    "v_fma_f32 v136, v152, v18, v136\n"
    "v_fma_f32 v137, v153, v18, v137\n"
    "v_mfma_f32_32x32x16_bf16 v[176:191], a[244:247], a[148:151], v[176:191]\n"
    "s_waitcnt lgkmcnt(6)\n"
    "ds_read_b64_tr_b16 a[232:233], v10 offset:36864\n"
    "ds_read_b64_tr_b16 a[234:235], v10 offset:37376\n"
    "buffer_load_dwordx4 v17, s[20:23], s53 offen offset:1024 lds\n"
    "v_fma_f32 v138, v154, v18, v138\n"
    "v_fma_f32 v139, v155, v18, v139\n"
    "v_mfma_f32_32x32x16_bf16 v[176:191], a[248:251], a[152:155], v[176:191]\n"
    "s_waitcnt lgkmcnt(6)\n"
    "ds_read_b64_tr_b16 a[236:237], v10 offset:38912\n"
    "ds_read_b64_tr_b16 a[238:239], v10 offset:39424\n"
    "v_cvt_pk_bf16_f32 v196, v136, v137\n"
    "v_cvt_pk_bf16_f32 v197, v138, v139\n"
    "v_mul_f32 v140, v140, v14\n"
    "v_mul_f32 v141, v141, v14\n"
    "v_mfma_f32_32x32x16_bf16 v[176:191], a[252:255], a[156:159], v[176:191]\n"
    "s_waitcnt lgkmcnt(6)\n"
    "ds_read_b64_tr_b16 a[240:241], v11 offset:32768\n"
    "ds_read_b64_tr_b16 a[242:243], v11 offset:33280\n"
    "buffer_load_dwordx4 v17, s[20:23], s54 offen offset:2048 lds\n"
    "v_mul_f32 v142, v142, v14\n"
    "v_mul_f32 v143, v143, v14\n"
    "v_mfma_f32_32x32x16_bf16 v[160:175], a[224:227], v[96:99], v[160:175]\n"
    "s_waitcnt lgkmcnt(6)\n"
    "ds_read_b64_tr_b16 a[244:245], v11 offset:34816\n"
    "ds_read_b64_tr_b16 a[246:247], v11 offset:35328\n"
    "v_fma_f32 v140, v156, v18, v140\n"
    "v_fma_f32 v141, v157, v18, v141\n"
    "v_fma_f32 v142, v158, v18, v142\n"
    "v_fma_f32 v143, v159, v18, v143\n"
    "v_mfma_f32_32x32x16_bf16 v[160:175], a[228:231], v[100:103], v[160:175]\n"
    "s_waitcnt lgkmcnt(6)\n"
    "ds_read_b64_tr_b16 a[248:249], v11 offset:36864\n"
    "ds_read_b64_tr_b16 a[250:251], v11 offset:37376\n"
    "buffer_load_dwordx4 v17, s[20:23], s55 offen offset:3072 lds\n"
    "v_add_u32 v17, 0xe000, v17\n"
    "v_cvt_pk_bf16_f32 v198, v140, v141\n"
    "v_mfma_f32_32x32x16_bf16 v[160:175], a[232:235], v[104:107], v[160:175]\n"
    "s_waitcnt lgkmcnt(6)\n"
    "ds_read_b64_tr_b16 a[252:253], v11 offset:38912\n"
    "ds_read_b64_tr_b16 a[254:255], v11 offset:39424\n"
    "v_cvt_pk_bf16_f32 v199, v142, v143\n"
    "buffer_store_dwordx4 v[196:199], v13, s[28:31], s63 offen offset:1024 sc1\n"
    "v_mfma_f32_32x32x16_bf16 v[160:175], a[236:239], v[108:111], v[160:175]\n"
    "s_waitcnt vmcnt(14) lgkmcnt(6)\n"
    "s_barrier\n"
    "s_add_u32 m0, s45, 0x4000\n"
    "ds_read_b64_tr_b16 a[224:225], v10 offset:49152\n"
    "ds_read_b64_tr_b16 a[226:227], v10 offset:49664\n"
    "buffer_load_dwordx4 v17, s[20:23], 0 offen lds\n"
    "v_mfma_f32_32x32x16_bf16 v[176:191], a[240:243], a[160:163], v[176:191]\n"
    "s_waitcnt lgkmcnt(6)\n"
    "ds_read_b64_tr_b16 a[228:229], v10 offset:51200\n"
    "ds_read_b64_tr_b16 a[230:231], v10 offset:51712\n"
    "v_mfma_f32_32x32x16_bf16 v[176:191], a[244:247], a[164:167], v[176:191]\n"
    "s_waitcnt lgkmcnt(6)\n"
    "ds_read_b64_tr_b16 a[232:233], v10 offset:53248\n"
    "ds_read_b64_tr_b16 a[234:235], v10 offset:53760\n"
    "buffer_load_dwordx4 v17, s[20:23], s53 offen offset:1024 lds\n"
    "v_mfma_f32_32x32x16_bf16 v[176:191], a[248:251], a[168:171], v[176:191]\n"
    "s_waitcnt lgkmcnt(6)\n"
    "ds_read_b64_tr_b16 a[236:237], v10 offset:55296\n"
    "ds_read_b64_tr_b16 a[238:239], v10 offset:55808\n"
    "v_mfma_f32_32x32x16_bf16 v[176:191], a[252:255], a[172:175], v[176:191]\n"
    "s_waitcnt lgkmcnt(6)\n"
    "ds_read_b64_tr_b16 a[240:241], v11 offset:49152\n"
    "ds_read_b64_tr_b16 a[242:243], v11 offset:49664\n"
    "buffer_load_dwordx4 v17, s[20:23], s54 offen offset:2048 lds\n"
    "v_mfma_f32_32x32x16_bf16 v[160:175], a[224:227], v[112:115], v[160:175]\n"
    "s_waitcnt lgkmcnt(6)\n"
    "ds_read_b64_tr_b16 a[244:245], v11 offset:51200\n"
    "ds_read_b64_tr_b16 a[246:247], v11 offset:51712\n"
    "v_mfma_f32_32x32x16_bf16 v[160:175], a[228:231], v[116:119], v[160:175]\n"
    "s_waitcnt lgkmcnt(6)\n"
    "ds_read_b64_tr_b16 a[248:249], v11 offset:53248\n"
    "ds_read_b64_tr_b16 a[250:251], v11 offset:53760\n"
    "buffer_load_dwordx4 v17, s[20:23], s55 offen offset:3072 lds\n"
    "v_add_u32 v17, 0xfffd6080, v17\n"
    "v_mfma_f32_32x32x16_bf16 v[160:175], a[232:235], v[120:123], v[160:175]\n"
    "s_waitcnt lgkmcnt(6)\n"
    "ds_read_b64_tr_b16 a[252:253], v11 offset:55296\n"
    "ds_read_b64_tr_b16 a[254:255], v11 offset:55808\n"
    "v_mfma_f32_32x32x16_bf16 v[160:175], a[236:239], v[124:127], v[160:175]\n"
    "s_waitcnt vmcnt(14) lgkmcnt(6)\n"
    "s_barrier\n"
    "s_add_u32 m0, s45, 0x8000\n"
    "v_xor_b32 v10, 0x10000, v10\n"
    "ds_read_b64_tr_b16 a[224:225], v10\n"
    "ds_read_b64_tr_b16 a[226:227], v10 offset:512\n"
    "buffer_load_dwordx4 v17, s[20:23], 0 offen lds\n"
    "v_mfma_f32_32x32x16_bf16 v[176:191], a[240:243], a[176:179], v[176:191]\n"
    "s_waitcnt lgkmcnt(6)\n"
    "ds_read_b64_tr_b16 a[228:229], v10 offset:2048\n"
    "ds_read_b64_tr_b16 a[230:231], v10 offset:2560\n"
    "v_mfma_f32_32x32x16_bf16 v[176:191], a[244:247], a[180:183], v[176:191]\n"
    "s_waitcnt lgkmcnt(6)\n"
    "ds_read_b64_tr_b16 a[232:233], v10 offset:4096\n"
    "ds_read_b64_tr_b16 a[234:235], v10 offset:4608\n"
    "buffer_load_dwordx4 v17, s[20:23], s53 offen offset:1024 lds\n"
    "v_mfma_f32_32x32x16_bf16 v[176:191], a[248:251], a[184:187], v[176:191]\n"
    "s_waitcnt lgkmcnt(6)\n"
    "ds_read_b64_tr_b16 a[236:237], v10 offset:6144\n"
    "ds_read_b64_tr_b16 a[238:239], v10 offset:6656\n"
    "v_mfma_f32_32x32x16_bf16 v[176:191], a[252:255], a[188:191], v[176:191]\n"
    "s_waitcnt lgkmcnt(6)\n"
    "v_xor_b32 v11, 0x10000, v11\n"
    "ds_read_b64_tr_b16 a[240:241], v11\n"
    "ds_read_b64_tr_b16 a[242:243], v11 offset:512\n"
    "buffer_load_dwordx4 v17, s[20:23], s54 offen offset:2048 lds\n"
    "v_mfma_f32_32x32x16_bf16 v[128:143], a[224:227], v[64:67], 0\n"
    "s_waitcnt lgkmcnt(6)\n"
    "ds_read_b64_tr_b16 a[244:245], v11 offset:2048\n"
    "ds_read_b64_tr_b16 a[246:247], v11 offset:2560\n"
    "v_mfma_f32_32x32x16_bf16 v[128:143], a[228:231], v[68:71], v[128:143]\n"
    "s_waitcnt lgkmcnt(6)\n"
    "ds_read_b64_tr_b16 a[248:249], v11 offset:4096\n"
    "ds_read_b64_tr_b16 a[250:251], v11 offset:4608\n"
    "buffer_load_dwordx4 v17, s[20:23], s55 offen offset:3072 lds\n"
    "v_add_u32 v17, 0xe000, v17\n"
    "v_mul_f32 v160, v160, v14\n"
    "v_mfma_f32_32x32x16_bf16 v[128:143], a[232:235], v[72:75], v[128:143]\n"
    "s_waitcnt lgkmcnt(6)\n"
    "ds_read_b64_tr_b16 a[252:253], v11 offset:6144\n"
    "ds_read_b64_tr_b16 a[254:255], v11 offset:6656\n"
    "v_mul_f32 v161, v161, v14\n"
    "v_mul_f32 v162, v162, v14\n"
    "v_mul_f32 v163, v163, v14\n"
    "v_fma_f32 v160, v176, v18, v160\n"
    "v_mfma_f32_32x32x16_bf16 v[128:143], a[236:239], v[76:79], v[128:143]\n"
    "s_waitcnt vmcnt(14) lgkmcnt(6)\n"
    "s_barrier\n"
    "s_add_u32 m0, s45, 0xc000\n"
    "ds_read_b64_tr_b16 a[224:225], v10 offset:16384\n"
    "ds_read_b64_tr_b16 a[226:227], v10 offset:16896\n"
    "buffer_load_dwordx4 v17, s[20:23], 0 offen lds\n"
    "v_mfma_f32_32x32x16_bf16 v[144:159], a[240:243], a[128:131], 0\n"
    "s_waitcnt lgkmcnt(6)\n"
    "ds_read_b64_tr_b16 a[228:229], v10 offset:18432\n"
    "ds_read_b64_tr_b16 a[230:231], v10 offset:18944\n"
    "v_fma_f32 v161, v177, v18, v161\n"
    "v_fma_f32 v162, v178, v18, v162\n"
    "v_fma_f32 v163, v179, v18, v163\n"
    "v_cvt_pk_bf16_f32 v200, v160, v161\n"
    "v_mfma_f32_32x32x16_bf16 v[144:159], a[244:247], a[132:135], v[144:159]\n"
    "s_waitcnt lgkmcnt(6)\n"
    "ds_read_b64_tr_b16 a[232:233], v10 offset:20480\n"
    "ds_read_b64_tr_b16 a[234:235], v10 offset:20992\n"
    "buffer_load_dwordx4 v17, s[20:23], s53 offen offset:1024 lds\n"
    "v_cvt_pk_bf16_f32 v201, v162, v163\n"
    "v_mul_f32 v164, v164, v14\n"
    "v_mfma_f32_32x32x16_bf16 v[144:159], a[248:251], a[136:139], v[144:159]\n"
    "s_waitcnt lgkmcnt(6)\n"
    "ds_read_b64_tr_b16 a[236:237], v10 offset:22528\n"
    "ds_read_b64_tr_b16 a[238:239], v10 offset:23040\n"
    "v_mul_f32 v165, v165, v14\n"
    "v_mul_f32 v166, v166, v14\n"
    "v_mul_f32 v167, v167, v14\n"
    "v_fma_f32 v164, v180, v18, v164\n"
    "v_mfma_f32_32x32x16_bf16 v[144:159], a[252:255], a[140:143], v[144:159]\n"
    "s_waitcnt lgkmcnt(6)\n"
    "ds_read_b64_tr_b16 a[240:241], v11 offset:16384\n"
    "ds_read_b64_tr_b16 a[242:243], v11 offset:16896\n"
    "buffer_load_dwordx4 v17, s[20:23], s54 offen offset:2048 lds\n"
    "v_fma_f32 v165, v181, v18, v165\n"
    "v_fma_f32 v166, v182, v18, v166\n"
    "v_mfma_f32_32x32x16_bf16 v[128:143], a[224:227], v[80:83], v[128:143]\n"
    "s_waitcnt lgkmcnt(6)\n"
    "ds_read_b64_tr_b16 a[244:245], v11 offset:18432\n"
    "ds_read_b64_tr_b16 a[246:247], v11 offset:18944\n"
    "v_fma_f32 v167, v183, v18, v167\n"
    "v_cvt_pk_bf16_f32 v202, v164, v165\n"
    "v_cvt_pk_bf16_f32 v203, v166, v167\n"
    "s_mov_b32 s63, 0x3000\n"
    "v_mfma_f32_32x32x16_bf16 v[128:143], a[228:231], v[84:87], v[128:143]\n"
    "s_waitcnt lgkmcnt(6)\n"
    "ds_read_b64_tr_b16 a[248:249], v11 offset:20480\n"
    "ds_read_b64_tr_b16 a[250:251], v11 offset:20992\n"
    "buffer_load_dwordx4 v17, s[20:23], s55 offen offset:3072 lds\n"
    "v_add_u32 v17, 0xe000, v17\n"
    "v_mfma_f32_32x32x16_bf16 v[128:143], a[232:235], v[88:91], v[128:143]\n"
    "s_waitcnt lgkmcnt(6)\n"
    "ds_read_b64_tr_b16 a[252:253], v11 offset:22528\n"
    "ds_read_b64_tr_b16 a[254:255], v11 offset:23040\n"
    "buffer_store_dwordx4 v[200:203], v13, s[28:31], s63 offen sc1\n"
    "v_mul_f32 v168, v168, v14\n"
    "v_mul_f32 v169, v169, v14\n"
    "v_mfma_f32_32x32x16_bf16 v[128:143], a[236:239], v[92:95], v[128:143]\n"
    "s_waitcnt vmcnt(14) lgkmcnt(6)\n"
    "s_barrier\n"
    "s_add_u32 m0, s45, 0x10000\n"
    "v_xor_b32 v10, 0x10000, v10\n"
    "ds_read_b64_tr_b16 a[224:225], v10\n"
    "ds_read_b64_tr_b16 a[226:227], v10 offset:512\n"
    "buffer_load_dwordx4 v17, s[20:23], 0 offen lds\n"
    "v_mfma_f32_32x32x16_bf16 v[144:159], a[240:243], a[144:147], v[144:159]\n"
    "s_waitcnt lgkmcnt(6)\n"
    "ds_read_b64_tr_b16 a[228:229], v10 offset:2048\n"
    "ds_read_b64_tr_b16 a[230:231], v10 offset:2560\n"
    "v_mul_f32 v170, v170, v14\n"
    "v_mul_f32 v171, v171, v14\n"
    "v_fma_f32 v168, v184, v18, v168\n"
    "v_fma_f32 v169, v185, v18, v169\n"
    "v_mfma_f32_32x32x16_bf16 v[144:159], a[244:247], a[148:151], v[144:159]\n"
    "s_waitcnt lgkmcnt(6)\n"
    "ds_read_b64_tr_b16 a[232:233], v10 offset:4096\n"
    "ds_read_b64_tr_b16 a[234:235], v10 offset:4608\n"
    "buffer_load_dwordx4 v17, s[20:23], s53 offen offset:1024 lds\n"
    "v_fma_f32 v170, v186, v18, v170\n"
    "v_fma_f32 v171, v187, v18, v171\n"
    "v_mfma_f32_32x32x16_bf16 v[144:159], a[248:251], a[152:155], v[144:159]\n"
    "s_waitcnt lgkmcnt(6)\n"
    "ds_read_b64_tr_b16 a[236:237], v10 offset:6144\n"
    "ds_read_b64_tr_b16 a[238:239], v10 offset:6656\n"
    "v_cvt_pk_bf16_f32 v204, v168, v169\n"
    "v_cvt_pk_bf16_f32 v205, v170, v171\n"
    "v_mul_f32 v172, v172, v14\n"
    "v_mul_f32 v173, v173, v14\n"
    "v_mfma_f32_32x32x16_bf16 v[144:159], a[252:255], a[156:159], v[144:159]\n"
    "s_waitcnt lgkmcnt(6)\n"
    "v_xor_b32 v11, 0x10000, v11\n"
    "ds_read_b64_tr_b16 a[240:241], v11\n"
    "ds_read_b64_tr_b16 a[242:243], v11 offset:512\n"
    "buffer_load_dwordx4 v17, s[20:23], s54 offen offset:2048 lds\n"
    "v_mul_f32 v174, v174, v14\n"
    "v_mfma_f32_32x32x16_bf16 v[128:143], a[224:227], v[96:99], v[128:143]\n"
    "s_waitcnt lgkmcnt(6)\n"
    "ds_read_b64_tr_b16 a[244:245], v11 offset:2048\n"
    "ds_read_b64_tr_b16 a[246:247], v11 offset:2560\n"
    "v_mul_f32 v175, v175, v14\n"
    "v_fma_f32 v172, v188, v18, v172\n"
    "v_fma_f32 v173, v189, v18, v173\n"
    "v_fma_f32 v174, v190, v18, v174\n"
    "v_mfma_f32_32x32x16_bf16 v[128:143], a[228:231], v[100:103], v[128:143]\n"
    "s_waitcnt lgkmcnt(6)\n"
    "ds_read_b64_tr_b16 a[248:249], v11 offset:4096\n"
    "ds_read_b64_tr_b16 a[250:251], v11 offset:4608\n"
    "buffer_load_dwordx4 v17, s[20:23], s55 offen offset:3072 lds\n"
    "v_add_u32 v17, 0xe000, v17\n"
    "v_fma_f32 v175, v191, v18, v175\n"
    "v_mfma_f32_32x32x16_bf16 v[128:143], a[232:235], v[104:107], v[128:143]\n"
    "s_waitcnt lgkmcnt(6)\n"
    "ds_read_b64_tr_b16 a[252:253], v11 offset:6144\n"
    "ds_read_b64_tr_b16 a[254:255], v11 offset:6656\n"
    "v_cvt_pk_bf16_f32 v206, v172, v173\n"
    "v_cvt_pk_bf16_f32 v207, v174, v175\n"
    "buffer_store_dwordx4 v[204:207], v13, s[28:31], s63 offen offset:1024 sc1\n"
    "v_mfma_f32_32x32x16_bf16 v[128:143], a[236:239], v[108:111], v[128:143]\n"
    "s_waitcnt vmcnt(14) lgkmcnt(6)\n"
    "s_barrier\n"
    "s_add_u32 m0, s45, 0x14000\n"
    "ds_read_b64_tr_b16 a[224:225], v10 offset:16384\n"
    "ds_read_b64_tr_b16 a[226:227], v10 offset:16896\n"
    "buffer_load_dwordx4 v17, s[20:23], 0 offen lds\n"
    "v_mfma_f32_32x32x16_bf16 v[144:159], a[240:243], a[160:163], v[144:159]\n"
    "s_waitcnt lgkmcnt(6)\n"
    "ds_read_b64_tr_b16 a[228:229], v10 offset:18432\n"
    "ds_read_b64_tr_b16 a[230:231], v10 offset:18944\n"
    "v_mfma_f32_32x32x16_bf16 v[144:159], a[244:247], a[164:167], v[144:159]\n"
    "s_waitcnt lgkmcnt(6)\n"
    "ds_read_b64_tr_b16 a[232:233], v10 offset:20480\n"
    "ds_read_b64_tr_b16 a[234:235], v10 offset:20992\n"
    "buffer_load_dwordx4 v17, s[20:23], s53 offen offset:1024 lds\n"
    "v_mfma_f32_32x32x16_bf16 v[144:159], a[248:251], a[168:171], v[144:159]\n"
    "s_waitcnt lgkmcnt(6)\n"
    "ds_read_b64_tr_b16 a[236:237], v10 offset:22528\n"
    "ds_read_b64_tr_b16 a[238:239], v10 offset:23040\n"
    "v_mfma_f32_32x32x16_bf16 v[144:159], a[252:255], a[172:175], v[144:159]\n"
    "s_waitcnt lgkmcnt(6)\n"
    "ds_read_b64_tr_b16 a[240:241], v11 offset:16384\n"
    "ds_read_b64_tr_b16 a[242:243], v11 offset:16896\n"
    "buffer_load_dwordx4 v17, s[20:23], s54 offen offset:2048 lds\n"
    "v_mfma_f32_32x32x16_bf16 v[128:143], a[224:227], v[112:115], v[128:143]\n"
    "s_waitcnt lgkmcnt(6)\n"
    "ds_read_b64_tr_b16 a[244:245], v11 offset:18432\n"
    "ds_read_b64_tr_b16 a[246:247], v11 offset:18944\n"
    "v_mfma_f32_32x32x16_bf16 v[128:143], a[228:231], v[116:119], v[128:143]\n"
    "s_waitcnt lgkmcnt(6)\n"
    "ds_read_b64_tr_b16 a[248:249], v11 offset:20480\n"
    "ds_read_b64_tr_b16 a[250:251], v11 offset:20992\n"
    "buffer_load_dwordx4 v17, s[20:23], s55 offen offset:3072 lds\n"
    "v_add_u32 v17, 0xfffd6080, v17\n"
    "v_mfma_f32_32x32x16_bf16 v[128:143], a[232:235], v[120:123], v[128:143]\n"
    "s_waitcnt lgkmcnt(6)\n"
    "ds_read_b64_tr_b16 a[252:253], v11 offset:22528\n"
    "ds_read_b64_tr_b16 a[254:255], v11 offset:23040\n"
    "v_mfma_f32_32x32x16_bf16 v[128:143], a[236:239], v[124:127], v[128:143]\n"
    "s_waitcnt vmcnt(14) lgkmcnt(6)\n"
    "s_barrier\n"
    "s_add_u32 m0, s45, 0\n"
    "ds_read_b64_tr_b16 a[224:225], v10 offset:32768\n"
    "ds_read_b64_tr_b16 a[226:227], v10 offset:33280\n"
    "buffer_load_dwordx4 v17, s[20:23], 0 offen lds\n"
    "v_mfma_f32_32x32x16_bf16 v[144:159], a[240:243], a[176:179], v[144:159]\n"
    "s_waitcnt lgkmcnt(6)\n"
    "ds_read_b64_tr_b16 a[228:229], v10 offset:34816\n"
    "ds_read_b64_tr_b16 a[230:231], v10 offset:35328\n"
    "v_mfma_f32_32x32x16_bf16 v[144:159], a[244:247], a[180:183], v[144:159]\n"
    "s_waitcnt lgkmcnt(6)\n"
    "ds_read_b64_tr_b16 a[232:233], v10 offset:36864\n"
    "ds_read_b64_tr_b16 a[234:235], v10 offset:37376\n"
    "buffer_load_dwordx4 v17, s[20:23], s53 offen offset:1024 lds\n"
    "v_mfma_f32_32x32x16_bf16 v[144:159], a[248:251], a[184:187], v[144:159]\n"
    "s_waitcnt lgkmcnt(6)\n"
    "ds_read_b64_tr_b16 a[236:237], v10 offset:38912\n"
    "ds_read_b64_tr_b16 a[238:239], v10 offset:39424\n"
    "v_mfma_f32_32x32x16_bf16 v[144:159], a[252:255], a[188:191], v[144:159]\n"
    "s_waitcnt lgkmcnt(6)\n"
    "ds_read_b64_tr_b16 a[240:241], v11 offset:32768\n"
    "ds_read_b64_tr_b16 a[242:243], v11 offset:33280\n"
    "buffer_load_dwordx4 v17, s[20:23], s54 offen offset:2048 lds\n"
    "v_mfma_f32_32x32x16_bf16 v[160:175], a[224:227], v[64:67], 0\n"
    "s_waitcnt lgkmcnt(6)\n"
    "ds_read_b64_tr_b16 a[244:245], v11 offset:34816\n"
    "ds_read_b64_tr_b16 a[246:247], v11 offset:35328\n"
    "v_mfma_f32_32x32x16_bf16 v[160:175], a[228:231], v[68:71], v[160:175]\n"
    "s_waitcnt lgkmcnt(6)\n"
    "ds_read_b64_tr_b16 a[248:249], v11 offset:36864\n"
    "ds_read_b64_tr_b16 a[250:251], v11 offset:37376\n"
    "buffer_load_dwordx4 v17, s[20:23], s55 offen offset:3072 lds\n"
    "v_add_u32 v17, 0xe000, v17\n"
    "v_mul_f32 v128, v128, v14\n"
    "v_mfma_f32_32x32x16_bf16 v[160:175], a[232:235], v[72:75], v[160:175]\n"
    "s_waitcnt lgkmcnt(6)\n"
    "ds_read_b64_tr_b16 a[252:253], v11 offset:38912\n"
    "ds_read_b64_tr_b16 a[254:255], v11 offset:39424\n"
    "v_mul_f32 v129, v129, v14\n"
    "v_mul_f32 v130, v130, v14\n"
    "v_mul_f32 v131, v131, v14\n"
    "v_fma_f32 v128, v144, v18, v128\n"
    "v_mfma_f32_32x32x16_bf16 v[160:175], a[236:239], v[76:79], v[160:175]\n"
    "s_waitcnt vmcnt(14) lgkmcnt(6)\n"
    "s_barrier\n"
    "s_add_u32 m0, s45, 0x4000\n"
    "ds_read_b64_tr_b16 a[224:225], v10 offset:49152\n"
    "ds_read_b64_tr_b16 a[226:227], v10 offset:49664\n"
    "buffer_load_dwordx4 v17, s[20:23], 0 offen lds\n"
    "v_mfma_f32_32x32x16_bf16 v[176:191], a[240:243], a[128:131], 0\n"
    "s_waitcnt lgkmcnt(6)\n"
    "ds_read_b64_tr_b16 a[228:229], v10 offset:51200\n"
    "ds_read_b64_tr_b16 a[230:231], v10 offset:51712\n"
    "v_fma_f32 v129, v145, v18, v129\n"
    "v_fma_f32 v130, v146, v18, v130\n"
    "v_fma_f32 v131, v147, v18, v131\n"
    "v_cvt_pk_bf16_f32 v192, v128, v129\n"
    "v_mfma_f32_32x32x16_bf16 v[176:191], a[244:247], a[132:135], v[176:191]\n"
    "s_waitcnt lgkmcnt(6)\n"
    "ds_read_b64_tr_b16 a[232:233], v10 offset:53248\n"
    "ds_read_b64_tr_b16 a[234:235], v10 offset:53760\n"
    "buffer_load_dwordx4 v17, s[20:23], s53 offen offset:1024 lds\n"
    "v_cvt_pk_bf16_f32 v193, v130, v131\n"
    "v_mul_f32 v132, v132, v14\n"
    "v_mfma_f32_32x32x16_bf16 v[176:191], a[248:251], a[136:139], v[176:191]\n"
    "s_waitcnt lgkmcnt(6)\n"
    "ds_read_b64_tr_b16 a[236:237], v10 offset:55296\n"
    "ds_read_b64_tr_b16 a[238:239], v10 offset:55808\n"
    "v_mul_f32 v133, v133, v14\n"
    "v_mul_f32 v134, v134, v14\n"
    "v_mul_f32 v135, v135, v14\n"
    "v_fma_f32 v132, v148, v18, v132\n"
    "v_mfma_f32_32x32x16_bf16 v[176:191], a[252:255], a[140:143], v[176:191]\n"
    "s_waitcnt lgkmcnt(6)\n"
    "ds_read_b64_tr_b16 a[240:241], v11 offset:49152\n"
    "ds_read_b64_tr_b16 a[242:243], v11 offset:49664\n"
    "buffer_load_dwordx4 v17, s[20:23], s54 offen offset:2048 lds\n"
    "v_fma_f32 v133, v149, v18, v133\n"
    "v_fma_f32 v134, v150, v18, v134\n"
    "v_mfma_f32_32x32x16_bf16 v[160:175], a[224:227], v[80:83], v[160:175]\n"
    "s_waitcnt lgkmcnt(6)\n"
    "ds_read_b64_tr_b16 a[244:245], v11 offset:51200\n"
    "ds_read_b64_tr_b16 a[246:247], v11 offset:51712\n"
    "v_fma_f32 v135, v151, v18, v135\n"
    "v_cvt_pk_bf16_f32 v194, v132, v133\n"
    "v_cvt_pk_bf16_f32 v195, v134, v135\n"
    "s_mov_b32 s63, 0x4000\n"
    "v_mfma_f32_32x32x16_bf16 v[160:175], a[228:231], v[84:87], v[160:175]\n"
    "s_waitcnt lgkmcnt(6)\n"
    "ds_read_b64_tr_b16 a[248:249], v11 offset:53248\n"
    "ds_read_b64_tr_b16 a[250:251], v11 offset:53760\n"
    "buffer_load_dwordx4 v17, s[20:23], s55 offen offset:3072 lds\n"
    "v_add_u32 v17, 0xe000, v17\n"
    "v_mfma_f32_32x32x16_bf16 v[160:175], a[232:235], v[88:91], v[160:175]\n"
    "s_waitcnt lgkmcnt(6)\n"
    "ds_read_b64_tr_b16 a[252:253], v11 offset:55296\n"
    "ds_read_b64_tr_b16 a[254:255], v11 offset:55808\n"
    "buffer_store_dwordx4 v[192:195], v13, s[28:31], s63 offen sc1\n"
    "v_mul_f32 v136, v136, v14\n"
    "v_mul_f32 v137, v137, v14\n"
    "v_mfma_f32_32x32x16_bf16 v[160:175], a[236:239], v[92:95], v[160:175]\n"
    "s_waitcnt vmcnt(14) lgkmcnt(6)\n"
    "s_barrier\n"
    "s_add_u32 m0, s45, 0x8000\n"
    "v_xor_b32 v10, 0x10000, v10\n"
    "ds_read_b64_tr_b16 a[224:225], v10\n"
    "ds_read_b64_tr_b16 a[226:227], v10 offset:512\n"
    "buffer_load_dwordx4 v17, s[20:23], 0 offen lds\n"
    "v_mfma_f32_32x32x16_bf16 v[176:191], a[240:243], a[144:147], v[176:191]\n"
    "s_waitcnt lgkmcnt(6)\n"
    "ds_read_b64_tr_b16 a[228:229], v10 offset:2048\n"
    "ds_read_b64_tr_b16 a[230:231], v10 offset:2560\n"
    "v_mul_f32 v138, v138, v14\n"
    "v_mul_f32 v139, v139, v14\n"
    "v_fma_f32 v136, v152, v18, v136\n"
    "v_fma_f32 v137, v153, v18, v137\n"
    "v_mfma_f32_32x32x16_bf16 v[176:191], a[244:247], a[148:151], v[176:191]\n"
    "s_waitcnt lgkmcnt(6)\n"
    "ds_read_b64_tr_b16 a[232:233], v10 offset:4096\n"
    "ds_read_b64_tr_b16 a[234:235], v10 offset:4608\n"
    "buffer_load_dwordx4 v17, s[20:23], s53 offen offset:1024 lds\n"
    "v_fma_f32 v138, v154, v18, v138\n"
    "v_fma_f32 v139, v155, v18, v139\n"
    "v_mfma_f32_32x32x16_bf16 v[176:191], a[248:251], a[152:155], v[176:191]\n"
    "s_waitcnt lgkmcnt(6)\n"
    "ds_read_b64_tr_b16 a[236:237], v10 offset:6144\n"
    "ds_read_b64_tr_b16 a[238:239], v10 offset:6656\n"
    "v_cvt_pk_bf16_f32 v196, v136, v137\n"
    "v_cvt_pk_bf16_f32 v197, v138, v139\n"
    "v_mul_f32 v140, v140, v14\n"
    "v_mul_f32 v141, v141, v14\n"
    "v_mfma_f32_32x32x16_bf16 v[176:191], a[252:255], a[156:159], v[176:191]\n"
    "s_waitcnt lgkmcnt(6)\n"
    "v_xor_b32 v11, 0x10000, v11\n"
    "ds_read_b64_tr_b16 a[240:241], v11\n"
    "ds_read_b64_tr_b16 a[242:243], v11 offset:512\n"
    "buffer_load_dwordx4 v17, s[20:23], s54 offen offset:2048 lds\n"
    "v_mul_f32 v142, v142, v14\n"
    "v_mfma_f32_32x32x16_bf16 v[160:175], a[224:227], v[96:99], v[160:175]\n"
    "s_waitcnt lgkmcnt(6)\n"
    "ds_read_b64_tr_b16 a[244:245], v11 offset:2048\n"
    "ds_read_b64_tr_b16 a[246:247], v11 offset:2560\n"
    "v_mul_f32 v143, v143, v14\n"
    "v_fma_f32 v140, v156, v18, v140\n"
    "v_fma_f32 v141, v157, v18, v141\n"
    "v_fma_f32 v142, v158, v18, v142\n"
    "v_mfma_f32_32x32x16_bf16 v[160:175], a[228:231], v[100:103], v[160:175]\n"
    "s_waitcnt lgkmcnt(6)\n"
    "ds_read_b64_tr_b16 a[248:249], v11 offset:4096\n"
    "ds_read_b64_tr_b16 a[250:251], v11 offset:4608\n"
    "buffer_load_dwordx4 v17, s[20:23], s55 offen offset:3072 lds\n"
    "v_add_u32 v17, 0xe000, v17\n"
    "v_fma_f32 v143, v159, v18, v143\n"
    "v_mfma_f32_32x32x16_bf16 v[160:175], a[232:235], v[104:107], v[160:175]\n"
    "s_waitcnt lgkmcnt(6)\n"
    "ds_read_b64_tr_b16 a[252:253], v11 offset:6144\n"
    "ds_read_b64_tr_b16 a[254:255], v11 offset:6656\n"
    "v_cvt_pk_bf16_f32 v198, v140, v141\n"
    "v_cvt_pk_bf16_f32 v199, v142, v143\n"
    "buffer_store_dwordx4 v[196:199], v13, s[28:31], s63 offen offset:1024 sc1\n"
    "v_mfma_f32_32x32x16_bf16 v[160:175], a[236:239], v[108:111], v[160:175]\n"
    "s_waitcnt vmcnt(14) lgkmcnt(6)\n"
    "s_barrier\n"
    "s_add_u32 m0, s45, 0xc000\n"
    "ds_read_b64_tr_b16 a[224:225], v10 offset:16384\n"
    "ds_read_b64_tr_b16 a[226:227], v10 offset:16896\n"
    "buffer_load_dwordx4 v17, s[20:23], 0 offen lds\n"
    "v_mfma_f32_32x32x16_bf16 v[176:191], a[240:243], a[160:163], v[176:191]\n"
    "s_waitcnt lgkmcnt(6)\n"
    "ds_read_b64_tr_b16 a[228:229], v10 offset:18432\n"
    "ds_read_b64_tr_b16 a[230:231], v10 offset:18944\n"
    "v_mfma_f32_32x32x16_bf16 v[176:191], a[244:247], a[164:167], v[176:191]\n"
    "s_waitcnt lgkmcnt(6)\n"
    "ds_read_b64_tr_b16 a[232:233], v10 offset:20480\n"
    "ds_read_b64_tr_b16 a[234:235], v10 offset:20992\n"
    "v_mfma_f32_32x32x16_bf16 v[176:191], a[248:251], a[168:171], v[176:191]\n"
    "s_waitcnt lgkmcnt(6)\n"
    "ds_read_b64_tr_b16 a[236:237], v10 offset:22528\n"
    "ds_read_b64_tr_b16 a[238:239], v10 offset:23040\n"
    "v_mfma_f32_32x32x16_bf16 v[176:191], a[252:255], a[172:175], v[176:191]\n"
    "s_waitcnt lgkmcnt(6)\n"
    "ds_read_b64_tr_b16 a[240:241], v11 offset:16384\n"
    "ds_read_b64_tr_b16 a[242:243], v11 offset:16896\n"
    "buffer_load_dwordx4 v17, s[20:23], s53 offen offset:1024 lds\n"
    "v_mfma_f32_32x32x16_bf16 v[160:175], a[224:227], v[112:115], v[160:175]\n"
    "s_waitcnt lgkmcnt(6)\n"
    "ds_read_b64_tr_b16 a[244:245], v11 offset:18432\n"
    "ds_read_b64_tr_b16 a[246:247], v11 offset:18944\n"
    "v_mfma_f32_32x32x16_bf16 v[160:175], a[228:231], v[116:119], v[160:175]\n"
    "s_waitcnt lgkmcnt(6)\n"
    "ds_read_b64_tr_b16 a[248:249], v11 offset:20480\n"
    "ds_read_b64_tr_b16 a[250:251], v11 offset:20992\n"
    "v_mfma_f32_32x32x16_bf16 v[160:175], a[232:235], v[120:123], v[160:175]\n"
    "s_waitcnt lgkmcnt(6)\n"
    "ds_read_b64_tr_b16 a[252:253], v11 offset:22528\n"
    "ds_read_b64_tr_b16 a[254:255], v11 offset:23040\n"
    "v_mfma_f32_32x32x16_bf16 v[160:175], a[236:239], v[124:127], v[160:175]\n"
    "s_waitcnt vmcnt(12) lgkmcnt(6)\n"
    "s_barrier\n"
    "v_xor_b32 v10, 0x10000, v10\n"
    "ds_read_b64_tr_b16 a[224:225], v10\n"
    "ds_read_b64_tr_b16 a[226:227], v10 offset:512\n"
    "buffer_load_dwordx4 v17, s[20:23], s54 offen offset:2048 lds\n"
    "v_mfma_f32_32x32x16_bf16 v[176:191], a[240:243], a[176:179], v[176:191]\n"
    "s_waitcnt lgkmcnt(6)\n"
    "ds_read_b64_tr_b16 a[228:229], v10 offset:2048\n"
    "ds_read_b64_tr_b16 a[230:231], v10 offset:2560\n"
    "v_mfma_f32_32x32x16_bf16 v[176:191], a[244:247], a[180:183], v[176:191]\n"
    "s_waitcnt lgkmcnt(6)\n"
    "ds_read_b64_tr_b16 a[232:233], v10 offset:4096\n"
    "ds_read_b64_tr_b16 a[234:235], v10 offset:4608\n"
    "v_mfma_f32_32x32x16_bf16 v[176:191], a[248:251], a[184:187], v[176:191]\n"
    "s_waitcnt lgkmcnt(6)\n"
    "ds_read_b64_tr_b16 a[236:237], v10 offset:6144\n"
    "ds_read_b64_tr_b16 a[238:239], v10 offset:6656\n"
    "v_mfma_f32_32x32x16_bf16 v[176:191], a[252:255], a[188:191], v[176:191]\n"
    "s_waitcnt lgkmcnt(6)\n"
    "v_xor_b32 v11, 0x10000, v11\n"
    "ds_read_b64_tr_b16 a[240:241], v11\n"
    "ds_read_b64_tr_b16 a[242:243], v11 offset:512\n"
    "buffer_load_dwordx4 v17, s[20:23], s55 offen offset:3072 lds\n"
    "v_add_u32 v17, 0xfffd6080, v17\n"
    "v_mfma_f32_32x32x16_bf16 v[128:143], a[224:227], v[64:67], 0\n"
    "s_waitcnt lgkmcnt(6)\n"
    "ds_read_b64_tr_b16 a[244:245], v11 offset:2048\n"
    "ds_read_b64_tr_b16 a[246:247], v11 offset:2560\n"
    "v_mfma_f32_32x32x16_bf16 v[128:143], a[228:231], v[68:71], v[128:143]\n"
    "s_waitcnt lgkmcnt(6)\n"
    "ds_read_b64_tr_b16 a[248:249], v11 offset:4096\n"
    "ds_read_b64_tr_b16 a[250:251], v11 offset:4608\n"
    "v_mul_f32 v160, v160, v14\n"
    "v_mul_f32 v161, v161, v14\n"
    "v_mul_f32 v162, v162, v14\n"
    "v_mul_f32 v163, v163, v14\n"
    "v_mfma_f32_32x32x16_bf16 v[128:143], a[232:235], v[72:75], v[128:143]\n"
    "s_waitcnt lgkmcnt(6)\n"
    "ds_read_b64_tr_b16 a[252:253], v11 offset:6144\n"
    "ds_read_b64_tr_b16 a[254:255], v11 offset:6656\n"
    "v_fma_f32 v160, v176, v18, v160\n"
    "v_fma_f32 v161, v177, v18, v161\n"
    "v_fma_f32 v162, v178, v18, v162\n"
    "v_fma_f32 v163, v179, v18, v163\n"
    "v_mfma_f32_32x32x16_bf16 v[128:143], a[236:239], v[76:79], v[128:143]\n"
    "s_waitcnt vmcnt(10) lgkmcnt(6)\n"
    "s_barrier\n"
    "ds_read_b64_tr_b16 a[224:225], v10 offset:16384\n"
    "ds_read_b64_tr_b16 a[226:227], v10 offset:16896\n"
    "v_cvt_pk_bf16_f32 v200, v160, v161\n"
    "v_cvt_pk_bf16_f32 v201, v162, v163\n"
    "v_mul_f32 v164, v164, v14\n"
    "v_mfma_f32_32x32x16_bf16 v[144:159], a[240:243], a[128:131], 0\n"
    "s_waitcnt lgkmcnt(6)\n"
    "ds_read_b64_tr_b16 a[228:229], v10 offset:18432\n"
    "ds_read_b64_tr_b16 a[230:231], v10 offset:18944\n"
    "v_mul_f32 v165, v165, v14\n"
    "v_mul_f32 v166, v166, v14\n"
    "v_mul_f32 v167, v167, v14\n"
    "v_fma_f32 v164, v180, v18, v164\n"
    "v_mfma_f32_32x32x16_bf16 v[144:159], a[244:247], a[132:135], v[144:159]\n"
    "s_waitcnt lgkmcnt(6)\n"
    "ds_read_b64_tr_b16 a[232:233], v10 offset:20480\n"
    "ds_read_b64_tr_b16 a[234:235], v10 offset:20992\n"
    "v_fma_f32 v165, v181, v18, v165\n"
    "v_fma_f32 v166, v182, v18, v166\n"
    "v_fma_f32 v167, v183, v18, v167\n"
    "v_cvt_pk_bf16_f32 v202, v164, v165\n"
    "v_mfma_f32_32x32x16_bf16 v[144:159], a[248:251], a[136:139], v[144:159]\n"
    "s_waitcnt lgkmcnt(6)\n"
    "ds_read_b64_tr_b16 a[236:237], v10 offset:22528\n"
    "ds_read_b64_tr_b16 a[238:239], v10 offset:23040\n"
    "v_cvt_pk_bf16_f32 v203, v166, v167\n"
    "s_mov_b32 s63, 0x5000\n"
    "buffer_store_dwordx4 v[200:203], v13, s[28:31], s63 offen sc1\n"
    "v_mfma_f32_32x32x16_bf16 v[144:159], a[252:255], a[140:143], v[144:159]\n"
    "s_waitcnt lgkmcnt(6)\n"
    "ds_read_b64_tr_b16 a[240:241], v11 offset:16384\n"
    "ds_read_b64_tr_b16 a[242:243], v11 offset:16896\n"
    "v_mul_f32 v168, v168, v14\n"
    "v_mul_f32 v169, v169, v14\n"
    "v_mul_f32 v170, v170, v14\n"
    "v_mul_f32 v171, v171, v14\n"
    "v_mfma_f32_32x32x16_bf16 v[128:143], a[224:227], v[80:83], v[128:143]\n"
    "s_waitcnt lgkmcnt(6)\n"
    "ds_read_b64_tr_b16 a[244:245], v11 offset:18432\n"
    "ds_read_b64_tr_b16 a[246:247], v11 offset:18944\n"
    "v_fma_f32 v168, v184, v18, v168\n"
    "v_fma_f32 v169, v185, v18, v169\n"
    "v_fma_f32 v170, v186, v18, v170\n"
    "v_fma_f32 v171, v187, v18, v171\n"
    "v_mfma_f32_32x32x16_bf16 v[128:143], a[228:231], v[84:87], v[128:143]\n"
    "s_waitcnt lgkmcnt(6)\n"
    "ds_read_b64_tr_b16 a[248:249], v11 offset:20480\n"
    "ds_read_b64_tr_b16 a[250:251], v11 offset:20992\n"
    "v_cvt_pk_bf16_f32 v204, v168, v169\n"
    "v_cvt_pk_bf16_f32 v205, v170, v171\n"
    "v_mul_f32 v172, v172, v14\n"
    "v_mul_f32 v173, v173, v14\n"
    "v_mfma_f32_32x32x16_bf16 v[128:143], a[232:235], v[88:91], v[128:143]\n"
    "s_waitcnt lgkmcnt(6)\n"
    "ds_read_b64_tr_b16 a[252:253], v11 offset:22528\n"
    "ds_read_b64_tr_b16 a[254:255], v11 offset:23040\n"
    "v_mul_f32 v174, v174, v14\n"
    "v_mul_f32 v175, v175, v14\n"
    "v_fma_f32 v172, v188, v18, v172\n"
    "v_fma_f32 v173, v189, v18, v173\n"
    "v_mfma_f32_32x32x16_bf16 v[128:143], a[236:239], v[92:95], v[128:143]\n"
    "s_waitcnt vmcnt(6) lgkmcnt(6)\n"
    "s_barrier\n"
    "ds_read_b64_tr_b16 a[224:225], v10 offset:32768\n"
    "ds_read_b64_tr_b16 a[226:227], v10 offset:33280\n"
    "v_fma_f32 v174, v190, v18, v174\n"
    "v_fma_f32 v175, v191, v18, v175\n"
    "v_cvt_pk_bf16_f32 v206, v172, v173\n"
    "v_mfma_f32_32x32x16_bf16 v[144:159], a[240:243], a[144:147], v[144:159]\n"
    "s_waitcnt lgkmcnt(6)\n"
    "ds_read_b64_tr_b16 a[228:229], v10 offset:34816\n"
    "ds_read_b64_tr_b16 a[230:231], v10 offset:35328\n"
    "v_cvt_pk_bf16_f32 v207, v174, v175\n"
    "buffer_store_dwordx4 v[204:207], v13, s[28:31], s63 offen offset:1024 sc1\n"
    "v_mfma_f32_32x32x16_bf16 v[144:159], a[244:247], a[148:151], v[144:159]\n"
    "s_waitcnt lgkmcnt(6)\n"
    "ds_read_b64_tr_b16 a[232:233], v10 offset:36864\n"
    "ds_read_b64_tr_b16 a[234:235], v10 offset:37376\n"
    "v_mfma_f32_32x32x16_bf16 v[144:159], a[248:251], a[152:155], v[144:159]\n"
    "s_waitcnt lgkmcnt(6)\n"
    "ds_read_b64_tr_b16 a[236:237], v10 offset:38912\n"
    "ds_read_b64_tr_b16 a[238:239], v10 offset:39424\n"
    "v_mfma_f32_32x32x16_bf16 v[144:159], a[252:255], a[156:159], v[144:159]\n"
    "s_waitcnt lgkmcnt(6)\n"
    "ds_read_b64_tr_b16 a[240:241], v11 offset:32768\n"
    "ds_read_b64_tr_b16 a[242:243], v11 offset:33280\n"
    "v_mfma_f32_32x32x16_bf16 v[128:143], a[224:227], v[96:99], v[128:143]\n"
    "s_waitcnt lgkmcnt(6)\n"
    "ds_read_b64_tr_b16 a[244:245], v11 offset:34816\n"
    "ds_read_b64_tr_b16 a[246:247], v11 offset:35328\n"
    "v_mfma_f32_32x32x16_bf16 v[128:143], a[228:231], v[100:103], v[128:143]\n"
    "s_waitcnt lgkmcnt(6)\n"
    "ds_read_b64_tr_b16 a[248:249], v11 offset:36864\n"
    "ds_read_b64_tr_b16 a[250:251], v11 offset:37376\n"
    "v_mfma_f32_32x32x16_bf16 v[128:143], a[232:235], v[104:107], v[128:143]\n"
    "s_waitcnt lgkmcnt(6)\n"
    "ds_read_b64_tr_b16 a[252:253], v11 offset:38912\n"
    "ds_read_b64_tr_b16 a[254:255], v11 offset:39424\n"
    "v_mfma_f32_32x32x16_bf16 v[128:143], a[236:239], v[108:111], v[128:143]\n"
    "s_waitcnt vmcnt(2) lgkmcnt(6)\n"
    "s_barrier\n"
    "ds_read_b64_tr_b16 a[224:225], v10 offset:49152\n"
    "ds_read_b64_tr_b16 a[226:227], v10 offset:49664\n"
    "v_mfma_f32_32x32x16_bf16 v[144:159], a[240:243], a[160:163], v[144:159]\n"
    "s_waitcnt lgkmcnt(6)\n"
    "ds_read_b64_tr_b16 a[228:229], v10 offset:51200\n"
    "ds_read_b64_tr_b16 a[230:231], v10 offset:51712\n"
    "v_mfma_f32_32x32x16_bf16 v[144:159], a[244:247], a[164:167], v[144:159]\n"
    "s_waitcnt lgkmcnt(6)\n"
    "ds_read_b64_tr_b16 a[232:233], v10 offset:53248\n"
    "ds_read_b64_tr_b16 a[234:235], v10 offset:53760\n"
    "v_mfma_f32_32x32x16_bf16 v[144:159], a[248:251], a[168:171], v[144:159]\n"
    "s_waitcnt lgkmcnt(6)\n"
    "ds_read_b64_tr_b16 a[236:237], v10 offset:55296\n"
    "ds_read_b64_tr_b16 a[238:239], v10 offset:55808\n"
    "v_mfma_f32_32x32x16_bf16 v[144:159], a[252:255], a[172:175], v[144:159]\n"
    "s_waitcnt lgkmcnt(6)\n"
    "ds_read_b64_tr_b16 a[240:241], v11 offset:49152\n"
    "ds_read_b64_tr_b16 a[242:243], v11 offset:49664\n"
    "v_mfma_f32_32x32x16_bf16 v[128:143], a[224:227], v[112:115], v[128:143]\n"
    "s_waitcnt lgkmcnt(6)\n"
    "ds_read_b64_tr_b16 a[244:245], v11 offset:51200\n"
    "ds_read_b64_tr_b16 a[246:247], v11 offset:51712\n"
    "v_mfma_f32_32x32x16_bf16 v[128:143], a[228:231], v[116:119], v[128:143]\n"
    "s_waitcnt lgkmcnt(6)\n"
    "ds_read_b64_tr_b16 a[248:249], v11 offset:53248\n"
    "ds_read_b64_tr_b16 a[250:251], v11 offset:53760\n"
    "v_mfma_f32_32x32x16_bf16 v[128:143], a[232:235], v[120:123], v[128:143]\n"
    "s_waitcnt lgkmcnt(6)\n"
    "ds_read_b64_tr_b16 a[252:253], v11 offset:55296\n"
    "ds_read_b64_tr_b16 a[254:255], v11 offset:55808\n"
    "v_mfma_f32_32x32x16_bf16 v[128:143], a[236:239], v[124:127], v[128:143]\n"
    "s_waitcnt lgkmcnt(6)\n"
    "v_mfma_f32_32x32x16_bf16 v[144:159], a[240:243], a[176:179], v[144:159]\n"
    "s_waitcnt lgkmcnt(4)\n"
    "v_mfma_f32_32x32x16_bf16 v[144:159], a[244:247], a[180:183], v[144:159]\n"
    "s_waitcnt lgkmcnt(2)\n"
    "v_mfma_f32_32x32x16_bf16 v[144:159], a[248:251], a[184:187], v[144:159]\n"
    "s_waitcnt lgkmcnt(0)\n"
    "v_mfma_f32_32x32x16_bf16 v[144:159], a[252:255], a[188:191], v[144:159]\n"
    "s_nop 3\n"
    "v_mul_f32 v128, v128, v14\n"
    "v_mul_f32 v129, v129, v14\n"
    "v_mul_f32 v130, v130, v14\n"
    "v_mul_f32 v131, v131, v14\n"
    "s_nop 3\n"
    "v_fma_f32 v128, v144, v18, v128\n"
    "v_fma_f32 v129, v145, v18, v129\n"
    "v_fma_f32 v130, v146, v18, v130\n"
    "v_fma_f32 v131, v147, v18, v131\n"
    "v_cvt_pk_bf16_f32 v192, v128, v129\n"
    "v_cvt_pk_bf16_f32 v193, v130, v131\n"
    "v_mul_f32 v132, v132, v14\n"
    "v_mul_f32 v133, v133, v14\n"
    "v_mul_f32 v134, v134, v14\n"
    "v_mul_f32 v135, v135, v14\n"
    "v_fma_f32 v132, v148, v18, v132\n"
    "v_fma_f32 v133, v149, v18, v133\n"
    "v_fma_f32 v134, v150, v18, v134\n"
    "v_fma_f32 v135, v151, v18, v135\n"
    "v_cvt_pk_bf16_f32 v194, v132, v133\n"
    "v_cvt_pk_bf16_f32 v195, v134, v135\n"
    "s_mov_b32 s63, 0x6000\n"
    "buffer_store_dwordx4 v[192:195], v13, s[28:31], s63 offen sc1\n"
    "v_mul_f32 v136, v136, v14\n"
    "v_mul_f32 v137, v137, v14\n"
    "v_mul_f32 v138, v138, v14\n"
    "v_mul_f32 v139, v139, v14\n"
    "v_fma_f32 v136, v152, v18, v136\n"
    "v_fma_f32 v137, v153, v18, v137\n"
    "v_fma_f32 v138, v154, v18, v138\n"
    "v_fma_f32 v139, v155, v18, v139\n"
    "v_cvt_pk_bf16_f32 v196, v136, v137\n"
    "v_cvt_pk_bf16_f32 v197, v138, v139\n"
    "v_mul_f32 v140, v140, v14\n"
    "v_mul_f32 v141, v141, v14\n"
    "v_mul_f32 v142, v142, v14\n"
    "v_mul_f32 v143, v143, v14\n"
    "v_fma_f32 v140, v156, v18, v140\n"
    "v_fma_f32 v141, v157, v18, v141\n"
    "v_fma_f32 v142, v158, v18, v142\n"
    "v_fma_f32 v143, v159, v18, v143\n"
    "v_cvt_pk_bf16_f32 v198, v140, v141\n"
    "v_cvt_pk_bf16_f32 v199, v142, v143\n"
    "buffer_store_dwordx4 v[196:199], v13, s[28:31], s63 offen offset:1024 sc1\n"
    ".LTAIL:\n"
    "s_nop 0\n"
    "s_lshl_b32 s58, s2, 3\n"
    "s_add_u32 s93, s58, 0x800\n"
    "s_cmp_lg_u32 s40, 0\n"
    "s_cbranch_scc1 .LF_w0_10\n"
    "v_min_u32 v20, 63, v1\n"
    "v_lshlrev_b32 v20, 3, v20\n"
    "s_mov_b32 s58, 0x100\n"
    "v_min_u32 v21, 63, v1\n"
    "v_lshlrev_b32 v21, 3, v21\n"
    "s_mov_b32 s59, 0x300\n"
    "v_min_u32 v22, 39, v1\n"
    "v_lshlrev_b32 v22, 3, v22\n"
    "s_mov_b32 s60, 0x500\n"
    "s_mov_b32 s92, 0\n"
    ".LF_poll_11:\n"
    "s_nop 0\n"
    "buffer_load_dwordx2 v[24:25], v20, s[28:31], s58 offen sc1\n"
    "buffer_load_dwordx2 v[26:27], v21, s[28:31], s59 offen sc1\n"
    "buffer_load_dwordx2 v[28:29], v22, s[28:31], s60 offen sc1\n"
    "s_waitcnt vmcnt(0)\n"
    "v_xor_b32 v24, s90, v24\n"
    "v_xor_b32 v25, s91, v25\n"
    "v_or_b32 v24, v24, v25\n"
    "v_xor_b32 v26, s90, v26\n"
    "v_xor_b32 v27, s91, v27\n"
    "v_or_b32 v26, v26, v27\n"
    "v_or_b32 v24, v24, v26\n"
    "v_xor_b32 v28, s90, v28\n"
    "v_xor_b32 v29, s91, v29\n"
    "v_or_b32 v28, v28, v29\n"
    "v_or_b32 v24, v24, v28\n"
    "v_cmp_ne_u32 vcc, 0, v24\n"
    "s_add_u32 s92, s92, 1\n"
    "s_nop 3\n"
    "s_cbranch_vccz .LF_go_12\n"
    "s_cmp_gt_u32 s92, 0x20000\n"
    "s_cbranch_scc1 .LF_go_12\n"
    "s_sleep 1\n"
    "s_branch .LF_poll_11\n"
    ".LF_go_12:\n"
    ".LF_w0_10:\n"
    "s_nop 4\n"
    "s_waitcnt vmcnt(0)\n"
    "s_barrier\n"
    "s_cmp_lg_u32 s40, 0\n"
    "s_cbranch_scc1 .LF_w0_13\n"
    "s_mov_b32 exec_lo, 0x1\n"
    "s_mov_b32 exec_hi, 0x0\n"
    "v_mov_b32 v28, s90\n"
    "v_mov_b32 v29, s91\n"
    "v_mov_b32 v30, 0\n"
    "buffer_store_dwordx2 v[28:29], v30, s[28:31], s93 offen sc1\n"
    "s_mov_b64 exec, -1\n"
    ".LF_w0_13:\n"
    "s_nop 4\n"
    "s_lshr_b32 s45, s42, 1\n"
    "s_and_b32 s58, s42, 1\n"
    "s_lshl_b32 s58, s58, 3\n"
    "s_add_u32 s46, s41, s58\n"
    "s_lshr_b32 s47, s41, 1\n"
    "s_lshl_b32 s47, s47, 3\n"
    "s_lshr_b32 s58, s42, 2\n"
    "s_add_u32 s47, s47, s58\n"
    "s_and_b32 s48, s41, 1\n"
    "s_lshl_b32 s48, s48, 2\n"
    "s_and_b32 s59, s42, 3\n"
    "s_add_u32 s48, s48, s59\n"
    "s_lshl_b32 s58, s58, 1\n"
    "s_lshl_b32 s59, s59, 2\n"
    "s_add_u32 s96, s58, s59\n"
    "s_and_b32 s96, s96, 15\n"
    "s_mul_hi_u32 s58, s42, 0x24924925\n"
    "s_mul_i32 s59, s58, 7\n"
    "s_sub_u32 s50, s42, s59\n"
    "s_lshl_b32 s49, s41, 2\n"
    "s_add_u32 s49, s49, s58\n"
    "s_cmp_ge_u32 s42, 28\n"
    "s_cselect_b32 s50, 7, s50\n"
    "s_and_b32 s51, s40, 1\n"
    "s_lshr_b32 s52, s40, 1\n"
    "s_mov_b32 s8, s74\n"
    "s_and_b32 s9, s75, 0xffff\n"
    "s_mov_b32 s10, 0x188000\n"
    "s_mov_b32 s11, 0x20000\n"
    "s_mov_b32 s12, s76\n"
    "s_and_b32 s13, s77, 0xffff\n"
    "s_mov_b32 s14, 0x800000\n"
    "s_mov_b32 s15, 0x20000\n"
    "s_mov_b32 s16, s80\n"
    "s_and_b32 s17, s81, 0xffff\n"
    "s_mov_b32 s18, 0xc4000\n"
    "s_mov_b32 s19, 0x20000\n"
    "s_mov_b32 s20, s78\n"
    "s_and_b32 s21, s79, 0xffff\n"
    "s_mov_b32 s22, 0x1000\n"
    "s_mov_b32 s23, 0x20000\n"
    "v_lshrrev_b32 v6, 4, v1\n"
    "v_bfe_u32 v7, v1, 2, 2\n"
    "v_xor_b32 v7, v7, v6\n"
    "v_and_b32 v8, 3, v1\n"
    "v_lshlrev_b32 v8, 4, v8\n"
    "v_and_b32 v2, 1, v7\n"
    "v_lshl_or_b32 v8, v2, 6, v8\n"
    "v_lshrrev_b32 v7, 1, v7\n"
    "v_xor_b32 v7, 1, v7\n"
    "v_mul_u32_u24 v7, 0x700, v7\n"
    "v_add_u32 v7, v7, v8\n"
    "v_mul_u32_u24 v6, 0xe00, v6\n"
    "v_add_u32 v2, v6, v7\n"
    "s_mul_i32 s58, s40, 0x3800\n"
    "s_lshl_b32 s59, s46, 7\n"
    "s_add_u32 s58, s58, s59\n"
    "s_cmp_ge_u32 s46, 14\n"
    "s_cselect_b32 s58, 0x40000000, s58\n"
    "v_add_u32 v2, s58, v2\n"
    "s_mov_b32 s53, 0\n"
    "s_lshl_b32 s58, s47, 6\n"
    "s_lshl_b32 s59, s51, 5\n"
    "s_add_u32 s58, s58, s59\n"
    "s_lshl_b32 s59, s52, 4\n"
    "s_add_u32 s58, s58, s59\n"
    "s_lshl_b32 s58, s58, 12\n"
    "s_lshl_b32 s59, s52, 11\n"
    "s_add_u32 s58, s58, s59\n"
    "v_and_b32 v6, 15, v1\n"
    "v_lshlrev_b32 v6, 12, v6\n"
    "v_lshrrev_b32 v7, 4, v1\n"
    "v_lshl_add_u32 v6, v7, 4, v6\n"
    "v_add_u32 v3, s58, v6\n"
    "s_lshl_b32 s58, s52, 17\n"
    "s_sub_u32 s58, 0x10000, s58\n"
    "v_add_u32 v4, s58, v3\n"
    "v_bfe_u32 v7, v1, 4, 1\n"
    "v_lshlrev_b32 v7, 2, v7\n"
    "v_xor_b32 v7, v7, v1\n"
    "v_and_b32 v7, 7, v7\n"
    "v_lshlrev_b32 v7, 4, v7\n"
    "v_lshrrev_b32 v6, 3, v1\n"
    "v_mul_u32_u24 v6, 0x380, v6\n"
    "v_add_u32 v5, v6, v7\n"
    "s_mul_i32 s58, s40, 0x1c00\n"
    "s_lshl_b32 s59, s50, 7\n"
    "s_add_u32 s58, s58, s59\n"
    "s_cmp_ge_u32 s50, 7\n"
    "s_cselect_b32 s58, 0x40000000, s58\n"
    "v_add_u32 v5, s58, v5\n"
    "s_lshl_b32 s63, s40, 10\n"
    "s_add_u32 m0, s63, 0\n"
    "s_nop 0\n"
    "buffer_load_dwordx4 v2, s[8:11], s53 offen lds\n"
    "s_add_u32 s53, s53, 0xe000\n"
    "s_lshl_b32 s63, s40, 10\n"
    "s_add_u32 m0, s63, 0x1000\n"
    "s_nop 0\n"
    "buffer_load_dwordx4 v2, s[8:11], s53 offen lds\n"
    "s_add_u32 s53, s53, 0xe000\n"
    "s_lshl_b32 s63, s40, 10\n"
    "s_add_u32 m0, s63, 0x2000\n"
    "s_nop 0\n"
    "buffer_load_dwordx4 v2, s[8:11], s53 offen lds\n"
    "s_add_u32 s53, s53, 0xe000\n"
    "s_lshl_b32 s63, s40, 10\n"
    "s_add_u32 m0, s63, 0x3000\n"
    "s_nop 0\n"
    "buffer_load_dwordx4 v2, s[8:11], s53 offen lds\n"
    "s_add_u32 s53, s53, 0xe000\n"
    "s_lshl_b32 s63, s40, 10\n"
    "s_add_u32 m0, s63, 0x4000\n"
    "s_nop 0\n"
    "buffer_load_dwordx4 v2, s[8:11], s53 offen lds\n"
    "s_add_u32 s53, s53, 0xe000\n"
    "s_lshl_b32 s63, s40, 10\n"
    "s_add_u32 m0, s63, 0x5000\n"
    "s_nop 0\n"
    "buffer_load_dwordx4 v2, s[8:11], s53 offen lds\n"
    "s_add_u32 s53, s53, 0xe000\n"
    "s_lshl_b32 s63, s40, 10\n"
    "s_add_u32 m0, s63, 0x6000\n"
    "s_nop 0\n"
    "buffer_load_dwordx4 v2, s[8:11], s53 offen lds\n"
    "s_add_u32 s53, s53, 0xe000\n"
    "s_lshl_b32 s63, s40, 10\n"
    "s_add_u32 m0, s63, 0x7000\n"
    "s_nop 0\n"
    "buffer_load_dwordx4 v2, s[8:11], s53 offen lds\n"
    "s_add_u32 s53, s53, 0xe000\n"
    "s_mov_b32 s56, 0\n"
    "s_cmp_eq_u32 s40, 3\n"
    "s_cselect_b32 s57, 0x40000000, 0\n"
    "v_lshrrev_b32 v6, 3, v1\n"
    "v_and_b32 v7, 7, v1\n"
    "s_lshl_b32 s58, s42, 1\n"
    "s_lshr_b32 s59, s41, 2\n"
    "s_add_u32 s58, s58, s59\n"
    "s_mul_i32 s58, s58, 56\n"
    "s_lshl_b32 s59, s40, 3\n"
    "s_add_u32 s58, s58, s59\n"
    "s_add_u32 s58, s58, 0\n"
    "s_lshl_b32 s58, s58, 9\n"
    "s_and_b32 s59, s41, 3\n"
    "s_lshl_b32 s59, s59, 7\n"
    "s_add_u32 s58, s58, s59\n"
    "s_add_u32 s58, s58, s56\n"
    "v_lshlrev_b32 v8, 9, v6\n"
    "v_lshl_add_u32 v8, v7, 4, v8\n"
    "v_add_u32 v10, s58, v8\n"
    "s_lshl_b32 s58, s42, 6\n"
    "s_lshl_b32 s59, s41, 3\n"
    "s_add_u32 s58, s58, s59\n"
    "s_mul_i32 s58, s58, 0x380\n"
    "s_lshl_b32 s59, s40, 7\n"
    "s_add_u32 s58, s58, s59\n"
    "s_add_u32 s58, s58, 0\n"
    "s_add_u32 s58, s58, s56\n"
    "v_mul_u32_u24 v8, 0x380, v7\n"
    "v_lshl_add_u32 v8, v6, 4, v8\n"
    "v_add_u32 v12, s58, v8\n"
    "s_lshl_b32 s58, s40, 8\n"
    "s_add_u32 s58, s58, 0x3000\n"
    "s_add_u32 s58, s58, s56\n"
    "v_lshlrev_b32 v8, 5, v6\n"
    "v_add_u32 v14, s58, v8\n"
    "buffer_load_dwordx4 v[64:67], v12, s[24:27], 0 offen\n"
    "s_mov_b32 s59, 0xe00\n"
    "buffer_load_dwordx4 v[68:71], v14, s[28:31], s59 offen sc1\n"
    "buffer_load_dwordx4 v[72:75], v14, s[28:31], s59 offen offset:16 sc1\n"
    "s_mov_b32 s59, 0x1500\n"
    "buffer_load_dwordx4 v[76:79], v14, s[28:31], s59 offen sc1\n"
    "buffer_load_dwordx4 v[80:83], v14, s[28:31], s59 offen offset:16 sc1\n"
    "s_mov_b32 s59, 0x1c00\n"
    "buffer_load_dwordx4 v[84:87], v14, s[28:31], s59 offen sc1\n"
    "buffer_load_dwordx4 v[88:91], v14, s[28:31], s59 offen offset:16 sc1\n"
    "s_lshl_b32 s58, s42, 1\n"
    "s_lshr_b32 s59, s41, 2\n"
    "s_add_u32 s58, s58, s59\n"
    "s_mul_i32 s58, s58, 56\n"
    "s_lshl_b32 s59, s40, 3\n"
    "s_add_u32 s58, s58, s59\n"
    "s_add_u32 s58, s58, 32\n"
    "s_lshl_b32 s58, s58, 9\n"
    "s_and_b32 s59, s41, 3\n"
    "s_lshl_b32 s59, s59, 7\n"
    "s_add_u32 s58, s58, s59\n"
    "s_add_u32 s58, s58, s57\n"
    "v_lshlrev_b32 v8, 9, v6\n"
    "v_lshl_add_u32 v8, v7, 4, v8\n"
    "v_add_u32 v11, s58, v8\n"
    "s_lshl_b32 s58, s42, 6\n"
    "s_lshl_b32 s59, s41, 3\n"
    "s_add_u32 s58, s58, s59\n"
    "s_mul_i32 s58, s58, 0x380\n"
    "s_lshl_b32 s59, s40, 7\n"
    "s_add_u32 s58, s58, s59\n"
    "s_add_u32 s58, s58, 0x200\n"
    "s_add_u32 s58, s58, s57\n"
    "v_mul_u32_u24 v8, 0x380, v7\n"
    "v_lshl_add_u32 v8, v6, 4, v8\n"
    "v_add_u32 v13, s58, v8\n"
    "s_lshl_b32 s58, s40, 8\n"
    "s_add_u32 s58, s58, 0x3400\n"
    "s_add_u32 s58, s58, s57\n"
    "v_lshlrev_b32 v8, 5, v6\n"
    "v_add_u32 v15, s58, v8\n"
    "buffer_load_dwordx4 v[128:131], v13, s[24:27], 0 offen\n"
    "s_mov_b32 s59, 0xe00\n"
    "buffer_load_dwordx4 v[132:135], v15, s[28:31], s59 offen sc1\n"
    "buffer_load_dwordx4 v[136:139], v15, s[28:31], s59 offen offset:16 sc1\n"
    "s_mov_b32 s59, 0x1500\n"
    "buffer_load_dwordx4 v[140:143], v15, s[28:31], s59 offen sc1\n"
    "buffer_load_dwordx4 v[144:147], v15, s[28:31], s59 offen offset:16 sc1\n"
    "s_mov_b32 s59, 0x1c00\n"
    "buffer_load_dwordx4 v[148:151], v15, s[28:31], s59 offen sc1\n"
    "buffer_load_dwordx4 v[152:155], v15, s[28:31], s59 offen offset:16 sc1\n"
    "s_lshl_b32 s93, s42, 6\n"
    "s_add_u32 s93, s93, 0x800\n"
    "s_cmp_lg_u32 s40, 0\n"
    "s_cbranch_scc1 .LF_w0_14\n"
    "v_min_u32 v20, 7, v1\n"
    "v_lshlrev_b32 v20, 3, v20\n"
    "s_mov_b32 s92, 0\n"
    ".LF_poll_15:\n"
    "s_nop 0\n"
    "buffer_load_dwordx2 v[24:25], v20, s[28:31], s93 offen sc1\n"
    "s_waitcnt vmcnt(0)\n"
    "v_xor_b32 v24, s90, v24\n"
    "v_xor_b32 v25, s91, v25\n"
    "v_or_b32 v24, v24, v25\n"
    "v_cmp_ne_u32 vcc, 0, v24\n"
    "s_add_u32 s92, s92, 1\n"
    "s_nop 3\n"
    "s_cbranch_vccz .LF_go_16\n"
    "s_cmp_gt_u32 s92, 0x20000\n"
    "s_cbranch_scc1 .LF_go_16\n"
    "s_sleep 1\n"
    "s_branch .LF_poll_15\n"
    ".LF_go_16:\n"
    ".LF_w0_14:\n"
    "s_nop 4\n"
    "s_barrier\n"
    "s_mov_b32 s59, 0x80000\n"
    "buffer_load_dwordx4 v[32:35], v10, s[28:31], s59 offen sc1\n"
    "buffer_load_dwordx4 v[96:99], v11, s[28:31], s59 offen sc1\n"
    "s_mov_b32 s59, 0x240000\n"
    "buffer_load_dwordx4 v[36:39], v10, s[28:31], s59 offen sc1\n"
    "buffer_load_dwordx4 v[100:103], v11, s[28:31], s59 offen sc1\n"
    "s_mov_b32 s59, 0x400000\n"
    "buffer_load_dwordx4 v[40:43], v10, s[28:31], s59 offen sc1\n"
    "buffer_load_dwordx4 v[104:107], v11, s[28:31], s59 offen sc1\n"
    "s_mov_b32 s59, 0x5c0000\n"
    "buffer_load_dwordx4 v[44:47], v10, s[28:31], s59 offen sc1\n"
    "buffer_load_dwordx4 v[108:111], v11, s[28:31], s59 offen sc1\n"
    "s_mov_b32 s59, 0x780000\n"
    "buffer_load_dwordx4 v[48:51], v10, s[28:31], s59 offen sc1\n"
    "buffer_load_dwordx4 v[112:115], v11, s[28:31], s59 offen sc1\n"
    "s_mov_b32 s59, 0x940000\n"
    "buffer_load_dwordx4 v[52:55], v10, s[28:31], s59 offen sc1\n"
    "buffer_load_dwordx4 v[116:119], v11, s[28:31], s59 offen sc1\n"
    "s_mov_b32 s59, 0xb00000\n"
    "buffer_load_dwordx4 v[56:59], v10, s[28:31], s59 offen sc1\n"
    "buffer_load_dwordx4 v[120:123], v11, s[28:31], s59 offen sc1\n"
    "s_mov_b32 s59, 0xcc0000\n"
    "buffer_load_dwordx4 v[60:63], v10, s[28:31], s59 offen sc1\n"
    "buffer_load_dwordx4 v[124:127], v11, s[28:31], s59 offen sc1\n"
    "s_mov_b32 s56, 0x3f80\n"
    "s_mov_b32 s57, 0x3f800000\n"
    "v_mov_b32 v16, 0\n"
    "v_mov_b32 v17, 0\n"
    "s_waitcnt vmcnt(15)\n"
    "v_dot2_f32_bf16 v160, v32, s56, 0\n"
    "v_dot2_f32_bf16 v161, v32, s57, 0\n"
    "v_dot2_f32_bf16 v162, v33, s56, 0\n"
    "v_dot2_f32_bf16 v163, v33, s57, 0\n"
    "v_dot2_f32_bf16 v164, v34, s56, 0\n"
    "v_dot2_f32_bf16 v165, v34, s57, 0\n"
    "v_dot2_f32_bf16 v166, v35, s56, 0\n"
    "v_dot2_f32_bf16 v167, v35, s57, 0\n"
    "s_waitcnt vmcnt(14)\n"
    "v_dot2_f32_bf16 v168, v96, s56, 0\n"
    "v_dot2_f32_bf16 v169, v96, s57, 0\n"
    "v_dot2_f32_bf16 v170, v97, s56, 0\n"
    "v_dot2_f32_bf16 v171, v97, s57, 0\n"
    "v_dot2_f32_bf16 v172, v98, s56, 0\n"
    "v_dot2_f32_bf16 v173, v98, s57, 0\n"
    "v_dot2_f32_bf16 v174, v99, s56, 0\n"
    "v_dot2_f32_bf16 v175, v99, s57, 0\n"
    "s_waitcnt vmcnt(13)\n"
    "v_dot2_f32_bf16 v160, v36, s56, v160\n"
    "v_dot2_f32_bf16 v161, v36, s57, v161\n"
    "v_dot2_f32_bf16 v162, v37, s56, v162\n"
    "v_dot2_f32_bf16 v163, v37, s57, v163\n"
    "v_dot2_f32_bf16 v164, v38, s56, v164\n"
    "v_dot2_f32_bf16 v165, v38, s57, v165\n"
    "v_dot2_f32_bf16 v166, v39, s56, v166\n"
    "v_dot2_f32_bf16 v167, v39, s57, v167\n"
    "s_waitcnt vmcnt(12)\n"
    "v_dot2_f32_bf16 v168, v100, s56, v168\n"
    "v_dot2_f32_bf16 v169, v100, s57, v169\n"
    "v_dot2_f32_bf16 v170, v101, s56, v170\n"
    "v_dot2_f32_bf16 v171, v101, s57, v171\n"
    "v_dot2_f32_bf16 v172, v102, s56, v172\n"
    "v_dot2_f32_bf16 v173, v102, s57, v173\n"
    "v_dot2_f32_bf16 v174, v103, s56, v174\n"
    "v_dot2_f32_bf16 v175, v103, s57, v175\n"
    "s_waitcnt vmcnt(11)\n"
    "v_dot2_f32_bf16 v160, v40, s56, v160\n"
    "v_dot2_f32_bf16 v161, v40, s57, v161\n"
    "v_dot2_f32_bf16 v162, v41, s56, v162\n"
    "v_dot2_f32_bf16 v163, v41, s57, v163\n"
    "v_dot2_f32_bf16 v164, v42, s56, v164\n"
    "v_dot2_f32_bf16 v165, v42, s57, v165\n"
    "v_dot2_f32_bf16 v166, v43, s56, v166\n"
    "v_dot2_f32_bf16 v167, v43, s57, v167\n"
    "s_waitcnt vmcnt(10)\n"
    "v_dot2_f32_bf16 v168, v104, s56, v168\n"
    "v_dot2_f32_bf16 v169, v104, s57, v169\n"
    "v_dot2_f32_bf16 v170, v105, s56, v170\n"
    "v_dot2_f32_bf16 v171, v105, s57, v171\n"
    "v_dot2_f32_bf16 v172, v106, s56, v172\n"
    "v_dot2_f32_bf16 v173, v106, s57, v173\n"
    "v_dot2_f32_bf16 v174, v107, s56, v174\n"
    "v_dot2_f32_bf16 v175, v107, s57, v175\n"
    "s_waitcnt vmcnt(9)\n"
    "v_dot2_f32_bf16 v160, v44, s56, v160\n"
    "v_dot2_f32_bf16 v161, v44, s57, v161\n"
    "v_dot2_f32_bf16 v162, v45, s56, v162\n"
    "v_dot2_f32_bf16 v163, v45, s57, v163\n"
    "v_dot2_f32_bf16 v164, v46, s56, v164\n"
    "v_dot2_f32_bf16 v165, v46, s57, v165\n"
    "v_dot2_f32_bf16 v166, v47, s56, v166\n"
    "v_dot2_f32_bf16 v167, v47, s57, v167\n"
    "s_waitcnt vmcnt(8)\n"
    "v_dot2_f32_bf16 v168, v108, s56, v168\n"
    "v_dot2_f32_bf16 v169, v108, s57, v169\n"
    "v_dot2_f32_bf16 v170, v109, s56, v170\n"
    "v_dot2_f32_bf16 v171, v109, s57, v171\n"
    "v_dot2_f32_bf16 v172, v110, s56, v172\n"
    "v_dot2_f32_bf16 v173, v110, s57, v173\n"
    "v_dot2_f32_bf16 v174, v111, s56, v174\n"
    "v_dot2_f32_bf16 v175, v111, s57, v175\n"
    "s_waitcnt vmcnt(7)\n"
    "v_dot2_f32_bf16 v160, v48, s56, v160\n"
    "v_dot2_f32_bf16 v161, v48, s57, v161\n"
    "v_dot2_f32_bf16 v162, v49, s56, v162\n"
    "v_dot2_f32_bf16 v163, v49, s57, v163\n"
    "v_dot2_f32_bf16 v164, v50, s56, v164\n"
    "v_dot2_f32_bf16 v165, v50, s57, v165\n"
    "v_dot2_f32_bf16 v166, v51, s56, v166\n"
    "v_dot2_f32_bf16 v167, v51, s57, v167\n"
    "s_waitcnt vmcnt(6)\n"
    "v_dot2_f32_bf16 v168, v112, s56, v168\n"
    "v_dot2_f32_bf16 v169, v112, s57, v169\n"
    "v_dot2_f32_bf16 v170, v113, s56, v170\n"
    "v_dot2_f32_bf16 v171, v113, s57, v171\n"
    "v_dot2_f32_bf16 v172, v114, s56, v172\n"
    "v_dot2_f32_bf16 v173, v114, s57, v173\n"
    "v_dot2_f32_bf16 v174, v115, s56, v174\n"
    "v_dot2_f32_bf16 v175, v115, s57, v175\n"
    "s_waitcnt vmcnt(5)\n"
    "v_dot2_f32_bf16 v160, v52, s56, v160\n"
    "v_dot2_f32_bf16 v161, v52, s57, v161\n"
    "v_dot2_f32_bf16 v162, v53, s56, v162\n"
    "v_dot2_f32_bf16 v163, v53, s57, v163\n"
    "v_dot2_f32_bf16 v164, v54, s56, v164\n"
    "v_dot2_f32_bf16 v165, v54, s57, v165\n"
    "v_dot2_f32_bf16 v166, v55, s56, v166\n"
    "v_dot2_f32_bf16 v167, v55, s57, v167\n"
    "s_waitcnt vmcnt(4)\n"
    "v_dot2_f32_bf16 v168, v116, s56, v168\n"
    "v_dot2_f32_bf16 v169, v116, s57, v169\n"
    "v_dot2_f32_bf16 v170, v117, s56, v170\n"
    "v_dot2_f32_bf16 v171, v117, s57, v171\n"
    "v_dot2_f32_bf16 v172, v118, s56, v172\n"
    "v_dot2_f32_bf16 v173, v118, s57, v173\n"
    "v_dot2_f32_bf16 v174, v119, s56, v174\n"
    "v_dot2_f32_bf16 v175, v119, s57, v175\n"
    "s_waitcnt vmcnt(3)\n"
    "v_dot2_f32_bf16 v160, v56, s56, v160\n"
    "v_dot2_f32_bf16 v161, v56, s57, v161\n"
    "v_dot2_f32_bf16 v162, v57, s56, v162\n"
    "v_dot2_f32_bf16 v163, v57, s57, v163\n"
    "v_dot2_f32_bf16 v164, v58, s56, v164\n"
    "v_dot2_f32_bf16 v165, v58, s57, v165\n"
    "v_dot2_f32_bf16 v166, v59, s56, v166\n"
    "v_dot2_f32_bf16 v167, v59, s57, v167\n"
    "s_waitcnt vmcnt(2)\n"
    "v_dot2_f32_bf16 v168, v120, s56, v168\n"
    "v_dot2_f32_bf16 v169, v120, s57, v169\n"
    "v_dot2_f32_bf16 v170, v121, s56, v170\n"
    "v_dot2_f32_bf16 v171, v121, s57, v171\n"
    "v_dot2_f32_bf16 v172, v122, s56, v172\n"
    "v_dot2_f32_bf16 v173, v122, s57, v173\n"
    "v_dot2_f32_bf16 v174, v123, s56, v174\n"
    "v_dot2_f32_bf16 v175, v123, s57, v175\n"
    "s_waitcnt vmcnt(1)\n"
    "v_dot2_f32_bf16 v160, v60, s56, v160\n"
    "v_dot2_f32_bf16 v161, v60, s57, v161\n"
    "v_dot2_f32_bf16 v162, v61, s56, v162\n"
    "v_dot2_f32_bf16 v163, v61, s57, v163\n"
    "v_dot2_f32_bf16 v164, v62, s56, v164\n"
    "v_dot2_f32_bf16 v165, v62, s57, v165\n"
    "v_dot2_f32_bf16 v166, v63, s56, v166\n"
    "v_dot2_f32_bf16 v167, v63, s57, v167\n"
    "s_waitcnt vmcnt(0)\n"
    "v_dot2_f32_bf16 v168, v124, s56, v168\n"
    "v_dot2_f32_bf16 v169, v124, s57, v169\n"
    "v_dot2_f32_bf16 v170, v125, s56, v170\n"
    "v_dot2_f32_bf16 v171, v125, s57, v171\n"
    "v_dot2_f32_bf16 v172, v126, s56, v172\n"
    "v_dot2_f32_bf16 v173, v126, s57, v173\n"
    "v_dot2_f32_bf16 v174, v127, s56, v174\n"
    "v_dot2_f32_bf16 v175, v127, s57, v175\n"
    "v_lshlrev_b32 v18, 16, v64\n"
    "v_and_b32 v19, s7, v64\n"
    "v_pk_fma_f32 v[160:161], v[160:161], v[68:69], v[18:19]\n"
    "v_cvt_pk_bf16_f32 v64, v160, v161\n"
    "v_lshlrev_b32 v18, 16, v65\n"
    "v_and_b32 v19, s7, v65\n"
    "v_pk_fma_f32 v[162:163], v[162:163], v[70:71], v[18:19]\n"
    "v_cvt_pk_bf16_f32 v65, v162, v163\n"
    "v_lshlrev_b32 v18, 16, v66\n"
    "v_and_b32 v19, s7, v66\n"
    "v_pk_fma_f32 v[164:165], v[164:165], v[72:73], v[18:19]\n"
    "v_cvt_pk_bf16_f32 v66, v164, v165\n"
    "v_lshlrev_b32 v18, 16, v67\n"
    "v_and_b32 v19, s7, v67\n"
    "v_pk_fma_f32 v[166:167], v[166:167], v[74:75], v[18:19]\n"
    "v_cvt_pk_bf16_f32 v67, v166, v167\n"
    "s_mov_b32 s59, 0xe80000\n"
    "buffer_store_dwordx4 v[64:67], v12, s[28:31], s59 offen sc1\n"
    "v_lshlrev_b32 v160, 16, v64\n"
    "v_and_b32 v161, s7, v64\n"
    "v_lshlrev_b32 v162, 16, v65\n"
    "v_and_b32 v163, s7, v65\n"
    "v_lshlrev_b32 v164, 16, v66\n"
    "v_and_b32 v165, s7, v66\n"
    "v_lshlrev_b32 v166, 16, v67\n"
    "v_and_b32 v167, s7, v67\n"
    "v_add_f32 v16, v16, v160\n"
    "v_fma_f32 v17, v160, v160, v17\n"
    "v_add_f32 v16, v16, v161\n"
    "v_fma_f32 v17, v161, v161, v17\n"
    "v_add_f32 v16, v16, v162\n"
    "v_fma_f32 v17, v162, v162, v17\n"
    "v_add_f32 v16, v16, v163\n"
    "v_fma_f32 v17, v163, v163, v17\n"
    "v_add_f32 v16, v16, v164\n"
    "v_fma_f32 v17, v164, v164, v17\n"
    "v_add_f32 v16, v16, v165\n"
    "v_fma_f32 v17, v165, v165, v17\n"
    "v_add_f32 v16, v16, v166\n"
    "v_fma_f32 v17, v166, v166, v17\n"
    "v_add_f32 v16, v16, v167\n"
    "v_fma_f32 v17, v167, v167, v17\n"
    "v_lshlrev_b32 v18, 16, v128\n"
    "v_and_b32 v19, s7, v128\n"
    "v_pk_fma_f32 v[168:169], v[168:169], v[132:133], v[18:19]\n"
    "v_cvt_pk_bf16_f32 v128, v168, v169\n"
    "v_lshlrev_b32 v18, 16, v129\n"
    "v_and_b32 v19, s7, v129\n"
    "v_pk_fma_f32 v[170:171], v[170:171], v[134:135], v[18:19]\n"
    "v_cvt_pk_bf16_f32 v129, v170, v171\n"
    "v_lshlrev_b32 v18, 16, v130\n"
    "v_and_b32 v19, s7, v130\n"
    "v_pk_fma_f32 v[172:173], v[172:173], v[136:137], v[18:19]\n"
    "v_cvt_pk_bf16_f32 v130, v172, v173\n"
    "v_lshlrev_b32 v18, 16, v131\n"
    "v_and_b32 v19, s7, v131\n"
    "v_pk_fma_f32 v[174:175], v[174:175], v[138:139], v[18:19]\n"
    "v_cvt_pk_bf16_f32 v131, v174, v175\n"
    "s_mov_b32 s59, 0xe80000\n"
    "buffer_store_dwordx4 v[128:131], v13, s[28:31], s59 offen sc1\n"
    "v_lshlrev_b32 v168, 16, v128\n"
    "v_and_b32 v169, s7, v128\n"
    "v_lshlrev_b32 v170, 16, v129\n"
    "v_and_b32 v171, s7, v129\n"
    "v_lshlrev_b32 v172, 16, v130\n"
    "v_and_b32 v173, s7, v130\n"
    "v_lshlrev_b32 v174, 16, v131\n"
    "v_and_b32 v175, s7, v131\n"
    "v_add_f32 v16, v16, v168\n"
    "v_fma_f32 v17, v168, v168, v17\n"
    "v_add_f32 v16, v16, v169\n"
    "v_fma_f32 v17, v169, v169, v17\n"
    "v_add_f32 v16, v16, v170\n"
    "v_fma_f32 v17, v170, v170, v17\n"
    "v_add_f32 v16, v16, v171\n"
    "v_fma_f32 v17, v171, v171, v17\n"
    "v_add_f32 v16, v16, v172\n"
    "v_fma_f32 v17, v172, v172, v17\n"
    "v_add_f32 v16, v16, v173\n"
    "v_fma_f32 v17, v173, v173, v17\n"
    "v_add_f32 v16, v16, v174\n"
    "v_fma_f32 v17, v174, v174, v17\n"
    "v_add_f32 v16, v16, v175\n"
    "v_fma_f32 v17, v175, v175, v17\n"
    "s_nop 1\n"
    "v_add_f32_dpp v16, v16, v16 row_ror:8 row_mask:0xf bank_mask:0xf\n"
    "v_mov_b32 v6, v16\n"
    "s_nop 1\n"
    "v_permlane16_swap_b32 v6, v16\n"
    "v_add_f32 v16, v16, v6\n"
    "v_mov_b32 v6, v16\n"
    "s_nop 1\n"
    "v_permlane32_swap_b32 v6, v16\n"
    "v_add_f32 v16, v16, v6\n"
    "s_nop 1\n"
    "v_add_f32_dpp v17, v17, v17 row_ror:8 row_mask:0xf bank_mask:0xf\n"
    "v_mov_b32 v6, v17\n"
    "s_nop 1\n"
    "v_permlane16_swap_b32 v6, v17\n"
    "v_add_f32 v17, v17, v6\n"
    "v_mov_b32 v6, v17\n"
    "s_nop 1\n"
    "v_permlane32_swap_b32 v6, v17\n"
    "v_add_f32 v17, v17, v6\n"
    "v_and_b32 v6, 7, v1\n"
    "v_lshlrev_b32 v6, 3, v6\n"
    "v_add_u32 v6, 0x27e00, v6\n"
    "s_lshl_b32 s58, s40, 6\n"
    "v_add_u32 v7, s58, v6\n"
    "s_mov_b32 exec_lo, 0xff\n"
    "s_mov_b32 exec_hi, 0x0\n"
    "ds_write_b64 v7, v[16:17]\n"
    "s_mov_b64 exec, -1\n"
    "s_waitcnt lgkmcnt(0)\n"
    "s_barrier\n"
    "ds_read_b64 v[176:177], v6\n"
    "ds_read_b64 v[178:179], v6 offset:64\n"
    "ds_read_b64 v[180:181], v6 offset:128\n"
    "ds_read_b64 v[182:183], v6 offset:192\n"
    "s_waitcnt lgkmcnt(2)\n"
    "v_add_f32 v16, v176, v178\n"
    "v_add_f32 v17, v177, v179\n"
    "s_waitcnt lgkmcnt(1)\n"
    "v_add_f32 v16, v16, v180\n"
    "v_add_f32 v17, v17, v181\n"
    "s_waitcnt lgkmcnt(0)\n"
    "v_add_f32 v16, v16, v182\n"
    "v_add_f32 v17, v17, v183\n"
    "v_mul_f32 v16, 0x3b124925, v16\n"
    "v_mul_f32 v17, 0x3b124925, v17\n"
    "v_fma_f32 v17, -v16, v16, v17\n"
    "v_max_f32 v17, 0, v17\n"
    "v_add_f32 v17, 0x3727c5ac, v17\n"
    "v_rsq_f32 v17, v17\n"
    "s_nop 0\n"
    "v_mul_f32 v16, -v16, v17\n"
    "v_pk_fma_f32 v[160:161], v[160:161], v[16:17], v[16:17] op_sel:[0,1,0] op_sel_hi:[1,1,0]\n"
    "v_pk_fma_f32 v[162:163], v[162:163], v[16:17], v[16:17] op_sel:[0,1,0] op_sel_hi:[1,1,0]\n"
    "v_pk_fma_f32 v[164:165], v[164:165], v[16:17], v[16:17] op_sel:[0,1,0] op_sel_hi:[1,1,0]\n"
    "v_pk_fma_f32 v[166:167], v[166:167], v[16:17], v[16:17] op_sel:[0,1,0] op_sel_hi:[1,1,0]\n"
    "v_pk_fma_f32 v[160:161], v[160:161], v[76:77], v[84:85]\n"
    "v_pk_fma_f32 v[162:163], v[162:163], v[78:79], v[86:87]\n"
    "v_pk_fma_f32 v[164:165], v[164:165], v[80:81], v[88:89]\n"
    "v_pk_fma_f32 v[166:167], v[166:167], v[82:83], v[90:91]\n"
    "v_cvt_pk_bf16_f32 v64, v160, v161\n"
    "v_cvt_pk_bf16_f32 v65, v162, v163\n"
    "v_cvt_pk_bf16_f32 v66, v164, v165\n"
    "v_cvt_pk_bf16_f32 v67, v166, v167\n"
    "s_mov_b32 s59, 0x1040000\n"
    "buffer_store_dwordx4 v[64:67], v10, s[28:31], s59 offen sc1\n"
    "v_pk_fma_f32 v[168:169], v[168:169], v[16:17], v[16:17] op_sel:[0,1,0] op_sel_hi:[1,1,0]\n"
    "v_pk_fma_f32 v[170:171], v[170:171], v[16:17], v[16:17] op_sel:[0,1,0] op_sel_hi:[1,1,0]\n"
    "v_pk_fma_f32 v[172:173], v[172:173], v[16:17], v[16:17] op_sel:[0,1,0] op_sel_hi:[1,1,0]\n"
    "v_pk_fma_f32 v[174:175], v[174:175], v[16:17], v[16:17] op_sel:[0,1,0] op_sel_hi:[1,1,0]\n"
    "v_pk_fma_f32 v[168:169], v[168:169], v[140:141], v[148:149]\n"
    "v_pk_fma_f32 v[170:171], v[170:171], v[142:143], v[150:151]\n"
    "v_pk_fma_f32 v[172:173], v[172:173], v[144:145], v[152:153]\n"
    "v_pk_fma_f32 v[174:175], v[174:175], v[146:147], v[154:155]\n"
    "v_cvt_pk_bf16_f32 v128, v168, v169\n"
    "v_cvt_pk_bf16_f32 v129, v170, v171\n"
    "v_cvt_pk_bf16_f32 v130, v172, v173\n"
    "v_cvt_pk_bf16_f32 v131, v174, v175\n"
    "s_mov_b32 s59, 0x1040000\n"
    "buffer_store_dwordx4 v[128:131], v11, s[28:31], s59 offen sc1\n"
    "s_lshl_b32 s93, s2, 3\n"
    "s_add_u32 s93, s93, 0x1000\n"
    "s_waitcnt vmcnt(0)\n"
    "s_barrier\n"
    "s_cmp_lg_u32 s40, 0\n"
    "s_cbranch_scc1 .LF_w0_17\n"
    "s_mov_b32 exec_lo, 0x1\n"
    "s_mov_b32 exec_hi, 0x0\n"
    "v_mov_b32 v28, s90\n"
    "v_mov_b32 v29, s91\n"
    "v_mov_b32 v30, 0\n"
    "buffer_store_dwordx2 v[28:29], v30, s[28:31], s93 offen sc1\n"
    "s_mov_b64 exec, -1\n"
    ".LF_w0_17:\n"
    "s_nop 4\n"
    "s_lshl_b32 s63, s40, 10\n"
    "s_add_u32 m0, s63, 0x8000\n"
    "s_nop 0\n"
    "buffer_load_dwordx4 v2, s[8:11], s53 offen lds\n"
    "s_add_u32 s53, s53, 0xe000\n"
    "s_lshl_b32 s63, s40, 10\n"
    "s_add_u32 m0, s63, 0x9000\n"
    "s_nop 0\n"
    "buffer_load_dwordx4 v2, s[8:11], s53 offen lds\n"
    "s_add_u32 s53, s53, 0xe000\n"
    "s_lshl_b32 s63, s40, 10\n"
    "s_add_u32 m0, s63, 0xa000\n"
    "s_nop 0\n"
    "buffer_load_dwordx4 v2, s[8:11], s53 offen lds\n"
    "s_add_u32 s53, s53, 0xe000\n"
    "s_lshl_b32 s63, s40, 10\n"
    "s_add_u32 m0, s63, 0xb000\n"
    "s_nop 0\n"
    "buffer_load_dwordx4 v2, s[8:11], s53 offen lds\n"
    "s_add_u32 s53, s53, 0xe000\n"
    "s_lshl_b32 s63, s40, 10\n"
    "s_add_u32 m0, s63, 0xc000\n"
    "s_nop 0\n"
    "buffer_load_dwordx4 v2, s[8:11], s53 offen lds\n"
    "s_add_u32 s53, s53, 0xe000\n"
    "s_lshl_b32 s63, s40, 10\n"
    "s_add_u32 m0, s63, 0xd000\n"
    "s_nop 0\n"
    "buffer_load_dwordx4 v2, s[8:11], s53 offen lds\n"
    "s_add_u32 s53, s53, 0xe000\n"
    "s_lshl_b32 s63, s40, 10\n"
    "s_add_u32 m0, s63, 0xe000\n"
    "s_nop 0\n"
    "buffer_load_dwordx4 v2, s[8:11], s53 offen lds\n"
    "s_add_u32 s53, s53, 0xe000\n"
    "s_lshl_b32 s63, s40, 10\n"
    "s_add_u32 m0, s63, 0xf000\n"
    "s_nop 0\n"
    "buffer_load_dwordx4 v2, s[8:11], s53 offen lds\n"
    "s_add_u32 s53, s53, 0xe000\n"
    "s_lshl_b32 s63, s40, 10\n"
    "s_add_u32 m0, s63, 0x10000\n"
    "s_nop 0\n"
    "buffer_load_dwordx4 v2, s[8:11], s53 offen lds\n"
    "s_add_u32 s53, s53, 0xe000\n"
    "s_lshl_b32 s63, s40, 10\n"
    "s_add_u32 m0, s63, 0x11000\n"
    "s_nop 0\n"
    "buffer_load_dwordx4 v2, s[8:11], s53 offen lds\n"
    "s_add_u32 s53, s53, 0xe000\n"
    "s_lshl_b32 s63, s40, 10\n"
    "s_add_u32 m0, s63, 0x12000\n"
    "s_nop 0\n"
    "buffer_load_dwordx4 v2, s[8:11], s53 offen lds\n"
    "s_add_u32 s53, s53, 0xe000\n"
    "s_lshl_b32 s63, s40, 10\n"
    "s_add_u32 m0, s63, 0x13000\n"
    "s_nop 0\n"
    "buffer_load_dwordx4 v2, s[8:11], s53 offen lds\n"
    "s_add_u32 s53, s53, 0xe000\n"
    "s_lshl_b32 s63, s40, 10\n"
    "s_add_u32 m0, s63, 0x14000\n"
    "s_nop 0\n"
    "buffer_load_dwordx4 v2, s[8:11], s53 offen lds\n"
    "s_add_u32 s53, s53, 0xe000\n"
    "s_lshl_b32 s63, s40, 10\n"
    "s_add_u32 m0, s63, 0x15000\n"
    "s_nop 0\n"
    "buffer_load_dwordx4 v2, s[8:11], s53 offen lds\n"
    "s_add_u32 s53, s53, 0xe000\n"
    "s_lshl_b32 s63, s40, 10\n"
    "s_add_u32 m0, s63, 0x16000\n"
    "s_nop 0\n"
    "buffer_load_dwordx4 v2, s[8:11], s53 offen lds\n"
    "s_add_u32 s53, s53, 0xe000\n"
    "s_lshl_b32 s63, s40, 10\n"
    "s_add_u32 m0, s63, 0x17000\n"
    "s_nop 0\n"
    "buffer_load_dwordx4 v2, s[8:11], s53 offen lds\n"
    "s_add_u32 s53, s53, 0xe000\n"
    "s_lshl_b32 s63, s40, 10\n"
    "s_add_u32 m0, s63, 0x18000\n"
    "s_nop 0\n"
    "buffer_load_dwordx4 v2, s[8:11], s53 offen lds\n"
    "s_add_u32 s53, s53, 0xe000\n"
    "s_lshl_b32 s63, s40, 10\n"
    "s_add_u32 m0, s63, 0x19000\n"
    "s_nop 0\n"
    "buffer_load_dwordx4 v2, s[8:11], s53 offen lds\n"
    "s_add_u32 s53, s53, 0xe000\n"
    "s_lshl_b32 s63, s40, 10\n"
    "s_add_u32 m0, s63, 0x1a000\n"
    "s_nop 0\n"
    "buffer_load_dwordx4 v2, s[8:11], s53 offen lds\n"
    "s_add_u32 s53, s53, 0xe000\n"
    "s_lshl_b32 s63, s40, 10\n"
    "s_add_u32 m0, s63, 0x1b000\n"
    "s_nop 0\n"
    "buffer_load_dwordx4 v2, s[8:11], s53 offen lds\n"
    "s_add_u32 s53, s53, 0xe000\n"
    "s_lshl_b32 s58, s45, 2\n"
    "s_add_u32 s58, s58, s40\n"
    "s_mul_i32 s58, s58, 0x7000\n"
    "s_add_u32 s58, s58, 0x1040000\n"
    "v_lshlrev_b32 v14, 4, v1\n"
    "v_add_u32 v9, s58, v14\n"
    "v_bfe_u32 v14, v1, 2, 2\n"
    "v_and_b32 v15, 3, v1\n"
    "v_bfe_u32 v16, v1, 4, 1\n"
    "v_lshrrev_b32 v17, 5, v1\n"
    "v_and_b32 v18, 1, v15\n"
    "v_lshlrev_b32 v18, 4, v18\n"
    "v_lshrrev_b32 v15, 1, v15\n"
    "v_lshl_or_b32 v18, v15, 3, v18\n"
    "v_lshl_or_b32 v18, v16, 5, v18\n"
    "v_lshl_add_u32 v17, v17, 3, v14\n"
    "v_lshl_add_u32 v18, v17, 8, v18\n"
    "v_xor_b32 v10, 0, v14\n"
    "v_lshl_add_u32 v10, v10, 6, v18\n"
    "v_xor_b32 v11, 1, v14\n"
    "v_lshl_add_u32 v11, v11, 6, v18\n"
    "v_xor_b32 v12, 2, v14\n"
    "v_lshl_add_u32 v12, v12, 6, v18\n"
    "v_xor_b32 v13, 3, v14\n"
    "v_lshl_add_u32 v13, v13, 6, v18\n"
    "s_mov_b32 s55, 0xbdd2d3e8\n"
    "v_mov_b32 v30, 0xc0135761\n"
    "v_mov_b32 v31, 0xbdd2d3e8\n"
    "v_mov_b32 v20, 0\n"
    "v_mov_b32 v21, 0\n"
    "v_cmp_gt_u32 s[56:57], 32, v1\n"
    "s_lshl_b32 s58, s45, 3\n"
    "s_lshl_b32 s59, s40, 1\n"
    "s_add_u32 s58, s58, s59\n"
    "v_bfe_u32 v14, v1, 4, 1\n"
    "v_add_u32 v14, s58, v14\n"
    "v_mul_u32_u24 v14, 56, v14\n"
    "s_lshl_b32 s59, s46, 2\n"
    "v_add_u32 v14, s59, v14\n"
    "v_lshlrev_b32 v14, 9, v14\n"
    "v_lshrrev_b32 v15, 5, v1\n"
    "v_lshl_add_u32 v14, v15, 8, v14\n"
    "v_and_b32 v15, 15, v1\n"
    "v_lshl_add_u32 v252, v15, 3, v14\n"
    "v_and_b32 v15, 31, v1\n"
    "v_lshrrev_b32 v17, 5, v1\n"
    "v_and_b32 v16, 7, v15\n"
    "v_xor_b32 v16, v16, v17\n"
    "v_lshlrev_b32 v16, 4, v16\n"
    "v_lshl_add_u32 v16, v15, 7, v16\n"
    "s_lshl_b32 s58, s40, 12\n"
    "s_add_u32 s58, s58, 0x21000\n"
    "v_add_u32 v233, s58, v16\n"
    "v_lshrrev_b32 v15, 3, v1\n"
    "v_and_b32 v16, 7, v1\n"
    "v_xor_b32 v17, v16, v15\n"
    "v_lshlrev_b32 v17, 4, v17\n"
    "v_lshl_add_u32 v17, v15, 7, v17\n"
    "v_add_u32 v234, s58, v17\n"
    "s_lshl_b32 s59, s46, 6\n"
    "v_lshlrev_b32 v16, 3, v16\n"
    "v_add_u32 v16, s59, v16\n"
    "s_mov_b32 s60, 0x2492493\n"
    "v_mul_hi_u32 v17, v16, s60\n"
    "v_mul_u32_u24 v18, 0x70, v17\n"
    "v_sub_u32 v16, v16, v18\n"
    "v_lshlrev_b32 v16, 1, v16\n"
    "v_mul_u32_u24 v17, 0x70000, v17\n"
    "v_add_u32 v235, v16, v17\n"
    "v_and_b32 v16, 3, v15\n"
    "v_lshrrev_b32 v15, 2, v15\n"
    "v_lshl_add_u32 v15, v15, 3, v16\n"
    "s_lshl_b32 s58, s45, 7\n"
    "s_lshl_b32 s59, s40, 5\n"
    "s_add_u32 s58, s58, s59\n"
    "v_add_u32 v15, s58, v15\n"
    "v_mul_u32_u24 v15, 0xe0, v15\n"
    "v_add_u32 v235, v235, v15\n"
    "v_add_u32 v235, 0x1580000, v235\n"
    "s_lshl_b32 s93, s45, 7\n"
    "s_add_u32 s93, s93, 0x1000\n"
    "s_waitcnt vmcnt(0)\n"
    "s_cmp_lg_u32 s40, 0\n"
    "s_cbranch_scc1 .LF_w0_19\n"
    "v_min_u32 v236, 15, v1\n"
    "v_lshlrev_b32 v236, 3, v236\n"
    "s_mov_b32 s92, 0\n"
    ".LF_poll_20:\n"
    "s_nop 0\n"
    "buffer_load_dwordx2 v[240:241], v236, s[28:31], s93 offen sc1\n"
    "s_waitcnt vmcnt(0)\n"
    "v_xor_b32 v240, s90, v240\n"
    "v_xor_b32 v241, s91, v241\n"
    "v_or_b32 v240, v240, v241\n"
    "v_cmp_ne_u32 vcc, 0, v240\n"
    "s_add_u32 s92, s92, 1\n"
    "s_nop 3\n"
    "s_cbranch_vccz .LF_go_21\n"
    "s_cmp_gt_u32 s92, 0x20000\n"
    "s_cbranch_scc1 .LF_go_21\n"
    "s_sleep 1\n"
    "s_branch .LF_poll_20\n"
    ".LF_go_21:\n"
    ".LF_w0_19:\n"
    "s_nop 4\n"
    "s_barrier\n"
    "s_cmp_ge_u32 s46, 14\n"
    "s_cbranch_scc1 .LF_t2idle_18\n"
    "buffer_load_dwordx4 v[96:99], v9, s[28:31], 0 offen sc1\n"
    "buffer_load_dwordx4 v[100:103], v9, s[28:31], 0 offen offset:1024 sc1\n"
    "buffer_load_dwordx4 v[104:107], v9, s[28:31], 0 offen offset:2048 sc1\n"
    "buffer_load_dwordx4 v[108:111], v9, s[28:31], 0 offen offset:3072 sc1\n"
    "s_mov_b32 s62, 0x1000\n"
    "buffer_load_dwordx4 v[112:115], v9, s[28:31], s62 offen sc1\n"
    "s_mov_b32 s62, 0x1400\n"
    "buffer_load_dwordx4 v[116:119], v9, s[28:31], s62 offen sc1\n"
    "s_mov_b32 s62, 0x1800\n"
    "buffer_load_dwordx4 v[120:123], v9, s[28:31], s62 offen sc1\n"
    "s_mov_b32 s62, 0x1c00\n"
    "buffer_load_dwordx4 v[124:127], v9, s[28:31], s62 offen sc1\n"
    "s_mov_b32 s62, 0x2000\n"
    "buffer_load_dwordx4 v[128:131], v9, s[28:31], s62 offen sc1\n"
    "s_mov_b32 s62, 0x2400\n"
    "buffer_load_dwordx4 v[132:135], v9, s[28:31], s62 offen sc1\n"
    "s_mov_b32 s62, 0x2800\n"
    "buffer_load_dwordx4 v[136:139], v9, s[28:31], s62 offen sc1\n"
    "s_mov_b32 s62, 0x2c00\n"
    "buffer_load_dwordx4 v[140:143], v9, s[28:31], s62 offen sc1\n"
    "s_lshl_b32 s58, s45, 2\n"
    "s_add_u32 s58, s58, s40\n"
    "s_mul_i32 s58, s58, 0x1c00\n"
    "s_add_u32 s58, s58, 0x8000\n"
    "v_and_b32 v232, 31, v1\n"
    "v_lshlrev_b32 v232, 4, v232\n"
    "v_add_u32 v232, s58, v232\n"
    "s_lshl_b32 s57, s46, 9\n"
    "v_mov_b32 v250, s90\n"
    "v_mov_b32 v251, s91\n"
    "ds_read_b64_tr_b16 v[208:209], v10\n"
    "ds_read_b64_tr_b16 v[210:211], v10 offset:1024\n"
    "ds_read_b64_tr_b16 v[212:213], v10 offset:4096\n"
    "ds_read_b64_tr_b16 v[214:215], v10 offset:5120\n"
    "ds_read_b64_tr_b16 v[216:217], v10 offset:8192\n"
    "ds_read_b64_tr_b16 v[218:219], v10 offset:9216\n"
    "ds_read_b64_tr_b16 v[220:221], v10 offset:12288\n"
    "ds_read_b64_tr_b16 v[222:223], v10 offset:13312\n"
    "ds_read_b64_tr_b16 v[224:225], v10 offset:16384\n"
    "ds_read_b64_tr_b16 v[226:227], v10 offset:17408\n"
    "s_mov_b32 s62, 0x3000\n"
    "buffer_load_dwordx4 v[144:147], v9, s[28:31], s62 offen sc1\n"
    "s_waitcnt vmcnt(12) lgkmcnt(8)\n"
    "v_mfma_f32_32x32x16_bf16 v[32:47], v[208:211], v[96:99], 0\n"
    "s_waitcnt lgkmcnt(6)\n"
    "ds_read_b64_tr_b16 v[228:229], v10 offset:20480\n"
    "ds_read_b64_tr_b16 v[230:231], v10 offset:21504\n"
    "s_mov_b32 s62, 0x3400\n"
    "buffer_load_dwordx4 v[148:151], v9, s[28:31], s62 offen sc1\n"
    "s_waitcnt vmcnt(12)\n"
    "v_mfma_f32_32x32x16_bf16 v[32:47], v[212:215], v[100:103], v[32:47]\n"
    "s_waitcnt lgkmcnt(6)\n"
    "ds_read_b64_tr_b16 v[208:209], v10 offset:24576\n"
    "ds_read_b64_tr_b16 v[210:211], v10 offset:25600\n"
    "s_mov_b32 s62, 0x3800\n"
    "buffer_load_dwordx4 v[152:155], v9, s[28:31], s62 offen sc1\n"
    "s_waitcnt vmcnt(12)\n"
    "v_mfma_f32_32x32x16_bf16 v[32:47], v[216:219], v[104:107], v[32:47]\n"
    "s_waitcnt lgkmcnt(6)\n"
    "ds_read_b64_tr_b16 v[212:213], v10 offset:28672\n"
    "ds_read_b64_tr_b16 v[214:215], v10 offset:29696\n"
    "s_mov_b32 s62, 0x3c00\n"
    "buffer_load_dwordx4 v[156:159], v9, s[28:31], s62 offen sc1\n"
    "s_waitcnt vmcnt(12)\n"
    "v_mfma_f32_32x32x16_bf16 v[32:47], v[220:223], v[108:111], v[32:47]\n"
    "s_waitcnt lgkmcnt(6)\n"
    "ds_read_b64_tr_b16 v[216:217], v10 offset:32768\n"
    "ds_read_b64_tr_b16 v[218:219], v10 offset:33792\n"
    "s_mov_b32 s62, 0x4000\n"
    "buffer_load_dwordx4 v[160:163], v9, s[28:31], s62 offen sc1\n"
    "s_waitcnt vmcnt(12)\n"
    "v_mfma_f32_32x32x16_bf16 v[32:47], v[224:227], v[112:115], v[32:47]\n"
    "s_waitcnt lgkmcnt(6)\n"
    "ds_read_b64_tr_b16 v[220:221], v10 offset:36864\n"
    "ds_read_b64_tr_b16 v[222:223], v10 offset:37888\n"
    "s_mov_b32 s62, 0x4400\n"
    "buffer_load_dwordx4 v[164:167], v9, s[28:31], s62 offen sc1\n"
    "s_waitcnt vmcnt(12)\n"
    "v_mfma_f32_32x32x16_bf16 v[32:47], v[228:231], v[116:119], v[32:47]\n"
    "s_waitcnt lgkmcnt(6)\n"
    "ds_read_b64_tr_b16 v[224:225], v10 offset:40960\n"
    "ds_read_b64_tr_b16 v[226:227], v10 offset:41984\n"
    "s_mov_b32 s62, 0x4800\n"
    "buffer_load_dwordx4 v[168:171], v9, s[28:31], s62 offen sc1\n"
    "s_waitcnt vmcnt(12)\n"
    "v_mfma_f32_32x32x16_bf16 v[32:47], v[208:211], v[120:123], v[32:47]\n"
    "s_waitcnt lgkmcnt(6)\n"
    "ds_read_b64_tr_b16 v[228:229], v10 offset:45056\n"
    "ds_read_b64_tr_b16 v[230:231], v10 offset:46080\n"
    "s_mov_b32 s62, 0x4c00\n"
    "buffer_load_dwordx4 v[172:175], v9, s[28:31], s62 offen sc1\n"
    "s_waitcnt vmcnt(12)\n"
    "v_mfma_f32_32x32x16_bf16 v[32:47], v[212:215], v[124:127], v[32:47]\n"
    "s_waitcnt lgkmcnt(6)\n"
    "ds_read_b64_tr_b16 v[208:209], v10 offset:49152\n"
    "ds_read_b64_tr_b16 v[210:211], v10 offset:50176\n"
    "s_mov_b32 s62, 0x5000\n"
    "buffer_load_dwordx4 v[176:179], v9, s[28:31], s62 offen sc1\n"
    "s_waitcnt vmcnt(12)\n"
    "v_mfma_f32_32x32x16_bf16 v[32:47], v[216:219], v[128:131], v[32:47]\n"
    "s_waitcnt lgkmcnt(6)\n"
    "ds_read_b64_tr_b16 v[212:213], v10 offset:53248\n"
    "ds_read_b64_tr_b16 v[214:215], v10 offset:54272\n"
    "s_mov_b32 s62, 0x5400\n"
    "buffer_load_dwordx4 v[180:183], v9, s[28:31], s62 offen sc1\n"
    "s_waitcnt vmcnt(12)\n"
    "v_mfma_f32_32x32x16_bf16 v[32:47], v[220:223], v[132:135], v[32:47]\n"
    "s_waitcnt lgkmcnt(6)\n"
    "ds_read_b64_tr_b16 v[216:217], v10 offset:57344\n"
    "ds_read_b64_tr_b16 v[218:219], v10 offset:58368\n"
    "s_mov_b32 s62, 0x5800\n"
    "buffer_load_dwordx4 v[184:187], v9, s[28:31], s62 offen sc1\n"
    "s_waitcnt vmcnt(12)\n"
    "v_mfma_f32_32x32x16_bf16 v[32:47], v[224:227], v[136:139], v[32:47]\n"
    "s_waitcnt lgkmcnt(6)\n"
    "v_add_u32 v10, 0xf000, v10\n"
    "ds_read_b64_tr_b16 v[220:221], v10\n"
    "ds_read_b64_tr_b16 v[222:223], v10 offset:1024\n"
    "s_mov_b32 s62, 0x5c00\n"
    "buffer_load_dwordx4 v[188:191], v9, s[28:31], s62 offen sc1\n"
    "s_waitcnt vmcnt(12)\n"
    "v_mfma_f32_32x32x16_bf16 v[32:47], v[228:231], v[140:143], v[32:47]\n"
    "s_waitcnt lgkmcnt(6)\n"
    "ds_read_b64_tr_b16 v[224:225], v10 offset:4096\n"
    "ds_read_b64_tr_b16 v[226:227], v10 offset:5120\n"
    "s_mov_b32 s62, 0x6000\n"
    "buffer_load_dwordx4 v[192:195], v9, s[28:31], s62 offen sc1\n"
    "s_waitcnt vmcnt(12)\n"
    "v_mfma_f32_32x32x16_bf16 v[32:47], v[208:211], v[144:147], v[32:47]\n"
    "s_waitcnt lgkmcnt(6)\n"
    "ds_read_b64_tr_b16 v[228:229], v10 offset:8192\n"
    "ds_read_b64_tr_b16 v[230:231], v10 offset:9216\n"
    "s_mov_b32 s62, 0x6400\n"
    "buffer_load_dwordx4 v[196:199], v9, s[28:31], s62 offen sc1\n"
    "s_waitcnt vmcnt(12)\n"
    "v_mfma_f32_32x32x16_bf16 v[32:47], v[212:215], v[148:151], v[32:47]\n"
    "s_waitcnt lgkmcnt(6)\n"
    "ds_read_b64_tr_b16 v[208:209], v10 offset:12288\n"
    "ds_read_b64_tr_b16 v[210:211], v10 offset:13312\n"
    "s_mov_b32 s62, 0x6800\n"
    "buffer_load_dwordx4 v[200:203], v9, s[28:31], s62 offen sc1\n"
    "s_waitcnt vmcnt(12)\n"
    "v_mfma_f32_32x32x16_bf16 v[32:47], v[216:219], v[152:155], v[32:47]\n"
    "s_waitcnt lgkmcnt(6)\n"
    "ds_read_b64_tr_b16 v[212:213], v10 offset:16384\n"
    "ds_read_b64_tr_b16 v[214:215], v10 offset:17408\n"
    "s_mov_b32 s62, 0x6c00\n"
    "buffer_load_dwordx4 v[204:207], v9, s[28:31], s62 offen sc1\n"
    "s_waitcnt vmcnt(12)\n"
    "v_mfma_f32_32x32x16_bf16 v[32:47], v[220:223], v[156:159], v[32:47]\n"
    "s_waitcnt lgkmcnt(6)\n"
    "ds_read_b64_tr_b16 v[216:217], v10 offset:20480\n"
    "ds_read_b64_tr_b16 v[218:219], v10 offset:21504\n"
    "s_waitcnt vmcnt(11)\n"
    "v_mfma_f32_32x32x16_bf16 v[32:47], v[224:227], v[160:163], v[32:47]\n"
    "s_waitcnt lgkmcnt(6)\n"
    "ds_read_b64_tr_b16 v[220:221], v10 offset:24576\n"
    "ds_read_b64_tr_b16 v[222:223], v10 offset:25600\n"
    "s_waitcnt vmcnt(10)\n"
    "v_mfma_f32_32x32x16_bf16 v[32:47], v[228:231], v[164:167], v[32:47]\n"
    "s_waitcnt lgkmcnt(6)\n"
    "ds_read_b64_tr_b16 v[224:225], v10 offset:28672\n"
    "ds_read_b64_tr_b16 v[226:227], v10 offset:29696\n"
    "s_waitcnt vmcnt(9)\n"
    "v_mfma_f32_32x32x16_bf16 v[32:47], v[208:211], v[168:171], v[32:47]\n"
    "s_waitcnt lgkmcnt(6)\n"
    "ds_read_b64_tr_b16 v[228:229], v10 offset:32768\n"
    "ds_read_b64_tr_b16 v[230:231], v10 offset:33792\n"
    "s_waitcnt vmcnt(8)\n"
    "v_mfma_f32_32x32x16_bf16 v[32:47], v[212:215], v[172:175], v[32:47]\n"
    "s_waitcnt lgkmcnt(6)\n"
    "ds_read_b64_tr_b16 v[208:209], v10 offset:36864\n"
    "ds_read_b64_tr_b16 v[210:211], v10 offset:37888\n"
    "s_waitcnt vmcnt(7)\n"
    "v_mfma_f32_32x32x16_bf16 v[32:47], v[216:219], v[176:179], v[32:47]\n"
    "s_waitcnt lgkmcnt(6)\n"
    "ds_read_b64_tr_b16 v[212:213], v10 offset:40960\n"
    "ds_read_b64_tr_b16 v[214:215], v10 offset:41984\n"
    "s_waitcnt vmcnt(6)\n"
    "v_mfma_f32_32x32x16_bf16 v[32:47], v[220:223], v[180:183], v[32:47]\n"
    "s_waitcnt lgkmcnt(6)\n"
    "ds_read_b64_tr_b16 v[216:217], v10 offset:45056\n"
    "ds_read_b64_tr_b16 v[218:219], v10 offset:46080\n"
    "s_waitcnt vmcnt(5)\n"
    "v_mfma_f32_32x32x16_bf16 v[32:47], v[224:227], v[184:187], v[32:47]\n"
    "s_waitcnt lgkmcnt(6)\n"
    "ds_read_b64_tr_b16 v[220:221], v10 offset:49152\n"
    "ds_read_b64_tr_b16 v[222:223], v10 offset:50176\n"
    "s_waitcnt vmcnt(4)\n"
    "v_mfma_f32_32x32x16_bf16 v[32:47], v[228:231], v[188:191], v[32:47]\n"
    "s_waitcnt lgkmcnt(6)\n"
    "ds_read_b64_tr_b16 v[224:225], v11\n"
    "ds_read_b64_tr_b16 v[226:227], v11 offset:1024\n"
    "s_waitcnt vmcnt(3)\n"
    "v_mfma_f32_32x32x16_bf16 v[32:47], v[208:211], v[192:195], v[32:47]\n"
    "s_waitcnt lgkmcnt(6)\n"
    "ds_read_b64_tr_b16 v[228:229], v11 offset:4096\n"
    "ds_read_b64_tr_b16 v[230:231], v11 offset:5120\n"
    "s_waitcnt vmcnt(2)\n"
    "v_mfma_f32_32x32x16_bf16 v[32:47], v[212:215], v[196:199], v[32:47]\n"
    "s_waitcnt lgkmcnt(6)\n"
    "ds_read_b64_tr_b16 v[208:209], v11 offset:8192\n"
    "ds_read_b64_tr_b16 v[210:211], v11 offset:9216\n"
    "s_waitcnt vmcnt(1)\n"
    "v_mfma_f32_32x32x16_bf16 v[32:47], v[216:219], v[200:203], v[32:47]\n"
    "s_waitcnt lgkmcnt(6)\n"
    "ds_read_b64_tr_b16 v[212:213], v11 offset:12288\n"
    "ds_read_b64_tr_b16 v[214:215], v11 offset:13312\n"
    "s_waitcnt vmcnt(0)\n"
    "v_mfma_f32_32x32x16_bf16 v[32:47], v[220:223], v[204:207], v[32:47]\n"
    "s_waitcnt lgkmcnt(6)\n"
    "ds_read_b64_tr_b16 v[216:217], v11 offset:16384\n"
    "ds_read_b64_tr_b16 v[218:219], v11 offset:17408\n"
    "v_mfma_f32_32x32x16_bf16 v[48:63], v[224:227], v[96:99], 0\n"
    "s_waitcnt lgkmcnt(6)\n"
    "ds_read_b64_tr_b16 v[220:221], v11 offset:20480\n"
    "ds_read_b64_tr_b16 v[222:223], v11 offset:21504\n"
    "v_mfma_f32_32x32x16_bf16 v[48:63], v[228:231], v[100:103], v[48:63]\n"
    "s_waitcnt lgkmcnt(6)\n"
    "ds_read_b64_tr_b16 v[224:225], v11 offset:24576\n"
    "ds_read_b64_tr_b16 v[226:227], v11 offset:25600\n"
    "s_nop 0\n"
    "v_mul_f32 v24, v32, v32\n"
    "v_mul_f32 v26, v33, v33\n"
    "v_mul_f32 v28, v34, v34\n"
    "v_fma_f32 v24, v24, s55, v30\n"
    "v_mfma_f32_32x32x16_bf16 v[48:63], v[208:211], v[104:107], v[48:63]\n"
    "s_waitcnt lgkmcnt(6)\n"
    "ds_read_b64_tr_b16 v[228:229], v11 offset:28672\n"
    "ds_read_b64_tr_b16 v[230:231], v11 offset:29696\n"
    "v_fma_f32 v26, v26, s55, v30\n"
    "v_fma_f32 v28, v28, s55, v30\n"
    "v_mul_f32 v24, v24, v32\n"
    "v_mul_f32 v26, v26, v33\n"
    "v_mfma_f32_32x32x16_bf16 v[48:63], v[212:215], v[108:111], v[48:63]\n"
    "s_waitcnt lgkmcnt(6)\n"
    "ds_read_b64_tr_b16 v[208:209], v11 offset:32768\n"
    "ds_read_b64_tr_b16 v[210:211], v11 offset:33792\n"
    "v_mul_f32 v28, v28, v34\n"
    "v_exp_f32 v24, v24\n"
    "v_mfma_f32_32x32x16_bf16 v[48:63], v[216:219], v[112:115], v[48:63]\n"
    "s_waitcnt lgkmcnt(6)\n"
    "ds_read_b64_tr_b16 v[212:213], v11 offset:36864\n"
    "ds_read_b64_tr_b16 v[214:215], v11 offset:37888\n"
    "v_exp_f32 v26, v26\n"
    "v_exp_f32 v28, v28\n"
    "v_mfma_f32_32x32x16_bf16 v[48:63], v[220:223], v[116:119], v[48:63]\n"
    "s_waitcnt lgkmcnt(6)\n"
    "ds_read_b64_tr_b16 v[216:217], v11 offset:40960\n"
    "ds_read_b64_tr_b16 v[218:219], v11 offset:41984\n"
    "v_add_f32 v24, 1.0, v24\n"
    "v_add_f32 v26, 1.0, v26\n"
    "v_add_f32 v28, 1.0, v28\n"
    "v_mfma_f32_32x32x16_bf16 v[48:63], v[224:227], v[120:123], v[48:63]\n"
    "s_waitcnt lgkmcnt(6)\n"
    "ds_read_b64_tr_b16 v[220:221], v11 offset:45056\n"
    "ds_read_b64_tr_b16 v[222:223], v11 offset:46080\n"
    "v_rcp_f32 v24, v24\n"
    "v_rcp_f32 v26, v26\n"
    "v_mfma_f32_32x32x16_bf16 v[48:63], v[228:231], v[124:127], v[48:63]\n"
    "s_waitcnt lgkmcnt(6)\n"
    "ds_read_b64_tr_b16 v[224:225], v11 offset:49152\n"
    "ds_read_b64_tr_b16 v[226:227], v11 offset:50176\n"
    "v_rcp_f32 v28, v28\n"
    "v_mul_f32 v32, v32, v24\n"
    "v_mul_f32 v33, v33, v26\n"
    "v_mfma_f32_32x32x16_bf16 v[48:63], v[208:211], v[128:131], v[48:63]\n"
    "s_waitcnt lgkmcnt(6)\n"
    "ds_read_b64_tr_b16 v[228:229], v11 offset:53248\n"
    "ds_read_b64_tr_b16 v[230:231], v11 offset:54272\n"
    "v_mul_f32 v34, v34, v28\n"
    "v_add_f32 v20, v20, v32\n"
    "v_fma_f32 v21, v32, v32, v21\n"
    "v_add_f32 v20, v20, v33\n"
    "v_mfma_f32_32x32x16_bf16 v[48:63], v[212:215], v[132:135], v[48:63]\n"
    "s_waitcnt lgkmcnt(6)\n"
    "ds_read_b64_tr_b16 v[208:209], v11 offset:57344\n"
    "ds_read_b64_tr_b16 v[210:211], v11 offset:58368\n"
    "v_fma_f32 v21, v33, v33, v21\n"
    "v_add_f32 v20, v20, v34\n"
    "v_fma_f32 v21, v34, v34, v21\n"
    "v_mul_f32 v24, v35, v35\n"
    "v_mfma_f32_32x32x16_bf16 v[48:63], v[216:219], v[136:139], v[48:63]\n"
    "s_waitcnt lgkmcnt(6)\n"
    "v_add_u32 v11, 0xf000, v11\n"
    "ds_read_b64_tr_b16 v[212:213], v11\n"
    "ds_read_b64_tr_b16 v[214:215], v11 offset:1024\n"
    "v_mul_f32 v26, v36, v36\n"
    "v_mul_f32 v28, v37, v37\n"
    "v_fma_f32 v24, v24, s55, v30\n"
    "v_mfma_f32_32x32x16_bf16 v[48:63], v[220:223], v[140:143], v[48:63]\n"
    "s_waitcnt lgkmcnt(6)\n"
    "ds_read_b64_tr_b16 v[216:217], v11 offset:4096\n"
    "ds_read_b64_tr_b16 v[218:219], v11 offset:5120\n"
    "v_fma_f32 v26, v26, s55, v30\n"
    "v_fma_f32 v28, v28, s55, v30\n"
    "v_mul_f32 v24, v24, v35\n"
    "v_mul_f32 v26, v26, v36\n"
    "v_mfma_f32_32x32x16_bf16 v[48:63], v[224:227], v[144:147], v[48:63]\n"
    "s_waitcnt lgkmcnt(6)\n"
    "ds_read_b64_tr_b16 v[220:221], v11 offset:8192\n"
    "ds_read_b64_tr_b16 v[222:223], v11 offset:9216\n"
    "v_mul_f32 v28, v28, v37\n"
    "v_exp_f32 v24, v24\n"
    "v_mfma_f32_32x32x16_bf16 v[48:63], v[228:231], v[148:151], v[48:63]\n"
    "s_waitcnt lgkmcnt(6)\n"
    "ds_read_b64_tr_b16 v[224:225], v11 offset:12288\n"
    "ds_read_b64_tr_b16 v[226:227], v11 offset:13312\n"
    "v_exp_f32 v26, v26\n"
    "v_exp_f32 v28, v28\n"
    "v_mfma_f32_32x32x16_bf16 v[48:63], v[208:211], v[152:155], v[48:63]\n"
    "s_waitcnt lgkmcnt(6)\n"
    "ds_read_b64_tr_b16 v[228:229], v11 offset:16384\n"
    "ds_read_b64_tr_b16 v[230:231], v11 offset:17408\n"
    "v_add_f32 v24, 1.0, v24\n"
    "v_add_f32 v26, 1.0, v26\n"
    "v_add_f32 v28, 1.0, v28\n"
    "v_mfma_f32_32x32x16_bf16 v[48:63], v[212:215], v[156:159], v[48:63]\n"
    "s_waitcnt lgkmcnt(6)\n"
    "ds_read_b64_tr_b16 v[208:209], v11 offset:20480\n"
    "ds_read_b64_tr_b16 v[210:211], v11 offset:21504\n"
    "v_rcp_f32 v24, v24\n"
    "v_rcp_f32 v26, v26\n"
    "v_mfma_f32_32x32x16_bf16 v[48:63], v[216:219], v[160:163], v[48:63]\n"
    "s_waitcnt lgkmcnt(6)\n"
    "ds_read_b64_tr_b16 v[212:213], v11 offset:24576\n"
    "ds_read_b64_tr_b16 v[214:215], v11 offset:25600\n"
    "v_rcp_f32 v28, v28\n"
    "v_mul_f32 v35, v35, v24\n"
    "v_mul_f32 v36, v36, v26\n"
    "v_mfma_f32_32x32x16_bf16 v[48:63], v[220:223], v[164:167], v[48:63]\n"
    "s_waitcnt lgkmcnt(6)\n"
    "ds_read_b64_tr_b16 v[216:217], v11 offset:28672\n"
    "ds_read_b64_tr_b16 v[218:219], v11 offset:29696\n"
    "v_mul_f32 v37, v37, v28\n"
    "v_add_f32 v20, v20, v35\n"
    "v_fma_f32 v21, v35, v35, v21\n"
    "v_add_f32 v20, v20, v36\n"
    "v_mfma_f32_32x32x16_bf16 v[48:63], v[224:227], v[168:171], v[48:63]\n"
    "s_waitcnt lgkmcnt(6)\n"
    "ds_read_b64_tr_b16 v[220:221], v11 offset:32768\n"
    "ds_read_b64_tr_b16 v[222:223], v11 offset:33792\n"
    "v_fma_f32 v21, v36, v36, v21\n"
    "v_add_f32 v20, v20, v37\n"
    "v_fma_f32 v21, v37, v37, v21\n"
    "v_mul_f32 v24, v38, v38\n"
    "v_mfma_f32_32x32x16_bf16 v[48:63], v[228:231], v[172:175], v[48:63]\n"
    "s_waitcnt lgkmcnt(6)\n"
    "ds_read_b64_tr_b16 v[224:225], v11 offset:36864\n"
    "ds_read_b64_tr_b16 v[226:227], v11 offset:37888\n"
    "v_mul_f32 v26, v39, v39\n"
    "v_mul_f32 v28, v40, v40\n"
    "v_fma_f32 v24, v24, s55, v30\n"
    "v_fma_f32 v26, v26, s55, v30\n"
    "v_mfma_f32_32x32x16_bf16 v[48:63], v[208:211], v[176:179], v[48:63]\n"
    "s_waitcnt lgkmcnt(6)\n"
    "ds_read_b64_tr_b16 v[228:229], v11 offset:40960\n"
    "ds_read_b64_tr_b16 v[230:231], v11 offset:41984\n"
    "v_fma_f32 v28, v28, s55, v30\n"
    "v_mul_f32 v24, v24, v38\n"
    "v_mul_f32 v26, v26, v39\n"
    "v_mul_f32 v28, v28, v40\n"
    "v_mfma_f32_32x32x16_bf16 v[48:63], v[212:215], v[180:183], v[48:63]\n"
    "s_waitcnt lgkmcnt(6)\n"
    "ds_read_b64_tr_b16 v[208:209], v11 offset:45056\n"
    "ds_read_b64_tr_b16 v[210:211], v11 offset:46080\n"
    "v_exp_f32 v24, v24\n"
    "v_exp_f32 v26, v26\n"
    "v_mfma_f32_32x32x16_bf16 v[48:63], v[216:219], v[184:187], v[48:63]\n"
    "s_waitcnt lgkmcnt(6)\n"
    "ds_read_b64_tr_b16 v[212:213], v11 offset:49152\n"
    "ds_read_b64_tr_b16 v[214:215], v11 offset:50176\n"
    "v_exp_f32 v28, v28\n"
    "v_add_f32 v24, 1.0, v24\n"
    "v_add_f32 v26, 1.0, v26\n"
    "v_mfma_f32_32x32x16_bf16 v[48:63], v[220:223], v[188:191], v[48:63]\n"
    "s_waitcnt lgkmcnt(6)\n"
    "ds_read_b64_tr_b16 v[216:217], v12\n"
    "ds_read_b64_tr_b16 v[218:219], v12 offset:1024\n"
    "v_add_f32 v28, 1.0, v28\n"
    "v_rcp_f32 v24, v24\n"
    "v_mfma_f32_32x32x16_bf16 v[48:63], v[224:227], v[192:195], v[48:63]\n"
    "s_waitcnt lgkmcnt(6)\n"
    "ds_read_b64_tr_b16 v[220:221], v12 offset:4096\n"
    "ds_read_b64_tr_b16 v[222:223], v12 offset:5120\n"
    "v_rcp_f32 v26, v26\n"
    "v_rcp_f32 v28, v28\n"
    "v_mfma_f32_32x32x16_bf16 v[48:63], v[228:231], v[196:199], v[48:63]\n"
    "s_waitcnt lgkmcnt(6)\n"
    "ds_read_b64_tr_b16 v[224:225], v12 offset:8192\n"
    "ds_read_b64_tr_b16 v[226:227], v12 offset:9216\n"
    "v_mul_f32 v38, v38, v24\n"
    "v_mul_f32 v39, v39, v26\n"
    "v_mul_f32 v40, v40, v28\n"
    "v_add_f32 v20, v20, v38\n"
    "v_mfma_f32_32x32x16_bf16 v[48:63], v[208:211], v[200:203], v[48:63]\n"
    "s_waitcnt lgkmcnt(6)\n"
    "ds_read_b64_tr_b16 v[228:229], v12 offset:12288\n"
    "ds_read_b64_tr_b16 v[230:231], v12 offset:13312\n"
    "v_fma_f32 v21, v38, v38, v21\n"
    "v_add_f32 v20, v20, v39\n"
    "v_fma_f32 v21, v39, v39, v21\n"
    "v_add_f32 v20, v20, v40\n"
    "v_mfma_f32_32x32x16_bf16 v[48:63], v[212:215], v[204:207], v[48:63]\n"
    "s_waitcnt lgkmcnt(6)\n"
    "ds_read_b64_tr_b16 v[208:209], v12 offset:16384\n"
    "ds_read_b64_tr_b16 v[210:211], v12 offset:17408\n"
    "v_fma_f32 v21, v40, v40, v21\n"
    "v_mul_f32 v24, v41, v41\n"
    "v_mul_f32 v26, v42, v42\n"
    "v_mul_f32 v28, v43, v43\n"
    "v_mfma_f32_32x32x16_bf16 v[64:79], v[216:219], v[96:99], 0\n"
    "s_waitcnt lgkmcnt(6)\n"
    "ds_read_b64_tr_b16 v[212:213], v12 offset:20480\n"
    "ds_read_b64_tr_b16 v[214:215], v12 offset:21504\n"
    "v_fma_f32 v24, v24, s55, v30\n"
    "v_fma_f32 v26, v26, s55, v30\n"
    "v_fma_f32 v28, v28, s55, v30\n"
    "v_mul_f32 v24, v24, v41\n"
    "v_mfma_f32_32x32x16_bf16 v[64:79], v[220:223], v[100:103], v[64:79]\n"
    "s_waitcnt lgkmcnt(6)\n"
    "ds_read_b64_tr_b16 v[216:217], v12 offset:24576\n"
    "ds_read_b64_tr_b16 v[218:219], v12 offset:25600\n"
    "v_mul_f32 v26, v26, v42\n"
    "v_mul_f32 v28, v28, v43\n"
    "v_exp_f32 v24, v24\n"
    "v_mfma_f32_32x32x16_bf16 v[64:79], v[224:227], v[104:107], v[64:79]\n"
    "s_waitcnt lgkmcnt(6)\n"
    "ds_read_b64_tr_b16 v[220:221], v12 offset:28672\n"
    "ds_read_b64_tr_b16 v[222:223], v12 offset:29696\n"
    "v_exp_f32 v26, v26\n"
    "v_exp_f32 v28, v28\n"
    "v_mfma_f32_32x32x16_bf16 v[64:79], v[228:231], v[108:111], v[64:79]\n"
    "s_waitcnt lgkmcnt(6)\n"
    "ds_read_b64_tr_b16 v[224:225], v12 offset:32768\n"
    "ds_read_b64_tr_b16 v[226:227], v12 offset:33792\n"
    "v_add_f32 v24, 1.0, v24\n"
    "v_add_f32 v26, 1.0, v26\n"
    "v_add_f32 v28, 1.0, v28\n"
    "v_rcp_f32 v24, v24\n"
    "v_rcp_f32 v26, v26\n"
    "v_rcp_f32 v28, v28\n"
    "v_mul_f32 v41, v41, v24\n"
    "v_mul_f32 v42, v42, v26\n"
    "v_mul_f32 v43, v43, v28\n"
    "v_add_f32 v20, v20, v41\n"
    "v_fma_f32 v21, v41, v41, v21\n"
    "v_add_f32 v20, v20, v42\n"
    "v_fma_f32 v21, v42, v42, v21\n"
    "v_add_f32 v20, v20, v43\n"
    "v_fma_f32 v21, v43, v43, v21\n"
    "v_mul_f32 v24, v44, v44\n"
    "v_mul_f32 v26, v45, v45\n"
    "v_mul_f32 v28, v46, v46\n"
    "v_fma_f32 v24, v24, s55, v30\n"
    "v_fma_f32 v26, v26, s55, v30\n"
    "v_fma_f32 v28, v28, s55, v30\n"
    "v_mul_f32 v24, v24, v44\n"
    "v_mul_f32 v26, v26, v45\n"
    "v_mul_f32 v28, v28, v46\n"
    "v_exp_f32 v24, v24\n"
    "v_exp_f32 v26, v26\n"
    "v_exp_f32 v28, v28\n"
    "v_add_f32 v24, 1.0, v24\n"
    "v_add_f32 v26, 1.0, v26\n"
    "v_add_f32 v28, 1.0, v28\n"
    "v_rcp_f32 v24, v24\n"
    "v_rcp_f32 v26, v26\n"
    "v_rcp_f32 v28, v28\n"
    "v_mul_f32 v44, v44, v24\n"
    "v_mul_f32 v45, v45, v26\n"
    "v_mul_f32 v46, v46, v28\n"
    "v_add_f32 v20, v20, v44\n"
    "v_fma_f32 v21, v44, v44, v21\n"
    "v_add_f32 v20, v20, v45\n"
    "v_fma_f32 v21, v45, v45, v21\n"
    "v_add_f32 v20, v20, v46\n"
    "v_fma_f32 v21, v46, v46, v21\n"
    "v_mul_f32 v24, v47, v47\n"
    "v_fma_f32 v24, v24, s55, v30\n"
    "v_mul_f32 v24, v24, v47\n"
    "v_exp_f32 v24, v24\n"
    "s_nop 0\n"
    "v_add_f32 v24, 1.0, v24\n"
    "v_rcp_f32 v24, v24\n"
    "s_nop 0\n"
    "v_mul_f32 v47, v47, v24\n"
    "v_add_f32 v20, v20, v47\n"
    "v_fma_f32 v21, v47, v47, v21\n"
    "v_pk_mul_f32 v[24:25], v[48:49], v[48:49]\n"
    "v_pk_mul_f32 v[26:27], v[50:51], v[50:51]\n"
    "v_pk_mul_f32 v[28:29], v[52:53], v[52:53]\n"
    "v_pk_fma_f32 v[24:25], v[24:25], v[30:31], v[30:31] op_sel:[0,1,0] op_sel_hi:[1,1,0]\n"
    "v_pk_fma_f32 v[26:27], v[26:27], v[30:31], v[30:31] op_sel:[0,1,0] op_sel_hi:[1,1,0]\n"
    "v_pk_fma_f32 v[28:29], v[28:29], v[30:31], v[30:31] op_sel:[0,1,0] op_sel_hi:[1,1,0]\n"
    "v_pk_mul_f32 v[24:25], v[24:25], v[48:49]\n"
    "v_pk_mul_f32 v[26:27], v[26:27], v[50:51]\n"
    "v_pk_mul_f32 v[28:29], v[28:29], v[52:53]\n"
    "v_exp_f32 v24, v24\n"
    "v_exp_f32 v25, v25\n"
    "v_exp_f32 v26, v26\n"
    "v_exp_f32 v27, v27\n"
    "v_exp_f32 v28, v28\n"
    "v_exp_f32 v29, v29\n"
    "v_add_f32 v24, 1.0, v24\n"
    "v_add_f32 v25, 1.0, v25\n"
    "v_add_f32 v26, 1.0, v26\n"
    "v_add_f32 v27, 1.0, v27\n"
    "v_add_f32 v28, 1.0, v28\n"
    "v_add_f32 v29, 1.0, v29\n"
    "v_rcp_f32 v24, v24\n"
    "v_rcp_f32 v25, v25\n"
    "v_rcp_f32 v26, v26\n"
    "v_rcp_f32 v27, v27\n"
    "v_rcp_f32 v28, v28\n"
    "v_rcp_f32 v29, v29\n"
    "v_pk_mul_f32 v[48:49], v[48:49], v[24:25]\n"
    "v_pk_mul_f32 v[50:51], v[50:51], v[26:27]\n"
    "v_pk_mul_f32 v[52:53], v[52:53], v[28:29]\n"
    "v_add_f32 v20, v20, v48\n"
    "v_fma_f32 v21, v48, v48, v21\n"
    "v_add_f32 v20, v20, v49\n"
    "v_fma_f32 v21, v49, v49, v21\n"
    "v_add_f32 v20, v20, v50\n"
    "v_fma_f32 v21, v50, v50, v21\n"
    "v_add_f32 v20, v20, v51\n"
    "v_fma_f32 v21, v51, v51, v21\n"
    "v_add_f32 v20, v20, v52\n"
    "v_fma_f32 v21, v52, v52, v21\n"
    "v_add_f32 v20, v20, v53\n"
    "v_fma_f32 v21, v53, v53, v21\n"
    "v_pk_mul_f32 v[24:25], v[54:55], v[54:55]\n"
    "v_pk_mul_f32 v[26:27], v[56:57], v[56:57]\n"
    "v_pk_mul_f32 v[28:29], v[58:59], v[58:59]\n"
    "v_pk_fma_f32 v[24:25], v[24:25], v[30:31], v[30:31] op_sel:[0,1,0] op_sel_hi:[1,1,0]\n"
    "v_pk_fma_f32 v[26:27], v[26:27], v[30:31], v[30:31] op_sel:[0,1,0] op_sel_hi:[1,1,0]\n"
    "v_pk_fma_f32 v[28:29], v[28:29], v[30:31], v[30:31] op_sel:[0,1,0] op_sel_hi:[1,1,0]\n"
    "v_pk_mul_f32 v[24:25], v[24:25], v[54:55]\n"
    "v_pk_mul_f32 v[26:27], v[26:27], v[56:57]\n"
    "v_pk_mul_f32 v[28:29], v[28:29], v[58:59]\n"
    "v_exp_f32 v24, v24\n"
    "v_exp_f32 v25, v25\n"
    "v_exp_f32 v26, v26\n"
    "v_exp_f32 v27, v27\n"
    "v_exp_f32 v28, v28\n"
    "v_exp_f32 v29, v29\n"
    "v_add_f32 v24, 1.0, v24\n"
    "v_add_f32 v25, 1.0, v25\n"
    "v_add_f32 v26, 1.0, v26\n"
    "v_add_f32 v27, 1.0, v27\n"
    "v_add_f32 v28, 1.0, v28\n"
    "v_add_f32 v29, 1.0, v29\n"
    "v_rcp_f32 v24, v24\n"
    "v_rcp_f32 v25, v25\n"
    "v_rcp_f32 v26, v26\n"
    "v_rcp_f32 v27, v27\n"
    "v_rcp_f32 v28, v28\n"
    "v_rcp_f32 v29, v29\n"
    "v_pk_mul_f32 v[54:55], v[54:55], v[24:25]\n"
    "v_pk_mul_f32 v[56:57], v[56:57], v[26:27]\n"
    "v_pk_mul_f32 v[58:59], v[58:59], v[28:29]\n"
    "v_add_f32 v20, v20, v54\n"
    "v_fma_f32 v21, v54, v54, v21\n"
    "v_add_f32 v20, v20, v55\n"
    "v_fma_f32 v21, v55, v55, v21\n"
    "v_add_f32 v20, v20, v56\n"
    "v_fma_f32 v21, v56, v56, v21\n"
    "v_add_f32 v20, v20, v57\n"
    "v_fma_f32 v21, v57, v57, v21\n"
    "v_add_f32 v20, v20, v58\n"
    "v_fma_f32 v21, v58, v58, v21\n"
    "v_add_f32 v20, v20, v59\n"
    "v_fma_f32 v21, v59, v59, v21\n"
    "v_pk_mul_f32 v[24:25], v[60:61], v[60:61]\n"
    "v_pk_mul_f32 v[26:27], v[62:63], v[62:63]\n"
    "v_pk_fma_f32 v[24:25], v[24:25], v[30:31], v[30:31] op_sel:[0,1,0] op_sel_hi:[1,1,0]\n"
    "v_pk_fma_f32 v[26:27], v[26:27], v[30:31], v[30:31] op_sel:[0,1,0] op_sel_hi:[1,1,0]\n"
    "v_pk_mul_f32 v[24:25], v[24:25], v[60:61]\n"
    "v_pk_mul_f32 v[26:27], v[26:27], v[62:63]\n"
    "v_exp_f32 v24, v24\n"
    "v_exp_f32 v25, v25\n"
    "v_exp_f32 v26, v26\n"
    "v_exp_f32 v27, v27\n"
    "v_add_f32 v24, 1.0, v24\n"
    "v_add_f32 v25, 1.0, v25\n"
    "v_add_f32 v26, 1.0, v26\n"
    "v_add_f32 v27, 1.0, v27\n"
    "v_rcp_f32 v24, v24\n"
    "v_rcp_f32 v25, v25\n"
    "v_rcp_f32 v26, v26\n"
    "v_rcp_f32 v27, v27\n"
    "v_pk_mul_f32 v[60:61], v[60:61], v[24:25]\n"
    "v_pk_mul_f32 v[62:63], v[62:63], v[26:27]\n"
    "v_add_f32 v20, v20, v60\n"
    "v_fma_f32 v21, v60, v60, v21\n"
    "v_add_f32 v20, v20, v61\n"
    "v_fma_f32 v21, v61, v61, v21\n"
    "v_add_f32 v20, v20, v62\n"
    "v_fma_f32 v21, v62, v62, v21\n"
    "v_add_f32 v20, v20, v63\n"
    "v_fma_f32 v21, v63, v63, v21\n"
    "v_mov_b32 v19, v20\n"
    "s_nop 1\n"
    "v_permlane32_swap_b32 v19, v20\n"
    "v_add_f32 v248, v20, v19\n"
    "v_mov_b32 v19, v21\n"
    "s_nop 1\n"
    "v_permlane32_swap_b32 v19, v21\n"
    "v_add_f32 v249, v21, v19\n"
    "buffer_store_dwordx4 v[248:251], v232, s[28:31], s57 offen sc1\n"
    "v_mfma_f32_32x32x16_bf16 v[64:79], v[208:211], v[112:115], v[64:79]\n"
    "s_waitcnt lgkmcnt(6)\n"
    "ds_read_b64_tr_b16 v[228:229], v12 offset:36864\n"
    "ds_read_b64_tr_b16 v[230:231], v12 offset:37888\n"
    "v_mfma_f32_32x32x16_bf16 v[64:79], v[212:215], v[116:119], v[64:79]\n"
    "s_waitcnt lgkmcnt(6)\n"
    "ds_read_b64_tr_b16 v[208:209], v12 offset:40960\n"
    "ds_read_b64_tr_b16 v[210:211], v12 offset:41984\n"
    "v_mfma_f32_32x32x16_bf16 v[64:79], v[216:219], v[120:123], v[64:79]\n"
    "s_waitcnt lgkmcnt(6)\n"
    "ds_read_b64_tr_b16 v[212:213], v12 offset:45056\n"
    "ds_read_b64_tr_b16 v[214:215], v12 offset:46080\n"
    "v_mfma_f32_32x32x16_bf16 v[64:79], v[220:223], v[124:127], v[64:79]\n"
    "s_waitcnt lgkmcnt(6)\n"
    "ds_read_b64_tr_b16 v[216:217], v12 offset:49152\n"
    "ds_read_b64_tr_b16 v[218:219], v12 offset:50176\n"
    "v_mfma_f32_32x32x16_bf16 v[64:79], v[224:227], v[128:131], v[64:79]\n"
    "s_waitcnt lgkmcnt(6)\n"
    "ds_read_b64_tr_b16 v[220:221], v12 offset:53248\n"
    "ds_read_b64_tr_b16 v[222:223], v12 offset:54272\n"
    "v_mfma_f32_32x32x16_bf16 v[64:79], v[228:231], v[132:135], v[64:79]\n"
    "s_waitcnt lgkmcnt(6)\n"
    "ds_read_b64_tr_b16 v[224:225], v12 offset:57344\n"
    "ds_read_b64_tr_b16 v[226:227], v12 offset:58368\n"
    "v_mfma_f32_32x32x16_bf16 v[64:79], v[208:211], v[136:139], v[64:79]\n"
    "s_waitcnt lgkmcnt(6)\n"
    "v_add_u32 v12, 0xf000, v12\n"
    "ds_read_b64_tr_b16 v[228:229], v12\n"
    "ds_read_b64_tr_b16 v[230:231], v12 offset:1024\n"
    "v_mfma_f32_32x32x16_bf16 v[64:79], v[212:215], v[140:143], v[64:79]\n"
    "s_waitcnt lgkmcnt(6)\n"
    "ds_read_b64_tr_b16 v[208:209], v12 offset:4096\n"
    "ds_read_b64_tr_b16 v[210:211], v12 offset:5120\n"
    "v_mfma_f32_32x32x16_bf16 v[64:79], v[216:219], v[144:147], v[64:79]\n"
    "s_waitcnt lgkmcnt(6)\n"
    "ds_read_b64_tr_b16 v[212:213], v12 offset:8192\n"
    "ds_read_b64_tr_b16 v[214:215], v12 offset:9216\n"
    "v_mfma_f32_32x32x16_bf16 v[64:79], v[220:223], v[148:151], v[64:79]\n"
    "s_waitcnt lgkmcnt(6)\n"
    "ds_read_b64_tr_b16 v[216:217], v12 offset:12288\n"
    "ds_read_b64_tr_b16 v[218:219], v12 offset:13312\n"
    "v_mfma_f32_32x32x16_bf16 v[64:79], v[224:227], v[152:155], v[64:79]\n"
    "s_waitcnt lgkmcnt(6)\n"
    "ds_read_b64_tr_b16 v[220:221], v12 offset:16384\n"
    "ds_read_b64_tr_b16 v[222:223], v12 offset:17408\n"
    "v_mfma_f32_32x32x16_bf16 v[64:79], v[228:231], v[156:159], v[64:79]\n"
    "s_waitcnt lgkmcnt(6)\n"
    "ds_read_b64_tr_b16 v[224:225], v12 offset:20480\n"
    "ds_read_b64_tr_b16 v[226:227], v12 offset:21504\n"
    "v_mfma_f32_32x32x16_bf16 v[64:79], v[208:211], v[160:163], v[64:79]\n"
    "s_waitcnt lgkmcnt(6)\n"
    "ds_read_b64_tr_b16 v[228:229], v12 offset:24576\n"
    "ds_read_b64_tr_b16 v[230:231], v12 offset:25600\n"
    "v_mfma_f32_32x32x16_bf16 v[64:79], v[212:215], v[164:167], v[64:79]\n"
    "s_waitcnt lgkmcnt(6)\n"
    "ds_read_b64_tr_b16 v[208:209], v12 offset:28672\n"
    "ds_read_b64_tr_b16 v[210:211], v12 offset:29696\n"
    "v_mfma_f32_32x32x16_bf16 v[64:79], v[216:219], v[168:171], v[64:79]\n"
    "s_waitcnt lgkmcnt(6)\n"
    "ds_read_b64_tr_b16 v[212:213], v12 offset:32768\n"
    "ds_read_b64_tr_b16 v[214:215], v12 offset:33792\n"
    "v_mfma_f32_32x32x16_bf16 v[64:79], v[220:223], v[172:175], v[64:79]\n"
    "s_waitcnt lgkmcnt(6)\n"
    "ds_read_b64_tr_b16 v[216:217], v12 offset:36864\n"
    "ds_read_b64_tr_b16 v[218:219], v12 offset:37888\n"
    "v_mfma_f32_32x32x16_bf16 v[64:79], v[224:227], v[176:179], v[64:79]\n"
    "s_waitcnt lgkmcnt(6)\n"
    "ds_read_b64_tr_b16 v[220:221], v12 offset:40960\n"
    "ds_read_b64_tr_b16 v[222:223], v12 offset:41984\n"
    "v_mfma_f32_32x32x16_bf16 v[64:79], v[228:231], v[180:183], v[64:79]\n"
    "s_waitcnt lgkmcnt(6)\n"
    "ds_read_b64_tr_b16 v[224:225], v12 offset:45056\n"
    "ds_read_b64_tr_b16 v[226:227], v12 offset:46080\n"
    "v_mfma_f32_32x32x16_bf16 v[64:79], v[208:211], v[184:187], v[64:79]\n"
    "s_waitcnt lgkmcnt(6)\n"
    "ds_read_b64_tr_b16 v[228:229], v12 offset:49152\n"
    "ds_read_b64_tr_b16 v[230:231], v12 offset:50176\n"
    "v_mfma_f32_32x32x16_bf16 v[64:79], v[212:215], v[188:191], v[64:79]\n"
    "s_waitcnt lgkmcnt(6)\n"
    "ds_read_b64_tr_b16 v[208:209], v13\n"
    "ds_read_b64_tr_b16 v[210:211], v13 offset:1024\n"
    "v_mfma_f32_32x32x16_bf16 v[64:79], v[216:219], v[192:195], v[64:79]\n"
    "s_waitcnt lgkmcnt(6)\n"
    "ds_read_b64_tr_b16 v[212:213], v13 offset:4096\n"
    "ds_read_b64_tr_b16 v[214:215], v13 offset:5120\n"
    "v_mfma_f32_32x32x16_bf16 v[64:79], v[220:223], v[196:199], v[64:79]\n"
    "s_waitcnt lgkmcnt(6)\n"
    "ds_read_b64_tr_b16 v[216:217], v13 offset:8192\n"
    "ds_read_b64_tr_b16 v[218:219], v13 offset:9216\n"
    "v_mfma_f32_32x32x16_bf16 v[64:79], v[224:227], v[200:203], v[64:79]\n"
    "s_waitcnt lgkmcnt(6)\n"
    "ds_read_b64_tr_b16 v[220:221], v13 offset:12288\n"
    "ds_read_b64_tr_b16 v[222:223], v13 offset:13312\n"
    "v_mfma_f32_32x32x16_bf16 v[64:79], v[228:231], v[204:207], v[64:79]\n"
    "s_waitcnt lgkmcnt(6)\n"
    "ds_read_b64_tr_b16 v[224:225], v13 offset:16384\n"
    "ds_read_b64_tr_b16 v[226:227], v13 offset:17408\n"
    "v_mfma_f32_32x32x16_bf16 v[80:95], v[208:211], v[96:99], 0\n"
    "s_waitcnt lgkmcnt(6)\n"
    "ds_read_b64_tr_b16 v[228:229], v13 offset:20480\n"
    "ds_read_b64_tr_b16 v[230:231], v13 offset:21504\n"
    "v_mfma_f32_32x32x16_bf16 v[80:95], v[212:215], v[100:103], v[80:95]\n"
    "s_waitcnt lgkmcnt(6)\n"
    "ds_read_b64_tr_b16 v[208:209], v13 offset:24576\n"
    "ds_read_b64_tr_b16 v[210:211], v13 offset:25600\n"
    "s_nop 0\n"
    "v_mul_f32 v24, v64, v64\n"
    "v_mul_f32 v26, v65, v65\n"
    "v_mul_f32 v28, v66, v66\n"
    "v_fma_f32 v24, v24, s55, v30\n"
    "v_mfma_f32_32x32x16_bf16 v[80:95], v[216:219], v[104:107], v[80:95]\n"
    "s_waitcnt lgkmcnt(6)\n"
    "ds_read_b64_tr_b16 v[212:213], v13 offset:28672\n"
    "ds_read_b64_tr_b16 v[214:215], v13 offset:29696\n"
    "v_fma_f32 v26, v26, s55, v30\n"
    "v_fma_f32 v28, v28, s55, v30\n"
    "v_mul_f32 v24, v24, v64\n"
    "v_mul_f32 v26, v26, v65\n"
    "v_mfma_f32_32x32x16_bf16 v[80:95], v[220:223], v[108:111], v[80:95]\n"
    "s_waitcnt lgkmcnt(6)\n"
    "ds_read_b64_tr_b16 v[216:217], v13 offset:32768\n"
    "ds_read_b64_tr_b16 v[218:219], v13 offset:33792\n"
    "v_mul_f32 v28, v28, v66\n"
    "v_exp_f32 v24, v24\n"
    "v_mfma_f32_32x32x16_bf16 v[80:95], v[224:227], v[112:115], v[80:95]\n"
    "s_waitcnt lgkmcnt(6)\n"
    "ds_read_b64_tr_b16 v[220:221], v13 offset:36864\n"
    "ds_read_b64_tr_b16 v[222:223], v13 offset:37888\n"
    "v_exp_f32 v26, v26\n"
    "v_exp_f32 v28, v28\n"
    "v_mfma_f32_32x32x16_bf16 v[80:95], v[228:231], v[116:119], v[80:95]\n"
    "s_waitcnt lgkmcnt(6)\n"
    "ds_read_b64_tr_b16 v[224:225], v13 offset:40960\n"
    "ds_read_b64_tr_b16 v[226:227], v13 offset:41984\n"
    "v_add_f32 v24, 1.0, v24\n"
    "v_add_f32 v26, 1.0, v26\n"
    "v_add_f32 v28, 1.0, v28\n"
    "v_mfma_f32_32x32x16_bf16 v[80:95], v[208:211], v[120:123], v[80:95]\n"
    "s_waitcnt lgkmcnt(6)\n"
    "ds_read_b64_tr_b16 v[228:229], v13 offset:45056\n"
    "ds_read_b64_tr_b16 v[230:231], v13 offset:46080\n"
    "v_rcp_f32 v24, v24\n"
    "v_rcp_f32 v26, v26\n"
    "v_mfma_f32_32x32x16_bf16 v[80:95], v[212:215], v[124:127], v[80:95]\n"
    "s_waitcnt lgkmcnt(6)\n"
    "ds_read_b64_tr_b16 v[208:209], v13 offset:49152\n"
    "ds_read_b64_tr_b16 v[210:211], v13 offset:50176\n"
    "v_rcp_f32 v28, v28\n"
    "v_mul_f32 v64, v64, v24\n"
    "v_mul_f32 v65, v65, v26\n"
    "v_mfma_f32_32x32x16_bf16 v[80:95], v[216:219], v[128:131], v[80:95]\n"
    "s_waitcnt lgkmcnt(6)\n"
    "ds_read_b64_tr_b16 v[212:213], v13 offset:53248\n"
    "ds_read_b64_tr_b16 v[214:215], v13 offset:54272\n"
    "v_mul_f32 v66, v66, v28\n"
    "v_mul_f32 v24, v67, v67\n"
    "v_mul_f32 v26, v68, v68\n"
    "v_mul_f32 v28, v69, v69\n"
    "v_mfma_f32_32x32x16_bf16 v[80:95], v[220:223], v[132:135], v[80:95]\n"
    "s_waitcnt lgkmcnt(6)\n"
    "ds_read_b64_tr_b16 v[216:217], v13 offset:57344\n"
    "ds_read_b64_tr_b16 v[218:219], v13 offset:58368\n"
    "v_fma_f32 v24, v24, s55, v30\n"
    "v_fma_f32 v26, v26, s55, v30\n"
    "v_fma_f32 v28, v28, s55, v30\n"
    "v_mul_f32 v24, v24, v67\n"
    "v_mfma_f32_32x32x16_bf16 v[80:95], v[224:227], v[136:139], v[80:95]\n"
    "s_waitcnt lgkmcnt(6)\n"
    "v_add_u32 v13, 0xf000, v13\n"
    "ds_read_b64_tr_b16 v[220:221], v13\n"
    "ds_read_b64_tr_b16 v[222:223], v13 offset:1024\n"
    "v_mul_f32 v26, v26, v68\n"
    "v_mul_f32 v28, v28, v69\n"
    "v_mfma_f32_32x32x16_bf16 v[80:95], v[228:231], v[140:143], v[80:95]\n"
    "s_waitcnt lgkmcnt(6)\n"
    "ds_read_b64_tr_b16 v[224:225], v13 offset:4096\n"
    "ds_read_b64_tr_b16 v[226:227], v13 offset:5120\n"
    "v_exp_f32 v24, v24\n"
    "v_exp_f32 v26, v26\n"
    "v_mfma_f32_32x32x16_bf16 v[80:95], v[208:211], v[144:147], v[80:95]\n"
    "s_waitcnt lgkmcnt(6)\n"
    "ds_read_b64_tr_b16 v[228:229], v13 offset:8192\n"
    "ds_read_b64_tr_b16 v[230:231], v13 offset:9216\n"
    "v_exp_f32 v28, v28\n"
    "v_add_f32 v24, 1.0, v24\n"
    "v_add_f32 v26, 1.0, v26\n"
    "v_mfma_f32_32x32x16_bf16 v[80:95], v[212:215], v[148:151], v[80:95]\n"
    "s_waitcnt lgkmcnt(6)\n"
    "ds_read_b64_tr_b16 v[208:209], v13 offset:12288\n"
    "ds_read_b64_tr_b16 v[210:211], v13 offset:13312\n"
    "v_add_f32 v28, 1.0, v28\n"
    "v_rcp_f32 v24, v24\n"
    "v_mfma_f32_32x32x16_bf16 v[80:95], v[216:219], v[152:155], v[80:95]\n"
    "s_waitcnt lgkmcnt(6)\n"
    "ds_read_b64_tr_b16 v[212:213], v13 offset:16384\n"
    "ds_read_b64_tr_b16 v[214:215], v13 offset:17408\n"
    "v_rcp_f32 v26, v26\n"
    "v_rcp_f32 v28, v28\n"
    "v_mfma_f32_32x32x16_bf16 v[80:95], v[220:223], v[156:159], v[80:95]\n"
    "s_waitcnt lgkmcnt(6)\n"
    "ds_read_b64_tr_b16 v[216:217], v13 offset:20480\n"
    "ds_read_b64_tr_b16 v[218:219], v13 offset:21504\n"
    "v_mul_f32 v67, v67, v24\n"
    "v_mul_f32 v68, v68, v26\n"
    "v_mul_f32 v69, v69, v28\n"
    "v_mul_f32 v24, v70, v70\n"
    "v_mfma_f32_32x32x16_bf16 v[80:95], v[224:227], v[160:163], v[80:95]\n"
    "s_waitcnt lgkmcnt(6)\n"
    "ds_read_b64_tr_b16 v[220:221], v13 offset:24576\n"
    "ds_read_b64_tr_b16 v[222:223], v13 offset:25600\n"
    "v_mul_f32 v26, v71, v71\n"
    "v_mul_f32 v28, v72, v72\n"
    "v_fma_f32 v24, v24, s55, v30\n"
    "v_fma_f32 v26, v26, s55, v30\n"
    "v_mfma_f32_32x32x16_bf16 v[80:95], v[228:231], v[164:167], v[80:95]\n"
    "s_waitcnt lgkmcnt(6)\n"
    "ds_read_b64_tr_b16 v[224:225], v13 offset:28672\n"
    "ds_read_b64_tr_b16 v[226:227], v13 offset:29696\n"
    "v_fma_f32 v28, v28, s55, v30\n"
    "v_mul_f32 v24, v24, v70\n"
    "v_mul_f32 v26, v26, v71\n"
    "v_mul_f32 v28, v28, v72\n"
    "v_mfma_f32_32x32x16_bf16 v[80:95], v[208:211], v[168:171], v[80:95]\n"
    "s_waitcnt lgkmcnt(6)\n"
    "ds_read_b64_tr_b16 v[228:229], v13 offset:32768\n"
    "ds_read_b64_tr_b16 v[230:231], v13 offset:33792\n"
    "v_exp_f32 v24, v24\n"
    "v_exp_f32 v26, v26\n"
    "v_mfma_f32_32x32x16_bf16 v[80:95], v[212:215], v[172:175], v[80:95]\n"
    "s_waitcnt lgkmcnt(6)\n"
    "ds_read_b64_tr_b16 v[208:209], v13 offset:36864\n"
    "ds_read_b64_tr_b16 v[210:211], v13 offset:37888\n"
    "v_exp_f32 v28, v28\n"
    "v_add_f32 v24, 1.0, v24\n"
    "v_add_f32 v26, 1.0, v26\n"
    "v_mfma_f32_32x32x16_bf16 v[80:95], v[216:219], v[176:179], v[80:95]\n"
    "s_waitcnt lgkmcnt(6)\n"
    "ds_read_b64_tr_b16 v[212:213], v13 offset:40960\n"
    "ds_read_b64_tr_b16 v[214:215], v13 offset:41984\n"
    "v_add_f32 v28, 1.0, v28\n"
    "v_rcp_f32 v24, v24\n"
    "v_mfma_f32_32x32x16_bf16 v[80:95], v[220:223], v[180:183], v[80:95]\n"
    "s_waitcnt lgkmcnt(6)\n"
    "ds_read_b64_tr_b16 v[216:217], v13 offset:45056\n"
    "ds_read_b64_tr_b16 v[218:219], v13 offset:46080\n"
    "v_rcp_f32 v26, v26\n"
    "v_rcp_f32 v28, v28\n"
    "v_mfma_f32_32x32x16_bf16 v[80:95], v[224:227], v[184:187], v[80:95]\n"
    "s_waitcnt lgkmcnt(6)\n"
    "ds_read_b64_tr_b16 v[220:221], v13 offset:49152\n"
    "ds_read_b64_tr_b16 v[222:223], v13 offset:50176\n"
    "v_mul_f32 v70, v70, v24\n"
    "v_mul_f32 v71, v71, v26\n"
    "v_mul_f32 v72, v72, v28\n"
    "v_mul_f32 v24, v73, v73\n"
    "v_mfma_f32_32x32x16_bf16 v[80:95], v[228:231], v[188:191], v[80:95]\n"
    "s_waitcnt lgkmcnt(6)\n"
    "v_mul_f32 v26, v74, v74\n"
    "v_mul_f32 v28, v75, v75\n"
    "v_fma_f32 v24, v24, s55, v30\n"
    "v_fma_f32 v26, v26, s55, v30\n"
    "v_fma_f32 v28, v28, s55, v30\n"
    "v_mul_f32 v24, v24, v73\n"
    "v_mfma_f32_32x32x16_bf16 v[80:95], v[208:211], v[192:195], v[80:95]\n"
    "s_waitcnt lgkmcnt(4)\n"
    "v_mul_f32 v26, v26, v74\n"
    "v_mul_f32 v28, v28, v75\n"
    "v_exp_f32 v24, v24\n"
    "v_exp_f32 v26, v26\n"
    "v_mfma_f32_32x32x16_bf16 v[80:95], v[212:215], v[196:199], v[80:95]\n"
    "s_waitcnt lgkmcnt(2)\n"
    "v_exp_f32 v28, v28\n"
    "v_add_f32 v24, 1.0, v24\n"
    "v_add_f32 v26, 1.0, v26\n"
    "v_add_f32 v28, 1.0, v28\n"
    "v_mfma_f32_32x32x16_bf16 v[80:95], v[216:219], v[200:203], v[80:95]\n"
    "s_waitcnt lgkmcnt(0)\n"
    "v_rcp_f32 v24, v24\n"
    "v_rcp_f32 v26, v26\n"
    "v_rcp_f32 v28, v28\n"
    "v_mfma_f32_32x32x16_bf16 v[80:95], v[220:223], v[204:207], v[80:95]\n"
    "v_mul_f32 v73, v73, v24\n"
    "v_mul_f32 v74, v74, v26\n"
    "v_mul_f32 v75, v75, v28\n"
    "v_mul_f32 v24, v76, v76\n"
    "v_mul_f32 v26, v77, v77\n"
    "v_mul_f32 v28, v78, v78\n"
    "v_fma_f32 v24, v24, s55, v30\n"
    "v_fma_f32 v26, v26, s55, v30\n"
    "v_fma_f32 v28, v28, s55, v30\n"
    "v_mul_f32 v24, v24, v76\n"
    "v_mul_f32 v26, v26, v77\n"
    "v_mul_f32 v28, v28, v78\n"
    "v_exp_f32 v24, v24\n"
    "v_exp_f32 v26, v26\n"
    "v_exp_f32 v28, v28\n"
    "v_add_f32 v24, 1.0, v24\n"
    "v_add_f32 v26, 1.0, v26\n"
    "v_add_f32 v28, 1.0, v28\n"
    "v_rcp_f32 v24, v24\n"
    "v_rcp_f32 v26, v26\n"
    "v_rcp_f32 v28, v28\n"
    "v_mul_f32 v76, v76, v24\n"
    "v_mul_f32 v77, v77, v26\n"
    "v_mul_f32 v78, v78, v28\n"
    "v_mul_f32 v24, v79, v79\n"
    "v_fma_f32 v24, v24, s55, v30\n"
    "v_mul_f32 v24, v24, v79\n"
    "v_exp_f32 v24, v24\n"
    "s_nop 0\n"
    "v_add_f32 v24, 1.0, v24\n"
    "v_rcp_f32 v24, v24\n"
    "s_nop 0\n"
    "v_mul_f32 v79, v79, v24\n"
    "s_mov_b32 s61, 0x1000\n"
    "buffer_load_dwordx4 v[96:99], v232, s[28:31], 0 offen sc1\n"
    "buffer_load_dwordx4 v[100:103], v232, s[28:31], 0 offen offset:512 sc1\n"
    "buffer_load_dwordx4 v[104:107], v232, s[28:31], 0 offen offset:1024 sc1\n"
    "buffer_load_dwordx4 v[108:111], v232, s[28:31], 0 offen offset:1536 sc1\n"
    "buffer_load_dwordx4 v[112:115], v232, s[28:31], 0 offen offset:2048 sc1\n"
    "buffer_load_dwordx4 v[116:119], v232, s[28:31], 0 offen offset:2560 sc1\n"
    "buffer_load_dwordx4 v[120:123], v232, s[28:31], 0 offen offset:3072 sc1\n"
    "buffer_load_dwordx4 v[124:127], v232, s[28:31], 0 offen offset:3584 sc1\n"
    "buffer_load_dwordx4 v[128:131], v232, s[28:31], s61 offen sc1\n"
    "buffer_load_dwordx4 v[132:135], v232, s[28:31], s61 offen offset:512 sc1\n"
    "buffer_load_dwordx4 v[136:139], v232, s[28:31], s61 offen offset:1024 sc1\n"
    "buffer_load_dwordx4 v[140:143], v232, s[28:31], s61 offen offset:1536 sc1\n"
    "buffer_load_dwordx4 v[144:147], v232, s[28:31], s61 offen offset:2048 sc1\n"
    "buffer_load_dwordx4 v[148:151], v232, s[28:31], s61 offen offset:2560 sc1\n"
    "s_mov_b32 s59, 0x1200000\n"
    "v_cvt_pk_bf16_f32 v64, v64, v65\n"
    "v_cvt_pk_bf16_f32 v65, v66, v67\n"
    "v_cvt_pk_bf16_f32 v66, v68, v69\n"
    "v_cvt_pk_bf16_f32 v67, v70, v71\n"
    "v_cvt_pk_bf16_f32 v68, v72, v73\n"
    "v_cvt_pk_bf16_f32 v69, v74, v75\n"
    "v_cvt_pk_bf16_f32 v70, v76, v77\n"
    "v_cvt_pk_bf16_f32 v71, v78, v79\n"
    "buffer_store_dwordx2 v[64:65], v252, s[28:31], s59 offen sc1\n"
    "buffer_store_dwordx2 v[66:67], v252, s[28:31], s59 offen offset:128 sc1\n"
    "buffer_store_dwordx2 v[68:69], v252, s[28:31], s59 offen offset:512 sc1\n"
    "buffer_store_dwordx2 v[70:71], v252, s[28:31], s59 offen offset:640 sc1\n"
    "v_pk_mul_f32 v[24:25], v[80:81], v[80:81]\n"
    "v_pk_mul_f32 v[26:27], v[82:83], v[82:83]\n"
    "v_pk_mul_f32 v[28:29], v[84:85], v[84:85]\n"
    "v_pk_fma_f32 v[24:25], v[24:25], v[30:31], v[30:31] op_sel:[0,1,0] op_sel_hi:[1,1,0]\n"
    "v_pk_fma_f32 v[26:27], v[26:27], v[30:31], v[30:31] op_sel:[0,1,0] op_sel_hi:[1,1,0]\n"
    "v_pk_fma_f32 v[28:29], v[28:29], v[30:31], v[30:31] op_sel:[0,1,0] op_sel_hi:[1,1,0]\n"
    "v_pk_mul_f32 v[24:25], v[24:25], v[80:81]\n"
    "v_pk_mul_f32 v[26:27], v[26:27], v[82:83]\n"
    "v_pk_mul_f32 v[28:29], v[28:29], v[84:85]\n"
    "v_exp_f32 v24, v24\n"
    "v_exp_f32 v25, v25\n"
    "v_exp_f32 v26, v26\n"
    "v_exp_f32 v27, v27\n"
    "v_exp_f32 v28, v28\n"
    "v_exp_f32 v29, v29\n"
    "v_add_f32 v24, 1.0, v24\n"
    "v_add_f32 v25, 1.0, v25\n"
    "v_add_f32 v26, 1.0, v26\n"
    "v_add_f32 v27, 1.0, v27\n"
    "v_add_f32 v28, 1.0, v28\n"
    "v_add_f32 v29, 1.0, v29\n"
    "v_rcp_f32 v24, v24\n"
    "v_rcp_f32 v25, v25\n"
    "v_rcp_f32 v26, v26\n"
    "v_rcp_f32 v27, v27\n"
    "v_rcp_f32 v28, v28\n"
    "v_rcp_f32 v29, v29\n"
    "v_pk_mul_f32 v[80:81], v[80:81], v[24:25]\n"
    "v_pk_mul_f32 v[82:83], v[82:83], v[26:27]\n"
    "v_pk_mul_f32 v[84:85], v[84:85], v[28:29]\n"
    "v_pk_mul_f32 v[24:25], v[86:87], v[86:87]\n"
    "v_pk_mul_f32 v[26:27], v[88:89], v[88:89]\n"
    "v_pk_mul_f32 v[28:29], v[90:91], v[90:91]\n"
    "v_pk_fma_f32 v[24:25], v[24:25], v[30:31], v[30:31] op_sel:[0,1,0] op_sel_hi:[1,1,0]\n"
    "v_pk_fma_f32 v[26:27], v[26:27], v[30:31], v[30:31] op_sel:[0,1,0] op_sel_hi:[1,1,0]\n"
    "v_pk_fma_f32 v[28:29], v[28:29], v[30:31], v[30:31] op_sel:[0,1,0] op_sel_hi:[1,1,0]\n"
    "v_pk_mul_f32 v[24:25], v[24:25], v[86:87]\n"
    "v_pk_mul_f32 v[26:27], v[26:27], v[88:89]\n"
    "v_pk_mul_f32 v[28:29], v[28:29], v[90:91]\n"
    "v_exp_f32 v24, v24\n"
    "v_exp_f32 v25, v25\n"
    "v_exp_f32 v26, v26\n"
    "v_exp_f32 v27, v27\n"
    "v_exp_f32 v28, v28\n"
    "v_exp_f32 v29, v29\n"
    "v_add_f32 v24, 1.0, v24\n"
    "v_add_f32 v25, 1.0, v25\n"
    "v_add_f32 v26, 1.0, v26\n"
    "v_add_f32 v27, 1.0, v27\n"
    "v_add_f32 v28, 1.0, v28\n"
    "v_add_f32 v29, 1.0, v29\n"
    "v_rcp_f32 v24, v24\n"
    "v_rcp_f32 v25, v25\n"
    "v_rcp_f32 v26, v26\n"
    "v_rcp_f32 v27, v27\n"
    "v_rcp_f32 v28, v28\n"
    "v_rcp_f32 v29, v29\n"
    "v_pk_mul_f32 v[86:87], v[86:87], v[24:25]\n"
    "v_pk_mul_f32 v[88:89], v[88:89], v[26:27]\n"
    "v_pk_mul_f32 v[90:91], v[90:91], v[28:29]\n"
    "v_pk_mul_f32 v[24:25], v[92:93], v[92:93]\n"
    "v_pk_mul_f32 v[26:27], v[94:95], v[94:95]\n"
    "v_pk_fma_f32 v[24:25], v[24:25], v[30:31], v[30:31] op_sel:[0,1,0] op_sel_hi:[1,1,0]\n"
    "v_pk_fma_f32 v[26:27], v[26:27], v[30:31], v[30:31] op_sel:[0,1,0] op_sel_hi:[1,1,0]\n"
    "v_pk_mul_f32 v[24:25], v[24:25], v[92:93]\n"
    "v_pk_mul_f32 v[26:27], v[26:27], v[94:95]\n"
    "v_exp_f32 v24, v24\n"
    "v_exp_f32 v25, v25\n"
    "v_exp_f32 v26, v26\n"
    "v_exp_f32 v27, v27\n"
    "v_add_f32 v24, 1.0, v24\n"
    "v_add_f32 v25, 1.0, v25\n"
    "v_add_f32 v26, 1.0, v26\n"
    "v_add_f32 v27, 1.0, v27\n"
    "v_rcp_f32 v24, v24\n"
    "v_rcp_f32 v25, v25\n"
    "v_rcp_f32 v26, v26\n"
    "v_rcp_f32 v27, v27\n"
    "v_pk_mul_f32 v[92:93], v[92:93], v[24:25]\n"
    "v_pk_mul_f32 v[94:95], v[94:95], v[26:27]\n"
    "v_cvt_pk_bf16_f32 v80, v80, v81\n"
    "v_cvt_pk_bf16_f32 v81, v82, v83\n"
    "v_cvt_pk_bf16_f32 v82, v84, v85\n"
    "v_cvt_pk_bf16_f32 v83, v86, v87\n"
    "v_cvt_pk_bf16_f32 v84, v88, v89\n"
    "v_cvt_pk_bf16_f32 v85, v90, v91\n"
    "v_cvt_pk_bf16_f32 v86, v92, v93\n"
    "v_cvt_pk_bf16_f32 v87, v94, v95\n"
    "buffer_store_dwordx2 v[80:81], v252, s[28:31], s59 offen offset:1024 sc1\n"
    "buffer_store_dwordx2 v[82:83], v252, s[28:31], s59 offen offset:1152 sc1\n"
    "buffer_store_dwordx2 v[84:85], v252, s[28:31], s59 offen offset:1536 sc1\n"
    "buffer_store_dwordx2 v[86:87], v252, s[28:31], s59 offen offset:1664 sc1\n"
    "s_lshl_b32 s93, s46, 4\n"
    "s_add_u32 s93, s93, s45\n"
    "s_lshl_b32 s93, s93, 3\n"
    "s_add_u32 s93, s93, 0x1800\n"
    "s_mov_b32 s92, 0\n"
    "s_waitcnt vmcnt(21)\n"
    "v_xor_b32 v15, s90, v98\n"
    "v_xor_b32 v16, s91, v99\n"
    "v_or_b32 v15, v15, v16\n"
    "s_waitcnt vmcnt(20)\n"
    "v_xor_b32 v16, s90, v102\n"
    "v_or_b32 v15, v15, v16\n"
    "v_xor_b32 v16, s91, v103\n"
    "v_or_b32 v15, v15, v16\n"
    "s_waitcnt vmcnt(19)\n"
    "v_xor_b32 v16, s90, v106\n"
    "v_or_b32 v15, v15, v16\n"
    "v_xor_b32 v16, s91, v107\n"
    "v_or_b32 v15, v15, v16\n"
    "s_waitcnt vmcnt(18)\n"
    "v_xor_b32 v16, s90, v110\n"
    "v_or_b32 v15, v15, v16\n"
    "v_xor_b32 v16, s91, v111\n"
    "v_or_b32 v15, v15, v16\n"
    "s_waitcnt vmcnt(17)\n"
    "v_xor_b32 v16, s90, v114\n"
    "v_or_b32 v15, v15, v16\n"
    "v_xor_b32 v16, s91, v115\n"
    "v_or_b32 v15, v15, v16\n"
    "s_waitcnt vmcnt(16)\n"
    "v_xor_b32 v16, s90, v118\n"
    "v_or_b32 v15, v15, v16\n"
    "v_xor_b32 v16, s91, v119\n"
    "v_or_b32 v15, v15, v16\n"
    "s_waitcnt vmcnt(15)\n"
    "v_xor_b32 v16, s90, v122\n"
    "v_or_b32 v15, v15, v16\n"
    "v_xor_b32 v16, s91, v123\n"
    "v_or_b32 v15, v15, v16\n"
    "s_waitcnt vmcnt(14)\n"
    "v_xor_b32 v16, s90, v126\n"
    "v_or_b32 v15, v15, v16\n"
    "v_xor_b32 v16, s91, v127\n"
    "v_or_b32 v15, v15, v16\n"
    "s_waitcnt vmcnt(13)\n"
    "v_xor_b32 v16, s90, v130\n"
    "v_or_b32 v15, v15, v16\n"
    "v_xor_b32 v16, s91, v131\n"
    "v_or_b32 v15, v15, v16\n"
    "s_waitcnt vmcnt(12)\n"
    "v_xor_b32 v16, s90, v134\n"
    "v_or_b32 v15, v15, v16\n"
    "v_xor_b32 v16, s91, v135\n"
    "v_or_b32 v15, v15, v16\n"
    "s_waitcnt vmcnt(11)\n"
    "v_xor_b32 v16, s90, v138\n"
    "v_or_b32 v15, v15, v16\n"
    "v_xor_b32 v16, s91, v139\n"
    "v_or_b32 v15, v15, v16\n"
    "s_waitcnt vmcnt(10)\n"
    "v_xor_b32 v16, s90, v142\n"
    "v_or_b32 v15, v15, v16\n"
    "v_xor_b32 v16, s91, v143\n"
    "v_or_b32 v15, v15, v16\n"
    "s_waitcnt vmcnt(9)\n"
    "v_xor_b32 v16, s90, v146\n"
    "v_or_b32 v15, v15, v16\n"
    "v_xor_b32 v16, s91, v147\n"
    "v_or_b32 v15, v15, v16\n"
    "s_waitcnt vmcnt(8)\n"
    "v_xor_b32 v16, s90, v150\n"
    "v_or_b32 v15, v15, v16\n"
    "v_xor_b32 v16, s91, v151\n"
    "v_or_b32 v15, v15, v16\n"
    "v_cmp_ne_u32 vcc, 0, v15\n"
    "s_add_u32 s92, s92, 1\n"
    "s_nop 3\n"
    "s_cbranch_vccz .LF_ggo_23\n"
    "s_waitcnt vmcnt(0)\n"
    ".LF_gpoll_22:\n"
    "s_nop 0\n"
    "s_mov_b32 s61, 0x1000\n"
    "buffer_load_dwordx4 v[96:99], v232, s[28:31], 0 offen sc1\n"
    "buffer_load_dwordx4 v[100:103], v232, s[28:31], 0 offen offset:512 sc1\n"
    "buffer_load_dwordx4 v[104:107], v232, s[28:31], 0 offen offset:1024 sc1\n"
    "buffer_load_dwordx4 v[108:111], v232, s[28:31], 0 offen offset:1536 sc1\n"
    "buffer_load_dwordx4 v[112:115], v232, s[28:31], 0 offen offset:2048 sc1\n"
    "buffer_load_dwordx4 v[116:119], v232, s[28:31], 0 offen offset:2560 sc1\n"
    "buffer_load_dwordx4 v[120:123], v232, s[28:31], 0 offen offset:3072 sc1\n"
    "buffer_load_dwordx4 v[124:127], v232, s[28:31], 0 offen offset:3584 sc1\n"
    "buffer_load_dwordx4 v[128:131], v232, s[28:31], s61 offen sc1\n"
    "buffer_load_dwordx4 v[132:135], v232, s[28:31], s61 offen offset:512 sc1\n"
    "buffer_load_dwordx4 v[136:139], v232, s[28:31], s61 offen offset:1024 sc1\n"
    "buffer_load_dwordx4 v[140:143], v232, s[28:31], s61 offen offset:1536 sc1\n"
    "buffer_load_dwordx4 v[144:147], v232, s[28:31], s61 offen offset:2048 sc1\n"
    "buffer_load_dwordx4 v[148:151], v232, s[28:31], s61 offen offset:2560 sc1\n"
    "s_waitcnt vmcnt(0)\n"
    "v_xor_b32 v15, s90, v98\n"
    "v_xor_b32 v16, s91, v99\n"
    "v_or_b32 v15, v15, v16\n"
    "v_xor_b32 v16, s90, v102\n"
    "v_or_b32 v15, v15, v16\n"
    "v_xor_b32 v16, s91, v103\n"
    "v_or_b32 v15, v15, v16\n"
    "v_xor_b32 v16, s90, v106\n"
    "v_or_b32 v15, v15, v16\n"
    "v_xor_b32 v16, s91, v107\n"
    "v_or_b32 v15, v15, v16\n"
    "v_xor_b32 v16, s90, v110\n"
    "v_or_b32 v15, v15, v16\n"
    "v_xor_b32 v16, s91, v111\n"
    "v_or_b32 v15, v15, v16\n"
    "v_xor_b32 v16, s90, v114\n"
    "v_or_b32 v15, v15, v16\n"
    "v_xor_b32 v16, s91, v115\n"
    "v_or_b32 v15, v15, v16\n"
    "v_xor_b32 v16, s90, v118\n"
    "v_or_b32 v15, v15, v16\n"
    "v_xor_b32 v16, s91, v119\n"
    "v_or_b32 v15, v15, v16\n"
    "v_xor_b32 v16, s90, v122\n"
    "v_or_b32 v15, v15, v16\n"
    "v_xor_b32 v16, s91, v123\n"
    "v_or_b32 v15, v15, v16\n"
    "v_xor_b32 v16, s90, v126\n"
    "v_or_b32 v15, v15, v16\n"
    "v_xor_b32 v16, s91, v127\n"
    "v_or_b32 v15, v15, v16\n"
    "v_xor_b32 v16, s90, v130\n"
    "v_or_b32 v15, v15, v16\n"
    "v_xor_b32 v16, s91, v131\n"
    "v_or_b32 v15, v15, v16\n"
    "v_xor_b32 v16, s90, v134\n"
    "v_or_b32 v15, v15, v16\n"
    "v_xor_b32 v16, s91, v135\n"
    "v_or_b32 v15, v15, v16\n"
    "v_xor_b32 v16, s90, v138\n"
    "v_or_b32 v15, v15, v16\n"
    "v_xor_b32 v16, s91, v139\n"
    "v_or_b32 v15, v15, v16\n"
    "v_xor_b32 v16, s90, v142\n"
    "v_or_b32 v15, v15, v16\n"
    "v_xor_b32 v16, s91, v143\n"
    "v_or_b32 v15, v15, v16\n"
    "v_xor_b32 v16, s90, v146\n"
    "v_or_b32 v15, v15, v16\n"
    "v_xor_b32 v16, s91, v147\n"
    "v_or_b32 v15, v15, v16\n"
    "v_xor_b32 v16, s90, v150\n"
    "v_or_b32 v15, v15, v16\n"
    "v_xor_b32 v16, s91, v151\n"
    "v_or_b32 v15, v15, v16\n"
    "v_cmp_ne_u32 vcc, 0, v15\n"
    "s_add_u32 s92, s92, 1\n"
    "s_nop 3\n"
    "s_cbranch_vccz .LF_ggo_23\n"
    "s_cmp_gt_u32 s92, 0x20000\n"
    "s_cbranch_scc1 .LF_ggo_23\n"
    "s_sleep 1\n"
    "s_branch .LF_gpoll_22\n"
    ".LF_ggo_23:\n"
    "s_nop 4\n"
    "v_add_f32 v22, v96, v100\n"
    "v_add_f32 v15, v97, v101\n"
    "v_add_f32 v22, v22, v104\n"
    "v_add_f32 v15, v15, v105\n"
    "v_add_f32 v22, v22, v108\n"
    "v_add_f32 v15, v15, v109\n"
    "v_add_f32 v22, v22, v112\n"
    "v_add_f32 v15, v15, v113\n"
    "v_add_f32 v22, v22, v116\n"
    "v_add_f32 v15, v15, v117\n"
    "v_add_f32 v22, v22, v120\n"
    "v_add_f32 v15, v15, v121\n"
    "v_add_f32 v22, v22, v124\n"
    "v_add_f32 v15, v15, v125\n"
    "v_add_f32 v22, v22, v128\n"
    "v_add_f32 v15, v15, v129\n"
    "v_add_f32 v22, v22, v132\n"
    "v_add_f32 v15, v15, v133\n"
    "v_add_f32 v22, v22, v136\n"
    "v_add_f32 v15, v15, v137\n"
    "v_add_f32 v22, v22, v140\n"
    "v_add_f32 v15, v15, v141\n"
    "v_add_f32 v22, v22, v144\n"
    "v_add_f32 v15, v15, v145\n"
    "v_add_f32 v22, v22, v148\n"
    "v_add_f32 v15, v15, v149\n"
    "v_mul_f32 v22, 0x3a924925, v22\n"
    "v_mul_f32 v15, 0x3a924925, v15\n"
    "v_fma_f32 v15, -v22, v22, v15\n"
    "v_max_f32 v15, 0, v15\n"
    "v_add_f32 v15, 0x3727c5ac, v15\n"
    "v_rsq_f32 v23, v15\n"
    "s_nop 0\n"
    "v_mul_f32 v22, -v22, v23\n"
    "v_pk_fma_f32 v[32:33], v[32:33], v[22:23], v[22:23] op_sel:[0,1,0] op_sel_hi:[1,1,0]\n"
    "v_pk_fma_f32 v[34:35], v[34:35], v[22:23], v[22:23] op_sel:[0,1,0] op_sel_hi:[1,1,0]\n"
    "v_pk_fma_f32 v[36:37], v[36:37], v[22:23], v[22:23] op_sel:[0,1,0] op_sel_hi:[1,1,0]\n"
    "v_pk_fma_f32 v[38:39], v[38:39], v[22:23], v[22:23] op_sel:[0,1,0] op_sel_hi:[1,1,0]\n"
    "v_pk_fma_f32 v[40:41], v[40:41], v[22:23], v[22:23] op_sel:[0,1,0] op_sel_hi:[1,1,0]\n"
    "v_pk_fma_f32 v[42:43], v[42:43], v[22:23], v[22:23] op_sel:[0,1,0] op_sel_hi:[1,1,0]\n"
    "v_pk_fma_f32 v[44:45], v[44:45], v[22:23], v[22:23] op_sel:[0,1,0] op_sel_hi:[1,1,0]\n"
    "v_pk_fma_f32 v[46:47], v[46:47], v[22:23], v[22:23] op_sel:[0,1,0] op_sel_hi:[1,1,0]\n"
    "v_cvt_pk_bf16_f32 v32, v32, v33\n"
    "v_cvt_pk_bf16_f32 v33, v34, v35\n"
    "v_cvt_pk_bf16_f32 v34, v36, v37\n"
    "v_cvt_pk_bf16_f32 v35, v38, v39\n"
    "v_cvt_pk_bf16_f32 v36, v40, v41\n"
    "v_cvt_pk_bf16_f32 v37, v42, v43\n"
    "v_cvt_pk_bf16_f32 v38, v44, v45\n"
    "v_cvt_pk_bf16_f32 v39, v46, v47\n"
    "v_xor_b32 v15, 0, v233\n"
    "ds_write_b128 v15, v[32:35]\n"
    "v_xor_b32 v15, 32, v233\n"
    "ds_write_b128 v15, v[36:39]\n"
    "v_pk_fma_f32 v[48:49], v[48:49], v[22:23], v[22:23] op_sel:[0,1,0] op_sel_hi:[1,1,0]\n"
    "v_pk_fma_f32 v[50:51], v[50:51], v[22:23], v[22:23] op_sel:[0,1,0] op_sel_hi:[1,1,0]\n"
    "v_pk_fma_f32 v[52:53], v[52:53], v[22:23], v[22:23] op_sel:[0,1,0] op_sel_hi:[1,1,0]\n"
    "v_pk_fma_f32 v[54:55], v[54:55], v[22:23], v[22:23] op_sel:[0,1,0] op_sel_hi:[1,1,0]\n"
    "v_pk_fma_f32 v[56:57], v[56:57], v[22:23], v[22:23] op_sel:[0,1,0] op_sel_hi:[1,1,0]\n"
    "v_pk_fma_f32 v[58:59], v[58:59], v[22:23], v[22:23] op_sel:[0,1,0] op_sel_hi:[1,1,0]\n"
    "v_pk_fma_f32 v[60:61], v[60:61], v[22:23], v[22:23] op_sel:[0,1,0] op_sel_hi:[1,1,0]\n"
    "v_pk_fma_f32 v[62:63], v[62:63], v[22:23], v[22:23] op_sel:[0,1,0] op_sel_hi:[1,1,0]\n"
    "v_cvt_pk_bf16_f32 v48, v48, v49\n"
    "v_cvt_pk_bf16_f32 v49, v50, v51\n"
    "v_cvt_pk_bf16_f32 v50, v52, v53\n"
    "v_cvt_pk_bf16_f32 v51, v54, v55\n"
    "v_cvt_pk_bf16_f32 v52, v56, v57\n"
    "v_cvt_pk_bf16_f32 v53, v58, v59\n"
    "v_cvt_pk_bf16_f32 v54, v60, v61\n"
    "v_cvt_pk_bf16_f32 v55, v62, v63\n"
    "v_xor_b32 v15, 64, v233\n"
    "ds_write_b128 v15, v[48:51]\n"
    "v_xor_b32 v15, 0x60, v233\n"
    "ds_write_b128 v15, v[52:55]\n"
    "ds_read_b128 v[160:163], v234\n"
    "ds_read_b128 v[164:167], v234 offset:1024\n"
    "ds_read_b128 v[168:171], v234 offset:2048\n"
    "ds_read_b128 v[172:175], v234 offset:3072\n"
    "s_mov_b32 s59, 0\n"
    "s_waitcnt lgkmcnt(3)\n"
    "buffer_store_dwordx4 v[160:163], v235, s[28:31], s59 offen sc1\n"
    "s_mov_b32 s59, 0x380\n"
    "s_waitcnt lgkmcnt(2)\n"
    "buffer_store_dwordx4 v[164:167], v235, s[28:31], s59 offen sc1\n"
    "s_mov_b32 s59, 0xe00\n"
    "s_waitcnt lgkmcnt(1)\n"
    "buffer_store_dwordx4 v[168:171], v235, s[28:31], s59 offen sc1\n"
    "s_mov_b32 s59, 0x1180\n"
    "s_waitcnt lgkmcnt(0)\n"
    "buffer_store_dwordx4 v[172:175], v235, s[28:31], s59 offen sc1\n"
    "s_waitcnt vmcnt(0)\n"
    "s_barrier\n"
    "s_cmp_lg_u32 s40, 0\n"
    "s_cbranch_scc1 .LF_w0_24\n"
    "s_mov_b32 exec_lo, 0x1\n"
    "s_mov_b32 exec_hi, 0x0\n"
    "v_mov_b32 v28, s90\n"
    "v_mov_b32 v29, s91\n"
    "v_mov_b32 v30, 0\n"
    "buffer_store_dwordx2 v[28:29], v30, s[28:31], s93 offen sc1\n"
    "s_mov_b64 exec, -1\n"
    ".LF_w0_24:\n"
    "s_nop 4\n"
    ".LF_t2idle_18:\n"
    "s_nop 4\n"
    "s_mul_i32 s55, s52, 0x5800\n"
    "s_mul_i32 s58, s51, 0x1c00\n"
    "s_add_u32 s55, s55, s58\n"
    "s_mov_b32 s56, 0x1000\n"
    "s_mul_i32 s57, s52, 0x5800\n"
    "s_lshl_b32 s58, s51, 12\n"
    "s_add_u32 s57, s57, s58\n"
    "s_add_u32 s57, s57, 0x3800\n"
    "s_mov_b32 s53, 0x7c00\n"
    "s_mov_b32 s54, 0xf800\n"
    "s_mov_b32 s97, 0x17400\n"
    "s_lshl_b32 s58, s47, 6\n"
    "s_lshl_b32 s59, s51, 5\n"
    "s_add_u32 s58, s58, s59\n"
    "s_lshl_b32 s58, s58, 12\n"
    "s_lshl_b32 s59, s52, 11\n"
    "s_add_u32 s58, s58, s59\n"
    "s_lshl_b32 s59, s96, 7\n"
    "s_add_u32 s58, s58, s59\n"
    "v_lshrrev_b32 v14, 3, v1\n"
    "v_lshlrev_b32 v14, 12, v14\n"
    "v_lshrrev_b32 v15, 4, v1\n"
    "v_xor_b32 v15, v15, v1\n"
    "v_and_b32 v15, 7, v15\n"
    "v_lshl_add_u32 v11, v15, 4, v14\n"
    "v_add_u32 v11, s58, v11\n"
    "v_xor_b32 v12, 64, v11\n"
    "s_mov_b32 s98, s96\n"
    "s_mov_b32 s99, s96\n"
    "s_add_u32 m0, s57, 0\n"
    "s_nop 0\n"
    "buffer_load_dwordx4 v11, s[12:15], 0 offen lds\n"
    "buffer_load_dwordx4 v12, s[12:15], s53 offen offset:1024 lds\n"
    "buffer_load_dwordx4 v11, s[12:15], s54 offen offset:2048 lds\n"
    "buffer_load_dwordx4 v12, s[12:15], s97 offen offset:3072 lds\n"
    "s_add_u32 s62, s99, 1\n"
    "s_and_b32 s62, s62, 15\n"
    "s_sub_u32 s63, s62, s99\n"
    "s_mov_b32 s99, s62\n"
    "s_mul_i32 s63, s63, 0x80\n"
    "v_add_u32 v11, s63, v11\n"
    "v_add_u32 v12, s63, v12\n"
    "s_add_u32 m0, s57, 0xb000\n"
    "s_nop 0\n"
    "buffer_load_dwordx4 v11, s[12:15], 0 offen lds\n"
    "buffer_load_dwordx4 v12, s[12:15], s53 offen offset:1024 lds\n"
    "buffer_load_dwordx4 v11, s[12:15], s54 offen offset:2048 lds\n"
    "buffer_load_dwordx4 v12, s[12:15], s97 offen offset:3072 lds\n"
    "s_add_u32 s62, s99, 1\n"
    "s_and_b32 s62, s62, 15\n"
    "s_sub_u32 s63, s62, s99\n"
    "s_mov_b32 s99, s62\n"
    "s_mul_i32 s63, s63, 0x80\n"
    "v_add_u32 v11, s63, v11\n"
    "v_add_u32 v12, s63, v12\n"
    "s_add_u32 m0, s57, 0x16000\n"
    "s_nop 0\n"
    "buffer_load_dwordx4 v11, s[12:15], 0 offen lds\n"
    "buffer_load_dwordx4 v12, s[12:15], s53 offen offset:1024 lds\n"
    "buffer_load_dwordx4 v11, s[12:15], s54 offen offset:2048 lds\n"
    "buffer_load_dwordx4 v12, s[12:15], s97 offen offset:3072 lds\n"
    "s_add_u32 s62, s99, 1\n"
    "s_and_b32 s62, s62, 15\n"
    "s_sub_u32 s63, s62, s99\n"
    "s_mov_b32 s99, s62\n"
    "s_mul_i32 s63, s63, 0x80\n"
    "v_add_u32 v11, s63, v11\n"
    "v_add_u32 v12, s63, v12\n"
    "s_lshl_b32 s58, s48, 11\n"
    "s_lshl_b32 s59, s52, 10\n"
    "s_add_u32 s58, s58, s59\n"
    "s_lshl_b32 s59, s96, 6\n"
    "s_add_u32 s58, s58, s59\n"
    "s_mul_i32 s58, s58, 0xe0\n"
    "s_mul_i32 s59, s51, 0x1c00\n"
    "s_add_u32 s58, s58, s59\n"
    "s_add_u32 s58, s58, 0x1580000\n"
    "v_lshlrev_b32 v14, 4, v1\n"
    "v_add_u32 v9, s58, v14\n"
    "v_bfe_u32 v14, v1, 5, 1\n"
    "v_lshlrev_b32 v14, 4, v14\n"
    "v_bfe_u32 v15, v1, 4, 1\n"
    "v_lshl_or_b32 v14, v15, 2, v14\n"
    "v_bfe_u32 v15, v1, 2, 2\n"
    "v_or_b32 v14, v14, v15\n"
    "v_mul_u32_u24 v14, 0xe0, v14\n"
    "v_and_b32 v15, 3, v1\n"
    "v_lshl_add_u32 v14, v15, 3, v14\n"
    "s_mul_i32 s58, s52, 0x5800\n"
    "v_add_u32 v10, s58, v14\n"
    "v_and_b32 v14, 15, v1\n"
    "v_lshrrev_b32 v15, 4, v1\n"
    "v_bfe_u32 v16, v1, 1, 3\n"
    "v_xor_b32 v15, v15, v16\n"
    "v_lshlrev_b32 v15, 4, v15\n"
    "v_lshl_add_u32 v14, v14, 7, v15\n"
    "s_mul_i32 s58, s52, 0x6000\n"
    "s_lshl_b32 s59, s51, 12\n"
    "s_add_u32 s58, s58, s59\n"
    "s_add_u32 s58, s58, 0x3800\n"
    "v_add_u32 v2, s58, v14\n"
    "v_xor_b32 v3, 64, v2\n"
    "s_lshl_b32 s58, s52, 12\n"
    "s_sub_u32 s58, 0x800, s58\n"
    "v_add_u32 v4, s58, v2\n"
    "v_add_u32 v13, s58, v3\n"
    "s_lshl_b32 s58, s47, 6\n"
    "s_lshl_b32 s59, s51, 5\n"
    "s_add_u32 s58, s58, s59\n"
    "s_lshl_b32 s59, s52, 4\n"
    "s_add_u32 s58, s58, s59\n"
    "v_and_b32 v14, 15, v1\n"
    "v_add_u32 v14, s58, v14\n"
    "v_lshlrev_b32 v15, 1, v14\n"
    "buffer_load_ushort v214, v15, s[20:23], 0 offen\n"
    "s_lshl_b32 s59, s47, 2\n"
    "s_lshl_b32 s60, s51, 1\n"
    "s_add_u32 s59, s59, s60\n"
    "s_add_u32 s59, s59, s52\n"
    "s_mul_i32 s59, s59, 56\n"
    "s_mul_i32 s60, s48, 7\n"
    "s_add_u32 s59, s59, s60\n"
    "s_lshl_b32 s59, s59, 9\n"
    "s_add_u32 s59, s59, 0x1200000\n"
    "v_lshlrev_b32 v15, 3, v1\n"
    "v_add_u32 v218, s59, v15\n"
    "s_lshl_b32 s58, s47, 1\n"
    "s_add_u32 s58, s58, s51\n"
    "s_mul_i32 s58, s58, 0x70\n"
    "s_mul_i32 s59, s48, 14\n"
    "s_add_u32 s58, s58, s59\n"
    "s_lshl_b32 s58, s58, 9\n"
    "s_lshl_b32 s59, s52, 8\n"
    "s_add_u32 s58, s58, s59\n"
    "s_add_u32 s58, s58, 0x1900000\n"
    "v_and_b32 v14, 15, v1\n"
    "v_lshlrev_b32 v14, 4, v14\n"
    "v_bfe_u32 v15, v1, 5, 1\n"
    "v_lshl_add_u32 v14, v15, 9, v14\n"
    "v_bfe_u32 v15, v1, 4, 1\n"
    "v_lshl_add_u32 v14, v15, 3, v14\n"
    "v_add_u32 v215, s58, v14\n"
    "s_mul_i32 s93, s48, 0x70\n"
    "s_lshr_b32 s93, s93, 6\n"
    "s_min_u32 s93, s93, 11\n"
    "s_lshl_b32 s93, s93, 7\n"
    "s_add_u32 s93, s93, 0x1800\n"
    "s_cmp_lg_u32 s40, 0\n"
    "s_cbranch_scc1 .LF_w0_25\n"
    "v_min_u32 v20, 47, v1\n"
    "v_lshlrev_b32 v20, 3, v20\n"
    "s_mov_b32 s92, 0\n"
    ".LF_poll_26:\n"
    "s_nop 0\n"
    "buffer_load_dwordx2 v[24:25], v20, s[28:31], s93 offen sc1\n"
    "s_waitcnt vmcnt(0)\n"
    "v_xor_b32 v24, s90, v24\n"
    "v_xor_b32 v25, s91, v25\n"
    "v_or_b32 v24, v24, v25\n"
    "v_cmp_ne_u32 vcc, 0, v24\n"
    "s_add_u32 s92, s92, 1\n"
    "s_nop 3\n"
    "s_cbranch_vccz .LF_go_27\n"
    "s_cmp_gt_u32 s92, 0x20000\n"
    "s_cbranch_scc1 .LF_go_27\n"
    "s_sleep 1\n"
    "s_branch .LF_poll_26\n"
    ".LF_go_27:\n"
    ".LF_w0_25:\n"
    "s_nop 4\n"
    "s_barrier\n"
    "s_add_u32 m0, s55, 0\n"
    "s_nop 0\n"
    "buffer_load_dwordx4 v9, s[28:31], 0 offen sc1 lds\n"
    "buffer_load_dwordx4 v9, s[28:31], 0 offen offset:1024 sc1 lds\n"
    "buffer_load_dwordx4 v9, s[28:31], 0 offen offset:2048 sc1 lds\n"
    "buffer_load_dwordx4 v9, s[28:31], 0 offen offset:3072 sc1 lds\n"
    "s_add_u32 m0, s55, 0x1000\n"
    "s_nop 0\n"
    "buffer_load_dwordx4 v9, s[28:31], s56 offen sc1 lds\n"
    "buffer_load_dwordx4 v9, s[28:31], s56 offen offset:1024 sc1 lds\n"
    "buffer_load_dwordx4 v9, s[28:31], s56 offen offset:2048 sc1 lds\n"
    "s_add_u32 s62, s98, 1\n"
    "s_and_b32 s62, s62, 15\n"
    "s_sub_u32 s63, s62, s98\n"
    "s_mov_b32 s98, s62\n"
    "s_mul_i32 s63, s63, 0x3800\n"
    "v_add_u32 v9, s63, v9\n"
    "s_add_u32 m0, s55, 0xb000\n"
    "s_nop 0\n"
    "buffer_load_dwordx4 v9, s[28:31], 0 offen sc1 lds\n"
    "buffer_load_dwordx4 v9, s[28:31], 0 offen offset:1024 sc1 lds\n"
    "buffer_load_dwordx4 v9, s[28:31], 0 offen offset:2048 sc1 lds\n"
    "buffer_load_dwordx4 v9, s[28:31], 0 offen offset:3072 sc1 lds\n"
    "s_add_u32 m0, s55, 0xc000\n"
    "s_nop 0\n"
    "buffer_load_dwordx4 v9, s[28:31], s56 offen sc1 lds\n"
    "buffer_load_dwordx4 v9, s[28:31], s56 offen offset:1024 sc1 lds\n"
    "buffer_load_dwordx4 v9, s[28:31], s56 offen offset:2048 sc1 lds\n"
    "s_add_u32 s62, s98, 1\n"
    "s_and_b32 s62, s62, 15\n"
    "s_sub_u32 s63, s62, s98\n"
    "s_mov_b32 s98, s62\n"
    "s_mul_i32 s63, s63, 0x3800\n"
    "v_add_u32 v9, s63, v9\n"
    "s_add_u32 m0, s55, 0x16000\n"
    "s_nop 0\n"
    "buffer_load_dwordx4 v9, s[28:31], 0 offen sc1 lds\n"
    "buffer_load_dwordx4 v9, s[28:31], 0 offen offset:1024 sc1 lds\n"
    "buffer_load_dwordx4 v9, s[28:31], 0 offen offset:2048 sc1 lds\n"
    "buffer_load_dwordx4 v9, s[28:31], 0 offen offset:3072 sc1 lds\n"
    "s_add_u32 m0, s55, 0x17000\n"
    "s_nop 0\n"
    "buffer_load_dwordx4 v9, s[28:31], s56 offen sc1 lds\n"
    "buffer_load_dwordx4 v9, s[28:31], s56 offen offset:1024 sc1 lds\n"
    "buffer_load_dwordx4 v9, s[28:31], s56 offen offset:2048 sc1 lds\n"
    "s_add_u32 s62, s98, 1\n"
    "s_and_b32 s62, s62, 15\n"
    "s_sub_u32 s63, s62, s98\n"
    "s_mov_b32 s98, s62\n"
    "s_mul_i32 s63, s63, 0x3800\n"
    "v_add_u32 v9, s63, v9\n"
    "buffer_load_dwordx2 v[200:201], v218, s[28:31], 0 offen sc1\n"
    "buffer_load_dwordx2 v[202:203], v218, s[28:31], 0 offen offset:512 sc1\n"
    "buffer_load_dwordx2 v[204:205], v218, s[28:31], 0 offen offset:1024 sc1\n"
    "buffer_load_dwordx2 v[206:207], v218, s[28:31], 0 offen offset:1536 sc1\n"
    "buffer_load_dwordx2 v[208:209], v218, s[28:31], 0 offen offset:2048 sc1\n"
    "buffer_load_dwordx2 v[210:211], v218, s[28:31], 0 offen offset:2560 sc1\n"
    "buffer_load_dwordx2 v[212:213], v218, s[28:31], 0 offen offset:3072 sc1\n"
    "s_waitcnt vmcnt(21)\n"
    "s_barrier\n"
    "ds_read_b64_tr_b16 v[96:97], v10\n"
    "ds_read_b64_tr_b16 v[98:99], v10 offset:1792\n"
    "ds_read_b64_tr_b16 v[100:101], v10 offset:32\n"
    "ds_read_b64_tr_b16 v[102:103], v10 offset:1824\n"
    "ds_read_b64_tr_b16 v[104:105], v10 offset:64\n"
    "ds_read_b64_tr_b16 v[106:107], v10 offset:1856\n"
    "ds_read_b64_tr_b16 v[108:109], v10 offset:96\n"
    "ds_read_b128 v[120:123], v2\n"
    "ds_read_b128 v[124:127], v4\n"
    "s_waitcnt lgkmcnt(1)\n"
    "v_mfma_f32_16x16x32_bf16 v[32:35], v[96:99], v[120:123], 0\n"
    "s_waitcnt lgkmcnt(0)\n"
    "ds_read_b64_tr_b16 v[110:111], v10 offset:1888\n"
    "v_mfma_f32_16x16x32_bf16 v[36:39], v[96:99], v[124:127], 0\n"
    "ds_read_b64_tr_b16 v[112:113], v10 offset:128\n"
    "v_mfma_f32_16x16x32_bf16 v[40:43], v[100:103], v[120:123], 0\n"
    "ds_read_b64_tr_b16 v[114:115], v10 offset:1920\n"
    "v_mfma_f32_16x16x32_bf16 v[44:47], v[100:103], v[124:127], 0\n"
    "ds_read_b64_tr_b16 v[116:117], v10 offset:160\n"
    "v_mfma_f32_16x16x32_bf16 v[48:51], v[104:107], v[120:123], 0\n"
    "ds_read_b64_tr_b16 v[118:119], v10 offset:1952\n"
    "ds_read_b128 v[128:131], v3\n"
    "v_mfma_f32_16x16x32_bf16 v[52:55], v[104:107], v[124:127], 0\n"
    "s_waitcnt lgkmcnt(5)\n"
    "ds_read_b64_tr_b16 v[96:97], v10 offset:192\n"
    "v_mfma_f32_16x16x32_bf16 v[56:59], v[108:111], v[120:123], 0\n"
    "ds_read_b64_tr_b16 v[98:99], v10 offset:1984\n"
    "ds_read_b128 v[132:135], v13\n"
    "v_mfma_f32_16x16x32_bf16 v[60:63], v[108:111], v[124:127], 0\n"
    "s_waitcnt lgkmcnt(6)\n"
    "ds_read_b64_tr_b16 v[100:101], v10 offset:7168\n"
    "v_mfma_f32_16x16x32_bf16 v[64:67], v[112:115], v[120:123], 0\n"
    "ds_read_b64_tr_b16 v[102:103], v10 offset:8960\n"
    "v_mfma_f32_16x16x32_bf16 v[68:71], v[112:115], v[124:127], 0\n"
    "s_waitcnt lgkmcnt(6)\n"
    "ds_read_b64_tr_b16 v[104:105], v10 offset:7200\n"
    "v_mfma_f32_16x16x32_bf16 v[72:75], v[116:119], v[120:123], 0\n"
    "ds_read_b64_tr_b16 v[106:107], v10 offset:8992\n"
    "v_mfma_f32_16x16x32_bf16 v[76:79], v[116:119], v[124:127], 0\n"
    "s_waitcnt lgkmcnt(5)\n"
    "ds_read_b64_tr_b16 v[108:109], v10 offset:7232\n"
    "v_mfma_f32_16x16x32_bf16 v[80:83], v[96:99], v[120:123], 0\n"
    "ds_read_b64_tr_b16 v[110:111], v10 offset:9024\n"
    "v_mfma_f32_16x16x32_bf16 v[84:87], v[96:99], v[124:127], 0\n"
    "s_waitcnt lgkmcnt(4)\n"
    "ds_read_b64_tr_b16 v[112:113], v10 offset:7264\n"
    "v_mfma_f32_16x16x32_bf16 v[32:35], v[100:103], v[128:131], v[32:35]\n"
    "ds_read_b64_tr_b16 v[114:115], v10 offset:9056\n"
    "v_mfma_f32_16x16x32_bf16 v[36:39], v[100:103], v[132:135], v[36:39]\n"
    "s_waitcnt lgkmcnt(4)\n"
    "ds_read_b64_tr_b16 v[116:117], v10 offset:7296\n"
    "v_mfma_f32_16x16x32_bf16 v[40:43], v[104:107], v[128:131], v[40:43]\n"
    "ds_read_b64_tr_b16 v[118:119], v10 offset:9088\n"
    "v_mfma_f32_16x16x32_bf16 v[44:47], v[104:107], v[132:135], v[44:47]\n"
    "s_waitcnt lgkmcnt(4)\n"
    "ds_read_b64_tr_b16 v[96:97], v10 offset:7328\n"
    "v_mfma_f32_16x16x32_bf16 v[48:51], v[108:111], v[128:131], v[48:51]\n"
    "s_waitcnt vmcnt(14)\n"
    "s_barrier\n"
    "v_add_u32 v2, 0xb000, v2\n"
    "v_add_u32 v3, 0xb000, v3\n"
    "v_add_u32 v4, 0xb000, v4\n"
    "v_add_u32 v13, 0xb000, v13\n"
    "ds_read_b64_tr_b16 v[98:99], v10 offset:9120\n"
    "ds_read_b128 v[120:123], v2\n"
    "v_mfma_f32_16x16x32_bf16 v[52:55], v[108:111], v[132:135], v[52:55]\n"
    "s_waitcnt lgkmcnt(5)\n"
    "ds_read_b64_tr_b16 v[100:101], v10 offset:7360\n"
    "v_mfma_f32_16x16x32_bf16 v[56:59], v[112:115], v[128:131], v[56:59]\n"
    "ds_read_b64_tr_b16 v[102:103], v10 offset:9152\n"
    "ds_read_b128 v[124:127], v4\n"
    "v_mfma_f32_16x16x32_bf16 v[60:63], v[112:115], v[132:135], v[60:63]\n"
    "s_waitcnt lgkmcnt(6)\n"
    "v_add_u32 v10, 0xb000, v10\n"
    "ds_read_b64_tr_b16 v[104:105], v10\n"
    "v_mfma_f32_16x16x32_bf16 v[64:67], v[116:119], v[128:131], v[64:67]\n"
    "ds_read_b64_tr_b16 v[106:107], v10 offset:1792\n"
    "v_mfma_f32_16x16x32_bf16 v[68:71], v[116:119], v[132:135], v[68:71]\n"
    "s_waitcnt lgkmcnt(6)\n"
    "ds_read_b64_tr_b16 v[108:109], v10 offset:32\n"
    "v_mfma_f32_16x16x32_bf16 v[72:75], v[96:99], v[128:131], v[72:75]\n"
    "ds_read_b64_tr_b16 v[110:111], v10 offset:1824\n"
    "v_mfma_f32_16x16x32_bf16 v[76:79], v[96:99], v[132:135], v[76:79]\n"
    "s_waitcnt lgkmcnt(5)\n"
    "ds_read_b64_tr_b16 v[112:113], v10 offset:64\n"
    "v_mfma_f32_16x16x32_bf16 v[80:83], v[100:103], v[128:131], v[80:83]\n"
    "ds_read_b64_tr_b16 v[114:115], v10 offset:1856\n"
    "v_mfma_f32_16x16x32_bf16 v[84:87], v[100:103], v[132:135], v[84:87]\n"
    "s_waitcnt lgkmcnt(4)\n"
    "s_barrier\n"
    "ds_read_b64_tr_b16 v[116:117], v10 offset:96\n"
    "s_add_u32 m0, s57, 0\n"
    "v_mfma_f32_16x16x32_bf16 v[32:35], v[104:107], v[120:123], v[32:35]\n"
    "ds_read_b64_tr_b16 v[118:119], v10 offset:1888\n"
    "buffer_load_dwordx4 v11, s[12:15], 0 offen lds\n"
    "v_mfma_f32_16x16x32_bf16 v[36:39], v[104:107], v[124:127], v[36:39]\n"
    "s_waitcnt lgkmcnt(4)\n"
    "ds_read_b64_tr_b16 v[96:97], v10 offset:128\n"
    "buffer_load_dwordx4 v12, s[12:15], s53 offen offset:1024 lds\n"
    "v_mfma_f32_16x16x32_bf16 v[40:43], v[108:111], v[120:123], v[40:43]\n"
    "ds_read_b64_tr_b16 v[98:99], v10 offset:1920\n"
    "buffer_load_dwordx4 v11, s[12:15], s54 offen offset:2048 lds\n"
    "v_mfma_f32_16x16x32_bf16 v[44:47], v[108:111], v[124:127], v[44:47]\n"
    "s_waitcnt lgkmcnt(4)\n"
    "ds_read_b64_tr_b16 v[100:101], v10 offset:160\n"
    "buffer_load_dwordx4 v12, s[12:15], s97 offen offset:3072 lds\n"
    "v_mfma_f32_16x16x32_bf16 v[48:51], v[112:115], v[120:123], v[48:51]\n"
    "ds_read_b64_tr_b16 v[102:103], v10 offset:1952\n"
    "ds_read_b128 v[128:131], v3\n"
    "s_add_u32 s62, s99, 1\n"
    "v_mfma_f32_16x16x32_bf16 v[52:55], v[112:115], v[124:127], v[52:55]\n"
    "s_waitcnt lgkmcnt(5)\n"
    "ds_read_b64_tr_b16 v[104:105], v10 offset:192\n"
    "s_and_b32 s62, s62, 15\n"
    "v_mfma_f32_16x16x32_bf16 v[56:59], v[116:119], v[120:123], v[56:59]\n"
    "ds_read_b64_tr_b16 v[106:107], v10 offset:1984\n"
    "ds_read_b128 v[132:135], v13\n"
    "s_sub_u32 s63, s62, s99\n"
    "v_mfma_f32_16x16x32_bf16 v[60:63], v[116:119], v[124:127], v[60:63]\n"
    "s_waitcnt lgkmcnt(6)\n"
    "ds_read_b64_tr_b16 v[108:109], v10 offset:7168\n"
    "s_mov_b32 s99, s62\n"
    "v_mfma_f32_16x16x32_bf16 v[64:67], v[96:99], v[120:123], v[64:67]\n"
    "ds_read_b64_tr_b16 v[110:111], v10 offset:8960\n"
    "s_mul_i32 s63, s63, 0x80\n"
    "v_mfma_f32_16x16x32_bf16 v[68:71], v[96:99], v[124:127], v[68:71]\n"
    "s_waitcnt lgkmcnt(6)\n"
    "ds_read_b64_tr_b16 v[112:113], v10 offset:7200\n"
    "v_add_u32 v11, s63, v11\n"
    "v_mfma_f32_16x16x32_bf16 v[72:75], v[100:103], v[120:123], v[72:75]\n"
    "ds_read_b64_tr_b16 v[114:115], v10 offset:8992\n"
    "v_add_u32 v12, s63, v12\n"
    "v_mfma_f32_16x16x32_bf16 v[76:79], v[100:103], v[124:127], v[76:79]\n"
    "s_waitcnt lgkmcnt(5)\n"
    "ds_read_b64_tr_b16 v[116:117], v10 offset:7232\n"
    "s_add_u32 m0, s55, 0\n"
    "v_mfma_f32_16x16x32_bf16 v[80:83], v[104:107], v[120:123], v[80:83]\n"
    "ds_read_b64_tr_b16 v[118:119], v10 offset:9024\n"
    "buffer_load_dwordx4 v9, s[28:31], 0 offen sc1 lds\n"
    "v_mfma_f32_16x16x32_bf16 v[84:87], v[104:107], v[124:127], v[84:87]\n"
    "s_waitcnt lgkmcnt(4)\n"
    "ds_read_b64_tr_b16 v[96:97], v10 offset:7264\n"
    "buffer_load_dwordx4 v9, s[28:31], 0 offen offset:1024 sc1 lds\n"
    "v_mfma_f32_16x16x32_bf16 v[32:35], v[108:111], v[128:131], v[32:35]\n"
    "ds_read_b64_tr_b16 v[98:99], v10 offset:9056\n"
    "buffer_load_dwordx4 v9, s[28:31], 0 offen offset:2048 sc1 lds\n"
    "v_mfma_f32_16x16x32_bf16 v[36:39], v[108:111], v[132:135], v[36:39]\n"
    "s_waitcnt lgkmcnt(4)\n"
    "ds_read_b64_tr_b16 v[100:101], v10 offset:7296\n"
    "buffer_load_dwordx4 v9, s[28:31], 0 offen offset:3072 sc1 lds\n"
    "v_mfma_f32_16x16x32_bf16 v[40:43], v[112:115], v[128:131], v[40:43]\n"
    "ds_read_b64_tr_b16 v[102:103], v10 offset:9088\n"
    "s_add_u32 m0, s55, 0x1000\n"
    "v_mfma_f32_16x16x32_bf16 v[44:47], v[112:115], v[132:135], v[44:47]\n"
    "s_waitcnt lgkmcnt(4)\n"
    "ds_read_b64_tr_b16 v[104:105], v10 offset:7328\n"
    "buffer_load_dwordx4 v9, s[28:31], s56 offen sc1 lds\n"
    "v_mfma_f32_16x16x32_bf16 v[48:51], v[116:119], v[128:131], v[48:51]\n"
    "s_waitcnt vmcnt(16)\n"
    "s_barrier\n"
    "v_add_u32 v2, 0xb000, v2\n"
    "v_add_u32 v3, 0xb000, v3\n"
    "v_add_u32 v4, 0xb000, v4\n"
    "v_add_u32 v13, 0xb000, v13\n"
    "ds_read_b64_tr_b16 v[106:107], v10 offset:9120\n"
    "ds_read_b128 v[120:123], v2\n"
    "buffer_load_dwordx4 v9, s[28:31], s56 offen offset:1024 sc1 lds\n"
    "v_mfma_f32_16x16x32_bf16 v[52:55], v[116:119], v[132:135], v[52:55]\n"
    "s_waitcnt lgkmcnt(5)\n"
    "ds_read_b64_tr_b16 v[108:109], v10 offset:7360\n"
    "buffer_load_dwordx4 v9, s[28:31], s56 offen offset:2048 sc1 lds\n"
    "v_mfma_f32_16x16x32_bf16 v[56:59], v[96:99], v[128:131], v[56:59]\n"
    "ds_read_b64_tr_b16 v[110:111], v10 offset:9152\n"
    "ds_read_b128 v[124:127], v4\n"
    "s_add_u32 s62, s98, 1\n"
    "v_mfma_f32_16x16x32_bf16 v[60:63], v[96:99], v[132:135], v[60:63]\n"
    "s_waitcnt lgkmcnt(6)\n"
    "v_add_u32 v10, 0xb000, v10\n"
    "ds_read_b64_tr_b16 v[112:113], v10\n"
    "s_and_b32 s62, s62, 15\n"
    "v_mfma_f32_16x16x32_bf16 v[64:67], v[100:103], v[128:131], v[64:67]\n"
    "ds_read_b64_tr_b16 v[114:115], v10 offset:1792\n"
    "s_sub_u32 s63, s62, s98\n"
    "v_mfma_f32_16x16x32_bf16 v[68:71], v[100:103], v[132:135], v[68:71]\n"
    "s_waitcnt lgkmcnt(6)\n"
    "ds_read_b64_tr_b16 v[116:117], v10 offset:32\n"
    "s_mov_b32 s98, s62\n"
    "v_mfma_f32_16x16x32_bf16 v[72:75], v[104:107], v[128:131], v[72:75]\n"
    "ds_read_b64_tr_b16 v[118:119], v10 offset:1824\n"
    "s_mul_i32 s63, s63, 0x3800\n"
    "v_mfma_f32_16x16x32_bf16 v[76:79], v[104:107], v[132:135], v[76:79]\n"
    "s_waitcnt lgkmcnt(5)\n"
    "ds_read_b64_tr_b16 v[96:97], v10 offset:64\n"
    "v_add_u32 v9, s63, v9\n"
    "v_mfma_f32_16x16x32_bf16 v[80:83], v[108:111], v[128:131], v[80:83]\n"
    "ds_read_b64_tr_b16 v[98:99], v10 offset:1856\n"
    "v_mfma_f32_16x16x32_bf16 v[84:87], v[108:111], v[132:135], v[84:87]\n"
    "s_waitcnt lgkmcnt(4)\n"
    "s_barrier\n"
    "ds_read_b64_tr_b16 v[100:101], v10 offset:96\n"
    "s_add_u32 m0, s57, 0xb000\n"
    "v_mfma_f32_16x16x32_bf16 v[32:35], v[112:115], v[120:123], v[32:35]\n"
    "ds_read_b64_tr_b16 v[102:103], v10 offset:1888\n"
    "buffer_load_dwordx4 v11, s[12:15], 0 offen lds\n"
    "v_mfma_f32_16x16x32_bf16 v[36:39], v[112:115], v[124:127], v[36:39]\n"
    "s_waitcnt lgkmcnt(4)\n"
    "ds_read_b64_tr_b16 v[104:105], v10 offset:128\n"
    "buffer_load_dwordx4 v12, s[12:15], s53 offen offset:1024 lds\n"
    "v_mfma_f32_16x16x32_bf16 v[40:43], v[116:119], v[120:123], v[40:43]\n"
    "ds_read_b64_tr_b16 v[106:107], v10 offset:1920\n"
    "buffer_load_dwordx4 v11, s[12:15], s54 offen offset:2048 lds\n"
    "v_mfma_f32_16x16x32_bf16 v[44:47], v[116:119], v[124:127], v[44:47]\n"
    "s_waitcnt lgkmcnt(4)\n"
    "ds_read_b64_tr_b16 v[108:109], v10 offset:160\n"
    "buffer_load_dwordx4 v12, s[12:15], s97 offen offset:3072 lds\n"
    "v_mfma_f32_16x16x32_bf16 v[48:51], v[96:99], v[120:123], v[48:51]\n"
    "ds_read_b64_tr_b16 v[110:111], v10 offset:1952\n"
    "ds_read_b128 v[128:131], v3\n"
    "s_add_u32 s62, s99, 1\n"
    "v_mfma_f32_16x16x32_bf16 v[52:55], v[96:99], v[124:127], v[52:55]\n"
    "s_waitcnt lgkmcnt(5)\n"
    "ds_read_b64_tr_b16 v[112:113], v10 offset:192\n"
    "s_and_b32 s62, s62, 15\n"
    "v_mfma_f32_16x16x32_bf16 v[56:59], v[100:103], v[120:123], v[56:59]\n"
    "ds_read_b64_tr_b16 v[114:115], v10 offset:1984\n"
    "ds_read_b128 v[132:135], v13\n"
    "s_sub_u32 s63, s62, s99\n"
    "v_mfma_f32_16x16x32_bf16 v[60:63], v[100:103], v[124:127], v[60:63]\n"
    "s_waitcnt lgkmcnt(6)\n"
    "ds_read_b64_tr_b16 v[116:117], v10 offset:7168\n"
    "s_mov_b32 s99, s62\n"
    "v_mfma_f32_16x16x32_bf16 v[64:67], v[104:107], v[120:123], v[64:67]\n"
    "ds_read_b64_tr_b16 v[118:119], v10 offset:8960\n"
    "s_mul_i32 s63, s63, 0x80\n"
    "v_mfma_f32_16x16x32_bf16 v[68:71], v[104:107], v[124:127], v[68:71]\n"
    "s_waitcnt lgkmcnt(6)\n"
    "ds_read_b64_tr_b16 v[96:97], v10 offset:7200\n"
    "v_add_u32 v11, s63, v11\n"
    "v_mfma_f32_16x16x32_bf16 v[72:75], v[108:111], v[120:123], v[72:75]\n"
    "ds_read_b64_tr_b16 v[98:99], v10 offset:8992\n"
    "v_add_u32 v12, s63, v12\n"
    "v_mfma_f32_16x16x32_bf16 v[76:79], v[108:111], v[124:127], v[76:79]\n"
    "s_waitcnt lgkmcnt(5)\n"
    "ds_read_b64_tr_b16 v[100:101], v10 offset:7232\n"
    "s_add_u32 m0, s55, 0xb000\n"
    "v_mfma_f32_16x16x32_bf16 v[80:83], v[112:115], v[120:123], v[80:83]\n"
    "ds_read_b64_tr_b16 v[102:103], v10 offset:9024\n"
    "buffer_load_dwordx4 v9, s[28:31], 0 offen sc1 lds\n"
    "v_mfma_f32_16x16x32_bf16 v[84:87], v[112:115], v[124:127], v[84:87]\n"
    "s_waitcnt lgkmcnt(4)\n"
    "ds_read_b64_tr_b16 v[104:105], v10 offset:7264\n"
    "buffer_load_dwordx4 v9, s[28:31], 0 offen offset:1024 sc1 lds\n"
    "v_mfma_f32_16x16x32_bf16 v[32:35], v[116:119], v[128:131], v[32:35]\n"
    "ds_read_b64_tr_b16 v[106:107], v10 offset:9056\n"
    "buffer_load_dwordx4 v9, s[28:31], 0 offen offset:2048 sc1 lds\n"
    "v_mfma_f32_16x16x32_bf16 v[36:39], v[116:119], v[132:135], v[36:39]\n"
    "s_waitcnt lgkmcnt(4)\n"
    "ds_read_b64_tr_b16 v[108:109], v10 offset:7296\n"
    "buffer_load_dwordx4 v9, s[28:31], 0 offen offset:3072 sc1 lds\n"
    "v_mfma_f32_16x16x32_bf16 v[40:43], v[96:99], v[128:131], v[40:43]\n"
    "ds_read_b64_tr_b16 v[110:111], v10 offset:9088\n"
    "s_add_u32 m0, s55, 0xc000\n"
    "v_mfma_f32_16x16x32_bf16 v[44:47], v[96:99], v[132:135], v[44:47]\n"
    "s_waitcnt lgkmcnt(4)\n"
    "ds_read_b64_tr_b16 v[112:113], v10 offset:7328\n"
    "buffer_load_dwordx4 v9, s[28:31], s56 offen sc1 lds\n"
    "v_mfma_f32_16x16x32_bf16 v[48:51], v[100:103], v[128:131], v[48:51]\n"
    "s_waitcnt vmcnt(9)\n"
    "s_barrier\n"
    "v_add_u32 v2, 0xfffea000, v2\n"
    "v_add_u32 v3, 0xfffea000, v3\n"
    "v_add_u32 v4, 0xfffea000, v4\n"
    "v_add_u32 v13, 0xfffea000, v13\n"
    "ds_read_b64_tr_b16 v[114:115], v10 offset:9120\n"
    "ds_read_b128 v[120:123], v2\n"
    "buffer_load_dwordx4 v9, s[28:31], s56 offen offset:1024 sc1 lds\n"
    "v_mfma_f32_16x16x32_bf16 v[52:55], v[100:103], v[132:135], v[52:55]\n"
    "s_waitcnt lgkmcnt(5)\n"
    "ds_read_b64_tr_b16 v[116:117], v10 offset:7360\n"
    "buffer_load_dwordx4 v9, s[28:31], s56 offen offset:2048 sc1 lds\n"
    "v_mfma_f32_16x16x32_bf16 v[56:59], v[104:107], v[128:131], v[56:59]\n"
    "ds_read_b64_tr_b16 v[118:119], v10 offset:9152\n"
    "ds_read_b128 v[124:127], v4\n"
    "s_add_u32 s62, s98, 1\n"
    "v_mfma_f32_16x16x32_bf16 v[60:63], v[104:107], v[132:135], v[60:63]\n"
    "s_waitcnt lgkmcnt(6)\n"
    "v_add_u32 v10, 0xfffea000, v10\n"
    "ds_read_b64_tr_b16 v[96:97], v10\n"
    "s_and_b32 s62, s62, 15\n"
    "v_mfma_f32_16x16x32_bf16 v[64:67], v[108:111], v[128:131], v[64:67]\n"
    "ds_read_b64_tr_b16 v[98:99], v10 offset:1792\n"
    "s_sub_u32 s63, s62, s98\n"
    "v_mfma_f32_16x16x32_bf16 v[68:71], v[108:111], v[132:135], v[68:71]\n"
    "s_waitcnt lgkmcnt(6)\n"
    "ds_read_b64_tr_b16 v[100:101], v10 offset:32\n"
    "s_mov_b32 s98, s62\n"
    "v_mfma_f32_16x16x32_bf16 v[72:75], v[112:115], v[128:131], v[72:75]\n"
    "ds_read_b64_tr_b16 v[102:103], v10 offset:1824\n"
    "s_mul_i32 s63, s63, 0x3800\n"
    "v_mfma_f32_16x16x32_bf16 v[76:79], v[112:115], v[132:135], v[76:79]\n"
    "s_waitcnt lgkmcnt(5)\n"
    "ds_read_b64_tr_b16 v[104:105], v10 offset:64\n"
    "v_add_u32 v9, s63, v9\n"
    "v_mfma_f32_16x16x32_bf16 v[80:83], v[116:119], v[128:131], v[80:83]\n"
    "ds_read_b64_tr_b16 v[106:107], v10 offset:1856\n"
    "v_mfma_f32_16x16x32_bf16 v[84:87], v[116:119], v[132:135], v[84:87]\n"
    "s_waitcnt lgkmcnt(4)\n"
    "s_barrier\n"
    "ds_read_b64_tr_b16 v[108:109], v10 offset:96\n"
    "s_add_u32 m0, s57, 0x16000\n"
    "v_mfma_f32_16x16x32_bf16 v[32:35], v[96:99], v[120:123], v[32:35]\n"
    "ds_read_b64_tr_b16 v[110:111], v10 offset:1888\n"
    "buffer_load_dwordx4 v11, s[12:15], 0 offen lds\n"
    "v_mfma_f32_16x16x32_bf16 v[36:39], v[96:99], v[124:127], v[36:39]\n"
    "s_waitcnt lgkmcnt(4)\n"
    "ds_read_b64_tr_b16 v[112:113], v10 offset:128\n"
    "buffer_load_dwordx4 v12, s[12:15], s53 offen offset:1024 lds\n"
    "v_mfma_f32_16x16x32_bf16 v[40:43], v[100:103], v[120:123], v[40:43]\n"
    "ds_read_b64_tr_b16 v[114:115], v10 offset:1920\n"
    "buffer_load_dwordx4 v11, s[12:15], s54 offen offset:2048 lds\n"
    "v_mfma_f32_16x16x32_bf16 v[44:47], v[100:103], v[124:127], v[44:47]\n"
    "s_waitcnt lgkmcnt(4)\n"
    "ds_read_b64_tr_b16 v[116:117], v10 offset:160\n"
    "buffer_load_dwordx4 v12, s[12:15], s97 offen offset:3072 lds\n"
    "v_mfma_f32_16x16x32_bf16 v[48:51], v[104:107], v[120:123], v[48:51]\n"
    "ds_read_b64_tr_b16 v[118:119], v10 offset:1952\n"
    "ds_read_b128 v[128:131], v3\n"
    "s_add_u32 s62, s99, 1\n"
    "v_mfma_f32_16x16x32_bf16 v[52:55], v[104:107], v[124:127], v[52:55]\n"
    "s_waitcnt lgkmcnt(5)\n"
    "ds_read_b64_tr_b16 v[96:97], v10 offset:192\n"
    "s_and_b32 s62, s62, 15\n"
    "v_mfma_f32_16x16x32_bf16 v[56:59], v[108:111], v[120:123], v[56:59]\n"
    "ds_read_b64_tr_b16 v[98:99], v10 offset:1984\n"
    "ds_read_b128 v[132:135], v13\n"
    "s_sub_u32 s63, s62, s99\n"
    "v_mfma_f32_16x16x32_bf16 v[60:63], v[108:111], v[124:127], v[60:63]\n"
    "s_waitcnt lgkmcnt(6)\n"
    "ds_read_b64_tr_b16 v[100:101], v10 offset:7168\n"
    "s_mov_b32 s99, s62\n"
    "v_mfma_f32_16x16x32_bf16 v[64:67], v[112:115], v[120:123], v[64:67]\n"
    "ds_read_b64_tr_b16 v[102:103], v10 offset:8960\n"
    "s_mul_i32 s63, s63, 0x80\n"
    "v_mfma_f32_16x16x32_bf16 v[68:71], v[112:115], v[124:127], v[68:71]\n"
    "s_waitcnt lgkmcnt(6)\n"
    "ds_read_b64_tr_b16 v[104:105], v10 offset:7200\n"
    "v_add_u32 v11, s63, v11\n"
    "v_mfma_f32_16x16x32_bf16 v[72:75], v[116:119], v[120:123], v[72:75]\n"
    "ds_read_b64_tr_b16 v[106:107], v10 offset:8992\n"
    "v_add_u32 v12, s63, v12\n"
    "v_mfma_f32_16x16x32_bf16 v[76:79], v[116:119], v[124:127], v[76:79]\n"
    "s_waitcnt lgkmcnt(5)\n"
    "ds_read_b64_tr_b16 v[108:109], v10 offset:7232\n"
    "s_add_u32 m0, s55, 0x16000\n"
    "v_mfma_f32_16x16x32_bf16 v[80:83], v[96:99], v[120:123], v[80:83]\n"
    "ds_read_b64_tr_b16 v[110:111], v10 offset:9024\n"
    "buffer_load_dwordx4 v9, s[28:31], 0 offen sc1 lds\n"
    "v_mfma_f32_16x16x32_bf16 v[84:87], v[96:99], v[124:127], v[84:87]\n"
    "s_waitcnt lgkmcnt(4)\n"
    "ds_read_b64_tr_b16 v[112:113], v10 offset:7264\n"
    "buffer_load_dwordx4 v9, s[28:31], 0 offen offset:1024 sc1 lds\n"
    "v_mfma_f32_16x16x32_bf16 v[32:35], v[100:103], v[128:131], v[32:35]\n"
    "ds_read_b64_tr_b16 v[114:115], v10 offset:9056\n"
    "buffer_load_dwordx4 v9, s[28:31], 0 offen offset:2048 sc1 lds\n"
    "v_mfma_f32_16x16x32_bf16 v[36:39], v[100:103], v[132:135], v[36:39]\n"
    "s_waitcnt lgkmcnt(4)\n"
    "ds_read_b64_tr_b16 v[116:117], v10 offset:7296\n"
    "buffer_load_dwordx4 v9, s[28:31], 0 offen offset:3072 sc1 lds\n"
    "v_mfma_f32_16x16x32_bf16 v[40:43], v[104:107], v[128:131], v[40:43]\n"
    "ds_read_b64_tr_b16 v[118:119], v10 offset:9088\n"
    "s_add_u32 m0, s55, 0x17000\n"
    "v_mfma_f32_16x16x32_bf16 v[44:47], v[104:107], v[132:135], v[44:47]\n"
    "s_waitcnt lgkmcnt(4)\n"
    "ds_read_b64_tr_b16 v[96:97], v10 offset:7328\n"
    "buffer_load_dwordx4 v9, s[28:31], s56 offen sc1 lds\n"
    "v_mfma_f32_16x16x32_bf16 v[48:51], v[108:111], v[128:131], v[48:51]\n"
    "s_waitcnt vmcnt(9)\n"
    "s_barrier\n"
    "v_add_u32 v2, 0xb000, v2\n"
    "v_add_u32 v3, 0xb000, v3\n"
    "v_add_u32 v4, 0xb000, v4\n"
    "v_add_u32 v13, 0xb000, v13\n"
    "ds_read_b64_tr_b16 v[98:99], v10 offset:9120\n"
    "ds_read_b128 v[120:123], v2\n"
    "buffer_load_dwordx4 v9, s[28:31], s56 offen offset:1024 sc1 lds\n"
    "v_mfma_f32_16x16x32_bf16 v[52:55], v[108:111], v[132:135], v[52:55]\n"
    "s_waitcnt lgkmcnt(5)\n"
    "ds_read_b64_tr_b16 v[100:101], v10 offset:7360\n"
    "buffer_load_dwordx4 v9, s[28:31], s56 offen offset:2048 sc1 lds\n"
    "v_mfma_f32_16x16x32_bf16 v[56:59], v[112:115], v[128:131], v[56:59]\n"
    "ds_read_b64_tr_b16 v[102:103], v10 offset:9152\n"
    "ds_read_b128 v[124:127], v4\n"
    "s_add_u32 s62, s98, 1\n"
    "v_mfma_f32_16x16x32_bf16 v[60:63], v[112:115], v[132:135], v[60:63]\n"
    "s_waitcnt lgkmcnt(6)\n"
    "v_add_u32 v10, 0xb000, v10\n"
    "ds_read_b64_tr_b16 v[104:105], v10\n"
    "s_and_b32 s62, s62, 15\n"
    "v_mfma_f32_16x16x32_bf16 v[64:67], v[116:119], v[128:131], v[64:67]\n"
    "ds_read_b64_tr_b16 v[106:107], v10 offset:1792\n"
    "s_sub_u32 s63, s62, s98\n"
    "v_mfma_f32_16x16x32_bf16 v[68:71], v[116:119], v[132:135], v[68:71]\n"
    "s_waitcnt lgkmcnt(6)\n"
    "ds_read_b64_tr_b16 v[108:109], v10 offset:32\n"
    "s_mov_b32 s98, s62\n"
    "v_mfma_f32_16x16x32_bf16 v[72:75], v[96:99], v[128:131], v[72:75]\n"
    "ds_read_b64_tr_b16 v[110:111], v10 offset:1824\n"
    "s_mul_i32 s63, s63, 0x3800\n"
    "v_mfma_f32_16x16x32_bf16 v[76:79], v[96:99], v[132:135], v[76:79]\n"
    "s_waitcnt lgkmcnt(5)\n"
    "ds_read_b64_tr_b16 v[112:113], v10 offset:64\n"
    "v_add_u32 v9, s63, v9\n"
    "v_mfma_f32_16x16x32_bf16 v[80:83], v[100:103], v[128:131], v[80:83]\n"
    "ds_read_b64_tr_b16 v[114:115], v10 offset:1856\n"
    "v_mfma_f32_16x16x32_bf16 v[84:87], v[100:103], v[132:135], v[84:87]\n"
    "s_waitcnt lgkmcnt(4)\n"
    "s_barrier\n"
    "ds_read_b64_tr_b16 v[116:117], v10 offset:96\n"
    "s_add_u32 m0, s57, 0\n"
    "v_mfma_f32_16x16x32_bf16 v[32:35], v[104:107], v[120:123], v[32:35]\n"
    "ds_read_b64_tr_b16 v[118:119], v10 offset:1888\n"
    "buffer_load_dwordx4 v11, s[12:15], 0 offen lds\n"
    "v_mfma_f32_16x16x32_bf16 v[36:39], v[104:107], v[124:127], v[36:39]\n"
    "s_waitcnt lgkmcnt(4)\n"
    "ds_read_b64_tr_b16 v[96:97], v10 offset:128\n"
    "buffer_load_dwordx4 v12, s[12:15], s53 offen offset:1024 lds\n"
    "v_mfma_f32_16x16x32_bf16 v[40:43], v[108:111], v[120:123], v[40:43]\n"
    "ds_read_b64_tr_b16 v[98:99], v10 offset:1920\n"
    "buffer_load_dwordx4 v11, s[12:15], s54 offen offset:2048 lds\n"
    "v_mfma_f32_16x16x32_bf16 v[44:47], v[108:111], v[124:127], v[44:47]\n"
    "s_waitcnt lgkmcnt(4)\n"
    "ds_read_b64_tr_b16 v[100:101], v10 offset:160\n"
    "buffer_load_dwordx4 v12, s[12:15], s97 offen offset:3072 lds\n"
    "v_mfma_f32_16x16x32_bf16 v[48:51], v[112:115], v[120:123], v[48:51]\n"
    "ds_read_b64_tr_b16 v[102:103], v10 offset:1952\n"
    "ds_read_b128 v[128:131], v3\n"
    "s_add_u32 s62, s99, 1\n"
    "v_mfma_f32_16x16x32_bf16 v[52:55], v[112:115], v[124:127], v[52:55]\n"
    "s_waitcnt lgkmcnt(5)\n"
    "ds_read_b64_tr_b16 v[104:105], v10 offset:192\n"
    "s_and_b32 s62, s62, 15\n"
    "v_mfma_f32_16x16x32_bf16 v[56:59], v[116:119], v[120:123], v[56:59]\n"
    "ds_read_b64_tr_b16 v[106:107], v10 offset:1984\n"
    "ds_read_b128 v[132:135], v13\n"
    "s_sub_u32 s63, s62, s99\n"
    "v_mfma_f32_16x16x32_bf16 v[60:63], v[116:119], v[124:127], v[60:63]\n"
    "s_waitcnt lgkmcnt(6)\n"
    "ds_read_b64_tr_b16 v[108:109], v10 offset:7168\n"
    "s_mov_b32 s99, s62\n"
    "v_mfma_f32_16x16x32_bf16 v[64:67], v[96:99], v[120:123], v[64:67]\n"
    "ds_read_b64_tr_b16 v[110:111], v10 offset:8960\n"
    "s_mul_i32 s63, s63, 0x80\n"
    "v_mfma_f32_16x16x32_bf16 v[68:71], v[96:99], v[124:127], v[68:71]\n"
    "s_waitcnt lgkmcnt(6)\n"
    "ds_read_b64_tr_b16 v[112:113], v10 offset:7200\n"
    "v_add_u32 v11, s63, v11\n"
    "v_mfma_f32_16x16x32_bf16 v[72:75], v[100:103], v[120:123], v[72:75]\n"
    "ds_read_b64_tr_b16 v[114:115], v10 offset:8992\n"
    "v_add_u32 v12, s63, v12\n"
    "v_mfma_f32_16x16x32_bf16 v[76:79], v[100:103], v[124:127], v[76:79]\n"
    "s_waitcnt lgkmcnt(5)\n"
    "ds_read_b64_tr_b16 v[116:117], v10 offset:7232\n"
    "s_add_u32 m0, s55, 0\n"
    "v_mfma_f32_16x16x32_bf16 v[80:83], v[104:107], v[120:123], v[80:83]\n"
    "ds_read_b64_tr_b16 v[118:119], v10 offset:9024\n"
    "buffer_load_dwordx4 v9, s[28:31], 0 offen sc1 lds\n"
    "v_mfma_f32_16x16x32_bf16 v[84:87], v[104:107], v[124:127], v[84:87]\n"
    "s_waitcnt lgkmcnt(4)\n"
    "ds_read_b64_tr_b16 v[96:97], v10 offset:7264\n"
    "buffer_load_dwordx4 v9, s[28:31], 0 offen offset:1024 sc1 lds\n"
    "v_mfma_f32_16x16x32_bf16 v[32:35], v[108:111], v[128:131], v[32:35]\n"
    "ds_read_b64_tr_b16 v[98:99], v10 offset:9056\n"
    "buffer_load_dwordx4 v9, s[28:31], 0 offen offset:2048 sc1 lds\n"
    "v_mfma_f32_16x16x32_bf16 v[36:39], v[108:111], v[132:135], v[36:39]\n"
    "s_waitcnt lgkmcnt(4)\n"
    "ds_read_b64_tr_b16 v[100:101], v10 offset:7296\n"
    "buffer_load_dwordx4 v9, s[28:31], 0 offen offset:3072 sc1 lds\n"
    "v_mfma_f32_16x16x32_bf16 v[40:43], v[112:115], v[128:131], v[40:43]\n"
    "ds_read_b64_tr_b16 v[102:103], v10 offset:9088\n"
    "s_add_u32 m0, s55, 0x1000\n"
    "v_mfma_f32_16x16x32_bf16 v[44:47], v[112:115], v[132:135], v[44:47]\n"
    "s_waitcnt lgkmcnt(4)\n"
    "ds_read_b64_tr_b16 v[104:105], v10 offset:7328\n"
    "buffer_load_dwordx4 v9, s[28:31], s56 offen sc1 lds\n"
    "v_mfma_f32_16x16x32_bf16 v[48:51], v[116:119], v[128:131], v[48:51]\n"
    "s_waitcnt vmcnt(9)\n"
    "s_barrier\n"
    "v_add_u32 v2, 0xb000, v2\n"
    "v_add_u32 v3, 0xb000, v3\n"
    "v_add_u32 v4, 0xb000, v4\n"
    "v_add_u32 v13, 0xb000, v13\n"
    "ds_read_b64_tr_b16 v[106:107], v10 offset:9120\n"
    "ds_read_b128 v[120:123], v2\n"
    "buffer_load_dwordx4 v9, s[28:31], s56 offen offset:1024 sc1 lds\n"
    "v_mfma_f32_16x16x32_bf16 v[52:55], v[116:119], v[132:135], v[52:55]\n"
    "s_waitcnt lgkmcnt(5)\n"
    "ds_read_b64_tr_b16 v[108:109], v10 offset:7360\n"
    "buffer_load_dwordx4 v9, s[28:31], s56 offen offset:2048 sc1 lds\n"
    "v_mfma_f32_16x16x32_bf16 v[56:59], v[96:99], v[128:131], v[56:59]\n"
    "ds_read_b64_tr_b16 v[110:111], v10 offset:9152\n"
    "ds_read_b128 v[124:127], v4\n"
    "s_add_u32 s62, s98, 1\n"
    "v_mfma_f32_16x16x32_bf16 v[60:63], v[96:99], v[132:135], v[60:63]\n"
    "s_waitcnt lgkmcnt(6)\n"
    "v_add_u32 v10, 0xb000, v10\n"
    "ds_read_b64_tr_b16 v[112:113], v10\n"
    "s_and_b32 s62, s62, 15\n"
    "v_mfma_f32_16x16x32_bf16 v[64:67], v[100:103], v[128:131], v[64:67]\n"
    "ds_read_b64_tr_b16 v[114:115], v10 offset:1792\n"
    "s_sub_u32 s63, s62, s98\n"
    "v_mfma_f32_16x16x32_bf16 v[68:71], v[100:103], v[132:135], v[68:71]\n"
    "s_waitcnt lgkmcnt(6)\n"
    "ds_read_b64_tr_b16 v[116:117], v10 offset:32\n"
    "s_mov_b32 s98, s62\n"
    "v_mfma_f32_16x16x32_bf16 v[72:75], v[104:107], v[128:131], v[72:75]\n"
    "ds_read_b64_tr_b16 v[118:119], v10 offset:1824\n"
    "s_mul_i32 s63, s63, 0x3800\n"
    "v_mfma_f32_16x16x32_bf16 v[76:79], v[104:107], v[132:135], v[76:79]\n"
    "s_waitcnt lgkmcnt(5)\n"
    "ds_read_b64_tr_b16 v[96:97], v10 offset:64\n"
    "v_add_u32 v9, s63, v9\n"
    "v_mfma_f32_16x16x32_bf16 v[80:83], v[108:111], v[128:131], v[80:83]\n"
    "ds_read_b64_tr_b16 v[98:99], v10 offset:1856\n"
    "v_mfma_f32_16x16x32_bf16 v[84:87], v[108:111], v[132:135], v[84:87]\n"
    "s_waitcnt lgkmcnt(4)\n"
    "s_barrier\n"
    "ds_read_b64_tr_b16 v[100:101], v10 offset:96\n"
    "s_add_u32 m0, s57, 0xb000\n"
    "v_mfma_f32_16x16x32_bf16 v[32:35], v[112:115], v[120:123], v[32:35]\n"
    "ds_read_b64_tr_b16 v[102:103], v10 offset:1888\n"
    "buffer_load_dwordx4 v11, s[12:15], 0 offen lds\n"
    "v_mfma_f32_16x16x32_bf16 v[36:39], v[112:115], v[124:127], v[36:39]\n"
    "s_waitcnt lgkmcnt(4)\n"
    "ds_read_b64_tr_b16 v[104:105], v10 offset:128\n"
    "buffer_load_dwordx4 v12, s[12:15], s53 offen offset:1024 lds\n"
    "v_mfma_f32_16x16x32_bf16 v[40:43], v[116:119], v[120:123], v[40:43]\n"
    "ds_read_b64_tr_b16 v[106:107], v10 offset:1920\n"
    "buffer_load_dwordx4 v11, s[12:15], s54 offen offset:2048 lds\n"
    "v_mfma_f32_16x16x32_bf16 v[44:47], v[116:119], v[124:127], v[44:47]\n"
    "s_waitcnt lgkmcnt(4)\n"
    "ds_read_b64_tr_b16 v[108:109], v10 offset:160\n"
    "buffer_load_dwordx4 v12, s[12:15], s97 offen offset:3072 lds\n"
    "v_mfma_f32_16x16x32_bf16 v[48:51], v[96:99], v[120:123], v[48:51]\n"
    "ds_read_b64_tr_b16 v[110:111], v10 offset:1952\n"
    "ds_read_b128 v[128:131], v3\n"
    "s_add_u32 s62, s99, 1\n"
    "v_mfma_f32_16x16x32_bf16 v[52:55], v[96:99], v[124:127], v[52:55]\n"
    "s_waitcnt lgkmcnt(5)\n"
    "ds_read_b64_tr_b16 v[112:113], v10 offset:192\n"
    "s_and_b32 s62, s62, 15\n"
    "v_mfma_f32_16x16x32_bf16 v[56:59], v[100:103], v[120:123], v[56:59]\n"
    "ds_read_b64_tr_b16 v[114:115], v10 offset:1984\n"
    "ds_read_b128 v[132:135], v13\n"
    "s_sub_u32 s63, s62, s99\n"
    "v_mfma_f32_16x16x32_bf16 v[60:63], v[100:103], v[124:127], v[60:63]\n"
    "s_waitcnt lgkmcnt(6)\n"
    "ds_read_b64_tr_b16 v[116:117], v10 offset:7168\n"
    "s_mov_b32 s99, s62\n"
    "v_mfma_f32_16x16x32_bf16 v[64:67], v[104:107], v[120:123], v[64:67]\n"
    "ds_read_b64_tr_b16 v[118:119], v10 offset:8960\n"
    "s_mul_i32 s63, s63, 0x80\n"
    "v_mfma_f32_16x16x32_bf16 v[68:71], v[104:107], v[124:127], v[68:71]\n"
    "s_waitcnt lgkmcnt(6)\n"
    "ds_read_b64_tr_b16 v[96:97], v10 offset:7200\n"
    "v_add_u32 v11, s63, v11\n"
    "v_mfma_f32_16x16x32_bf16 v[72:75], v[108:111], v[120:123], v[72:75]\n"
    "ds_read_b64_tr_b16 v[98:99], v10 offset:8992\n"
    "v_add_u32 v12, s63, v12\n"
    "v_mfma_f32_16x16x32_bf16 v[76:79], v[108:111], v[124:127], v[76:79]\n"
    "s_waitcnt lgkmcnt(5)\n"
    "ds_read_b64_tr_b16 v[100:101], v10 offset:7232\n"
    "s_add_u32 m0, s55, 0xb000\n"
    "v_mfma_f32_16x16x32_bf16 v[80:83], v[112:115], v[120:123], v[80:83]\n"
    "ds_read_b64_tr_b16 v[102:103], v10 offset:9024\n"
    "buffer_load_dwordx4 v9, s[28:31], 0 offen sc1 lds\n"
    "v_mfma_f32_16x16x32_bf16 v[84:87], v[112:115], v[124:127], v[84:87]\n"
    "s_waitcnt lgkmcnt(4)\n"
    "ds_read_b64_tr_b16 v[104:105], v10 offset:7264\n"
    "buffer_load_dwordx4 v9, s[28:31], 0 offen offset:1024 sc1 lds\n"
    "v_mfma_f32_16x16x32_bf16 v[32:35], v[116:119], v[128:131], v[32:35]\n"
    "ds_read_b64_tr_b16 v[106:107], v10 offset:9056\n"
    "buffer_load_dwordx4 v9, s[28:31], 0 offen offset:2048 sc1 lds\n"
    "v_mfma_f32_16x16x32_bf16 v[36:39], v[116:119], v[132:135], v[36:39]\n"
    "s_waitcnt lgkmcnt(4)\n"
    "ds_read_b64_tr_b16 v[108:109], v10 offset:7296\n"
    "buffer_load_dwordx4 v9, s[28:31], 0 offen offset:3072 sc1 lds\n"
    "v_mfma_f32_16x16x32_bf16 v[40:43], v[96:99], v[128:131], v[40:43]\n"
    "ds_read_b64_tr_b16 v[110:111], v10 offset:9088\n"
    "s_add_u32 m0, s55, 0xc000\n"
    "v_mfma_f32_16x16x32_bf16 v[44:47], v[96:99], v[132:135], v[44:47]\n"
    "s_waitcnt lgkmcnt(4)\n"
    "ds_read_b64_tr_b16 v[112:113], v10 offset:7328\n"
    "buffer_load_dwordx4 v9, s[28:31], s56 offen sc1 lds\n"
    "v_mfma_f32_16x16x32_bf16 v[48:51], v[100:103], v[128:131], v[48:51]\n"
    "s_waitcnt vmcnt(9)\n"
    "s_barrier\n"
    "v_add_u32 v2, 0xfffea000, v2\n"
    "v_add_u32 v3, 0xfffea000, v3\n"
    "v_add_u32 v4, 0xfffea000, v4\n"
    "v_add_u32 v13, 0xfffea000, v13\n"
    "ds_read_b64_tr_b16 v[114:115], v10 offset:9120\n"
    "ds_read_b128 v[120:123], v2\n"
    "buffer_load_dwordx4 v9, s[28:31], s56 offen offset:1024 sc1 lds\n"
    "v_mfma_f32_16x16x32_bf16 v[52:55], v[100:103], v[132:135], v[52:55]\n"
    "s_waitcnt lgkmcnt(5)\n"
    "ds_read_b64_tr_b16 v[116:117], v10 offset:7360\n"
    "buffer_load_dwordx4 v9, s[28:31], s56 offen offset:2048 sc1 lds\n"
    "v_mfma_f32_16x16x32_bf16 v[56:59], v[104:107], v[128:131], v[56:59]\n"
    "ds_read_b64_tr_b16 v[118:119], v10 offset:9152\n"
    "ds_read_b128 v[124:127], v4\n"
    "s_add_u32 s62, s98, 1\n"
    "v_mfma_f32_16x16x32_bf16 v[60:63], v[104:107], v[132:135], v[60:63]\n"
    "s_waitcnt lgkmcnt(6)\n"
    "v_add_u32 v10, 0xfffea000, v10\n"
    "ds_read_b64_tr_b16 v[96:97], v10\n"
    "s_and_b32 s62, s62, 15\n"
    "v_mfma_f32_16x16x32_bf16 v[64:67], v[108:111], v[128:131], v[64:67]\n"
    "ds_read_b64_tr_b16 v[98:99], v10 offset:1792\n"
    "s_sub_u32 s63, s62, s98\n"
    "v_mfma_f32_16x16x32_bf16 v[68:71], v[108:111], v[132:135], v[68:71]\n"
    "s_waitcnt lgkmcnt(6)\n"
    "ds_read_b64_tr_b16 v[100:101], v10 offset:32\n"
    "s_mov_b32 s98, s62\n"
    "v_mfma_f32_16x16x32_bf16 v[72:75], v[112:115], v[128:131], v[72:75]\n"
    "ds_read_b64_tr_b16 v[102:103], v10 offset:1824\n"
    "s_mul_i32 s63, s63, 0x3800\n"
    "v_mfma_f32_16x16x32_bf16 v[76:79], v[112:115], v[132:135], v[76:79]\n"
    "s_waitcnt lgkmcnt(5)\n"
    "ds_read_b64_tr_b16 v[104:105], v10 offset:64\n"
    "v_add_u32 v9, s63, v9\n"
    "v_mfma_f32_16x16x32_bf16 v[80:83], v[116:119], v[128:131], v[80:83]\n"
    "ds_read_b64_tr_b16 v[106:107], v10 offset:1856\n"
    "v_mfma_f32_16x16x32_bf16 v[84:87], v[116:119], v[132:135], v[84:87]\n"
    "s_waitcnt lgkmcnt(4)\n"
    "s_barrier\n"
    "ds_read_b64_tr_b16 v[108:109], v10 offset:96\n"
    "s_add_u32 m0, s57, 0x16000\n"
    "v_mfma_f32_16x16x32_bf16 v[32:35], v[96:99], v[120:123], v[32:35]\n"
    "ds_read_b64_tr_b16 v[110:111], v10 offset:1888\n"
    "buffer_load_dwordx4 v11, s[12:15], 0 offen lds\n"
    "v_mfma_f32_16x16x32_bf16 v[36:39], v[96:99], v[124:127], v[36:39]\n"
    "s_waitcnt lgkmcnt(4)\n"
    "ds_read_b64_tr_b16 v[112:113], v10 offset:128\n"
    "buffer_load_dwordx4 v12, s[12:15], s53 offen offset:1024 lds\n"
    "v_mfma_f32_16x16x32_bf16 v[40:43], v[100:103], v[120:123], v[40:43]\n"
    "ds_read_b64_tr_b16 v[114:115], v10 offset:1920\n"
    "buffer_load_dwordx4 v11, s[12:15], s54 offen offset:2048 lds\n"
    "v_mfma_f32_16x16x32_bf16 v[44:47], v[100:103], v[124:127], v[44:47]\n"
    "s_waitcnt lgkmcnt(4)\n"
    "ds_read_b64_tr_b16 v[116:117], v10 offset:160\n"
    "buffer_load_dwordx4 v12, s[12:15], s97 offen offset:3072 lds\n"
    "v_mfma_f32_16x16x32_bf16 v[48:51], v[104:107], v[120:123], v[48:51]\n"
    "ds_read_b64_tr_b16 v[118:119], v10 offset:1952\n"
    "ds_read_b128 v[128:131], v3\n"
    "s_add_u32 s62, s99, 1\n"
    "v_mfma_f32_16x16x32_bf16 v[52:55], v[104:107], v[124:127], v[52:55]\n"
    "s_waitcnt lgkmcnt(5)\n"
    "ds_read_b64_tr_b16 v[96:97], v10 offset:192\n"
    "s_and_b32 s62, s62, 15\n"
    "v_mfma_f32_16x16x32_bf16 v[56:59], v[108:111], v[120:123], v[56:59]\n"
    "ds_read_b64_tr_b16 v[98:99], v10 offset:1984\n"
    "ds_read_b128 v[132:135], v13\n"
    "s_sub_u32 s63, s62, s99\n"
    "v_mfma_f32_16x16x32_bf16 v[60:63], v[108:111], v[124:127], v[60:63]\n"
    "s_waitcnt lgkmcnt(6)\n"
    "ds_read_b64_tr_b16 v[100:101], v10 offset:7168\n"
    "s_mov_b32 s99, s62\n"
    "v_mfma_f32_16x16x32_bf16 v[64:67], v[112:115], v[120:123], v[64:67]\n"
    "ds_read_b64_tr_b16 v[102:103], v10 offset:8960\n"
    "s_mul_i32 s63, s63, 0x80\n"
    "v_mfma_f32_16x16x32_bf16 v[68:71], v[112:115], v[124:127], v[68:71]\n"
    "s_waitcnt lgkmcnt(6)\n"
    "ds_read_b64_tr_b16 v[104:105], v10 offset:7200\n"
    "v_add_u32 v11, s63, v11\n"
    "v_mfma_f32_16x16x32_bf16 v[72:75], v[116:119], v[120:123], v[72:75]\n"
    "ds_read_b64_tr_b16 v[106:107], v10 offset:8992\n"
    "v_add_u32 v12, s63, v12\n"
    "v_mfma_f32_16x16x32_bf16 v[76:79], v[116:119], v[124:127], v[76:79]\n"
    "s_waitcnt lgkmcnt(5)\n"
    "ds_read_b64_tr_b16 v[108:109], v10 offset:7232\n"
    "s_add_u32 m0, s55, 0x16000\n"
    "v_mfma_f32_16x16x32_bf16 v[80:83], v[96:99], v[120:123], v[80:83]\n"
    "ds_read_b64_tr_b16 v[110:111], v10 offset:9024\n"
    "buffer_load_dwordx4 v9, s[28:31], 0 offen sc1 lds\n"
    "v_mfma_f32_16x16x32_bf16 v[84:87], v[96:99], v[124:127], v[84:87]\n"
    "s_waitcnt lgkmcnt(4)\n"
    "ds_read_b64_tr_b16 v[112:113], v10 offset:7264\n"
    "buffer_load_dwordx4 v9, s[28:31], 0 offen offset:1024 sc1 lds\n"
    "v_mfma_f32_16x16x32_bf16 v[32:35], v[100:103], v[128:131], v[32:35]\n"
    "ds_read_b64_tr_b16 v[114:115], v10 offset:9056\n"
    "buffer_load_dwordx4 v9, s[28:31], 0 offen offset:2048 sc1 lds\n"
    "v_mfma_f32_16x16x32_bf16 v[36:39], v[100:103], v[132:135], v[36:39]\n"
    "s_waitcnt lgkmcnt(4)\n"
    "ds_read_b64_tr_b16 v[116:117], v10 offset:7296\n"
    "buffer_load_dwordx4 v9, s[28:31], 0 offen offset:3072 sc1 lds\n"
    "v_mfma_f32_16x16x32_bf16 v[40:43], v[104:107], v[128:131], v[40:43]\n"
    "ds_read_b64_tr_b16 v[118:119], v10 offset:9088\n"
    "s_add_u32 m0, s55, 0x17000\n"
    "v_mfma_f32_16x16x32_bf16 v[44:47], v[104:107], v[132:135], v[44:47]\n"
    "s_waitcnt lgkmcnt(4)\n"
    "ds_read_b64_tr_b16 v[96:97], v10 offset:7328\n"
    "buffer_load_dwordx4 v9, s[28:31], s56 offen sc1 lds\n"
    "v_mfma_f32_16x16x32_bf16 v[48:51], v[108:111], v[128:131], v[48:51]\n"
    "s_waitcnt vmcnt(9)\n"
    "s_barrier\n"
    "v_add_u32 v2, 0xb000, v2\n"
    "v_add_u32 v3, 0xb000, v3\n"
    "v_add_u32 v4, 0xb000, v4\n"
    "v_add_u32 v13, 0xb000, v13\n"
    "ds_read_b64_tr_b16 v[98:99], v10 offset:9120\n"
    "ds_read_b128 v[120:123], v2\n"
    "buffer_load_dwordx4 v9, s[28:31], s56 offen offset:1024 sc1 lds\n"
    "v_mfma_f32_16x16x32_bf16 v[52:55], v[108:111], v[132:135], v[52:55]\n"
    "s_waitcnt lgkmcnt(5)\n"
    "ds_read_b64_tr_b16 v[100:101], v10 offset:7360\n"
    "buffer_load_dwordx4 v9, s[28:31], s56 offen offset:2048 sc1 lds\n"
    "v_mfma_f32_16x16x32_bf16 v[56:59], v[112:115], v[128:131], v[56:59]\n"
    "ds_read_b64_tr_b16 v[102:103], v10 offset:9152\n"
    "ds_read_b128 v[124:127], v4\n"
    "s_add_u32 s62, s98, 1\n"
    "v_mfma_f32_16x16x32_bf16 v[60:63], v[112:115], v[132:135], v[60:63]\n"
    "s_waitcnt lgkmcnt(6)\n"
    "v_add_u32 v10, 0xb000, v10\n"
    "ds_read_b64_tr_b16 v[104:105], v10\n"
    "s_and_b32 s62, s62, 15\n"
    "v_mfma_f32_16x16x32_bf16 v[64:67], v[116:119], v[128:131], v[64:67]\n"
    "ds_read_b64_tr_b16 v[106:107], v10 offset:1792\n"
    "s_sub_u32 s63, s62, s98\n"
    "v_mfma_f32_16x16x32_bf16 v[68:71], v[116:119], v[132:135], v[68:71]\n"
    "s_waitcnt lgkmcnt(6)\n"
    "ds_read_b64_tr_b16 v[108:109], v10 offset:32\n"
    "s_mov_b32 s98, s62\n"
    "v_mfma_f32_16x16x32_bf16 v[72:75], v[96:99], v[128:131], v[72:75]\n"
    "ds_read_b64_tr_b16 v[110:111], v10 offset:1824\n"
    "s_mul_i32 s63, s63, 0x3800\n"
    "v_mfma_f32_16x16x32_bf16 v[76:79], v[96:99], v[132:135], v[76:79]\n"
    "s_waitcnt lgkmcnt(5)\n"
    "ds_read_b64_tr_b16 v[112:113], v10 offset:64\n"
    "v_add_u32 v9, s63, v9\n"
    "v_mfma_f32_16x16x32_bf16 v[80:83], v[100:103], v[128:131], v[80:83]\n"
    "ds_read_b64_tr_b16 v[114:115], v10 offset:1856\n"
    "v_mfma_f32_16x16x32_bf16 v[84:87], v[100:103], v[132:135], v[84:87]\n"
    "s_waitcnt lgkmcnt(4)\n"
    "s_barrier\n"
    "ds_read_b64_tr_b16 v[116:117], v10 offset:96\n"
    "s_add_u32 m0, s57, 0\n"
    "v_mfma_f32_16x16x32_bf16 v[32:35], v[104:107], v[120:123], v[32:35]\n"
    "ds_read_b64_tr_b16 v[118:119], v10 offset:1888\n"
    "buffer_load_dwordx4 v11, s[12:15], 0 offen lds\n"
    "v_mfma_f32_16x16x32_bf16 v[36:39], v[104:107], v[124:127], v[36:39]\n"
    "s_waitcnt lgkmcnt(4)\n"
    "ds_read_b64_tr_b16 v[96:97], v10 offset:128\n"
    "buffer_load_dwordx4 v12, s[12:15], s53 offen offset:1024 lds\n"
    "v_mfma_f32_16x16x32_bf16 v[40:43], v[108:111], v[120:123], v[40:43]\n"
    "ds_read_b64_tr_b16 v[98:99], v10 offset:1920\n"
    "buffer_load_dwordx4 v11, s[12:15], s54 offen offset:2048 lds\n"
    "v_mfma_f32_16x16x32_bf16 v[44:47], v[108:111], v[124:127], v[44:47]\n"
    "s_waitcnt lgkmcnt(4)\n"
    "ds_read_b64_tr_b16 v[100:101], v10 offset:160\n"
    "buffer_load_dwordx4 v12, s[12:15], s97 offen offset:3072 lds\n"
    "v_mfma_f32_16x16x32_bf16 v[48:51], v[112:115], v[120:123], v[48:51]\n"
    "ds_read_b64_tr_b16 v[102:103], v10 offset:1952\n"
    "ds_read_b128 v[128:131], v3\n"
    "s_add_u32 s62, s99, 1\n"
    "v_mfma_f32_16x16x32_bf16 v[52:55], v[112:115], v[124:127], v[52:55]\n"
    "s_waitcnt lgkmcnt(5)\n"
    "ds_read_b64_tr_b16 v[104:105], v10 offset:192\n"
    "s_and_b32 s62, s62, 15\n"
    "v_mfma_f32_16x16x32_bf16 v[56:59], v[116:119], v[120:123], v[56:59]\n"
    "ds_read_b64_tr_b16 v[106:107], v10 offset:1984\n"
    "ds_read_b128 v[132:135], v13\n"
    "s_sub_u32 s63, s62, s99\n"
    "v_mfma_f32_16x16x32_bf16 v[60:63], v[116:119], v[124:127], v[60:63]\n"
    "s_waitcnt lgkmcnt(6)\n"
    "ds_read_b64_tr_b16 v[108:109], v10 offset:7168\n"
    "s_mov_b32 s99, s62\n"
    "v_mfma_f32_16x16x32_bf16 v[64:67], v[96:99], v[120:123], v[64:67]\n"
    "ds_read_b64_tr_b16 v[110:111], v10 offset:8960\n"
    "s_mul_i32 s63, s63, 0x80\n"
    "v_mfma_f32_16x16x32_bf16 v[68:71], v[96:99], v[124:127], v[68:71]\n"
    "s_waitcnt lgkmcnt(6)\n"
    "ds_read_b64_tr_b16 v[112:113], v10 offset:7200\n"
    "v_add_u32 v11, s63, v11\n"
    "v_mfma_f32_16x16x32_bf16 v[72:75], v[100:103], v[120:123], v[72:75]\n"
    "ds_read_b64_tr_b16 v[114:115], v10 offset:8992\n"
    "v_add_u32 v12, s63, v12\n"
    "v_mfma_f32_16x16x32_bf16 v[76:79], v[100:103], v[124:127], v[76:79]\n"
    "s_waitcnt lgkmcnt(5)\n"
    "ds_read_b64_tr_b16 v[116:117], v10 offset:7232\n"
    "s_add_u32 m0, s55, 0\n"
    "v_mfma_f32_16x16x32_bf16 v[80:83], v[104:107], v[120:123], v[80:83]\n"
    "ds_read_b64_tr_b16 v[118:119], v10 offset:9024\n"
    "buffer_load_dwordx4 v9, s[28:31], 0 offen sc1 lds\n"
    "v_mfma_f32_16x16x32_bf16 v[84:87], v[104:107], v[124:127], v[84:87]\n"
    "s_waitcnt lgkmcnt(4)\n"
    "ds_read_b64_tr_b16 v[96:97], v10 offset:7264\n"
    "buffer_load_dwordx4 v9, s[28:31], 0 offen offset:1024 sc1 lds\n"
    "v_mfma_f32_16x16x32_bf16 v[32:35], v[108:111], v[128:131], v[32:35]\n"
    "ds_read_b64_tr_b16 v[98:99], v10 offset:9056\n"
    "buffer_load_dwordx4 v9, s[28:31], 0 offen offset:2048 sc1 lds\n"
    "v_mfma_f32_16x16x32_bf16 v[36:39], v[108:111], v[132:135], v[36:39]\n"
    "s_waitcnt lgkmcnt(4)\n"
    "ds_read_b64_tr_b16 v[100:101], v10 offset:7296\n"
    "buffer_load_dwordx4 v9, s[28:31], 0 offen offset:3072 sc1 lds\n"
    "v_mfma_f32_16x16x32_bf16 v[40:43], v[112:115], v[128:131], v[40:43]\n"
    "ds_read_b64_tr_b16 v[102:103], v10 offset:9088\n"
    "s_add_u32 m0, s55, 0x1000\n"
    "v_mfma_f32_16x16x32_bf16 v[44:47], v[112:115], v[132:135], v[44:47]\n"
    "s_waitcnt lgkmcnt(4)\n"
    "ds_read_b64_tr_b16 v[104:105], v10 offset:7328\n"
    "buffer_load_dwordx4 v9, s[28:31], s56 offen sc1 lds\n"
    "v_mfma_f32_16x16x32_bf16 v[48:51], v[116:119], v[128:131], v[48:51]\n"
    "s_waitcnt vmcnt(9)\n"
    "s_barrier\n"
    "v_add_u32 v2, 0xb000, v2\n"
    "v_add_u32 v3, 0xb000, v3\n"
    "v_add_u32 v4, 0xb000, v4\n"
    "v_add_u32 v13, 0xb000, v13\n"
    "ds_read_b64_tr_b16 v[106:107], v10 offset:9120\n"
    "ds_read_b128 v[120:123], v2\n"
    "buffer_load_dwordx4 v9, s[28:31], s56 offen offset:1024 sc1 lds\n"
    "v_mfma_f32_16x16x32_bf16 v[52:55], v[116:119], v[132:135], v[52:55]\n"
    "s_waitcnt lgkmcnt(5)\n"
    "ds_read_b64_tr_b16 v[108:109], v10 offset:7360\n"
    "buffer_load_dwordx4 v9, s[28:31], s56 offen offset:2048 sc1 lds\n"
    "v_mfma_f32_16x16x32_bf16 v[56:59], v[96:99], v[128:131], v[56:59]\n"
    "ds_read_b64_tr_b16 v[110:111], v10 offset:9152\n"
    "ds_read_b128 v[124:127], v4\n"
    "s_add_u32 s62, s98, 1\n"
    "v_mfma_f32_16x16x32_bf16 v[60:63], v[96:99], v[132:135], v[60:63]\n"
    "s_waitcnt lgkmcnt(6)\n"
    "v_add_u32 v10, 0xb000, v10\n"
    "ds_read_b64_tr_b16 v[112:113], v10\n"
    "s_and_b32 s62, s62, 15\n"
    "v_mfma_f32_16x16x32_bf16 v[64:67], v[100:103], v[128:131], v[64:67]\n"
    "ds_read_b64_tr_b16 v[114:115], v10 offset:1792\n"
    "s_sub_u32 s63, s62, s98\n"
    "v_mfma_f32_16x16x32_bf16 v[68:71], v[100:103], v[132:135], v[68:71]\n"
    "s_waitcnt lgkmcnt(6)\n"
    "ds_read_b64_tr_b16 v[116:117], v10 offset:32\n"
    "s_mov_b32 s98, s62\n"
    "v_mfma_f32_16x16x32_bf16 v[72:75], v[104:107], v[128:131], v[72:75]\n"
    "ds_read_b64_tr_b16 v[118:119], v10 offset:1824\n"
    "s_mul_i32 s63, s63, 0x3800\n"
    "v_mfma_f32_16x16x32_bf16 v[76:79], v[104:107], v[132:135], v[76:79]\n"
    "s_waitcnt lgkmcnt(5)\n"
    "ds_read_b64_tr_b16 v[96:97], v10 offset:64\n"
    "v_add_u32 v9, s63, v9\n"
    "v_mfma_f32_16x16x32_bf16 v[80:83], v[108:111], v[128:131], v[80:83]\n"
    "ds_read_b64_tr_b16 v[98:99], v10 offset:1856\n"
    "v_mfma_f32_16x16x32_bf16 v[84:87], v[108:111], v[132:135], v[84:87]\n"
    "s_waitcnt lgkmcnt(4)\n"
    "s_barrier\n"
    "ds_read_b64_tr_b16 v[100:101], v10 offset:96\n"
    "s_add_u32 m0, s57, 0xb000\n"
    "v_mfma_f32_16x16x32_bf16 v[32:35], v[112:115], v[120:123], v[32:35]\n"
    "ds_read_b64_tr_b16 v[102:103], v10 offset:1888\n"
    "buffer_load_dwordx4 v11, s[12:15], 0 offen lds\n"
    "v_mfma_f32_16x16x32_bf16 v[36:39], v[112:115], v[124:127], v[36:39]\n"
    "s_waitcnt lgkmcnt(4)\n"
    "ds_read_b64_tr_b16 v[104:105], v10 offset:128\n"
    "buffer_load_dwordx4 v12, s[12:15], s53 offen offset:1024 lds\n"
    "v_mfma_f32_16x16x32_bf16 v[40:43], v[116:119], v[120:123], v[40:43]\n"
    "ds_read_b64_tr_b16 v[106:107], v10 offset:1920\n"
    "buffer_load_dwordx4 v11, s[12:15], s54 offen offset:2048 lds\n"
    "v_mfma_f32_16x16x32_bf16 v[44:47], v[116:119], v[124:127], v[44:47]\n"
    "s_waitcnt lgkmcnt(4)\n"
    "ds_read_b64_tr_b16 v[108:109], v10 offset:160\n"
    "buffer_load_dwordx4 v12, s[12:15], s97 offen offset:3072 lds\n"
    "v_mfma_f32_16x16x32_bf16 v[48:51], v[96:99], v[120:123], v[48:51]\n"
    "ds_read_b64_tr_b16 v[110:111], v10 offset:1952\n"
    "ds_read_b128 v[128:131], v3\n"
    "s_add_u32 s62, s99, 1\n"
    "v_mfma_f32_16x16x32_bf16 v[52:55], v[96:99], v[124:127], v[52:55]\n"
    "s_waitcnt lgkmcnt(5)\n"
    "ds_read_b64_tr_b16 v[112:113], v10 offset:192\n"
    "s_and_b32 s62, s62, 15\n"
    "v_mfma_f32_16x16x32_bf16 v[56:59], v[100:103], v[120:123], v[56:59]\n"
    "ds_read_b64_tr_b16 v[114:115], v10 offset:1984\n"
    "ds_read_b128 v[132:135], v13\n"
    "s_sub_u32 s63, s62, s99\n"
    "v_mfma_f32_16x16x32_bf16 v[60:63], v[100:103], v[124:127], v[60:63]\n"
    "s_waitcnt lgkmcnt(6)\n"
    "ds_read_b64_tr_b16 v[116:117], v10 offset:7168\n"
    "s_mov_b32 s99, s62\n"
    "v_mfma_f32_16x16x32_bf16 v[64:67], v[104:107], v[120:123], v[64:67]\n"
    "ds_read_b64_tr_b16 v[118:119], v10 offset:8960\n"
    "s_mul_i32 s63, s63, 0x80\n"
    "v_mfma_f32_16x16x32_bf16 v[68:71], v[104:107], v[124:127], v[68:71]\n"
    "s_waitcnt lgkmcnt(6)\n"
    "ds_read_b64_tr_b16 v[96:97], v10 offset:7200\n"
    "v_add_u32 v11, s63, v11\n"
    "v_mfma_f32_16x16x32_bf16 v[72:75], v[108:111], v[120:123], v[72:75]\n"
    "ds_read_b64_tr_b16 v[98:99], v10 offset:8992\n"
    "v_add_u32 v12, s63, v12\n"
    "v_mfma_f32_16x16x32_bf16 v[76:79], v[108:111], v[124:127], v[76:79]\n"
    "s_waitcnt lgkmcnt(5)\n"
    "ds_read_b64_tr_b16 v[100:101], v10 offset:7232\n"
    "s_add_u32 m0, s55, 0xb000\n"
    "v_mfma_f32_16x16x32_bf16 v[80:83], v[112:115], v[120:123], v[80:83]\n"
    "ds_read_b64_tr_b16 v[102:103], v10 offset:9024\n"
    "buffer_load_dwordx4 v9, s[28:31], 0 offen sc1 lds\n"
    "v_mfma_f32_16x16x32_bf16 v[84:87], v[112:115], v[124:127], v[84:87]\n"
    "s_waitcnt lgkmcnt(4)\n"
    "ds_read_b64_tr_b16 v[104:105], v10 offset:7264\n"
    "buffer_load_dwordx4 v9, s[28:31], 0 offen offset:1024 sc1 lds\n"
    "v_mfma_f32_16x16x32_bf16 v[32:35], v[116:119], v[128:131], v[32:35]\n"
    "ds_read_b64_tr_b16 v[106:107], v10 offset:9056\n"
    "buffer_load_dwordx4 v9, s[28:31], 0 offen offset:2048 sc1 lds\n"
    "v_mfma_f32_16x16x32_bf16 v[36:39], v[116:119], v[132:135], v[36:39]\n"
    "s_waitcnt lgkmcnt(4)\n"
    "ds_read_b64_tr_b16 v[108:109], v10 offset:7296\n"
    "buffer_load_dwordx4 v9, s[28:31], 0 offen offset:3072 sc1 lds\n"
    "v_mfma_f32_16x16x32_bf16 v[40:43], v[96:99], v[128:131], v[40:43]\n"
    "ds_read_b64_tr_b16 v[110:111], v10 offset:9088\n"
    "s_add_u32 m0, s55, 0xc000\n"
    "v_mfma_f32_16x16x32_bf16 v[44:47], v[96:99], v[132:135], v[44:47]\n"
    "s_waitcnt lgkmcnt(4)\n"
    "ds_read_b64_tr_b16 v[112:113], v10 offset:7328\n"
    "buffer_load_dwordx4 v9, s[28:31], s56 offen sc1 lds\n"
    "v_mfma_f32_16x16x32_bf16 v[48:51], v[100:103], v[128:131], v[48:51]\n"
    "s_waitcnt vmcnt(9)\n"
    "s_barrier\n"
    "v_add_u32 v2, 0xfffea000, v2\n"
    "v_add_u32 v3, 0xfffea000, v3\n"
    "v_add_u32 v4, 0xfffea000, v4\n"
    "v_add_u32 v13, 0xfffea000, v13\n"
    "ds_read_b64_tr_b16 v[114:115], v10 offset:9120\n"
    "ds_read_b128 v[120:123], v2\n"
    "buffer_load_dwordx4 v9, s[28:31], s56 offen offset:1024 sc1 lds\n"
    "v_mfma_f32_16x16x32_bf16 v[52:55], v[100:103], v[132:135], v[52:55]\n"
    "s_waitcnt lgkmcnt(5)\n"
    "ds_read_b64_tr_b16 v[116:117], v10 offset:7360\n"
    "buffer_load_dwordx4 v9, s[28:31], s56 offen offset:2048 sc1 lds\n"
    "v_mfma_f32_16x16x32_bf16 v[56:59], v[104:107], v[128:131], v[56:59]\n"
    "ds_read_b64_tr_b16 v[118:119], v10 offset:9152\n"
    "ds_read_b128 v[124:127], v4\n"
    "s_add_u32 s62, s98, 1\n"
    "v_mfma_f32_16x16x32_bf16 v[60:63], v[104:107], v[132:135], v[60:63]\n"
    "s_waitcnt lgkmcnt(6)\n"
    "v_add_u32 v10, 0xfffea000, v10\n"
    "ds_read_b64_tr_b16 v[96:97], v10\n"
    "s_and_b32 s62, s62, 15\n"
    "v_mfma_f32_16x16x32_bf16 v[64:67], v[108:111], v[128:131], v[64:67]\n"
    "ds_read_b64_tr_b16 v[98:99], v10 offset:1792\n"
    "s_sub_u32 s63, s62, s98\n"
    "v_mfma_f32_16x16x32_bf16 v[68:71], v[108:111], v[132:135], v[68:71]\n"
    "s_waitcnt lgkmcnt(6)\n"
    "ds_read_b64_tr_b16 v[100:101], v10 offset:32\n"
    "s_mov_b32 s98, s62\n"
    "v_mfma_f32_16x16x32_bf16 v[72:75], v[112:115], v[128:131], v[72:75]\n"
    "ds_read_b64_tr_b16 v[102:103], v10 offset:1824\n"
    "s_mul_i32 s63, s63, 0x3800\n"
    "v_mfma_f32_16x16x32_bf16 v[76:79], v[112:115], v[132:135], v[76:79]\n"
    "s_waitcnt lgkmcnt(5)\n"
    "ds_read_b64_tr_b16 v[104:105], v10 offset:64\n"
    "v_add_u32 v9, s63, v9\n"
    "v_mfma_f32_16x16x32_bf16 v[80:83], v[116:119], v[128:131], v[80:83]\n"
    "ds_read_b64_tr_b16 v[106:107], v10 offset:1856\n"
    "v_mfma_f32_16x16x32_bf16 v[84:87], v[116:119], v[132:135], v[84:87]\n"
    "s_waitcnt lgkmcnt(4)\n"
    "s_barrier\n"
    "ds_read_b64_tr_b16 v[108:109], v10 offset:96\n"
    "s_add_u32 m0, s57, 0x16000\n"
    "v_mfma_f32_16x16x32_bf16 v[32:35], v[96:99], v[120:123], v[32:35]\n"
    "ds_read_b64_tr_b16 v[110:111], v10 offset:1888\n"
    "buffer_load_dwordx4 v11, s[12:15], 0 offen lds\n"
    "v_mfma_f32_16x16x32_bf16 v[36:39], v[96:99], v[124:127], v[36:39]\n"
    "s_waitcnt lgkmcnt(4)\n"
    "ds_read_b64_tr_b16 v[112:113], v10 offset:128\n"
    "buffer_load_dwordx4 v12, s[12:15], s53 offen offset:1024 lds\n"
    "v_mfma_f32_16x16x32_bf16 v[40:43], v[100:103], v[120:123], v[40:43]\n"
    "ds_read_b64_tr_b16 v[114:115], v10 offset:1920\n"
    "buffer_load_dwordx4 v11, s[12:15], s54 offen offset:2048 lds\n"
    "v_mfma_f32_16x16x32_bf16 v[44:47], v[100:103], v[124:127], v[44:47]\n"
    "s_waitcnt lgkmcnt(4)\n"
    "ds_read_b64_tr_b16 v[116:117], v10 offset:160\n"
    "buffer_load_dwordx4 v12, s[12:15], s97 offen offset:3072 lds\n"
    "v_mfma_f32_16x16x32_bf16 v[48:51], v[104:107], v[120:123], v[48:51]\n"
    "ds_read_b64_tr_b16 v[118:119], v10 offset:1952\n"
    "ds_read_b128 v[128:131], v3\n"
    "s_add_u32 s62, s99, 1\n"
    "v_mfma_f32_16x16x32_bf16 v[52:55], v[104:107], v[124:127], v[52:55]\n"
    "s_waitcnt lgkmcnt(5)\n"
    "ds_read_b64_tr_b16 v[96:97], v10 offset:192\n"
    "s_and_b32 s62, s62, 15\n"
    "v_mfma_f32_16x16x32_bf16 v[56:59], v[108:111], v[120:123], v[56:59]\n"
    "ds_read_b64_tr_b16 v[98:99], v10 offset:1984\n"
    "ds_read_b128 v[132:135], v13\n"
    "s_sub_u32 s63, s62, s99\n"
    "v_mfma_f32_16x16x32_bf16 v[60:63], v[108:111], v[124:127], v[60:63]\n"
    "s_waitcnt lgkmcnt(6)\n"
    "ds_read_b64_tr_b16 v[100:101], v10 offset:7168\n"
    "s_mov_b32 s99, s62\n"
    "v_mfma_f32_16x16x32_bf16 v[64:67], v[112:115], v[120:123], v[64:67]\n"
    "ds_read_b64_tr_b16 v[102:103], v10 offset:8960\n"
    "s_mul_i32 s63, s63, 0x80\n"
    "v_mfma_f32_16x16x32_bf16 v[68:71], v[112:115], v[124:127], v[68:71]\n"
    "s_waitcnt lgkmcnt(6)\n"
    "ds_read_b64_tr_b16 v[104:105], v10 offset:7200\n"
    "v_add_u32 v11, s63, v11\n"
    "v_mfma_f32_16x16x32_bf16 v[72:75], v[116:119], v[120:123], v[72:75]\n"
    "ds_read_b64_tr_b16 v[106:107], v10 offset:8992\n"
    "v_add_u32 v12, s63, v12\n"
    "v_mfma_f32_16x16x32_bf16 v[76:79], v[116:119], v[124:127], v[76:79]\n"
    "s_waitcnt lgkmcnt(5)\n"
    "ds_read_b64_tr_b16 v[108:109], v10 offset:7232\n"
    "s_add_u32 m0, s55, 0x16000\n"
    "v_mfma_f32_16x16x32_bf16 v[80:83], v[96:99], v[120:123], v[80:83]\n"
    "ds_read_b64_tr_b16 v[110:111], v10 offset:9024\n"
    "buffer_load_dwordx4 v9, s[28:31], 0 offen sc1 lds\n"
    "v_mfma_f32_16x16x32_bf16 v[84:87], v[96:99], v[124:127], v[84:87]\n"
    "s_waitcnt lgkmcnt(4)\n"
    "ds_read_b64_tr_b16 v[112:113], v10 offset:7264\n"
    "buffer_load_dwordx4 v9, s[28:31], 0 offen offset:1024 sc1 lds\n"
    "v_mfma_f32_16x16x32_bf16 v[32:35], v[100:103], v[128:131], v[32:35]\n"
    "ds_read_b64_tr_b16 v[114:115], v10 offset:9056\n"
    "buffer_load_dwordx4 v9, s[28:31], 0 offen offset:2048 sc1 lds\n"
    "v_mfma_f32_16x16x32_bf16 v[36:39], v[100:103], v[132:135], v[36:39]\n"
    "s_waitcnt lgkmcnt(4)\n"
    "ds_read_b64_tr_b16 v[116:117], v10 offset:7296\n"
    "buffer_load_dwordx4 v9, s[28:31], 0 offen offset:3072 sc1 lds\n"
    "v_mfma_f32_16x16x32_bf16 v[40:43], v[104:107], v[128:131], v[40:43]\n"
    "ds_read_b64_tr_b16 v[118:119], v10 offset:9088\n"
    "s_add_u32 m0, s55, 0x17000\n"
    "v_mfma_f32_16x16x32_bf16 v[44:47], v[104:107], v[132:135], v[44:47]\n"
    "s_waitcnt lgkmcnt(4)\n"
    "ds_read_b64_tr_b16 v[96:97], v10 offset:7328\n"
    "buffer_load_dwordx4 v9, s[28:31], s56 offen sc1 lds\n"
    "v_mfma_f32_16x16x32_bf16 v[48:51], v[108:111], v[128:131], v[48:51]\n"
    "s_waitcnt vmcnt(9)\n"
    "s_barrier\n"
    "v_add_u32 v2, 0xb000, v2\n"
    "v_add_u32 v3, 0xb000, v3\n"
    "v_add_u32 v4, 0xb000, v4\n"
    "v_add_u32 v13, 0xb000, v13\n"
    "ds_read_b64_tr_b16 v[98:99], v10 offset:9120\n"
    "ds_read_b128 v[120:123], v2\n"
    "buffer_load_dwordx4 v9, s[28:31], s56 offen offset:1024 sc1 lds\n"
    "v_mfma_f32_16x16x32_bf16 v[52:55], v[108:111], v[132:135], v[52:55]\n"
    "s_waitcnt lgkmcnt(5)\n"
    "ds_read_b64_tr_b16 v[100:101], v10 offset:7360\n"
    "buffer_load_dwordx4 v9, s[28:31], s56 offen offset:2048 sc1 lds\n"
    "v_mfma_f32_16x16x32_bf16 v[56:59], v[112:115], v[128:131], v[56:59]\n"
    "ds_read_b64_tr_b16 v[102:103], v10 offset:9152\n"
    "ds_read_b128 v[124:127], v4\n"
    "s_add_u32 s62, s98, 1\n"
    "v_mfma_f32_16x16x32_bf16 v[60:63], v[112:115], v[132:135], v[60:63]\n"
    "s_waitcnt lgkmcnt(6)\n"
    "v_add_u32 v10, 0xb000, v10\n"
    "ds_read_b64_tr_b16 v[104:105], v10\n"
    "s_and_b32 s62, s62, 15\n"
    "v_mfma_f32_16x16x32_bf16 v[64:67], v[116:119], v[128:131], v[64:67]\n"
    "ds_read_b64_tr_b16 v[106:107], v10 offset:1792\n"
    "s_sub_u32 s63, s62, s98\n"
    "v_mfma_f32_16x16x32_bf16 v[68:71], v[116:119], v[132:135], v[68:71]\n"
    "s_waitcnt lgkmcnt(6)\n"
    "ds_read_b64_tr_b16 v[108:109], v10 offset:32\n"
    "s_mov_b32 s98, s62\n"
    "v_mfma_f32_16x16x32_bf16 v[72:75], v[96:99], v[128:131], v[72:75]\n"
    "ds_read_b64_tr_b16 v[110:111], v10 offset:1824\n"
    "s_mul_i32 s63, s63, 0x3800\n"
    "v_mfma_f32_16x16x32_bf16 v[76:79], v[96:99], v[132:135], v[76:79]\n"
    "s_waitcnt lgkmcnt(5)\n"
    "ds_read_b64_tr_b16 v[112:113], v10 offset:64\n"
    "v_add_u32 v9, s63, v9\n"
    "v_mfma_f32_16x16x32_bf16 v[80:83], v[100:103], v[128:131], v[80:83]\n"
    "ds_read_b64_tr_b16 v[114:115], v10 offset:1856\n"
    "v_mfma_f32_16x16x32_bf16 v[84:87], v[100:103], v[132:135], v[84:87]\n"
    "s_waitcnt lgkmcnt(4)\n"
    "s_barrier\n"
    "ds_read_b64_tr_b16 v[116:117], v10 offset:96\n"
    "s_add_u32 m0, s57, 0\n"
    "v_mfma_f32_16x16x32_bf16 v[32:35], v[104:107], v[120:123], v[32:35]\n"
    "ds_read_b64_tr_b16 v[118:119], v10 offset:1888\n"
    "buffer_load_dwordx4 v11, s[12:15], 0 offen lds\n"
    "v_mfma_f32_16x16x32_bf16 v[36:39], v[104:107], v[124:127], v[36:39]\n"
    "s_waitcnt lgkmcnt(4)\n"
    "ds_read_b64_tr_b16 v[96:97], v10 offset:128\n"
    "buffer_load_dwordx4 v12, s[12:15], s53 offen offset:1024 lds\n"
    "v_mfma_f32_16x16x32_bf16 v[40:43], v[108:111], v[120:123], v[40:43]\n"
    "ds_read_b64_tr_b16 v[98:99], v10 offset:1920\n"
    "buffer_load_dwordx4 v11, s[12:15], s54 offen offset:2048 lds\n"
    "v_mfma_f32_16x16x32_bf16 v[44:47], v[108:111], v[124:127], v[44:47]\n"
    "s_waitcnt lgkmcnt(4)\n"
    "ds_read_b64_tr_b16 v[100:101], v10 offset:160\n"
    "buffer_load_dwordx4 v12, s[12:15], s97 offen offset:3072 lds\n"
    "v_mfma_f32_16x16x32_bf16 v[48:51], v[112:115], v[120:123], v[48:51]\n"
    "ds_read_b64_tr_b16 v[102:103], v10 offset:1952\n"
    "ds_read_b128 v[128:131], v3\n"
    "s_add_u32 s62, s99, 1\n"
    "v_mfma_f32_16x16x32_bf16 v[52:55], v[112:115], v[124:127], v[52:55]\n"
    "s_waitcnt lgkmcnt(5)\n"
    "ds_read_b64_tr_b16 v[104:105], v10 offset:192\n"
    "s_and_b32 s62, s62, 15\n"
    "v_mfma_f32_16x16x32_bf16 v[56:59], v[116:119], v[120:123], v[56:59]\n"
    "ds_read_b64_tr_b16 v[106:107], v10 offset:1984\n"
    "ds_read_b128 v[132:135], v13\n"
    "s_sub_u32 s63, s62, s99\n"
    "v_mfma_f32_16x16x32_bf16 v[60:63], v[116:119], v[124:127], v[60:63]\n"
    "s_waitcnt lgkmcnt(6)\n"
    "ds_read_b64_tr_b16 v[108:109], v10 offset:7168\n"
    "s_mov_b32 s99, s62\n"
    "v_mfma_f32_16x16x32_bf16 v[64:67], v[96:99], v[120:123], v[64:67]\n"
    "ds_read_b64_tr_b16 v[110:111], v10 offset:8960\n"
    "s_mul_i32 s63, s63, 0x80\n"
    "v_mfma_f32_16x16x32_bf16 v[68:71], v[96:99], v[124:127], v[68:71]\n"
    "s_waitcnt lgkmcnt(6)\n"
    "ds_read_b64_tr_b16 v[112:113], v10 offset:7200\n"
    "v_add_u32 v11, s63, v11\n"
    "v_mfma_f32_16x16x32_bf16 v[72:75], v[100:103], v[120:123], v[72:75]\n"
    "ds_read_b64_tr_b16 v[114:115], v10 offset:8992\n"
    "v_add_u32 v12, s63, v12\n"
    "v_mfma_f32_16x16x32_bf16 v[76:79], v[100:103], v[124:127], v[76:79]\n"
    "s_waitcnt lgkmcnt(5)\n"
    "ds_read_b64_tr_b16 v[116:117], v10 offset:7232\n"
    "s_add_u32 m0, s55, 0\n"
    "v_mfma_f32_16x16x32_bf16 v[80:83], v[104:107], v[120:123], v[80:83]\n"
    "ds_read_b64_tr_b16 v[118:119], v10 offset:9024\n"
    "buffer_load_dwordx4 v9, s[28:31], 0 offen sc1 lds\n"
    "v_mfma_f32_16x16x32_bf16 v[84:87], v[104:107], v[124:127], v[84:87]\n"
    "s_waitcnt lgkmcnt(4)\n"
    "ds_read_b64_tr_b16 v[96:97], v10 offset:7264\n"
    "buffer_load_dwordx4 v9, s[28:31], 0 offen offset:1024 sc1 lds\n"
    "v_mfma_f32_16x16x32_bf16 v[32:35], v[108:111], v[128:131], v[32:35]\n"
    "ds_read_b64_tr_b16 v[98:99], v10 offset:9056\n"
    "buffer_load_dwordx4 v9, s[28:31], 0 offen offset:2048 sc1 lds\n"
    "v_mfma_f32_16x16x32_bf16 v[36:39], v[108:111], v[132:135], v[36:39]\n"
    "s_waitcnt lgkmcnt(4)\n"
    "ds_read_b64_tr_b16 v[100:101], v10 offset:7296\n"
    "buffer_load_dwordx4 v9, s[28:31], 0 offen offset:3072 sc1 lds\n"
    "v_mfma_f32_16x16x32_bf16 v[40:43], v[112:115], v[128:131], v[40:43]\n"
    "ds_read_b64_tr_b16 v[102:103], v10 offset:9088\n"
    "s_add_u32 m0, s55, 0x1000\n"
    "v_mfma_f32_16x16x32_bf16 v[44:47], v[112:115], v[132:135], v[44:47]\n"
    "s_waitcnt lgkmcnt(4)\n"
    "ds_read_b64_tr_b16 v[104:105], v10 offset:7328\n"
    "buffer_load_dwordx4 v9, s[28:31], s56 offen sc1 lds\n"
    "v_mfma_f32_16x16x32_bf16 v[48:51], v[116:119], v[128:131], v[48:51]\n"
    "s_waitcnt vmcnt(9)\n"
    "s_barrier\n"
    "v_add_u32 v2, 0xb000, v2\n"
    "v_add_u32 v3, 0xb000, v3\n"
    "v_add_u32 v4, 0xb000, v4\n"
    "v_add_u32 v13, 0xb000, v13\n"
    "ds_read_b64_tr_b16 v[106:107], v10 offset:9120\n"
    "ds_read_b128 v[120:123], v2\n"
    "buffer_load_dwordx4 v9, s[28:31], s56 offen offset:1024 sc1 lds\n"
    "v_mfma_f32_16x16x32_bf16 v[52:55], v[116:119], v[132:135], v[52:55]\n"
    "s_waitcnt lgkmcnt(5)\n"
    "ds_read_b64_tr_b16 v[108:109], v10 offset:7360\n"
    "buffer_load_dwordx4 v9, s[28:31], s56 offen offset:2048 sc1 lds\n"
    "v_mfma_f32_16x16x32_bf16 v[56:59], v[96:99], v[128:131], v[56:59]\n"
    "ds_read_b64_tr_b16 v[110:111], v10 offset:9152\n"
    "ds_read_b128 v[124:127], v4\n"
    "s_add_u32 s62, s98, 1\n"
    "v_mfma_f32_16x16x32_bf16 v[60:63], v[96:99], v[132:135], v[60:63]\n"
    "s_waitcnt lgkmcnt(6)\n"
    "v_add_u32 v10, 0xb000, v10\n"
    "ds_read_b64_tr_b16 v[112:113], v10\n"
    "s_and_b32 s62, s62, 15\n"
    "v_mfma_f32_16x16x32_bf16 v[64:67], v[100:103], v[128:131], v[64:67]\n"
    "ds_read_b64_tr_b16 v[114:115], v10 offset:1792\n"
    "s_sub_u32 s63, s62, s98\n"
    "v_mfma_f32_16x16x32_bf16 v[68:71], v[100:103], v[132:135], v[68:71]\n"
    "s_waitcnt lgkmcnt(6)\n"
    "ds_read_b64_tr_b16 v[116:117], v10 offset:32\n"
    "s_mov_b32 s98, s62\n"
    "v_mfma_f32_16x16x32_bf16 v[72:75], v[104:107], v[128:131], v[72:75]\n"
    "ds_read_b64_tr_b16 v[118:119], v10 offset:1824\n"
    "s_mul_i32 s63, s63, 0x3800\n"
    "v_mfma_f32_16x16x32_bf16 v[76:79], v[104:107], v[132:135], v[76:79]\n"
    "s_waitcnt lgkmcnt(5)\n"
    "ds_read_b64_tr_b16 v[96:97], v10 offset:64\n"
    "v_add_u32 v9, s63, v9\n"
    "v_mfma_f32_16x16x32_bf16 v[80:83], v[108:111], v[128:131], v[80:83]\n"
    "ds_read_b64_tr_b16 v[98:99], v10 offset:1856\n"
    "v_mfma_f32_16x16x32_bf16 v[84:87], v[108:111], v[132:135], v[84:87]\n"
    "s_waitcnt lgkmcnt(4)\n"
    "s_barrier\n"
    "ds_read_b64_tr_b16 v[100:101], v10 offset:96\n"
    "s_add_u32 m0, s57, 0xb000\n"
    "v_mfma_f32_16x16x32_bf16 v[32:35], v[112:115], v[120:123], v[32:35]\n"
    "ds_read_b64_tr_b16 v[102:103], v10 offset:1888\n"
    "buffer_load_dwordx4 v11, s[12:15], 0 offen lds\n"
    "v_mfma_f32_16x16x32_bf16 v[36:39], v[112:115], v[124:127], v[36:39]\n"
    "s_waitcnt lgkmcnt(4)\n"
    "ds_read_b64_tr_b16 v[104:105], v10 offset:128\n"
    "buffer_load_dwordx4 v12, s[12:15], s53 offen offset:1024 lds\n"
    "v_mfma_f32_16x16x32_bf16 v[40:43], v[116:119], v[120:123], v[40:43]\n"
    "ds_read_b64_tr_b16 v[106:107], v10 offset:1920\n"
    "buffer_load_dwordx4 v11, s[12:15], s54 offen offset:2048 lds\n"
    "v_mfma_f32_16x16x32_bf16 v[44:47], v[116:119], v[124:127], v[44:47]\n"
    "s_waitcnt lgkmcnt(4)\n"
    "ds_read_b64_tr_b16 v[108:109], v10 offset:160\n"
    "buffer_load_dwordx4 v12, s[12:15], s97 offen offset:3072 lds\n"
    "v_mfma_f32_16x16x32_bf16 v[48:51], v[96:99], v[120:123], v[48:51]\n"
    "ds_read_b64_tr_b16 v[110:111], v10 offset:1952\n"
    "ds_read_b128 v[128:131], v3\n"
    "s_add_u32 s62, s99, 1\n"
    "v_mfma_f32_16x16x32_bf16 v[52:55], v[96:99], v[124:127], v[52:55]\n"
    "s_waitcnt lgkmcnt(5)\n"
    "ds_read_b64_tr_b16 v[112:113], v10 offset:192\n"
    "s_and_b32 s62, s62, 15\n"
    "v_mfma_f32_16x16x32_bf16 v[56:59], v[100:103], v[120:123], v[56:59]\n"
    "ds_read_b64_tr_b16 v[114:115], v10 offset:1984\n"
    "ds_read_b128 v[132:135], v13\n"
    "s_sub_u32 s63, s62, s99\n"
    "v_mfma_f32_16x16x32_bf16 v[60:63], v[100:103], v[124:127], v[60:63]\n"
    "s_waitcnt lgkmcnt(6)\n"
    "ds_read_b64_tr_b16 v[116:117], v10 offset:7168\n"
    "s_mov_b32 s99, s62\n"
    "v_mfma_f32_16x16x32_bf16 v[64:67], v[104:107], v[120:123], v[64:67]\n"
    "ds_read_b64_tr_b16 v[118:119], v10 offset:8960\n"
    "s_mul_i32 s63, s63, 0x80\n"
    "v_mfma_f32_16x16x32_bf16 v[68:71], v[104:107], v[124:127], v[68:71]\n"
    "s_waitcnt lgkmcnt(6)\n"
    "ds_read_b64_tr_b16 v[96:97], v10 offset:7200\n"
    "v_add_u32 v11, s63, v11\n"
    "v_mfma_f32_16x16x32_bf16 v[72:75], v[108:111], v[120:123], v[72:75]\n"
    "ds_read_b64_tr_b16 v[98:99], v10 offset:8992\n"
    "v_add_u32 v12, s63, v12\n"
    "v_mfma_f32_16x16x32_bf16 v[76:79], v[108:111], v[124:127], v[76:79]\n"
    "s_waitcnt lgkmcnt(5)\n"
    "ds_read_b64_tr_b16 v[100:101], v10 offset:7232\n"
    "s_add_u32 m0, s55, 0xb000\n"
    "v_mfma_f32_16x16x32_bf16 v[80:83], v[112:115], v[120:123], v[80:83]\n"
    "ds_read_b64_tr_b16 v[102:103], v10 offset:9024\n"
    "buffer_load_dwordx4 v9, s[28:31], 0 offen sc1 lds\n"
    "v_mfma_f32_16x16x32_bf16 v[84:87], v[112:115], v[124:127], v[84:87]\n"
    "s_waitcnt lgkmcnt(4)\n"
    "ds_read_b64_tr_b16 v[104:105], v10 offset:7264\n"
    "buffer_load_dwordx4 v9, s[28:31], 0 offen offset:1024 sc1 lds\n"
    "v_mfma_f32_16x16x32_bf16 v[32:35], v[116:119], v[128:131], v[32:35]\n"
    "ds_read_b64_tr_b16 v[106:107], v10 offset:9056\n"
    "buffer_load_dwordx4 v9, s[28:31], 0 offen offset:2048 sc1 lds\n"
    "v_mfma_f32_16x16x32_bf16 v[36:39], v[116:119], v[132:135], v[36:39]\n"
    "s_waitcnt lgkmcnt(4)\n"
    "ds_read_b64_tr_b16 v[108:109], v10 offset:7296\n"
    "buffer_load_dwordx4 v9, s[28:31], 0 offen offset:3072 sc1 lds\n"
    "v_mfma_f32_16x16x32_bf16 v[40:43], v[96:99], v[128:131], v[40:43]\n"
    "ds_read_b64_tr_b16 v[110:111], v10 offset:9088\n"
    "s_add_u32 m0, s55, 0xc000\n"
    "v_mfma_f32_16x16x32_bf16 v[44:47], v[96:99], v[132:135], v[44:47]\n"
    "s_waitcnt lgkmcnt(4)\n"
    "ds_read_b64_tr_b16 v[112:113], v10 offset:7328\n"
    "buffer_load_dwordx4 v9, s[28:31], s56 offen sc1 lds\n"
    "v_mfma_f32_16x16x32_bf16 v[48:51], v[100:103], v[128:131], v[48:51]\n"
    "s_waitcnt vmcnt(9)\n"
    "s_barrier\n"
    "v_add_u32 v2, 0xfffea000, v2\n"
    "v_add_u32 v3, 0xfffea000, v3\n"
    "v_add_u32 v4, 0xfffea000, v4\n"
    "v_add_u32 v13, 0xfffea000, v13\n"
    "ds_read_b64_tr_b16 v[114:115], v10 offset:9120\n"
    "ds_read_b128 v[120:123], v2\n"
    "buffer_load_dwordx4 v9, s[28:31], s56 offen offset:1024 sc1 lds\n"
    "v_mfma_f32_16x16x32_bf16 v[52:55], v[100:103], v[132:135], v[52:55]\n"
    "s_waitcnt lgkmcnt(5)\n"
    "ds_read_b64_tr_b16 v[116:117], v10 offset:7360\n"
    "buffer_load_dwordx4 v9, s[28:31], s56 offen offset:2048 sc1 lds\n"
    "v_mfma_f32_16x16x32_bf16 v[56:59], v[104:107], v[128:131], v[56:59]\n"
    "ds_read_b64_tr_b16 v[118:119], v10 offset:9152\n"
    "ds_read_b128 v[124:127], v4\n"
    "s_add_u32 s62, s98, 1\n"
    "v_mfma_f32_16x16x32_bf16 v[60:63], v[104:107], v[132:135], v[60:63]\n"
    "s_waitcnt lgkmcnt(6)\n"
    "v_add_u32 v10, 0xfffea000, v10\n"
    "ds_read_b64_tr_b16 v[96:97], v10\n"
    "s_and_b32 s62, s62, 15\n"
    "v_mfma_f32_16x16x32_bf16 v[64:67], v[108:111], v[128:131], v[64:67]\n"
    "ds_read_b64_tr_b16 v[98:99], v10 offset:1792\n"
    "s_sub_u32 s63, s62, s98\n"
    "v_mfma_f32_16x16x32_bf16 v[68:71], v[108:111], v[132:135], v[68:71]\n"
    "s_waitcnt lgkmcnt(6)\n"
    "ds_read_b64_tr_b16 v[100:101], v10 offset:32\n"
    "s_mov_b32 s98, s62\n"
    "v_mfma_f32_16x16x32_bf16 v[72:75], v[112:115], v[128:131], v[72:75]\n"
    "ds_read_b64_tr_b16 v[102:103], v10 offset:1824\n"
    "s_mul_i32 s63, s63, 0x3800\n"
    "v_mfma_f32_16x16x32_bf16 v[76:79], v[112:115], v[132:135], v[76:79]\n"
    "s_waitcnt lgkmcnt(5)\n"
    "ds_read_b64_tr_b16 v[104:105], v10 offset:64\n"
    "v_add_u32 v9, s63, v9\n"
    "v_mfma_f32_16x16x32_bf16 v[80:83], v[116:119], v[128:131], v[80:83]\n"
    "ds_read_b64_tr_b16 v[106:107], v10 offset:1856\n"
    "v_mfma_f32_16x16x32_bf16 v[84:87], v[116:119], v[132:135], v[84:87]\n"
    "s_waitcnt lgkmcnt(4)\n"
    "s_barrier\n"
    "ds_read_b64_tr_b16 v[108:109], v10 offset:96\n"
    "s_add_u32 m0, s57, 0x16000\n"
    "v_mfma_f32_16x16x32_bf16 v[32:35], v[96:99], v[120:123], v[32:35]\n"
    "ds_read_b64_tr_b16 v[110:111], v10 offset:1888\n"
    "buffer_load_dwordx4 v11, s[12:15], 0 offen lds\n"
    "v_mfma_f32_16x16x32_bf16 v[36:39], v[96:99], v[124:127], v[36:39]\n"
    "s_waitcnt lgkmcnt(4)\n"
    "ds_read_b64_tr_b16 v[112:113], v10 offset:128\n"
    "buffer_load_dwordx4 v12, s[12:15], s53 offen offset:1024 lds\n"
    "v_mfma_f32_16x16x32_bf16 v[40:43], v[100:103], v[120:123], v[40:43]\n"
    "ds_read_b64_tr_b16 v[114:115], v10 offset:1920\n"
    "buffer_load_dwordx4 v11, s[12:15], s54 offen offset:2048 lds\n"
    "v_mfma_f32_16x16x32_bf16 v[44:47], v[100:103], v[124:127], v[44:47]\n"
    "s_waitcnt lgkmcnt(4)\n"
    "ds_read_b64_tr_b16 v[116:117], v10 offset:160\n"
    "buffer_load_dwordx4 v12, s[12:15], s97 offen offset:3072 lds\n"
    "v_mfma_f32_16x16x32_bf16 v[48:51], v[104:107], v[120:123], v[48:51]\n"
    "ds_read_b64_tr_b16 v[118:119], v10 offset:1952\n"
    "ds_read_b128 v[128:131], v3\n"
    "s_add_u32 s62, s99, 1\n"
    "v_mfma_f32_16x16x32_bf16 v[52:55], v[104:107], v[124:127], v[52:55]\n"
    "s_waitcnt lgkmcnt(5)\n"
    "ds_read_b64_tr_b16 v[96:97], v10 offset:192\n"
    "s_and_b32 s62, s62, 15\n"
    "v_mfma_f32_16x16x32_bf16 v[56:59], v[108:111], v[120:123], v[56:59]\n"
    "ds_read_b64_tr_b16 v[98:99], v10 offset:1984\n"
    "ds_read_b128 v[132:135], v13\n"
    "s_sub_u32 s63, s62, s99\n"
    "v_mfma_f32_16x16x32_bf16 v[60:63], v[108:111], v[124:127], v[60:63]\n"
    "s_waitcnt lgkmcnt(6)\n"
    "ds_read_b64_tr_b16 v[100:101], v10 offset:7168\n"
    "s_mov_b32 s99, s62\n"
    "v_mfma_f32_16x16x32_bf16 v[64:67], v[112:115], v[120:123], v[64:67]\n"
    "ds_read_b64_tr_b16 v[102:103], v10 offset:8960\n"
    "s_mul_i32 s63, s63, 0x80\n"
    "v_mfma_f32_16x16x32_bf16 v[68:71], v[112:115], v[124:127], v[68:71]\n"
    "s_waitcnt lgkmcnt(6)\n"
    "ds_read_b64_tr_b16 v[104:105], v10 offset:7200\n"
    "v_add_u32 v11, s63, v11\n"
    "v_mfma_f32_16x16x32_bf16 v[72:75], v[116:119], v[120:123], v[72:75]\n"
    "ds_read_b64_tr_b16 v[106:107], v10 offset:8992\n"
    "v_add_u32 v12, s63, v12\n"
    "v_mfma_f32_16x16x32_bf16 v[76:79], v[116:119], v[124:127], v[76:79]\n"
    "s_waitcnt lgkmcnt(5)\n"
    "ds_read_b64_tr_b16 v[108:109], v10 offset:7232\n"
    "s_add_u32 m0, s55, 0x16000\n"
    "v_mfma_f32_16x16x32_bf16 v[80:83], v[96:99], v[120:123], v[80:83]\n"
    "ds_read_b64_tr_b16 v[110:111], v10 offset:9024\n"
    "buffer_load_dwordx4 v9, s[28:31], 0 offen sc1 lds\n"
    "v_mfma_f32_16x16x32_bf16 v[84:87], v[96:99], v[124:127], v[84:87]\n"
    "s_waitcnt lgkmcnt(4)\n"
    "ds_read_b64_tr_b16 v[112:113], v10 offset:7264\n"
    "buffer_load_dwordx4 v9, s[28:31], 0 offen offset:1024 sc1 lds\n"
    "v_mfma_f32_16x16x32_bf16 v[32:35], v[100:103], v[128:131], v[32:35]\n"
    "ds_read_b64_tr_b16 v[114:115], v10 offset:9056\n"
    "buffer_load_dwordx4 v9, s[28:31], 0 offen offset:2048 sc1 lds\n"
    "v_mfma_f32_16x16x32_bf16 v[36:39], v[100:103], v[132:135], v[36:39]\n"
    "s_waitcnt lgkmcnt(4)\n"
    "ds_read_b64_tr_b16 v[116:117], v10 offset:7296\n"
    "buffer_load_dwordx4 v9, s[28:31], 0 offen offset:3072 sc1 lds\n"
    "v_mfma_f32_16x16x32_bf16 v[40:43], v[104:107], v[128:131], v[40:43]\n"
    "ds_read_b64_tr_b16 v[118:119], v10 offset:9088\n"
    "s_add_u32 m0, s55, 0x17000\n"
    "v_mfma_f32_16x16x32_bf16 v[44:47], v[104:107], v[132:135], v[44:47]\n"
    "s_waitcnt lgkmcnt(4)\n"
    "ds_read_b64_tr_b16 v[96:97], v10 offset:7328\n"
    "buffer_load_dwordx4 v9, s[28:31], s56 offen sc1 lds\n"
    "v_mfma_f32_16x16x32_bf16 v[48:51], v[108:111], v[128:131], v[48:51]\n"
    "s_waitcnt vmcnt(9)\n"
    "s_barrier\n"
    "v_add_u32 v2, 0xb000, v2\n"
    "v_add_u32 v3, 0xb000, v3\n"
    "v_add_u32 v4, 0xb000, v4\n"
    "v_add_u32 v13, 0xb000, v13\n"
    "ds_read_b64_tr_b16 v[98:99], v10 offset:9120\n"
    "ds_read_b128 v[120:123], v2\n"
    "buffer_load_dwordx4 v9, s[28:31], s56 offen offset:1024 sc1 lds\n"
    "v_mfma_f32_16x16x32_bf16 v[52:55], v[108:111], v[132:135], v[52:55]\n"
    "s_waitcnt lgkmcnt(5)\n"
    "ds_read_b64_tr_b16 v[100:101], v10 offset:7360\n"
    "buffer_load_dwordx4 v9, s[28:31], s56 offen offset:2048 sc1 lds\n"
    "v_mfma_f32_16x16x32_bf16 v[56:59], v[112:115], v[128:131], v[56:59]\n"
    "ds_read_b64_tr_b16 v[102:103], v10 offset:9152\n"
    "ds_read_b128 v[124:127], v4\n"
    "s_add_u32 s62, s98, 1\n"
    "v_mfma_f32_16x16x32_bf16 v[60:63], v[112:115], v[132:135], v[60:63]\n"
    "s_waitcnt lgkmcnt(6)\n"
    "v_add_u32 v10, 0xb000, v10\n"
    "ds_read_b64_tr_b16 v[104:105], v10\n"
    "s_and_b32 s62, s62, 15\n"
    "v_mfma_f32_16x16x32_bf16 v[64:67], v[116:119], v[128:131], v[64:67]\n"
    "ds_read_b64_tr_b16 v[106:107], v10 offset:1792\n"
    "s_sub_u32 s63, s62, s98\n"
    "v_mfma_f32_16x16x32_bf16 v[68:71], v[116:119], v[132:135], v[68:71]\n"
    "s_waitcnt lgkmcnt(6)\n"
    "ds_read_b64_tr_b16 v[108:109], v10 offset:32\n"
    "s_mov_b32 s98, s62\n"
    "v_mfma_f32_16x16x32_bf16 v[72:75], v[96:99], v[128:131], v[72:75]\n"
    "ds_read_b64_tr_b16 v[110:111], v10 offset:1824\n"
    "s_mul_i32 s63, s63, 0x3800\n"
    "v_mfma_f32_16x16x32_bf16 v[76:79], v[96:99], v[132:135], v[76:79]\n"
    "s_waitcnt lgkmcnt(5)\n"
    "ds_read_b64_tr_b16 v[112:113], v10 offset:64\n"
    "v_add_u32 v9, s63, v9\n"
    "v_mfma_f32_16x16x32_bf16 v[80:83], v[100:103], v[128:131], v[80:83]\n"
    "ds_read_b64_tr_b16 v[114:115], v10 offset:1856\n"
    "v_mfma_f32_16x16x32_bf16 v[84:87], v[100:103], v[132:135], v[84:87]\n"
    "s_waitcnt lgkmcnt(4)\n"
    "s_barrier\n"
    "ds_read_b64_tr_b16 v[116:117], v10 offset:96\n"
    "s_add_u32 m0, s57, 0\n"
    "v_mfma_f32_16x16x32_bf16 v[32:35], v[104:107], v[120:123], v[32:35]\n"
    "ds_read_b64_tr_b16 v[118:119], v10 offset:1888\n"
    "buffer_load_dwordx4 v11, s[12:15], 0 offen lds\n"
    "v_mfma_f32_16x16x32_bf16 v[36:39], v[104:107], v[124:127], v[36:39]\n"
    "s_waitcnt lgkmcnt(4)\n"
    "ds_read_b64_tr_b16 v[96:97], v10 offset:128\n"
    "buffer_load_dwordx4 v12, s[12:15], s53 offen offset:1024 lds\n"
    "v_mfma_f32_16x16x32_bf16 v[40:43], v[108:111], v[120:123], v[40:43]\n"
    "ds_read_b64_tr_b16 v[98:99], v10 offset:1920\n"
    "buffer_load_dwordx4 v11, s[12:15], s54 offen offset:2048 lds\n"
    "v_mfma_f32_16x16x32_bf16 v[44:47], v[108:111], v[124:127], v[44:47]\n"
    "s_waitcnt lgkmcnt(4)\n"
    "ds_read_b64_tr_b16 v[100:101], v10 offset:160\n"
    "buffer_load_dwordx4 v12, s[12:15], s97 offen offset:3072 lds\n"
    "v_mfma_f32_16x16x32_bf16 v[48:51], v[112:115], v[120:123], v[48:51]\n"
    "ds_read_b64_tr_b16 v[102:103], v10 offset:1952\n"
    "ds_read_b128 v[128:131], v3\n"
    "s_add_u32 s62, s99, 1\n"
    "v_mfma_f32_16x16x32_bf16 v[52:55], v[112:115], v[124:127], v[52:55]\n"
    "s_waitcnt lgkmcnt(5)\n"
    "ds_read_b64_tr_b16 v[104:105], v10 offset:192\n"
    "s_and_b32 s62, s62, 15\n"
    "v_mfma_f32_16x16x32_bf16 v[56:59], v[116:119], v[120:123], v[56:59]\n"
    "ds_read_b64_tr_b16 v[106:107], v10 offset:1984\n"
    "ds_read_b128 v[132:135], v13\n"
    "s_sub_u32 s63, s62, s99\n"
    "v_mfma_f32_16x16x32_bf16 v[60:63], v[116:119], v[124:127], v[60:63]\n"
    "s_waitcnt lgkmcnt(6)\n"
    "ds_read_b64_tr_b16 v[108:109], v10 offset:7168\n"
    "s_mov_b32 s99, s62\n"
    "v_mfma_f32_16x16x32_bf16 v[64:67], v[96:99], v[120:123], v[64:67]\n"
    "ds_read_b64_tr_b16 v[110:111], v10 offset:8960\n"
    "s_mul_i32 s63, s63, 0x80\n"
    "v_mfma_f32_16x16x32_bf16 v[68:71], v[96:99], v[124:127], v[68:71]\n"
    "s_waitcnt lgkmcnt(6)\n"
    "ds_read_b64_tr_b16 v[112:113], v10 offset:7200\n"
    "v_add_u32 v11, s63, v11\n"
    "v_mfma_f32_16x16x32_bf16 v[72:75], v[100:103], v[120:123], v[72:75]\n"
    "ds_read_b64_tr_b16 v[114:115], v10 offset:8992\n"
    "v_add_u32 v12, s63, v12\n"
    "v_mfma_f32_16x16x32_bf16 v[76:79], v[100:103], v[124:127], v[76:79]\n"
    "s_waitcnt lgkmcnt(5)\n"
    "ds_read_b64_tr_b16 v[116:117], v10 offset:7232\n"
    "s_add_u32 m0, s55, 0\n"
    "v_mfma_f32_16x16x32_bf16 v[80:83], v[104:107], v[120:123], v[80:83]\n"
    "ds_read_b64_tr_b16 v[118:119], v10 offset:9024\n"
    "buffer_load_dwordx4 v9, s[28:31], 0 offen sc1 lds\n"
    "v_mfma_f32_16x16x32_bf16 v[84:87], v[104:107], v[124:127], v[84:87]\n"
    "s_waitcnt lgkmcnt(4)\n"
    "ds_read_b64_tr_b16 v[96:97], v10 offset:7264\n"
    "buffer_load_dwordx4 v9, s[28:31], 0 offen offset:1024 sc1 lds\n"
    "v_mfma_f32_16x16x32_bf16 v[32:35], v[108:111], v[128:131], v[32:35]\n"
    "ds_read_b64_tr_b16 v[98:99], v10 offset:9056\n"
    "buffer_load_dwordx4 v9, s[28:31], 0 offen offset:2048 sc1 lds\n"
    "v_mfma_f32_16x16x32_bf16 v[36:39], v[108:111], v[132:135], v[36:39]\n"
    "s_waitcnt lgkmcnt(4)\n"
    "ds_read_b64_tr_b16 v[100:101], v10 offset:7296\n"
    "buffer_load_dwordx4 v9, s[28:31], 0 offen offset:3072 sc1 lds\n"
    "v_mfma_f32_16x16x32_bf16 v[40:43], v[112:115], v[128:131], v[40:43]\n"
    "ds_read_b64_tr_b16 v[102:103], v10 offset:9088\n"
    "s_add_u32 m0, s55, 0x1000\n"
    "v_mfma_f32_16x16x32_bf16 v[44:47], v[112:115], v[132:135], v[44:47]\n"
    "s_waitcnt lgkmcnt(4)\n"
    "ds_read_b64_tr_b16 v[104:105], v10 offset:7328\n"
    "buffer_load_dwordx4 v9, s[28:31], s56 offen sc1 lds\n"
    "v_mfma_f32_16x16x32_bf16 v[48:51], v[116:119], v[128:131], v[48:51]\n"
    "s_waitcnt vmcnt(9)\n"
    "s_barrier\n"
    "v_add_u32 v2, 0xb000, v2\n"
    "v_add_u32 v3, 0xb000, v3\n"
    "v_add_u32 v4, 0xb000, v4\n"
    "v_add_u32 v13, 0xb000, v13\n"
    "ds_read_b64_tr_b16 v[106:107], v10 offset:9120\n"
    "ds_read_b128 v[120:123], v2\n"
    "buffer_load_dwordx4 v9, s[28:31], s56 offen offset:1024 sc1 lds\n"
    "v_mfma_f32_16x16x32_bf16 v[52:55], v[116:119], v[132:135], v[52:55]\n"
    "s_waitcnt lgkmcnt(5)\n"
    "ds_read_b64_tr_b16 v[108:109], v10 offset:7360\n"
    "buffer_load_dwordx4 v9, s[28:31], s56 offen offset:2048 sc1 lds\n"
    "v_mfma_f32_16x16x32_bf16 v[56:59], v[96:99], v[128:131], v[56:59]\n"
    "ds_read_b64_tr_b16 v[110:111], v10 offset:9152\n"
    "ds_read_b128 v[124:127], v4\n"
    "s_add_u32 s62, s98, 1\n"
    "v_mfma_f32_16x16x32_bf16 v[60:63], v[96:99], v[132:135], v[60:63]\n"
    "s_waitcnt lgkmcnt(6)\n"
    "v_add_u32 v10, 0xb000, v10\n"
    "ds_read_b64_tr_b16 v[112:113], v10\n"
    "s_and_b32 s62, s62, 15\n"
    "v_mfma_f32_16x16x32_bf16 v[64:67], v[100:103], v[128:131], v[64:67]\n"
    "ds_read_b64_tr_b16 v[114:115], v10 offset:1792\n"
    "s_sub_u32 s63, s62, s98\n"
    "v_mfma_f32_16x16x32_bf16 v[68:71], v[100:103], v[132:135], v[68:71]\n"
    "s_waitcnt lgkmcnt(6)\n"
    "ds_read_b64_tr_b16 v[116:117], v10 offset:32\n"
    "s_mov_b32 s98, s62\n"
    "v_mfma_f32_16x16x32_bf16 v[72:75], v[104:107], v[128:131], v[72:75]\n"
    "ds_read_b64_tr_b16 v[118:119], v10 offset:1824\n"
    "s_mul_i32 s63, s63, 0x3800\n"
    "v_mfma_f32_16x16x32_bf16 v[76:79], v[104:107], v[132:135], v[76:79]\n"
    "s_waitcnt lgkmcnt(5)\n"
    "ds_read_b64_tr_b16 v[96:97], v10 offset:64\n"
    "v_add_u32 v9, s63, v9\n"
    "v_mfma_f32_16x16x32_bf16 v[80:83], v[108:111], v[128:131], v[80:83]\n"
    "ds_read_b64_tr_b16 v[98:99], v10 offset:1856\n"
    "v_mfma_f32_16x16x32_bf16 v[84:87], v[108:111], v[132:135], v[84:87]\n"
    "s_waitcnt lgkmcnt(4)\n"
    "ds_read_b64_tr_b16 v[100:101], v10 offset:96\n"
    "v_mfma_f32_16x16x32_bf16 v[32:35], v[112:115], v[120:123], v[32:35]\n"
    "ds_read_b64_tr_b16 v[102:103], v10 offset:1888\n"
    "v_mfma_f32_16x16x32_bf16 v[36:39], v[112:115], v[124:127], v[36:39]\n"
    "s_waitcnt lgkmcnt(4)\n"
    "ds_read_b64_tr_b16 v[104:105], v10 offset:128\n"
    "v_mfma_f32_16x16x32_bf16 v[40:43], v[116:119], v[120:123], v[40:43]\n"
    "ds_read_b64_tr_b16 v[106:107], v10 offset:1920\n"
    "v_mfma_f32_16x16x32_bf16 v[44:47], v[116:119], v[124:127], v[44:47]\n"
    "s_waitcnt lgkmcnt(4)\n"
    "ds_read_b64_tr_b16 v[108:109], v10 offset:160\n"
    "v_mfma_f32_16x16x32_bf16 v[48:51], v[96:99], v[120:123], v[48:51]\n"
    "ds_read_b64_tr_b16 v[110:111], v10 offset:1952\n"
    "ds_read_b128 v[128:131], v3\n"
    "v_mfma_f32_16x16x32_bf16 v[52:55], v[96:99], v[124:127], v[52:55]\n"
    "s_waitcnt lgkmcnt(5)\n"
    "ds_read_b64_tr_b16 v[112:113], v10 offset:192\n"
    "v_mfma_f32_16x16x32_bf16 v[56:59], v[100:103], v[120:123], v[56:59]\n"
    "ds_read_b64_tr_b16 v[114:115], v10 offset:1984\n"
    "ds_read_b128 v[132:135], v13\n"
    "v_mfma_f32_16x16x32_bf16 v[60:63], v[100:103], v[124:127], v[60:63]\n"
    "s_waitcnt lgkmcnt(6)\n"
    "ds_read_b64_tr_b16 v[116:117], v10 offset:7168\n"
    "v_mfma_f32_16x16x32_bf16 v[64:67], v[104:107], v[120:123], v[64:67]\n"
    "ds_read_b64_tr_b16 v[118:119], v10 offset:8960\n"
    "v_mfma_f32_16x16x32_bf16 v[68:71], v[104:107], v[124:127], v[68:71]\n"
    "s_waitcnt lgkmcnt(6)\n"
    "ds_read_b64_tr_b16 v[96:97], v10 offset:7200\n"
    "v_mfma_f32_16x16x32_bf16 v[72:75], v[108:111], v[120:123], v[72:75]\n"
    "ds_read_b64_tr_b16 v[98:99], v10 offset:8992\n"
    "v_mfma_f32_16x16x32_bf16 v[76:79], v[108:111], v[124:127], v[76:79]\n"
    "s_waitcnt lgkmcnt(5)\n"
    "ds_read_b64_tr_b16 v[100:101], v10 offset:7232\n"
    "v_mfma_f32_16x16x32_bf16 v[80:83], v[112:115], v[120:123], v[80:83]\n"
    "ds_read_b64_tr_b16 v[102:103], v10 offset:9024\n"
    "v_mfma_f32_16x16x32_bf16 v[84:87], v[112:115], v[124:127], v[84:87]\n"
    "s_waitcnt lgkmcnt(4)\n"
    "ds_read_b64_tr_b16 v[104:105], v10 offset:7264\n"
    "v_mfma_f32_16x16x32_bf16 v[32:35], v[116:119], v[128:131], v[32:35]\n"
    "ds_read_b64_tr_b16 v[106:107], v10 offset:9056\n"
    "v_mfma_f32_16x16x32_bf16 v[36:39], v[116:119], v[132:135], v[36:39]\n"
    "s_waitcnt lgkmcnt(4)\n"
    "ds_read_b64_tr_b16 v[108:109], v10 offset:7296\n"
    "v_mfma_f32_16x16x32_bf16 v[40:43], v[96:99], v[128:131], v[40:43]\n"
    "ds_read_b64_tr_b16 v[110:111], v10 offset:9088\n"
    "v_mfma_f32_16x16x32_bf16 v[44:47], v[96:99], v[132:135], v[44:47]\n"
    "s_waitcnt lgkmcnt(4)\n"
    "ds_read_b64_tr_b16 v[112:113], v10 offset:7328\n"
    "v_mfma_f32_16x16x32_bf16 v[48:51], v[100:103], v[128:131], v[48:51]\n"
    "s_waitcnt vmcnt(0)\n"
    "s_barrier\n"
    "v_add_u32 v2, 0xfffea000, v2\n"
    "v_add_u32 v3, 0xfffea000, v3\n"
    "v_add_u32 v4, 0xfffea000, v4\n"
    "v_add_u32 v13, 0xfffea000, v13\n"
    "ds_read_b64_tr_b16 v[114:115], v10 offset:9120\n"
    "ds_read_b128 v[120:123], v2\n"
    "v_mfma_f32_16x16x32_bf16 v[52:55], v[100:103], v[132:135], v[52:55]\n"
    "s_waitcnt lgkmcnt(5)\n"
    "ds_read_b64_tr_b16 v[116:117], v10 offset:7360\n"
    "v_mfma_f32_16x16x32_bf16 v[56:59], v[104:107], v[128:131], v[56:59]\n"
    "ds_read_b64_tr_b16 v[118:119], v10 offset:9152\n"
    "ds_read_b128 v[124:127], v4\n"
    "v_mfma_f32_16x16x32_bf16 v[60:63], v[104:107], v[132:135], v[60:63]\n"
    "s_waitcnt lgkmcnt(6)\n"
    "v_add_u32 v10, 0xfffea000, v10\n"
    "ds_read_b64_tr_b16 v[96:97], v10\n"
    "v_mfma_f32_16x16x32_bf16 v[64:67], v[108:111], v[128:131], v[64:67]\n"
    "ds_read_b64_tr_b16 v[98:99], v10 offset:1792\n"
    "v_mfma_f32_16x16x32_bf16 v[68:71], v[108:111], v[132:135], v[68:71]\n"
    "s_waitcnt lgkmcnt(6)\n"
    "ds_read_b64_tr_b16 v[100:101], v10 offset:32\n"
    "v_mfma_f32_16x16x32_bf16 v[72:75], v[112:115], v[128:131], v[72:75]\n"
    "ds_read_b64_tr_b16 v[102:103], v10 offset:1824\n"
    "v_mfma_f32_16x16x32_bf16 v[76:79], v[112:115], v[132:135], v[76:79]\n"
    "s_waitcnt lgkmcnt(5)\n"
    "ds_read_b64_tr_b16 v[104:105], v10 offset:64\n"
    "v_mfma_f32_16x16x32_bf16 v[80:83], v[116:119], v[128:131], v[80:83]\n"
    "ds_read_b64_tr_b16 v[106:107], v10 offset:1856\n"
    "v_mfma_f32_16x16x32_bf16 v[84:87], v[116:119], v[132:135], v[84:87]\n"
    "s_waitcnt lgkmcnt(4)\n"
    "ds_read_b64_tr_b16 v[108:109], v10 offset:96\n"
    "v_mfma_f32_16x16x32_bf16 v[32:35], v[96:99], v[120:123], v[32:35]\n"
    "ds_read_b64_tr_b16 v[110:111], v10 offset:1888\n"
    "v_mfma_f32_16x16x32_bf16 v[36:39], v[96:99], v[124:127], v[36:39]\n"
    "s_waitcnt lgkmcnt(4)\n"
    "ds_read_b64_tr_b16 v[112:113], v10 offset:128\n"
    "v_mfma_f32_16x16x32_bf16 v[40:43], v[100:103], v[120:123], v[40:43]\n"
    "ds_read_b64_tr_b16 v[114:115], v10 offset:1920\n"
    "v_mfma_f32_16x16x32_bf16 v[44:47], v[100:103], v[124:127], v[44:47]\n"
    "s_waitcnt lgkmcnt(4)\n"
    "ds_read_b64_tr_b16 v[116:117], v10 offset:160\n"
    "v_mfma_f32_16x16x32_bf16 v[48:51], v[104:107], v[120:123], v[48:51]\n"
    "ds_read_b64_tr_b16 v[118:119], v10 offset:1952\n"
    "ds_read_b128 v[128:131], v3\n"
    "v_mfma_f32_16x16x32_bf16 v[52:55], v[104:107], v[124:127], v[52:55]\n"
    "s_waitcnt lgkmcnt(5)\n"
    "ds_read_b64_tr_b16 v[96:97], v10 offset:192\n"
    "v_mfma_f32_16x16x32_bf16 v[56:59], v[108:111], v[120:123], v[56:59]\n"
    "ds_read_b64_tr_b16 v[98:99], v10 offset:1984\n"
    "ds_read_b128 v[132:135], v13\n"
    "v_mfma_f32_16x16x32_bf16 v[60:63], v[108:111], v[124:127], v[60:63]\n"
    "s_waitcnt lgkmcnt(6)\n"
    "ds_read_b64_tr_b16 v[100:101], v10 offset:7168\n"
    "v_mfma_f32_16x16x32_bf16 v[64:67], v[112:115], v[120:123], v[64:67]\n"
    "ds_read_b64_tr_b16 v[102:103], v10 offset:8960\n"
    "v_mfma_f32_16x16x32_bf16 v[68:71], v[112:115], v[124:127], v[68:71]\n"
    "s_waitcnt lgkmcnt(6)\n"
    "ds_read_b64_tr_b16 v[104:105], v10 offset:7200\n"
    "v_mfma_f32_16x16x32_bf16 v[72:75], v[116:119], v[120:123], v[72:75]\n"
    "ds_read_b64_tr_b16 v[106:107], v10 offset:8992\n"
    "v_mfma_f32_16x16x32_bf16 v[76:79], v[116:119], v[124:127], v[76:79]\n"
    "s_waitcnt lgkmcnt(5)\n"
    "ds_read_b64_tr_b16 v[108:109], v10 offset:7232\n"
    "v_mfma_f32_16x16x32_bf16 v[80:83], v[96:99], v[120:123], v[80:83]\n"
    "ds_read_b64_tr_b16 v[110:111], v10 offset:9024\n"
    "v_mfma_f32_16x16x32_bf16 v[84:87], v[96:99], v[124:127], v[84:87]\n"
    "s_waitcnt lgkmcnt(4)\n"
    "ds_read_b64_tr_b16 v[112:113], v10 offset:7264\n"
    "v_mfma_f32_16x16x32_bf16 v[32:35], v[100:103], v[128:131], v[32:35]\n"
    "ds_read_b64_tr_b16 v[114:115], v10 offset:9056\n"
    "v_mfma_f32_16x16x32_bf16 v[36:39], v[100:103], v[132:135], v[36:39]\n"
    "s_waitcnt lgkmcnt(4)\n"
    "ds_read_b64_tr_b16 v[116:117], v10 offset:7296\n"
    "v_mfma_f32_16x16x32_bf16 v[40:43], v[104:107], v[128:131], v[40:43]\n"
    "ds_read_b64_tr_b16 v[118:119], v10 offset:9088\n"
    "v_mfma_f32_16x16x32_bf16 v[44:47], v[104:107], v[132:135], v[44:47]\n"
    "s_waitcnt lgkmcnt(4)\n"
    "ds_read_b64_tr_b16 v[96:97], v10 offset:7328\n"
    "v_mfma_f32_16x16x32_bf16 v[48:51], v[108:111], v[128:131], v[48:51]\n"
    "ds_read_b64_tr_b16 v[98:99], v10 offset:9120\n"
    "v_mfma_f32_16x16x32_bf16 v[52:55], v[108:111], v[132:135], v[52:55]\n"
    "s_waitcnt lgkmcnt(4)\n"
    "ds_read_b64_tr_b16 v[100:101], v10 offset:7360\n"
    "v_mfma_f32_16x16x32_bf16 v[56:59], v[112:115], v[128:131], v[56:59]\n"
    "ds_read_b64_tr_b16 v[102:103], v10 offset:9152\n"
    "v_mfma_f32_16x16x32_bf16 v[60:63], v[112:115], v[132:135], v[60:63]\n"
    "s_waitcnt lgkmcnt(4)\n"
    "v_mfma_f32_16x16x32_bf16 v[64:67], v[116:119], v[128:131], v[64:67]\n"
    "v_mfma_f32_16x16x32_bf16 v[68:71], v[116:119], v[132:135], v[68:71]\n"
    "s_waitcnt lgkmcnt(2)\n"
    "v_mfma_f32_16x16x32_bf16 v[72:75], v[96:99], v[128:131], v[72:75]\n"
    "v_mfma_f32_16x16x32_bf16 v[76:79], v[96:99], v[132:135], v[76:79]\n"
    "s_waitcnt lgkmcnt(0)\n"
    "v_mfma_f32_16x16x32_bf16 v[80:83], v[100:103], v[128:131], v[80:83]\n"
    "v_mfma_f32_16x16x32_bf16 v[84:87], v[100:103], v[132:135], v[84:87]\n"
    "s_mul_i32 s58, s40, 0x1c00\n"
    "s_add_u32 s58, s58, 0x21000\n"
    "v_lshlrev_b32 v14, 4, v1\n"
    "v_add_u32 v216, s58, v14\n"
    "s_xor_b32 s59, s40, 2\n"
    "s_mul_i32 s59, s59, 0x1c00\n"
    "s_add_u32 s59, s59, 0x21000\n"
    "v_add_u32 v217, s59, v14\n"
    "ds_write_b128 v216, v[36:39]\n"
    "ds_write_b128 v216, v[44:47] offset:1024\n"
    "ds_write_b128 v216, v[52:55] offset:2048\n"
    "ds_write_b128 v216, v[60:63] offset:3072\n"
    "ds_write_b128 v216, v[68:71] offset:4096\n"
    "ds_write_b128 v216, v[76:79] offset:5120\n"
    "ds_write_b128 v216, v[84:87] offset:6144\n"
    "s_waitcnt lgkmcnt(0)\n"
    "s_barrier\n"
    "ds_read_b128 v[160:163], v217\n"
    "ds_read_b128 v[164:167], v217 offset:1024\n"
    "ds_read_b128 v[168:171], v217 offset:2048\n"
    "ds_read_b128 v[172:175], v217 offset:3072\n"
    "ds_read_b128 v[176:179], v217 offset:4096\n"
    "ds_read_b128 v[180:183], v217 offset:5120\n"
    "ds_read_b128 v[184:187], v217 offset:6144\n"
    "v_lshlrev_b32 v214, 16, v214\n"
    "s_waitcnt lgkmcnt(6)\n"
    "v_pk_add_f32 v[32:33], v[32:33], v[160:161]\n"
    "v_pk_add_f32 v[34:35], v[34:35], v[162:163]\n"
    "v_pk_add_f32 v[32:33], v[32:33], v[214:215] op_sel_hi:[1,0]\n"
    "v_pk_add_f32 v[34:35], v[34:35], v[214:215] op_sel_hi:[1,0]\n"
    "v_lshlrev_b32 v14, 16, v200\n"
    "v_and_b32 v15, s7, v200\n"
    "v_pk_mul_f32 v[32:33], v[32:33], v[14:15]\n"
    "v_lshlrev_b32 v14, 16, v201\n"
    "v_and_b32 v15, s7, v201\n"
    "v_pk_mul_f32 v[34:35], v[34:35], v[14:15]\n"
    "v_cvt_pk_bf16_f32 v32, v32, v33\n"
    "v_cvt_pk_bf16_f32 v33, v34, v35\n"
    "buffer_store_dwordx2 v[32:33], v215, s[28:31], 0 offen sc1\n"
    "s_waitcnt lgkmcnt(5)\n"
    "v_pk_add_f32 v[40:41], v[40:41], v[164:165]\n"
    "v_pk_add_f32 v[42:43], v[42:43], v[166:167]\n"
    "v_pk_add_f32 v[40:41], v[40:41], v[214:215] op_sel_hi:[1,0]\n"
    "v_pk_add_f32 v[42:43], v[42:43], v[214:215] op_sel_hi:[1,0]\n"
    "v_lshlrev_b32 v14, 16, v202\n"
    "v_and_b32 v15, s7, v202\n"
    "v_pk_mul_f32 v[40:41], v[40:41], v[14:15]\n"
    "v_lshlrev_b32 v14, 16, v203\n"
    "v_and_b32 v15, s7, v203\n"
    "v_pk_mul_f32 v[42:43], v[42:43], v[14:15]\n"
    "v_cvt_pk_bf16_f32 v40, v40, v41\n"
    "v_cvt_pk_bf16_f32 v41, v42, v43\n"
    "buffer_store_dwordx2 v[40:41], v215, s[28:31], 0 offen offset:1024 sc1\n"
    "s_waitcnt lgkmcnt(4)\n"
    "v_pk_add_f32 v[48:49], v[48:49], v[168:169]\n"
    "v_pk_add_f32 v[50:51], v[50:51], v[170:171]\n"
    "v_pk_add_f32 v[48:49], v[48:49], v[214:215] op_sel_hi:[1,0]\n"
    "v_pk_add_f32 v[50:51], v[50:51], v[214:215] op_sel_hi:[1,0]\n"
    "v_lshlrev_b32 v14, 16, v204\n"
    "v_and_b32 v15, s7, v204\n"
    "v_pk_mul_f32 v[48:49], v[48:49], v[14:15]\n"
    "v_lshlrev_b32 v14, 16, v205\n"
    "v_and_b32 v15, s7, v205\n"
    "v_pk_mul_f32 v[50:51], v[50:51], v[14:15]\n"
    "v_cvt_pk_bf16_f32 v48, v48, v49\n"
    "v_cvt_pk_bf16_f32 v49, v50, v51\n"
    "buffer_store_dwordx2 v[48:49], v215, s[28:31], 0 offen offset:2048 sc1\n"
    "s_waitcnt lgkmcnt(3)\n"
    "v_pk_add_f32 v[56:57], v[56:57], v[172:173]\n"
    "v_pk_add_f32 v[58:59], v[58:59], v[174:175]\n"
    "v_pk_add_f32 v[56:57], v[56:57], v[214:215] op_sel_hi:[1,0]\n"
    "v_pk_add_f32 v[58:59], v[58:59], v[214:215] op_sel_hi:[1,0]\n"
    "v_lshlrev_b32 v14, 16, v206\n"
    "v_and_b32 v15, s7, v206\n"
    "v_pk_mul_f32 v[56:57], v[56:57], v[14:15]\n"
    "v_lshlrev_b32 v14, 16, v207\n"
    "v_and_b32 v15, s7, v207\n"
    "v_pk_mul_f32 v[58:59], v[58:59], v[14:15]\n"
    "v_cvt_pk_bf16_f32 v56, v56, v57\n"
    "v_cvt_pk_bf16_f32 v57, v58, v59\n"
    "buffer_store_dwordx2 v[56:57], v215, s[28:31], 0 offen offset:3072 sc1\n"
    "s_waitcnt lgkmcnt(2)\n"
    "v_pk_add_f32 v[64:65], v[64:65], v[176:177]\n"
    "v_pk_add_f32 v[66:67], v[66:67], v[178:179]\n"
    "v_pk_add_f32 v[64:65], v[64:65], v[214:215] op_sel_hi:[1,0]\n"
    "v_pk_add_f32 v[66:67], v[66:67], v[214:215] op_sel_hi:[1,0]\n"
    "v_lshlrev_b32 v14, 16, v208\n"
    "v_and_b32 v15, s7, v208\n"
    "v_pk_mul_f32 v[64:65], v[64:65], v[14:15]\n"
    "v_lshlrev_b32 v14, 16, v209\n"
    "v_and_b32 v15, s7, v209\n"
    "v_pk_mul_f32 v[66:67], v[66:67], v[14:15]\n"
    "v_cvt_pk_bf16_f32 v64, v64, v65\n"
    "v_cvt_pk_bf16_f32 v65, v66, v67\n"
    "buffer_store_dwordx2 v[64:65], v215, s[28:31], s56 offen sc1\n"
    "s_waitcnt lgkmcnt(1)\n"
    "v_pk_add_f32 v[72:73], v[72:73], v[180:181]\n"
    "v_pk_add_f32 v[74:75], v[74:75], v[182:183]\n"
    "v_pk_add_f32 v[72:73], v[72:73], v[214:215] op_sel_hi:[1,0]\n"
    "v_pk_add_f32 v[74:75], v[74:75], v[214:215] op_sel_hi:[1,0]\n"
    "v_lshlrev_b32 v14, 16, v210\n"
    "v_and_b32 v15, s7, v210\n"
    "v_pk_mul_f32 v[72:73], v[72:73], v[14:15]\n"
    "v_lshlrev_b32 v14, 16, v211\n"
    "v_and_b32 v15, s7, v211\n"
    "v_pk_mul_f32 v[74:75], v[74:75], v[14:15]\n"
    "v_cvt_pk_bf16_f32 v72, v72, v73\n"
    "v_cvt_pk_bf16_f32 v73, v74, v75\n"
    "buffer_store_dwordx2 v[72:73], v215, s[28:31], s56 offen offset:1024 sc1\n"
    "s_waitcnt lgkmcnt(0)\n"
    "v_pk_add_f32 v[80:81], v[80:81], v[184:185]\n"
    "v_pk_add_f32 v[82:83], v[82:83], v[186:187]\n"
    "v_pk_add_f32 v[80:81], v[80:81], v[214:215] op_sel_hi:[1,0]\n"
    "v_pk_add_f32 v[82:83], v[82:83], v[214:215] op_sel_hi:[1,0]\n"
    "v_lshlrev_b32 v14, 16, v212\n"
    "v_and_b32 v15, s7, v212\n"
    "v_pk_mul_f32 v[80:81], v[80:81], v[14:15]\n"
    "v_lshlrev_b32 v14, 16, v213\n"
    "v_and_b32 v15, s7, v213\n"
    "v_pk_mul_f32 v[82:83], v[82:83], v[14:15]\n"
    "v_cvt_pk_bf16_f32 v80, v80, v81\n"
    "v_cvt_pk_bf16_f32 v81, v82, v83\n"
    "buffer_store_dwordx2 v[80:81], v215, s[28:31], s56 offen offset:2048 sc1\n"
    "s_lshl_b32 s93, s47, 3\n"
    "s_add_u32 s93, s93, s48\n"
    "s_lshl_b32 s93, s93, 3\n"
    "s_add_u32 s93, s93, 0x2000\n"
    "s_waitcnt vmcnt(0)\n"
    "s_barrier\n"
    "s_cmp_lg_u32 s40, 0\n"
    "s_cbranch_scc1 .LF_w0_28\n"
    "s_mov_b32 exec_lo, 0x1\n"
    "s_mov_b32 exec_hi, 0x0\n"
    "v_mov_b32 v28, s90\n"
    "v_mov_b32 v29, s91\n"
    "v_mov_b32 v30, 0\n"
    "buffer_store_dwordx2 v[28:29], v30, s[28:31], s93 offen sc1\n"
    "s_mov_b64 exec, -1\n"
    ".LF_w0_28:\n"
    "s_nop 4\n"
    "s_lshl_b32 s63, s40, 10\n"
    "s_mov_b32 s54, 0\n"
    "s_add_u32 m0, s63, 0\n"
    "s_nop 0\n"
    "buffer_load_dwordx4 v5, s[16:19], s54 offen lds\n"
    "s_lshl_b32 s63, s40, 10\n"
    "s_mov_b32 s54, 0x62000\n"
    "s_add_u32 m0, s63, 0xe000\n"
    "s_nop 0\n"
    "buffer_load_dwordx4 v5, s[16:19], s54 offen lds\n"
    "s_lshl_b32 s63, s40, 10\n"
    "s_mov_b32 s54, 0x7000\n"
    "s_add_u32 m0, s63, 0x1000\n"
    "s_nop 0\n"
    "buffer_load_dwordx4 v5, s[16:19], s54 offen lds\n"
    "s_lshl_b32 s63, s40, 10\n"
    "s_mov_b32 s54, 0x69000\n"
    "s_add_u32 m0, s63, 0xf000\n"
    "s_nop 0\n"
    "buffer_load_dwordx4 v5, s[16:19], s54 offen lds\n"
    "s_lshl_b32 s63, s40, 10\n"
    "s_mov_b32 s54, 0xe000\n"
    "s_add_u32 m0, s63, 0x2000\n"
    "s_nop 0\n"
    "buffer_load_dwordx4 v5, s[16:19], s54 offen lds\n"
    "s_lshl_b32 s63, s40, 10\n"
    "s_mov_b32 s54, 0x70000\n"
    "s_add_u32 m0, s63, 0x10000\n"
    "s_nop 0\n"
    "buffer_load_dwordx4 v5, s[16:19], s54 offen lds\n"
    "s_lshl_b32 s63, s40, 10\n"
    "s_mov_b32 s54, 0x15000\n"
    "s_add_u32 m0, s63, 0x3000\n"
    "s_nop 0\n"
    "buffer_load_dwordx4 v5, s[16:19], s54 offen lds\n"
    "s_lshl_b32 s63, s40, 10\n"
    "s_mov_b32 s54, 0x77000\n"
    "s_add_u32 m0, s63, 0x11000\n"
    "s_nop 0\n"
    "buffer_load_dwordx4 v5, s[16:19], s54 offen lds\n"
    "s_lshl_b32 s63, s40, 10\n"
    "s_mov_b32 s54, 0x1c000\n"
    "s_add_u32 m0, s63, 0x4000\n"
    "s_nop 0\n"
    "buffer_load_dwordx4 v5, s[16:19], s54 offen lds\n"
    "s_lshl_b32 s63, s40, 10\n"
    "s_mov_b32 s54, 0x7e000\n"
    "s_add_u32 m0, s63, 0x12000\n"
    "s_nop 0\n"
    "buffer_load_dwordx4 v5, s[16:19], s54 offen lds\n"
    "s_lshl_b32 s63, s40, 10\n"
    "s_mov_b32 s54, 0x23000\n"
    "s_add_u32 m0, s63, 0x5000\n"
    "s_nop 0\n"
    "buffer_load_dwordx4 v5, s[16:19], s54 offen lds\n"
    "s_lshl_b32 s63, s40, 10\n"
    "s_mov_b32 s54, 0x85000\n"
    "s_add_u32 m0, s63, 0x13000\n"
    "s_nop 0\n"
    "buffer_load_dwordx4 v5, s[16:19], s54 offen lds\n"
    "s_lshl_b32 s63, s40, 10\n"
    "s_mov_b32 s54, 0x2a000\n"
    "s_add_u32 m0, s63, 0x6000\n"
    "s_nop 0\n"
    "buffer_load_dwordx4 v5, s[16:19], s54 offen lds\n"
    "s_lshl_b32 s63, s40, 10\n"
    "s_mov_b32 s54, 0x8c000\n"
    "s_add_u32 m0, s63, 0x14000\n"
    "s_nop 0\n"
    "buffer_load_dwordx4 v5, s[16:19], s54 offen lds\n"
    "s_cmp_ge_u32 s50, 7\n"
    "s_cbranch_scc1 .LF_t5end_29\n"
    "s_mov_b32 s24, s86\n"
    "s_and_b32 s25, s87, 0xffff\n"
    "s_mov_b32 s26, 0x1c0000\n"
    "s_mov_b32 s27, 0x20000\n"
    "s_lshl_b32 s58, s49, 1\n"
    "s_add_u32 s58, s58, s51\n"
    "s_mul_i32 s58, s58, 56\n"
    "s_mul_i32 s59, s52, 28\n"
    "s_add_u32 s58, s58, s59\n"
    "s_lshl_b32 s58, s58, 10\n"
    "s_add_u32 s58, s58, 0x1900000\n"
    "v_lshlrev_b32 v14, 4, v1\n"
    "v_add_u32 v9, s58, v14\n"
    "v_bfe_u32 v14, v1, 2, 2\n"
    "v_and_b32 v15, 3, v1\n"
    "v_bfe_u32 v16, v1, 4, 1\n"
    "v_lshrrev_b32 v17, 5, v1\n"
    "v_and_b32 v18, 1, v15\n"
    "v_lshlrev_b32 v18, 4, v18\n"
    "v_lshrrev_b32 v15, 1, v15\n"
    "v_lshl_or_b32 v18, v15, 3, v18\n"
    "v_lshl_or_b32 v18, v16, 5, v18\n"
    "v_lshl_add_u32 v17, v17, 3, v14\n"
    "v_lshl_add_u32 v18, v17, 7, v18\n"
    "v_lshrrev_b32 v14, 1, v14\n"
    "v_xor_b32 v14, s52, v14\n"
    "v_lshl_add_u32 v18, v14, 6, v18\n"
    "s_mul_i32 s58, s52, 0xe000\n"
    "v_add_u32 v10, s58, v18\n"
    "v_xor_b32 v11, 64, v10\n"
    "s_lshl_b32 s58, s49, 6\n"
    "s_lshl_b32 s59, s51, 5\n"
    "s_add_u32 s58, s58, s59\n"
    "v_and_b32 v14, 31, v1\n"
    "v_add_u32 v14, s58, v14\n"
    "v_mul_u32_u24 v14, 0x380, v14\n"
    "s_lshl_b32 s58, s50, 7\n"
    "s_lshl_b32 s59, s52, 6\n"
    "s_add_u32 s58, s58, s59\n"
    "v_lshrrev_b32 v15, 5, v1\n"
    "v_lshl_add_u32 v14, v15, 4, v14\n"
    "v_add_u32 v232, s58, v14\n"
    "s_mov_b32 s60, 0xe80000\n"
    "buffer_load_dwordx4 v[208:211], v232, s[28:31], s60 offen sc1\n"
    "buffer_load_dwordx4 v[212:215], v232, s[28:31], s60 offen offset:32 sc1\n"
    "s_lshl_b32 s58, s58, 1\n"
    "v_lshlrev_b32 v15, 5, v15\n"
    "v_add_u32 v15, s58, v15\n"
    "s_mov_b32 s59, 0x5300\n"
    "buffer_load_dwordx4 v[216:219], v15, s[28:31], s59 offen sc1\n"
    "buffer_load_dwordx4 v[220:223], v15, s[28:31], s59 offen offset:16 sc1\n"
    "buffer_load_dwordx4 v[224:227], v15, s[28:31], s59 offen offset:64 sc1\n"
    "buffer_load_dwordx4 v[228:231], v15, s[28:31], s59 offen offset:80 sc1\n"
    "s_lshl_b32 s93, s49, 6\n"
    "s_add_u32 s93, s93, 0x2000\n"
    "s_cmp_lg_u32 s40, 0\n"
    "s_cbranch_scc1 .LF_w0_30\n"
    "v_min_u32 v20, 7, v1\n"
    "v_lshlrev_b32 v20, 3, v20\n"
    "s_mov_b32 s92, 0\n"
    ".LF_poll_31:\n"
    "s_nop 0\n"
    "buffer_load_dwordx2 v[24:25], v20, s[28:31], s93 offen sc1\n"
    "s_waitcnt vmcnt(0)\n"
    "v_xor_b32 v24, s90, v24\n"
    "v_xor_b32 v25, s91, v25\n"
    "v_or_b32 v24, v24, v25\n"
    "v_cmp_ne_u32 vcc, 0, v24\n"
    "s_add_u32 s92, s92, 1\n"
    "s_nop 3\n"
    "s_cbranch_vccz .LF_go_32\n"
    "s_cmp_gt_u32 s92, 0x20000\n"
    "s_cbranch_scc1 .LF_go_32\n"
    "s_sleep 1\n"
    "s_branch .LF_poll_31\n"
    ".LF_go_32:\n"
    ".LF_w0_30:\n"
    "s_nop 4\n"
    "s_barrier\n"
    "buffer_load_dwordx4 v[96:99], v9, s[28:31], 0 offen sc1\n"
    "buffer_load_dwordx4 v[100:103], v9, s[28:31], 0 offen offset:1024 sc1\n"
    "buffer_load_dwordx4 v[104:107], v9, s[28:31], 0 offen offset:2048 sc1\n"
    "buffer_load_dwordx4 v[108:111], v9, s[28:31], 0 offen offset:3072 sc1\n"
    "s_mov_b32 s62, 0x1000\n"
    "buffer_load_dwordx4 v[112:115], v9, s[28:31], s62 offen sc1\n"
    "s_mov_b32 s62, 0x1400\n"
    "buffer_load_dwordx4 v[116:119], v9, s[28:31], s62 offen sc1\n"
    "s_mov_b32 s62, 0x1800\n"
    "buffer_load_dwordx4 v[120:123], v9, s[28:31], s62 offen sc1\n"
    "s_mov_b32 s62, 0x1c00\n"
    "buffer_load_dwordx4 v[124:127], v9, s[28:31], s62 offen sc1\n"
    "s_mov_b32 s62, 0x2000\n"
    "buffer_load_dwordx4 v[128:131], v9, s[28:31], s62 offen sc1\n"
    "s_mov_b32 s62, 0x2400\n"
    "buffer_load_dwordx4 v[132:135], v9, s[28:31], s62 offen sc1\n"
    "s_mov_b32 s62, 0x2800\n"
    "buffer_load_dwordx4 v[136:139], v9, s[28:31], s62 offen sc1\n"
    "s_mov_b32 s62, 0x2c00\n"
    "buffer_load_dwordx4 v[140:143], v9, s[28:31], s62 offen sc1\n"
    "s_mov_b32 s62, 0x3000\n"
    "buffer_load_dwordx4 v[144:147], v9, s[28:31], s62 offen sc1\n"
    "s_mov_b32 s62, 0x3400\n"
    "buffer_load_dwordx4 v[148:151], v9, s[28:31], s62 offen sc1\n"
    "s_mov_b32 s62, 0x3800\n"
    "buffer_load_dwordx4 v[152:155], v9, s[28:31], s62 offen sc1\n"
    "s_mov_b32 s62, 0x3c00\n"
    "buffer_load_dwordx4 v[156:159], v9, s[28:31], s62 offen sc1\n"
    "s_mov_b32 s62, 0x4000\n"
    "buffer_load_dwordx4 v[160:163], v9, s[28:31], s62 offen sc1\n"
    "s_mov_b32 s62, 0x4400\n"
    "buffer_load_dwordx4 v[164:167], v9, s[28:31], s62 offen sc1\n"
    "s_mov_b32 s62, 0x4800\n"
    "buffer_load_dwordx4 v[168:171], v9, s[28:31], s62 offen sc1\n"
    "s_mov_b32 s62, 0x4c00\n"
    "buffer_load_dwordx4 v[172:175], v9, s[28:31], s62 offen sc1\n"
    "s_mov_b32 s62, 0x5000\n"
    "buffer_load_dwordx4 v[176:179], v9, s[28:31], s62 offen sc1\n"
    "s_mov_b32 s62, 0x5400\n"
    "buffer_load_dwordx4 v[180:183], v9, s[28:31], s62 offen sc1\n"
    "s_mov_b32 s62, 0x5800\n"
    "buffer_load_dwordx4 v[184:187], v9, s[28:31], s62 offen sc1\n"
    "s_mov_b32 s62, 0x5c00\n"
    "buffer_load_dwordx4 v[188:191], v9, s[28:31], s62 offen sc1\n"
    "s_mov_b32 s62, 0x6000\n"
    "buffer_load_dwordx4 v[192:195], v9, s[28:31], s62 offen sc1\n"
    "s_mov_b32 s62, 0x6400\n"
    "buffer_load_dwordx4 v[196:199], v9, s[28:31], s62 offen sc1\n"
    "s_mov_b32 s62, 0x6800\n"
    "buffer_load_dwordx4 v[200:203], v9, s[28:31], s62 offen sc1\n"
    "s_mov_b32 s62, 0x6c00\n"
    "buffer_load_dwordx4 v[204:207], v9, s[28:31], s62 offen sc1\n"
    "s_lshl_b32 s63, s40, 10\n"
    "s_mov_b32 s54, 0x31000\n"
    "s_add_u32 m0, s63, 0x7000\n"
    "s_nop 0\n"
    "buffer_load_dwordx4 v5, s[16:19], s54 offen lds\n"
    "s_lshl_b32 s63, s40, 10\n"
    "s_mov_b32 s54, 0x93000\n"
    "s_add_u32 m0, s63, 0x15000\n"
    "s_nop 0\n"
    "buffer_load_dwordx4 v5, s[16:19], s54 offen lds\n"
    "s_lshl_b32 s63, s40, 10\n"
    "s_mov_b32 s54, 0x38000\n"
    "s_add_u32 m0, s63, 0x8000\n"
    "s_nop 0\n"
    "buffer_load_dwordx4 v5, s[16:19], s54 offen lds\n"
    "s_lshl_b32 s63, s40, 10\n"
    "s_mov_b32 s54, 0x9a000\n"
    "s_add_u32 m0, s63, 0x16000\n"
    "s_nop 0\n"
    "buffer_load_dwordx4 v5, s[16:19], s54 offen lds\n"
    "s_lshl_b32 s63, s40, 10\n"
    "s_mov_b32 s54, 0x3f000\n"
    "s_add_u32 m0, s63, 0x9000\n"
    "s_nop 0\n"
    "buffer_load_dwordx4 v5, s[16:19], s54 offen lds\n"
    "s_lshl_b32 s63, s40, 10\n"
    "s_mov_b32 s54, 0xa1000\n"
    "s_add_u32 m0, s63, 0x17000\n"
    "s_nop 0\n"
    "buffer_load_dwordx4 v5, s[16:19], s54 offen lds\n"
    "s_lshl_b32 s63, s40, 10\n"
    "s_mov_b32 s54, 0x46000\n"
    "s_add_u32 m0, s63, 0xa000\n"
    "s_nop 0\n"
    "buffer_load_dwordx4 v5, s[16:19], s54 offen lds\n"
    "s_lshl_b32 s63, s40, 10\n"
    "s_mov_b32 s54, 0xa8000\n"
    "s_add_u32 m0, s63, 0x18000\n"
    "s_nop 0\n"
    "buffer_load_dwordx4 v5, s[16:19], s54 offen lds\n"
    "s_lshl_b32 s63, s40, 10\n"
    "s_mov_b32 s54, 0x4d000\n"
    "s_add_u32 m0, s63, 0xb000\n"
    "s_nop 0\n"
    "buffer_load_dwordx4 v5, s[16:19], s54 offen lds\n"
    "s_lshl_b32 s63, s40, 10\n"
    "s_mov_b32 s54, 0xaf000\n"
    "s_add_u32 m0, s63, 0x19000\n"
    "s_nop 0\n"
    "buffer_load_dwordx4 v5, s[16:19], s54 offen lds\n"
    "s_lshl_b32 s63, s40, 10\n"
    "s_mov_b32 s54, 0x54000\n"
    "s_add_u32 m0, s63, 0xc000\n"
    "s_nop 0\n"
    "buffer_load_dwordx4 v5, s[16:19], s54 offen lds\n"
    "s_lshl_b32 s63, s40, 10\n"
    "s_mov_b32 s54, 0xb6000\n"
    "s_add_u32 m0, s63, 0x1a000\n"
    "s_nop 0\n"
    "buffer_load_dwordx4 v5, s[16:19], s54 offen lds\n"
    "s_lshl_b32 s63, s40, 10\n"
    "s_mov_b32 s54, 0x5b000\n"
    "s_add_u32 m0, s63, 0xd000\n"
    "s_nop 0\n"
    "buffer_load_dwordx4 v5, s[16:19], s54 offen lds\n"
    "s_lshl_b32 s63, s40, 10\n"
    "s_mov_b32 s54, 0xbd000\n"
    "s_add_u32 m0, s63, 0x1b000\n"
    "s_nop 0\n"
    "buffer_load_dwordx4 v5, s[16:19], s54 offen lds\n"
    "s_waitcnt vmcnt(48)\n"
    "s_barrier\n"
    "ds_read_b64_tr_b16 v[64:65], v10\n"
    "ds_read_b64_tr_b16 v[66:67], v10 offset:512\n"
    "ds_read_b64_tr_b16 v[68:69], v11\n"
    "ds_read_b64_tr_b16 v[70:71], v11 offset:512\n"
    "ds_read_b64_tr_b16 v[72:73], v10 offset:2048\n"
    "ds_read_b64_tr_b16 v[74:75], v10 offset:2560\n"
    "ds_read_b64_tr_b16 v[76:77], v11 offset:2048\n"
    "ds_read_b64_tr_b16 v[78:79], v11 offset:2560\n"
    "ds_read_b64_tr_b16 v[80:81], v10 offset:4096\n"
    "ds_read_b64_tr_b16 v[82:83], v10 offset:4608\n"
    "s_waitcnt vmcnt(41) lgkmcnt(8)\n"
    "v_mfma_f32_32x32x16_bf16 v[32:47], v[64:67], v[96:99], 0\n"
    "s_waitcnt lgkmcnt(6)\n"
    "ds_read_b64_tr_b16 v[84:85], v11 offset:4096\n"
    "ds_read_b64_tr_b16 v[86:87], v11 offset:4608\n"
    "v_mfma_f32_32x32x16_bf16 v[48:63], v[68:71], v[96:99], 0\n"
    "s_waitcnt lgkmcnt(6)\n"
    "ds_read_b64_tr_b16 v[88:89], v10 offset:6144\n"
    "ds_read_b64_tr_b16 v[90:91], v10 offset:6656\n"
    "s_waitcnt vmcnt(40)\n"
    "v_mfma_f32_32x32x16_bf16 v[32:47], v[72:75], v[100:103], v[32:47]\n"
    "s_waitcnt lgkmcnt(6)\n"
    "ds_read_b64_tr_b16 v[92:93], v11 offset:6144\n"
    "ds_read_b64_tr_b16 v[94:95], v11 offset:6656\n"
    "v_mfma_f32_32x32x16_bf16 v[48:63], v[76:79], v[100:103], v[48:63]\n"
    "s_waitcnt lgkmcnt(6)\n"
    "ds_read_b64_tr_b16 v[64:65], v10 offset:8192\n"
    "ds_read_b64_tr_b16 v[66:67], v10 offset:8704\n"
    "s_waitcnt vmcnt(39)\n"
    "v_mfma_f32_32x32x16_bf16 v[32:47], v[80:83], v[104:107], v[32:47]\n"
    "s_waitcnt lgkmcnt(6)\n"
    "ds_read_b64_tr_b16 v[68:69], v11 offset:8192\n"
    "ds_read_b64_tr_b16 v[70:71], v11 offset:8704\n"
    "v_mfma_f32_32x32x16_bf16 v[48:63], v[84:87], v[104:107], v[48:63]\n"
    "s_waitcnt lgkmcnt(6)\n"
    "ds_read_b64_tr_b16 v[72:73], v10 offset:10240\n"
    "ds_read_b64_tr_b16 v[74:75], v10 offset:10752\n"
    "s_waitcnt vmcnt(38)\n"
    "v_mfma_f32_32x32x16_bf16 v[32:47], v[88:91], v[108:111], v[32:47]\n"
    "s_waitcnt lgkmcnt(6)\n"
    "ds_read_b64_tr_b16 v[76:77], v11 offset:10240\n"
    "ds_read_b64_tr_b16 v[78:79], v11 offset:10752\n"
    "v_mfma_f32_32x32x16_bf16 v[48:63], v[92:95], v[108:111], v[48:63]\n"
    "s_waitcnt lgkmcnt(6)\n"
    "ds_read_b64_tr_b16 v[80:81], v10 offset:12288\n"
    "ds_read_b64_tr_b16 v[82:83], v10 offset:12800\n"
    "s_waitcnt vmcnt(37)\n"
    "v_mfma_f32_32x32x16_bf16 v[32:47], v[64:67], v[112:115], v[32:47]\n"
    "s_waitcnt lgkmcnt(6)\n"
    "ds_read_b64_tr_b16 v[84:85], v11 offset:12288\n"
    "ds_read_b64_tr_b16 v[86:87], v11 offset:12800\n"
    "v_mfma_f32_32x32x16_bf16 v[48:63], v[68:71], v[112:115], v[48:63]\n"
    "s_waitcnt lgkmcnt(6)\n"
    "ds_read_b64_tr_b16 v[88:89], v10 offset:14336\n"
    "ds_read_b64_tr_b16 v[90:91], v10 offset:14848\n"
    "s_waitcnt vmcnt(36)\n"
    "v_mfma_f32_32x32x16_bf16 v[32:47], v[72:75], v[116:119], v[32:47]\n"
    "s_waitcnt lgkmcnt(6)\n"
    "ds_read_b64_tr_b16 v[92:93], v11 offset:14336\n"
    "ds_read_b64_tr_b16 v[94:95], v11 offset:14848\n"
    "v_mfma_f32_32x32x16_bf16 v[48:63], v[76:79], v[116:119], v[48:63]\n"
    "s_waitcnt lgkmcnt(6)\n"
    "ds_read_b64_tr_b16 v[64:65], v10 offset:16384\n"
    "ds_read_b64_tr_b16 v[66:67], v10 offset:16896\n"
    "s_waitcnt vmcnt(35)\n"
    "v_mfma_f32_32x32x16_bf16 v[32:47], v[80:83], v[120:123], v[32:47]\n"
    "s_waitcnt lgkmcnt(6)\n"
    "ds_read_b64_tr_b16 v[68:69], v11 offset:16384\n"
    "ds_read_b64_tr_b16 v[70:71], v11 offset:16896\n"
    "v_mfma_f32_32x32x16_bf16 v[48:63], v[84:87], v[120:123], v[48:63]\n"
    "s_waitcnt lgkmcnt(6)\n"
    "ds_read_b64_tr_b16 v[72:73], v10 offset:18432\n"
    "ds_read_b64_tr_b16 v[74:75], v10 offset:18944\n"
    "s_waitcnt vmcnt(34)\n"
    "v_mfma_f32_32x32x16_bf16 v[32:47], v[88:91], v[124:127], v[32:47]\n"
    "s_waitcnt lgkmcnt(6)\n"
    "ds_read_b64_tr_b16 v[76:77], v11 offset:18432\n"
    "ds_read_b64_tr_b16 v[78:79], v11 offset:18944\n"
    "v_mfma_f32_32x32x16_bf16 v[48:63], v[92:95], v[124:127], v[48:63]\n"
    "s_waitcnt lgkmcnt(6)\n"
    "ds_read_b64_tr_b16 v[80:81], v10 offset:20480\n"
    "ds_read_b64_tr_b16 v[82:83], v10 offset:20992\n"
    "s_waitcnt vmcnt(33)\n"
    "v_mfma_f32_32x32x16_bf16 v[32:47], v[64:67], v[128:131], v[32:47]\n"
    "s_waitcnt lgkmcnt(6)\n"
    "ds_read_b64_tr_b16 v[84:85], v11 offset:20480\n"
    "ds_read_b64_tr_b16 v[86:87], v11 offset:20992\n"
    "v_mfma_f32_32x32x16_bf16 v[48:63], v[68:71], v[128:131], v[48:63]\n"
    "s_waitcnt lgkmcnt(6)\n"
    "ds_read_b64_tr_b16 v[88:89], v10 offset:22528\n"
    "ds_read_b64_tr_b16 v[90:91], v10 offset:23040\n"
    "s_waitcnt vmcnt(32)\n"
    "v_mfma_f32_32x32x16_bf16 v[32:47], v[72:75], v[132:135], v[32:47]\n"
    "s_waitcnt lgkmcnt(6)\n"
    "ds_read_b64_tr_b16 v[92:93], v11 offset:22528\n"
    "ds_read_b64_tr_b16 v[94:95], v11 offset:23040\n"
    "v_mfma_f32_32x32x16_bf16 v[48:63], v[76:79], v[132:135], v[48:63]\n"
    "s_waitcnt lgkmcnt(6)\n"
    "ds_read_b64_tr_b16 v[64:65], v10 offset:24576\n"
    "ds_read_b64_tr_b16 v[66:67], v10 offset:25088\n"
    "s_waitcnt vmcnt(31)\n"
    "v_mfma_f32_32x32x16_bf16 v[32:47], v[80:83], v[136:139], v[32:47]\n"
    "s_waitcnt lgkmcnt(6)\n"
    "ds_read_b64_tr_b16 v[68:69], v11 offset:24576\n"
    "ds_read_b64_tr_b16 v[70:71], v11 offset:25088\n"
    "v_mfma_f32_32x32x16_bf16 v[48:63], v[84:87], v[136:139], v[48:63]\n"
    "s_waitcnt lgkmcnt(6)\n"
    "ds_read_b64_tr_b16 v[72:73], v10 offset:26624\n"
    "ds_read_b64_tr_b16 v[74:75], v10 offset:27136\n"
    "s_waitcnt vmcnt(30)\n"
    "v_mfma_f32_32x32x16_bf16 v[32:47], v[88:91], v[140:143], v[32:47]\n"
    "s_waitcnt lgkmcnt(6)\n"
    "ds_read_b64_tr_b16 v[76:77], v11 offset:26624\n"
    "ds_read_b64_tr_b16 v[78:79], v11 offset:27136\n"
    "v_mfma_f32_32x32x16_bf16 v[48:63], v[92:95], v[140:143], v[48:63]\n"
    "s_waitcnt vmcnt(0) lgkmcnt(6)\n"
    "s_barrier\n"
    "ds_read_b64_tr_b16 v[80:81], v10 offset:28672\n"
    "ds_read_b64_tr_b16 v[82:83], v10 offset:29184\n"
    "v_mfma_f32_32x32x16_bf16 v[32:47], v[64:67], v[144:147], v[32:47]\n"
    "s_waitcnt lgkmcnt(6)\n"
    "ds_read_b64_tr_b16 v[84:85], v11 offset:28672\n"
    "ds_read_b64_tr_b16 v[86:87], v11 offset:29184\n"
    "v_mfma_f32_32x32x16_bf16 v[48:63], v[68:71], v[144:147], v[48:63]\n"
    "s_waitcnt lgkmcnt(6)\n"
    "ds_read_b64_tr_b16 v[88:89], v10 offset:30720\n"
    "ds_read_b64_tr_b16 v[90:91], v10 offset:31232\n"
    "v_mfma_f32_32x32x16_bf16 v[32:47], v[72:75], v[148:151], v[32:47]\n"
    "s_waitcnt lgkmcnt(6)\n"
    "ds_read_b64_tr_b16 v[92:93], v11 offset:30720\n"
    "ds_read_b64_tr_b16 v[94:95], v11 offset:31232\n"
    "v_mfma_f32_32x32x16_bf16 v[48:63], v[76:79], v[148:151], v[48:63]\n"
    "s_waitcnt lgkmcnt(6)\n"
    "ds_read_b64_tr_b16 v[64:65], v10 offset:32768\n"
    "ds_read_b64_tr_b16 v[66:67], v10 offset:33280\n"
    "v_mfma_f32_32x32x16_bf16 v[32:47], v[80:83], v[152:155], v[32:47]\n"
    "s_waitcnt lgkmcnt(6)\n"
    "ds_read_b64_tr_b16 v[68:69], v11 offset:32768\n"
    "ds_read_b64_tr_b16 v[70:71], v11 offset:33280\n"
    "v_mfma_f32_32x32x16_bf16 v[48:63], v[84:87], v[152:155], v[48:63]\n"
    "s_waitcnt lgkmcnt(6)\n"
    "ds_read_b64_tr_b16 v[72:73], v10 offset:34816\n"
    "ds_read_b64_tr_b16 v[74:75], v10 offset:35328\n"
    "v_mfma_f32_32x32x16_bf16 v[32:47], v[88:91], v[156:159], v[32:47]\n"
    "s_waitcnt lgkmcnt(6)\n"
    "ds_read_b64_tr_b16 v[76:77], v11 offset:34816\n"
    "ds_read_b64_tr_b16 v[78:79], v11 offset:35328\n"
    "v_mfma_f32_32x32x16_bf16 v[48:63], v[92:95], v[156:159], v[48:63]\n"
    "s_waitcnt lgkmcnt(6)\n"
    "ds_read_b64_tr_b16 v[80:81], v10 offset:36864\n"
    "ds_read_b64_tr_b16 v[82:83], v10 offset:37376\n"
    "v_mfma_f32_32x32x16_bf16 v[32:47], v[64:67], v[160:163], v[32:47]\n"
    "s_waitcnt lgkmcnt(6)\n"
    "ds_read_b64_tr_b16 v[84:85], v11 offset:36864\n"
    "ds_read_b64_tr_b16 v[86:87], v11 offset:37376\n"
    "v_mfma_f32_32x32x16_bf16 v[48:63], v[68:71], v[160:163], v[48:63]\n"
    "s_waitcnt lgkmcnt(6)\n"
    "ds_read_b64_tr_b16 v[88:89], v10 offset:38912\n"
    "ds_read_b64_tr_b16 v[90:91], v10 offset:39424\n"
    "v_mfma_f32_32x32x16_bf16 v[32:47], v[72:75], v[164:167], v[32:47]\n"
    "s_waitcnt lgkmcnt(6)\n"
    "ds_read_b64_tr_b16 v[92:93], v11 offset:38912\n"
    "ds_read_b64_tr_b16 v[94:95], v11 offset:39424\n"
    "v_mfma_f32_32x32x16_bf16 v[48:63], v[76:79], v[164:167], v[48:63]\n"
    "s_waitcnt lgkmcnt(6)\n"
    "ds_read_b64_tr_b16 v[64:65], v10 offset:40960\n"
    "ds_read_b64_tr_b16 v[66:67], v10 offset:41472\n"
    "v_mfma_f32_32x32x16_bf16 v[32:47], v[80:83], v[168:171], v[32:47]\n"
    "s_waitcnt lgkmcnt(6)\n"
    "ds_read_b64_tr_b16 v[68:69], v11 offset:40960\n"
    "ds_read_b64_tr_b16 v[70:71], v11 offset:41472\n"
    "v_mfma_f32_32x32x16_bf16 v[48:63], v[84:87], v[168:171], v[48:63]\n"
    "s_waitcnt lgkmcnt(6)\n"
    "ds_read_b64_tr_b16 v[72:73], v10 offset:43008\n"
    "ds_read_b64_tr_b16 v[74:75], v10 offset:43520\n"
    "v_mfma_f32_32x32x16_bf16 v[32:47], v[88:91], v[172:175], v[32:47]\n"
    "s_waitcnt lgkmcnt(6)\n"
    "ds_read_b64_tr_b16 v[76:77], v11 offset:43008\n"
    "ds_read_b64_tr_b16 v[78:79], v11 offset:43520\n"
    "v_mfma_f32_32x32x16_bf16 v[48:63], v[92:95], v[172:175], v[48:63]\n"
    "s_waitcnt lgkmcnt(6)\n"
    "ds_read_b64_tr_b16 v[80:81], v10 offset:45056\n"
    "ds_read_b64_tr_b16 v[82:83], v10 offset:45568\n"
    "v_mfma_f32_32x32x16_bf16 v[32:47], v[64:67], v[176:179], v[32:47]\n"
    "s_waitcnt lgkmcnt(6)\n"
    "ds_read_b64_tr_b16 v[84:85], v11 offset:45056\n"
    "ds_read_b64_tr_b16 v[86:87], v11 offset:45568\n"
    "v_mfma_f32_32x32x16_bf16 v[48:63], v[68:71], v[176:179], v[48:63]\n"
    "s_waitcnt lgkmcnt(6)\n"
    "ds_read_b64_tr_b16 v[88:89], v10 offset:47104\n"
    "ds_read_b64_tr_b16 v[90:91], v10 offset:47616\n"
    "v_mfma_f32_32x32x16_bf16 v[32:47], v[72:75], v[180:183], v[32:47]\n"
    "s_waitcnt lgkmcnt(6)\n"
    "ds_read_b64_tr_b16 v[92:93], v11 offset:47104\n"
    "ds_read_b64_tr_b16 v[94:95], v11 offset:47616\n"
    "v_mfma_f32_32x32x16_bf16 v[48:63], v[76:79], v[180:183], v[48:63]\n"
    "s_waitcnt lgkmcnt(6)\n"
    "ds_read_b64_tr_b16 v[64:65], v10 offset:49152\n"
    "ds_read_b64_tr_b16 v[66:67], v10 offset:49664\n"
    "v_mfma_f32_32x32x16_bf16 v[32:47], v[80:83], v[184:187], v[32:47]\n"
    "s_waitcnt lgkmcnt(6)\n"
    "ds_read_b64_tr_b16 v[68:69], v11 offset:49152\n"
    "ds_read_b64_tr_b16 v[70:71], v11 offset:49664\n"
    "v_mfma_f32_32x32x16_bf16 v[48:63], v[84:87], v[184:187], v[48:63]\n"
    "s_waitcnt lgkmcnt(6)\n"
    "ds_read_b64_tr_b16 v[72:73], v10 offset:51200\n"
    "ds_read_b64_tr_b16 v[74:75], v10 offset:51712\n"
    "v_mfma_f32_32x32x16_bf16 v[32:47], v[88:91], v[188:191], v[32:47]\n"
    "s_waitcnt lgkmcnt(6)\n"
    "ds_read_b64_tr_b16 v[76:77], v11 offset:51200\n"
    "ds_read_b64_tr_b16 v[78:79], v11 offset:51712\n"
    "v_mfma_f32_32x32x16_bf16 v[48:63], v[92:95], v[188:191], v[48:63]\n"
    "s_waitcnt lgkmcnt(6)\n"
    "ds_read_b64_tr_b16 v[80:81], v10 offset:53248\n"
    "ds_read_b64_tr_b16 v[82:83], v10 offset:53760\n"
    "v_mfma_f32_32x32x16_bf16 v[32:47], v[64:67], v[192:195], v[32:47]\n"
    "s_waitcnt lgkmcnt(6)\n"
    "ds_read_b64_tr_b16 v[84:85], v11 offset:53248\n"
    "ds_read_b64_tr_b16 v[86:87], v11 offset:53760\n"
    "v_mfma_f32_32x32x16_bf16 v[48:63], v[68:71], v[192:195], v[48:63]\n"
    "s_waitcnt lgkmcnt(6)\n"
    "ds_read_b64_tr_b16 v[88:89], v10 offset:55296\n"
    "ds_read_b64_tr_b16 v[90:91], v10 offset:55808\n"
    "v_mfma_f32_32x32x16_bf16 v[32:47], v[72:75], v[196:199], v[32:47]\n"
    "s_waitcnt lgkmcnt(6)\n"
    "ds_read_b64_tr_b16 v[92:93], v11 offset:55296\n"
    "ds_read_b64_tr_b16 v[94:95], v11 offset:55808\n"
    "v_mfma_f32_32x32x16_bf16 v[48:63], v[76:79], v[196:199], v[48:63]\n"
    "s_waitcnt lgkmcnt(6)\n"
    "v_mfma_f32_32x32x16_bf16 v[32:47], v[80:83], v[200:203], v[32:47]\n"
    "s_waitcnt lgkmcnt(4)\n"
    "v_mfma_f32_32x32x16_bf16 v[48:63], v[84:87], v[200:203], v[48:63]\n"
    "s_waitcnt lgkmcnt(2)\n"
    "v_mfma_f32_32x32x16_bf16 v[32:47], v[88:91], v[204:207], v[32:47]\n"
    "s_waitcnt lgkmcnt(0)\n"
    "v_mfma_f32_32x32x16_bf16 v[48:63], v[92:95], v[204:207], v[48:63]\n"
    "s_lshl_b32 s58, s40, 12\n"
    "s_add_u32 s58, s58, 0x1c000\n"
    "v_lshlrev_b32 v14, 4, v1\n"
    "v_add_u32 v233, s58, v14\n"
    "s_xor_b32 s59, s40, 2\n"
    "s_lshl_b32 s59, s59, 12\n"
    "s_add_u32 s59, s59, 0x1c000\n"
    "v_add_u32 v234, s59, v14\n"
    "s_nop 3\n"
    "ds_write_b128 v233, v[48:51]\n"
    "ds_write_b128 v233, v[52:55] offset:1024\n"
    "ds_write_b128 v233, v[56:59] offset:2048\n"
    "ds_write_b128 v233, v[60:63] offset:3072\n"
    "s_waitcnt lgkmcnt(0)\n"
    "s_barrier\n"
    "ds_read_b128 v[236:239], v234\n"
    "ds_read_b128 v[240:243], v234 offset:1024\n"
    "ds_read_b128 v[244:247], v234 offset:2048\n"
    "ds_read_b128 v[248:251], v234 offset:3072\n"
    "s_waitcnt lgkmcnt(3)\n"
    "v_pk_add_f32 v[32:33], v[32:33], v[236:237]\n"
    "v_pk_add_f32 v[34:35], v[34:35], v[238:239]\n"
    "s_waitcnt lgkmcnt(2)\n"
    "v_pk_add_f32 v[36:37], v[36:37], v[240:241]\n"
    "v_pk_add_f32 v[38:39], v[38:39], v[242:243]\n"
    "s_waitcnt lgkmcnt(1)\n"
    "v_pk_add_f32 v[40:41], v[40:41], v[244:245]\n"
    "v_pk_add_f32 v[42:43], v[42:43], v[246:247]\n"
    "s_waitcnt lgkmcnt(0)\n"
    "v_pk_add_f32 v[44:45], v[44:45], v[248:249]\n"
    "v_pk_add_f32 v[46:47], v[46:47], v[250:251]\n"
    "v_lshlrev_b32 v14, 16, v208\n"
    "v_and_b32 v15, s7, v208\n"
    "v_pk_fma_f32 v[32:33], v[32:33], v[216:217], v[14:15]\n"
    "v_cvt_pk_bf16_f32 v208, v32, v33\n"
    "v_lshlrev_b32 v14, 16, v209\n"
    "v_and_b32 v15, s7, v209\n"
    "v_pk_fma_f32 v[34:35], v[34:35], v[218:219], v[14:15]\n"
    "v_cvt_pk_bf16_f32 v209, v34, v35\n"
    "v_lshlrev_b32 v14, 16, v210\n"
    "v_and_b32 v15, s7, v210\n"
    "v_pk_fma_f32 v[36:37], v[36:37], v[220:221], v[14:15]\n"
    "v_cvt_pk_bf16_f32 v210, v36, v37\n"
    "v_lshlrev_b32 v14, 16, v211\n"
    "v_and_b32 v15, s7, v211\n"
    "v_pk_fma_f32 v[38:39], v[38:39], v[222:223], v[14:15]\n"
    "v_cvt_pk_bf16_f32 v211, v38, v39\n"
    "buffer_store_dwordx4 v[208:211], v232, s[24:27], 0 offen sc1\n"
    "v_lshlrev_b32 v14, 16, v212\n"
    "v_and_b32 v15, s7, v212\n"
    "v_pk_fma_f32 v[40:41], v[40:41], v[224:225], v[14:15]\n"
    "v_cvt_pk_bf16_f32 v212, v40, v41\n"
    "v_lshlrev_b32 v14, 16, v213\n"
    "v_and_b32 v15, s7, v213\n"
    "v_pk_fma_f32 v[42:43], v[42:43], v[226:227], v[14:15]\n"
    "v_cvt_pk_bf16_f32 v213, v42, v43\n"
    "v_lshlrev_b32 v14, 16, v214\n"
    "v_and_b32 v15, s7, v214\n"
    "v_pk_fma_f32 v[44:45], v[44:45], v[228:229], v[14:15]\n"
    "v_cvt_pk_bf16_f32 v214, v44, v45\n"
    "v_lshlrev_b32 v14, 16, v215\n"
    "v_and_b32 v15, s7, v215\n"
    "v_pk_fma_f32 v[46:47], v[46:47], v[230:231], v[14:15]\n"
    "v_cvt_pk_bf16_f32 v215, v46, v47\n"
    "buffer_store_dwordx4 v[212:215], v232, s[24:27], 0 offen offset:32 sc1\n"
    "s_waitcnt vmcnt(0) lgkmcnt(0)\n"
    ".LF_t5end_29:\n"
    "s_nop 4\n"
    "s_waitcnt vmcnt(0) lgkmcnt(0)\n"
    "s_cmp_lg_u32 s2, 0\n"
    "s_cbranch_scc1 .LF_end_33\n"
    "s_cmp_lg_u32 s40, 0\n"
    "s_cbranch_scc1 .LF_end_33\n"
    "s_mov_b32 exec_lo, 0x1\n"
    "s_mov_b32 exec_hi, 0x0\n"
    "v_mov_b32 v8, s90\n"
    "v_mov_b32 v9, s91\n"
    "v_mov_b32 v10, 0\n"
    "buffer_store_dwordx2 v[8:9], v10, s[28:31], 0 offen sc1\n"
    "s_mov_b64 exec, -1\n"
    ".LF_end_33:\n"
    "s_waitcnt vmcnt(0) lgkmcnt(0)\n"
    ".LCODE_END:\n"
    :: "{s[0:1]}"(ka), "{s2}"(bid), "{v0}"(tid)
    : "v1", "v2", "v3", "v4", "v5", "v6", "v7", "v8", "v9", "v10", "v11", "v12", "v13", "v14", "v15", "v16", "v17", "v18", "v19", "v20", "v21", "v22", "v23", "v24", "v25", "v26", "v27", "v28", "v29", "v30", "v31", "v32", "v33", "v34", "v35", "v36", "v37", "v38", "v39", "v40", "v41", "v42", "v43", "v44", "v45", "v46", "v47", "v48", "v49", "v50", "v51", "v52", "v53", "v54", "v55", "v56", "v57", "v58", "v59", "v60", "v61", "v62", "v63", "v64", "v65", "v66", "v67", "v68", "v69", "v70", "v71", "v72", "v73", "v74", "v75", "v76", "v77", "v78", "v79", "v80", "v81", "v82", "v83", "v84", "v85", "v86", "v87", "v88", "v89", "v90", "v91", "v92", "v93", "v94", "v95", "v96", "v97", "v98", "v99", "v100", "v101", "v102", "v103", "v104", "v105", "v106", "v107", "v108", "v109", "v110", "v111", "v112", "v113", "v114", "v115", "v116", "v117", "v118", "v119", "v120", "v121", "v122", "v123", "v124", "v125", "v126", "v127", "v128", "v129", "v130", "v131", "v132", "v133", "v134", "v135", "v136", "v137", "v138", "v139", "v140", "v141", "v142", "v143", "v144", "v145", "v146", "v147", "v148", "v149", "v150", "v151", "v152", "v153", "v154", "v155", "v156", "v157", "v158", "v159", "v160", "v161", "v162", "v163", "v164", "v165", "v166", "v167", "v168", "v169", "v170", "v171", "v172", "v173", "v174", "v175", "v176", "v177", "v178", "v179", "v180", "v181", "v182", "v183", "v184", "v185", "v186", "v187", "v188", "v189", "v190", "v191", "v192", "v193", "v194", "v195", "v196", "v197", "v198", "v199", "v200", "v201", "v202", "v203", "v204", "v205", "v206", "v207", "v208", "v209", "v210", "v211", "v212", "v213", "v214", "v215", "v216", "v217", "v218", "v219", "v220", "v221", "v222", "v223", "v224", "v225", "v226", "v227", "v228", "v229", "v230", "v231", "v232", "v233", "v234", "v235", "v236", "v237", "v238", "v239", "v240", "v241", "v242", "v243", "v244", "v245", "v246", "v247", "v248", "v249", "v250", "v251", "v252", "v253", "v254", "v255", "a0", "a1", "a2", "a3", "a4", "a5", "a6", "a7", "a8", "a9", "a10", "a11", "a12", "a13", "a14", "a15", "a16", "a17", "a18", "a19", "a20", "a21", "a22", "a23", "a24", "a25", "a26", "a27", "a28", "a29", "a30", "a31", "a32", "a33", "a34", "a35", "a36", "a37", "a38", "a39", "a40", "a41", "a42", "a43", "a44", "a45", "a46", "a47", "a48", "a49", "a50", "a51", "a52", "a53", "a54", "a55", "a56", "a57", "a58", "a59", "a60", "a61", "a62", "a63", "a64", "a65", "a66", "a67", "a68", "a69", "a70", "a71", "a72", "a73", "a74", "a75", "a76", "a77", "a78", "a79", "a80", "a81", "a82", "a83", "a84", "a85", "a86", "a87", "a88", "a89", "a90", "a91", "a92", "a93", "a94", "a95", "a96", "a97", "a98", "a99", "a100", "a101", "a102", "a103", "a104", "a105", "a106", "a107", "a108", "a109", "a110", "a111", "a112", "a113", "a114", "a115", "a116", "a117", "a118", "a119", "a120", "a121", "a122", "a123", "a124", "a125", "a126", "a127", "a128", "a129", "a130", "a131", "a132", "a133", "a134", "a135", "a136", "a137", "a138", "a139", "a140", "a141", "a142", "a143", "a144", "a145", "a146", "a147", "a148", "a149", "a150", "a151", "a152", "a153", "a154", "a155", "a156", "a157", "a158", "a159", "a160", "a161", "a162", "a163", "a164", "a165", "a166", "a167", "a168", "a169", "a170", "a171", "a172", "a173", "a174", "a175", "a176", "a177", "a178", "a179", "a180", "a181", "a182", "a183", "a184", "a185", "a186", "a187", "a188", "a189", "a190", "a191", "a192", "a193", "a194", "a195", "a196", "a197", "a198", "a199", "a200", "a201", "a202", "a203", "a204", "a205", "a206", "a207", "a208", "a209", "a210", "a211", "a212", "a213", "a214", "a215", "a216", "a217", "a218", "a219", "a220", "a221", "a222", "a223", "a224", "a225", "a226", "a227", "a228", "a229", "a230", "a231", "a232", "a233", "a234", "a235", "a236", "a237", "a238", "a239", "a240", "a241", "a242", "a243", "a244", "a245", "a246", "a247", "a248", "a249", "a250", "a251", "a252", "a253", "a254", "a255", "s3", "s4", "s5", "s6", "s7", "s8", "s9", "s10", "s11", "s12", "s13", "s14", "s15", "s16", "s17", "s18", "s19", "s20", "s21", "s22", "s23", "s24", "s25", "s26", "s27", "s28", "s29", "s30", "s31", "s33", "s34", "s35", "s36", "s37", "s38", "s39", "s40", "s41", "s42", "s43", "s44", "s45", "s46", "s47", "s48", "s49", "s50", "s51", "s52", "s53", "s54", "s55", "s56", "s57", "s58", "s59", "s60", "s61", "s62", "s63", "s64", "s65", "s66", "s67", "s68", "s69", "s70", "s71", "s72", "s73", "s74", "s75", "s76", "s77", "s78", "s79", "s80", "s81", "s82", "s83", "s84", "s85", "s86", "s87", "s88", "s89", "s90", "s91", "s92", "s93", "s94", "s95", "s96", "s97", "s98", "s99", "vcc", "memory");
}

extern "C" void kernel_launch(void* const* d_in, const int* in_sizes, int n_in, void* d_out, int out_size, void* d_ws, size_t ws_size, hipStream_t stream) {
  (void)hipFuncSetAttribute((const void*)fused_layer, hipFuncAttributeMaxDynamicSharedMemorySize, 163840);
  fused_layer<<<256, 256, 163840, stream>>>(d_in[0], d_in[1], d_in[2], d_in[3], d_in[4], d_in[6], d_in[7], d_in[8], d_in[9], d_in[10], d_in[11], d_out, d_ws);
}
```
